```python
import torch

def _softmax(x, axis=-1):
    return torch.softmax(x, dim=axis)
def _gelu(x):
    return torch.nn.functional.gelu(x, approximate="tanh")
def _silu(x):
    return torch.nn.functional.silu(x)
def _sigmoid(x):
    return torch.sigmoid(x)
def _layernorm(x, axis=-1, eps=1e-5):
    m = x.mean(dim=axis, keepdim=True)
    v = x.var(dim=axis, keepdim=True, unbiased=False)
    return (x - m) / torch.sqrt(v + eps)
def _scatter_add(tgt, idx, src, axis=0):
    return tgt.clone().index_add_(axis, idx, src)
def _segment_sum(src, seg, n):
    out = torch.zeros((n,) + src.shape[1:], dtype=src.dtype)
    return out.index_add_(0, seg, src)
def _pad(x, pw):
    flat = tuple(v for pair in reversed(pw) for v in pair)
    return torch.nn.functional.pad(x, flat)

kernel_name = 'concat_heads__mha_block_cached__gqa_block_cached__near_ridge'

def setup_inputs():

    _w = (lambda a: (lambda t: t.to(torch.bfloat16) if t.is_floating_point() else t)(torch.as_tensor(a)))
    def _A_setup():
        B, Sq, D = 16, 144, 640
        H, Sk, Dh = 32, 512, 192
        return {
            "x": _w((lambda *s: torch.randn(*s, dtype=torch.bfloat16) * 0.8)(B, Sq, D)),
            "k_cache": _w((lambda *s: torch.randn(*s, dtype=torch.bfloat16) * 0.8)(B, H, Sk, Dh)),
            "v_cache": _w((lambda *s: torch.randn(*s, dtype=torch.bfloat16) * 0.05)(B, H, Sk, Dh)),
            "wq": _w((lambda *s: torch.randn(*s, dtype=torch.bfloat16) * 0.8)(D, H * Dh) * (D ** -0.5)),
            "wo": _w((lambda *s: torch.randn(*s, dtype=torch.bfloat16) * 0.05)(H * Dh, D) * ((H * Dh) ** -0.5)),
        }
    _A_in = _A_setup()

    def _B_setup():
        B, Sq, D = 16, 144, 640
        Hq, Hkv, Sk, Dh = 32, 32, 512, 192
        assert Hq % Hkv == 0, (Hq, Hkv)
        return {
            "x": _w((lambda *s: torch.randn(*s, dtype=torch.bfloat16) * 0.8)(B, Sq, D)),
            "k_cache": _w((lambda *s: torch.randn(*s, dtype=torch.bfloat16) * 0.8)(B, Hkv, Sk, Dh)),
            "v_cache": _w((lambda *s: torch.randn(*s, dtype=torch.bfloat16) * 0.05)(B, Hkv, Sk, Dh)),
            "wq": _w((lambda *s: torch.randn(*s, dtype=torch.bfloat16) * 0.8)(D, Hq * Dh) * (D ** -0.5)),
            "wo": _w((lambda *s: torch.randn(*s, dtype=torch.bfloat16) * 0.05)(Hq * Dh, D) * ((Hq * Dh) ** -0.5)),
        }
    _B_in = _B_setup()

    return {
        "A_x": _A_in["x"],
        "A_k_cache": _A_in["k_cache"],
        "A_v_cache": _A_in["v_cache"],
        "A_wq": _A_in["wq"],
        "A_wo": _A_in["wo"],
        "B_x": _B_in["x"],
        "B_k_cache": _B_in["k_cache"],
        "B_v_cache": _B_in["v_cache"],
        "B_wq": _B_in["wq"],
        "B_wo": _B_in["wo"],
    }

def reference(A_x, A_k_cache, A_v_cache, A_wq, A_wo, B_x, B_k_cache, B_v_cache, B_wq, B_wo):
    def _A_ref(x, k_cache, v_cache, wq, wo):
        B, Sq, D = x.shape
        H, Dh = 32, 192
        q = (lambda x, *d: x.permute(*d))((x @ wq).reshape(B, Sq, H, Dh), 0, 2, 1, 3)
        scores = torch.einsum("bhqd,bhkd->bhqk", q, k_cache) * (Dh ** -0.5)
        p = _softmax(scores, axis=-1)
        o = torch.einsum("bhqk,bhkd->bhqd", p, v_cache)
        return (lambda x, *d: x.permute(*d))(o, 0, 2, 1, 3).reshape(B, Sq, H * Dh) @ wo

    def _B_ref(x, k_cache, v_cache, wq, wo):
        B, Sq, D = x.shape
        Hq, Dh = 32, 192
        Hkv = k_cache.shape[1]
        rep = Hq // Hkv
        q = (lambda x, *d: x.permute(*d))((x @ wq).reshape(B, Sq, Hq, Dh), 0, 2, 1, 3)
        k = (lambda x, n, axis: torch.repeat_interleave(x, n, dim=axis))(k_cache, rep, axis=1)
        v = (lambda x, n, axis: torch.repeat_interleave(x, n, dim=axis))(v_cache, rep, axis=1)
        scores = torch.einsum("bhqd,bhkd->bhqk", q, k) * (Dh ** -0.5)
        p = _softmax(scores, axis=-1)
        o = torch.einsum("bhqk,bhkd->bhqd", p, v)
        return (lambda x, *d: x.permute(*d))(o, 0, 2, 1, 3).reshape(B, Sq, Hq * Dh) @ wo

    a = _A_ref(A_x, A_k_cache, A_v_cache, A_wq, A_wo)
    b = _B_ref(B_x, B_k_cache, B_v_cache, B_wq, B_wo)
    return torch.concatenate([a, b], axis=-1)
```

```cpp
#include <hip/hip_runtime.h>
#include <hip/hip_bf16.h>
#include <stdint.h>

typedef short bf16x8 __attribute__((ext_vector_type(8)));
typedef float f32x4 __attribute__((ext_vector_type(4)));
typedef unsigned short u16;

static constexpr int NB = 16, SQ = 144, DM = 640, NH = 32, SK = 512, DH = 192, HD = NH * DH;

__device__ __forceinline__ u16 f2bf(float x) { __hip_bfloat16 b = __float2bfloat16(x); return *reinterpret_cast<u16*>(&b); }

__global__ __launch_bounds__(64) void gemm_naive(const u16* A, const u16* B, u16* C, int K, int N, int ldc) {
  int lane = threadIdx.x, r = lane & 15, g = lane >> 4;
  int m0 = blockIdx.y * 16, n0 = blockIdx.x * 16;
  f32x4 acc = {0, 0, 0, 0};
  for (int k0 = 0; k0 < K; k0 += 32) {
    bf16x8 a = *(const bf16x8*)(A + (size_t)(m0 + r) * K + k0 + 8 * g);
    bf16x8 b;
    for (int j = 0; j < 8; ++j) b[j] = (short)B[(size_t)(k0 + 8 * g + j) * N + n0 + r];
    acc = __builtin_amdgcn_mfma_f32_16x16x32_bf16(a, b, acc, 0, 0, 0);
  }
  for (int j = 0; j < 4; ++j) C[(size_t)(m0 + 4 * g + j) * ldc + n0 + r] = f2bf(acc[j]);
}

__global__ __launch_bounds__(64) void attn_naive(const u16* Q, const u16* Kc, const u16* Vc, u16* O) {
  int lane = threadIdx.x, r = lane & 15, g = lane >> 4;
  int qt = blockIdx.x, h = blockIdx.y, b = blockIdx.z;
  const u16* Qp = Q + (size_t)(b * SQ + qt * 16 + r) * HD + h * DH;
  const u16* Kp = Kc + (size_t)(b * NH + h) * SK * DH;
  const u16* Vp = Vc + (size_t)(b * NH + h) * SK * DH;
  bf16x8 qf[6];
#pragma unroll
  for (int s = 0; s < 6; ++s) qf[s] = *(const bf16x8*)(Qp + 32 * s + 8 * g);
  f32x4 S[32];
#pragma unroll
  for (int t = 0; t < 32; ++t) {
    f32x4 acc = {0, 0, 0, 0};
#pragma unroll
    for (int s = 0; s < 6; ++s) {
      bf16x8 a = *(const bf16x8*)(Kp + (size_t)(16 * t + r) * DH + 32 * s + 8 * g);
      acc = __builtin_amdgcn_mfma_f32_16x16x32_bf16(a, qf[s], acc, 0, 0, 0);
    }
    S[t] = acc;
  }
  float m = -1e30f;
#pragma unroll
  for (int t = 0; t < 32; ++t)
#pragma unroll
    for (int j = 0; j < 4; ++j) m = fmaxf(m, S[t][j]);
  m = fmaxf(m, __shfl_xor(m, 16));
  m = fmaxf(m, __shfl_xor(m, 32));
  const float c = 0.07216878364870322f * 1.4426950408889634f;
  float l = 0;
#pragma unroll
  for (int t = 0; t < 32; ++t)
#pragma unroll
    for (int j = 0; j < 4; ++j) { float p = __builtin_amdgcn_exp2f((S[t][j] - m) * c); S[t][j] = p; l += p; }
  l += __shfl_xor(l, 16);
  l += __shfl_xor(l, 32);
  f32x4 o[12];
#pragma unroll
  for (int t = 0; t < 12; ++t) o[t] = (f32x4){0, 0, 0, 0};
#pragma unroll
  for (int i = 0; i < 16; ++i) {
    bf16x8 pb;
#pragma unroll
    for (int j = 0; j < 4; ++j) { pb[j] = (short)f2bf(S[2 * i][j]); pb[4 + j] = (short)f2bf(S[2 * i + 1][j]); }
#pragma unroll
    for (int t = 0; t < 12; ++t) {
      bf16x8 a;
#pragma unroll
      for (int j = 0; j < 4; ++j) {
        a[j] = (short)Vp[(size_t)(32 * i + 4 * g + j) * DH + 16 * t + r];
        a[4 + j] = (short)Vp[(size_t)(32 * i + 16 + 4 * g + j) * DH + 16 * t + r];
      }
      o[t] = __builtin_amdgcn_mfma_f32_16x16x32_bf16(a, pb, o[t], 0, 0, 0);
    }
  }
  float inv = 1.0f / l;
  u16* Op = O + (size_t)(b * SQ + qt * 16 + r) * HD + h * DH;
#pragma unroll
  for (int t = 0; t < 12; ++t)
#pragma unroll
    for (int j = 0; j < 4; ++j) Op[16 * t + 4 * g + j] = f2bf(o[t][j] * inv);
}

extern "C" void kernel_launch(void* const* d_in, const int* in_sizes, int n_in, void* d_out, int out_size, void* d_ws,
                              size_t ws_size, hipStream_t stream) {
  u16* ws = (u16*)d_ws;
  const size_t QSZ = (size_t)NB * SQ * HD;
  for (int br = 0; br < 2; ++br) {
    const u16* x = (const u16*)d_in[5 * br + 0];
    const u16* kc = (const u16*)d_in[5 * br + 1];
    const u16* vc = (const u16*)d_in[5 * br + 2];
    const u16* wq = (const u16*)d_in[5 * br + 3];
    const u16* wo = (const u16*)d_in[5 * br + 4];
    u16* Q = ws + (size_t)(2 * br) * QSZ;
    u16* O = ws + (size_t)(2 * br + 1) * QSZ;
    gemm_naive<<<dim3(HD / 16, NB * SQ / 16), 64, 0, stream>>>(x, wq, Q, DM, HD, HD);
    attn_naive<<<dim3(9, NH, NB), 64, 0, stream>>>(Q, kc, vc, O);
    gemm_naive<<<dim3(DM / 16, NB * SQ / 16), 64, 0, stream>>>(O, wo, (u16*)d_out + br * DM, HD, DM, 2 * DM);
  }
}
```

```cpp
#include <hip/hip_runtime.h>
#include <stdint.h>

__global__ __launch_bounds__(256, 1) void k1_attn(const void* xA, const void* kA, const void* vA, const void* wqA, const void* xB, const void* kB, const void* vB, const void* wqB, void* O) {
  unsigned tid = threadIdx.x;
  asm volatile(R"ASM(
s_getpc_b64 s[12:13]
K1_pcw:
v_and_b32 v1, 63, v0
v_lshrrev_b32 v2, 6, v0
v_lshlrev_b32 v1, 7, v1
v_lshl_add_u32 v1, v2, 14, v1
v_add_u32 v2, 8192, v1
v_min_u32 v1, K1_end-K1_pcw-4, v1
v_min_u32 v2, K1_end-K1_pcw-4, v2
v_and_b32 v1, -4, v1
v_and_b32 v2, -4, v2
global_load_dword v3, v1, s[12:13]
global_load_dword v4, v2, s[12:13]
; prologue
v_and_b32 v108, 63, v0
v_lshrrev_b32 v111, 6, v0
s_nop 0
v_readfirstlane_b32 s19, v111
s_and_b32 s67, s18, 7
s_lshr_b32 s88, s67, 2
s_lshl_b32 s90, s88, 5
s_load_dwordx8 s[20:27], s[16:17], s90
s_load_dwordx2 s[28:29], s[16:17], 0x40
s_and_b32 s67, s67, 3
s_lshl_b32 s67, s67, 3
s_lshr_b32 s68, s18, 3
s_and_b32 s89, s68, 7
s_add_u32 s86, s67, s89
s_lshr_b32 s87, s68, 3
v_and_b32 v109, 15, v108
v_lshrrev_b32 v110, 4, v108
v_lshrrev_b32 v119, 3, v108
v_and_b32 v120, 7, v108
v_lshrrev_b32 v121, 4, v108
v_mul_u32_u24 v111, 384, v119
v_xor_b32 v112, v120, v121
v_xor_b32 v112, 0, v112
v_lshl_add_u32 v112, v112, 4, v111
v_add_u32 v232, 4096, v112
v_lshlrev_b32 v112, 1, v121
v_xor_b32 v112, v120, v112
v_lshl_add_u32 v112, v112, 4, v111
v_add_u32 v234, 4096, v112
v_xor_b32 v112, v120, v121
v_xor_b32 v112, 4, v112
v_lshl_add_u32 v112, v112, 4, v111
v_add_u32 v233, 6144, v112
v_lshlrev_b32 v112, 1, v121
v_xor_b32 v112, v120, v112
v_lshl_add_u32 v112, v112, 4, v111
v_add_u32 v235, 6144, v112
v_mul_u32_u24 v111, 12288, v119
v_and_b32 v112, 1, v121
s_and_b32 s67, s19, 1
s_lshl_b32 s67, s67, 1
v_or_b32 v112, s67, v112
v_lshlrev_b32 v112, 1, v112
v_xor_b32 v112, v120, v112
v_lshl_add_u32 v112, v112, 4, v111
v_add_u32 v236, 4096, v112
v_add_u32 v237, 3200, v112
v_add_u32 v238, 2304, v112
v_mul_u32_u24 v111, 1280, v119
v_xor_b32 v112, v120, v121
v_xor_b32 v112, 0, v112
v_lshl_add_u32 v112, v112, 4, v111
v_add_u32 v239, 4096, v112
v_xor_b32 v112, v120, v121
v_xor_b32 v112, 4, v112
v_lshl_add_u32 v112, v112, 4, v111
v_add_u32 v240, 13312, v112
s_and_b32 s67, s19, 1
s_lshl_b32 s67, s67, 2
v_xor_b32 v112, v120, v121
v_xor_b32 v112, s67, v112
v_lshl_add_u32 v112, v112, 4, v111
s_mov_b32 s68, 2146435072
s_cmp_lt_u32 s19, 2
s_cselect_b32 s67, 4096, s68
v_add_u32 v245, s67, v112
v_lshlrev_b32 v241, 4, v108
s_mul_i32 s67, s19, 3072
s_add_u32 s56, s67, 49152
s_lshl_b32 s67, s19, 12
s_add_u32 s57, s67, 122880
s_lshl_b32 s67, s19, 10
s_add_u32 s67, s67, 139264
s_cmp_lt_u32 s19, 2
s_cselect_b32 s92, s67, 160768
s_cselect_b32 s72, -1, 0
s_mul_i32 s67, s19, 6144
s_add_u32 s58, s67, 0
s_add_u32 s59, s67, 73728
s_mov_b32 s69, 1090519040
s_mov_b32 s70, 1037384597
s_mov_b32 s71, 1037384597
s_waitcnt lgkmcnt(0)
s_sub_u32 s36, s20, 4096
s_subb_u32 s37, s21, 0
s_and_b32 s37, s37, 65535
s_mov_b32 s38, 2953216
s_mov_b32 s39, 131072
s_sub_u32 s40, s22, 4096
s_subb_u32 s41, s23, 0
s_and_b32 s41, s41, 65535
s_mov_b32 s42, 100667392
s_mov_b32 s43, 131072
s_sub_u32 s44, s24, 4096
s_subb_u32 s45, s25, 0
s_and_b32 s45, s45, 65535
s_mov_b32 s46, 100667392
s_mov_b32 s47, 131072
s_sub_u32 s48, s26, 4096
s_subb_u32 s49, s27, 0
s_and_b32 s49, s49, 65535
s_mov_b32 s50, 7868416
s_mov_b32 s51, 131072
s_sub_u32 s52, s28, 0
s_subb_u32 s53, s29, 0
s_and_b32 s53, s53, 65535
s_mov_b32 s54, 56623104
s_mov_b32 s55, 131072
s_lshl_b32 s67, s87, 5
s_add_u32 s67, s67, s86
s_mul_i32 s67, s67, 196608
s_mul_i32 s68, s19, 6144
s_add_u32 s63, s67, s68
s_mul_i32 s67, s86, 384
s_mul_i32 s68, s19, 98304
s_add_u32 s65, s67, s68
s_mov_b32 s60, s65
s_mul_i32 s67, s87, 184320
s_mul_i32 s68, s19, 40960
s_add_u32 s61, s67, s68
s_and_b32 s68, s19, 1
s_mul_i32 s68, s68, 10240
s_add_u32 s62, s67, s68
s_add_u32 s62, s62, 163840
s_mov_b32 s66, 0
s_mov_b32 s64, s63
s_cmp_lt_u32 s19, 3
s_cselect_b32 s4, s48, s36
s_cselect_b32 s5, s49, s37
s_cselect_b32 s6, s50, s38
s_cselect_b32 s7, s51, s39
s_mov_b32 s10, s6
s_mov_b32 s89, 8192
s_cselect_b32 s9, 128, s89
s_mov_b32 s89, 128
s_cselect_b32 s89, 12288, s89
v_mul_lo_u32 v162, s89, v108
s_lshr_b32 s67, s18, 6
s_mul_i32 s67, s67, 160
s_lshl_b32 s68, s19, 6
s_add_u32 s67, s67, s68
s_mul_i32 s67, s67, 12288
s_mul_i32 s68, s86, 384
s_add_u32 s67, s67, s68
s_add_u32 s67, s67, 4096
s_lshr_b32 s68, s18, 3
s_and_b32 s68, s68, 7
s_mul_i32 s68, s68, 23040
s_mul_i32 s90, s87, 184320
s_add_u32 s68, s68, s90
s_add_u32 s68, s68, 4096
s_cmp_lt_u32 s19, 3
s_cselect_b32 s8, s67, s68
s_cmp_ge_u32 s18, 128
s_cbranch_scc1 K1_l1
s_cmp_lg_u32 s19, 0
s_cbranch_scc1 K1_l1
s_lshl_b32 s67, s18, 8
s_add_u32 s67, s67, 58720256
v_mov_b32 v122, s67
v_mov_b32 v123, 0
s_mov_b64 exec, 1
global_store_dword v122, v123, s[28:29]
s_mov_b64 exec, -1
K1_l1:
s_add_u32 m0, s56, 0
s_nop 0
buffer_load_dwordx4 v236, s[48:51], s60 offen lds
buffer_load_dwordx4 v237, s[48:51], s60 offen offset:1024 lds
buffer_load_dwordx4 v238, s[48:51], s60 offen offset:2048 lds
s_add_u32 s60, s60, 393216
s_add_u32 m0, s57, 0
s_add_u32 s67, s61, 0
buffer_load_dwordx4 v239, s[36:39], s67 offen lds
buffer_load_dwordx4 v240, s[36:39], s67 offen offset:1024 lds
s_add_u32 m0, s57, 2048
s_add_u32 s67, s61, 20480
buffer_load_dwordx4 v239, s[36:39], s67 offen lds
buffer_load_dwordx4 v240, s[36:39], s67 offen offset:1024 lds
s_and_b32 s67, s72, 0
s_add_u32 m0, s92, s67
s_add_u32 s61, s61, 128
buffer_load_dwordx4 v245, s[36:39], s62 offen lds
s_add_u32 s62, s62, 128
s_add_u32 m0, s56, 12288
s_nop 0
buffer_load_dwordx4 v236, s[48:51], s60 offen lds
buffer_load_dwordx4 v237, s[48:51], s60 offen offset:1024 lds
buffer_load_dwordx4 v238, s[48:51], s60 offen offset:2048 lds
s_add_u32 s60, s60, 393216
s_add_u32 m0, s57, 18432
s_add_u32 s67, s61, 0
buffer_load_dwordx4 v239, s[36:39], s67 offen lds
buffer_load_dwordx4 v240, s[36:39], s67 offen offset:1024 lds
s_add_u32 m0, s57, 20480
s_add_u32 s67, s61, 20480
buffer_load_dwordx4 v239, s[36:39], s67 offen lds
buffer_load_dwordx4 v240, s[36:39], s67 offen offset:1024 lds
s_and_b32 s67, s72, 18432
s_add_u32 m0, s92, s67
s_add_u32 s61, s61, 128
buffer_load_dwordx4 v245, s[36:39], s62 offen lds
s_add_u32 s62, s62, 128
v_lshrrev_b32 v111, 3, v109
v_lshlrev_b32 v111, 10, v111
v_and_b32 v112, 7, v109
v_lshl_add_u32 v111, v112, 7, v111
v_lshrrev_b32 v112, 1, v109
v_add_u32 v113, 0, v110
v_xor_b32 v113, v113, v112
v_lshl_add_u32 v113, v113, 4, v111
s_xor_b32 s67, s19, 0
s_mul_i32 s67, s67, 6144
s_add_u32 s67, s67, 0
v_add_u32 v204, s67, v113
s_xor_b32 s67, s19, 1
s_mul_i32 s67, s67, 6144
s_add_u32 s67, s67, 0
v_add_u32 v205, s67, v113
s_xor_b32 s67, s19, 2
s_mul_i32 s67, s67, 6144
s_add_u32 s67, s67, 0
v_add_u32 v206, s67, v113
s_xor_b32 s67, s19, 3
s_mul_i32 s67, s67, 6144
s_add_u32 s67, s67, 0
v_add_u32 v207, s67, v113
v_add_u32 v113, 4, v110
v_xor_b32 v113, v113, v112
v_lshl_add_u32 v113, v113, 4, v111
s_xor_b32 s67, s19, 0
s_mul_i32 s67, s67, 6144
s_add_u32 s67, s67, 0
v_add_u32 v208, s67, v113
s_xor_b32 s67, s19, 1
s_mul_i32 s67, s67, 6144
s_add_u32 s67, s67, 0
v_add_u32 v209, s67, v113
s_xor_b32 s67, s19, 2
s_mul_i32 s67, s67, 6144
s_add_u32 s67, s67, 0
v_add_u32 v210, s67, v113
s_xor_b32 s67, s19, 3
s_mul_i32 s67, s67, 6144
s_add_u32 s67, s67, 0
v_add_u32 v211, s67, v113
v_lshrrev_b32 v115, 2, v108
v_and_b32 v115, 3, v115
v_and_b32 v116, 3, v108
v_and_b32 v111, 1, v110
v_lshlrev_b32 v117, 1, v111
v_lshrrev_b32 v112, 1, v115
v_add_u32 v117, v117, v112
v_lshlrev_b32 v118, 3, v116
v_lshrrev_b32 v112, 1, v110
v_lshlrev_b32 v112, 10, v112
v_lshl_add_u32 v113, v111, 2, v115
v_lshl_add_u32 v112, v113, 7, v112
v_add_u32 v112, v112, v118
v_xor_b32 v113, 0, v117
v_lshl_add_u32 v113, v113, 5, v112
s_xor_b32 s67, s19, 0
s_mul_i32 s67, s67, 6144
s_add_u32 s67, s67, 73728
v_add_u32 v212, s67, v113
s_xor_b32 s67, s19, 1
s_mul_i32 s67, s67, 6144
s_add_u32 s67, s67, 73728
v_add_u32 v216, s67, v113
s_xor_b32 s67, s19, 2
s_mul_i32 s67, s67, 6144
s_add_u32 s67, s67, 73728
v_add_u32 v220, s67, v113
s_xor_b32 s67, s19, 3
s_mul_i32 s67, s67, 6144
s_add_u32 s67, s67, 73728
v_add_u32 v224, s67, v113
v_xor_b32 v113, 1, v117
v_lshl_add_u32 v113, v113, 5, v112
s_xor_b32 s67, s19, 0
s_mul_i32 s67, s67, 6144
s_add_u32 s67, s67, 73728
v_add_u32 v213, s67, v113
s_xor_b32 s67, s19, 1
s_mul_i32 s67, s67, 6144
s_add_u32 s67, s67, 73728
v_add_u32 v217, s67, v113
s_xor_b32 s67, s19, 2
s_mul_i32 s67, s67, 6144
s_add_u32 s67, s67, 73728
v_add_u32 v221, s67, v113
s_xor_b32 s67, s19, 3
s_mul_i32 s67, s67, 6144
s_add_u32 s67, s67, 73728
v_add_u32 v225, s67, v113
v_xor_b32 v113, 2, v117
v_lshl_add_u32 v113, v113, 5, v112
s_xor_b32 s67, s19, 0
s_mul_i32 s67, s67, 6144
s_add_u32 s67, s67, 73728
v_add_u32 v214, s67, v113
s_xor_b32 s67, s19, 1
s_mul_i32 s67, s67, 6144
s_add_u32 s67, s67, 73728
v_add_u32 v218, s67, v113
s_xor_b32 s67, s19, 2
s_mul_i32 s67, s67, 6144
s_add_u32 s67, s67, 73728
v_add_u32 v222, s67, v113
s_xor_b32 s67, s19, 3
s_mul_i32 s67, s67, 6144
s_add_u32 s67, s67, 73728
v_add_u32 v226, s67, v113
v_xor_b32 v113, 3, v117
v_lshl_add_u32 v113, v113, 5, v112
s_xor_b32 s67, s19, 0
s_mul_i32 s67, s67, 6144
s_add_u32 s67, s67, 73728
v_add_u32 v215, s67, v113
s_xor_b32 s67, s19, 1
s_mul_i32 s67, s67, 6144
s_add_u32 s67, s67, 73728
v_add_u32 v219, s67, v113
s_xor_b32 s67, s19, 2
s_mul_i32 s67, s67, 6144
s_add_u32 s67, s67, 73728
v_add_u32 v223, s67, v113
s_xor_b32 s67, s19, 3
s_mul_i32 s67, s67, 6144
s_add_u32 s67, s67, 73728
v_add_u32 v227, s67, v113
v_mul_u32_u24 v112, 3072, v110
v_lshl_add_u32 v112, v115, 7, v112
v_add_u32 v112, v112, v118
s_mul_i32 s67, s19, 3
s_add_u32 s67, s67, 0
s_and_b32 s68, s67, 3
v_xor_b32 v113, s68, v117
v_lshl_add_u32 v113, v113, 5, v112
s_lshr_b32 s67, s67, 2
s_lshl_b32 s67, s67, 10
s_add_u32 s67, s67, 49152
v_add_u32 v228, s67, v113
s_mul_i32 s67, s19, 3
s_add_u32 s67, s67, 1
s_and_b32 s68, s67, 3
v_xor_b32 v113, s68, v117
v_lshl_add_u32 v113, v113, 5, v112
s_lshr_b32 s67, s67, 2
s_lshl_b32 s67, s67, 10
s_add_u32 s67, s67, 49152
v_add_u32 v229, s67, v113
s_mul_i32 s67, s19, 3
s_add_u32 s67, s67, 2
s_and_b32 s68, s67, 3
v_xor_b32 v113, s68, v117
v_lshl_add_u32 v113, v113, 5, v112
s_lshr_b32 s67, s67, 2
s_lshl_b32 s67, s67, 10
s_add_u32 s67, s67, 49152
v_add_u32 v230, s67, v113
v_lshrrev_b32 v111, 3, v109
v_lshlrev_b32 v111, 10, v111
v_and_b32 v112, 7, v109
v_lshl_add_u32 v111, v112, 7, v111
v_lshrrev_b32 v112, 1, v109
v_add_u32 v113, 0, v110
v_xor_b32 v113, v113, v112
v_lshl_add_u32 v113, v113, 4, v111
v_add_u32 v231, 122880, v113
v_add_u32 v164, 24576, v113
v_add_u32 v113, 4, v110
v_xor_b32 v113, v113, v112
v_lshl_add_u32 v113, v113, 4, v111
v_add_u32 v246, 122880, v113
v_add_u32 v247, 24576, v113
s_getpc_b64 s[76:77]
K1_pc_rare_e:
s_add_u32 s76, s76, K1_rare_e-K1_pc_rare_e
s_addc_u32 s77, s77, 0
s_getpc_b64 s[78:79]
K1_pc_rare_o:
s_add_u32 s78, s78, K1_rare_o-K1_pc_rare_o
s_addc_u32 s79, s79, 0
s_getpc_b64 s[80:81]
K1_pc_pend:
s_add_u32 s80, s80, K1_pend-K1_pc_pend
s_addc_u32 s81, s81, 0
s_waitcnt vmcnt(0)
s_mul_i32 s90, s9, 0
s_add_u32 s90, s90, s8
buffer_load_dword v163, v162, s[4:7], s90 offen
s_mul_i32 s90, s9, 1
s_add_u32 s90, s90, s8
buffer_load_dword v163, v162, s[4:7], s90 offen
s_mul_i32 s90, s9, 2
s_add_u32 s90, s90, s8
buffer_load_dword v163, v162, s[4:7], s90 offen
s_add_u32 m0, s58, 0
s_add_u32 s67, s64, 0
buffer_load_dwordx4 v232, s[40:43], s67 offen nt lds
buffer_load_dwordx4 v233, s[40:43], s67 offen offset:1024 nt lds
s_add_u32 m0, s58, 2048
s_add_u32 s67, s64, 128
buffer_load_dwordx4 v232, s[40:43], s67 offen nt lds
buffer_load_dwordx4 v233, s[40:43], s67 offen offset:1024 nt lds
s_add_u32 m0, s58, 4096
s_add_u32 s67, s64, 256
buffer_load_dwordx4 v232, s[40:43], s67 offen nt lds
buffer_load_dwordx4 v233, s[40:43], s67 offen offset:1024 nt lds
s_add_u32 m0, s59, 0
s_add_u32 s67, s64, 0
buffer_load_dwordx4 v234, s[44:47], s67 offen nt lds
buffer_load_dwordx4 v235, s[44:47], s67 offen offset:1024 nt lds
s_add_u32 m0, s59, 2048
s_add_u32 s67, s64, 128
buffer_load_dwordx4 v234, s[44:47], s67 offen nt lds
buffer_load_dwordx4 v235, s[44:47], s67 offen offset:1024 nt lds
s_add_u32 m0, s59, 4096
s_add_u32 s67, s64, 256
buffer_load_dwordx4 v234, s[44:47], s67 offen nt lds
buffer_load_dwordx4 v235, s[44:47], s67 offen offset:1024 nt lds
s_mov_b32 s54, 0
s_barrier
K1_unit:
; unit setup
s_cmp_lt_u32 s66, 3
s_cselect_b32 s91, 100667392, 0
s_cselect_b32 s93, 7868416, 0
s_cselect_b32 s32, 2953216, 0
s_cselect_b32 s67, 25165824, 0
s_add_u32 s64, s63, s67
s_mov_b32 s42, 100667392
s_mov_b32 s46, 100667392
ds_read_b64_tr_b16 a[192:193], v228 offset:0
ds_read_b64_tr_b16 a[194:195], v228 offset:512
ds_read_b128 a[196:199], v231 offset:0
s_waitcnt lgkmcnt(0)
v_mfma_f32_16x16x32_bf16 v[0:3], a[192:195], a[196:199], 0
ds_read_b64_tr_b16 a[200:201], v229 offset:0
ds_read_b64_tr_b16 a[202:203], v229 offset:512
s_waitcnt lgkmcnt(0)
v_mfma_f32_16x16x32_bf16 v[36:39], a[200:203], a[196:199], 0
ds_read_b64_tr_b16 a[204:205], v230 offset:0
ds_read_b64_tr_b16 a[206:207], v230 offset:512
s_waitcnt lgkmcnt(0)
v_mfma_f32_16x16x32_bf16 v[72:75], a[204:207], a[196:199], 0
ds_read_b128 a[208:211], v231 offset:2048
ds_read_b128 a[212:215], v231 offset:4096
s_waitcnt lgkmcnt(1)
v_mfma_f32_16x16x32_bf16 v[4:7], a[192:195], a[208:211], 0
ds_read_b128 a[216:219], v231 offset:6144
ds_read_b128 a[220:223], v231 offset:8192
v_mfma_f32_16x16x32_bf16 v[40:43], a[200:203], a[208:211], 0
ds_read_b128 a[224:227], v231 offset:10240
ds_read_b128 a[228:231], v231 offset:12288
v_mfma_f32_16x16x32_bf16 v[76:79], a[204:207], a[208:211], 0
s_add_u32 m0, s56, 49152
v_max_f32 v116, v108, v109
v_max3_f32 v116, v116, v110, v111
s_waitcnt lgkmcnt(4)
v_mfma_f32_16x16x32_bf16 v[8:11], a[192:195], a[212:215], 0
buffer_load_dwordx4 v236, s[48:51], s60 offen lds
v_mfma_f32_16x16x32_bf16 v[44:47], a[200:203], a[212:215], 0
ds_read_b128 a[232:235], v231 offset:14336
v_sub_f32 v118, v108, v116
v_mfma_f32_16x16x32_bf16 v[80:83], a[204:207], a[212:215], 0
ds_read_b128 a[236:239], v231 offset:16384
s_waitcnt lgkmcnt(5)
v_mfma_f32_16x16x32_bf16 v[12:15], a[192:195], a[216:219], 0
buffer_load_dwordx4 v237, s[48:51], s60 offen offset:1024 lds
v_mfma_f32_16x16x32_bf16 v[48:51], a[200:203], a[216:219], 0
buffer_load_dwordx4 v238, s[48:51], s60 offen offset:2048 lds
v_mfma_f32_16x16x32_bf16 v[84:87], a[204:207], a[216:219], 0
s_waitcnt lgkmcnt(4)
s_add_u32 s60, s60, 393216
s_add_u32 m0, s57, 4294868992
v_mfma_f32_16x16x32_bf16 v[16:19], a[192:195], a[220:223], 0
s_waitcnt lgkmcnt(2)
s_add_u32 s67, s61, 0
v_mfma_f32_16x16x32_bf16 v[52:55], a[200:203], a[220:223], 0
s_waitcnt vmcnt(22) lgkmcnt(0)
s_barrier
v_mfma_f32_16x16x32_bf16 v[88:91], a[204:207], a[220:223], 0
ds_read_b64_tr_b16 a[240:241], v228 offset:12288
ds_read_b64_tr_b16 a[242:243], v228 offset:12800
v_mfma_f32_16x16x32_bf16 v[20:23], a[192:195], a[224:227], 0
ds_read_b128 a[244:247], v246 offset:0
ds_read_b64_tr_b16 a[248:249], v229 offset:12288
v_mfma_f32_16x16x32_bf16 v[56:59], a[200:203], a[224:227], 0
ds_read_b64_tr_b16 a[250:251], v229 offset:12800
ds_read_b64_tr_b16 a[252:253], v230 offset:12288
v_mfma_f32_16x16x32_bf16 v[92:95], a[204:207], a[224:227], 0
ds_read_b64_tr_b16 a[254:255], v230 offset:12800
ds_read_b128 a[196:199], v246 offset:2048
v_mfma_f32_16x16x32_bf16 v[24:27], a[192:195], a[228:231], 0
buffer_load_dwordx4 v239, s[36:39], s67 offen lds
v_mfma_f32_16x16x32_bf16 v[60:63], a[200:203], a[228:231], 0
ds_read_b128 a[208:211], v246 offset:4096
v_mfma_f32_16x16x32_bf16 v[96:99], a[204:207], a[228:231], 0
ds_read_b128 a[212:215], v246 offset:6144
v_mfma_f32_16x16x32_bf16 v[28:31], a[192:195], a[232:235], 0
buffer_load_dwordx4 v240, s[36:39], s67 offen offset:1024 lds
v_mfma_f32_16x16x32_bf16 v[64:67], a[200:203], a[232:235], 0
ds_read_b128 a[216:219], v246 offset:8192
s_add_u32 m0, s57, 4294871040
v_mfma_f32_16x16x32_bf16 v[100:103], a[204:207], a[232:235], 0
s_add_u32 s67, s61, 20480
v_exp_f32 v118, v118
v_mfma_f32_16x16x32_bf16 v[32:35], a[192:195], a[236:239], 0
s_waitcnt lgkmcnt(5)
v_sub_f32 v119, v109, v116
v_mfma_f32_16x16x32_bf16 v[68:71], a[200:203], a[236:239], 0
buffer_load_dwordx4 v239, s[36:39], s67 offen lds
v_mfma_f32_16x16x32_bf16 v[104:107], a[204:207], a[236:239], 0
s_waitcnt lgkmcnt(3)
ds_read_b128 a[220:223], v246 offset:10240
v_mfma_f32_16x16x32_bf16 v[0:3], a[240:243], a[244:247], v[0:3]
buffer_load_dwordx4 v240, s[36:39], s67 offen offset:1024 lds
v_mfma_f32_16x16x32_bf16 v[36:39], a[248:251], a[244:247], v[36:39]
ds_read_b128 a[224:227], v246 offset:12288
v_mfma_f32_16x16x32_bf16 v[72:75], a[252:255], a[244:247], v[72:75]
ds_read_b128 a[228:231], v246 offset:14336
s_and_b32 s67, s72, 4294868992
v_mfma_f32_16x16x32_bf16 v[4:7], a[240:243], a[196:199], v[4:7]
s_waitcnt lgkmcnt(3)
s_add_u32 m0, s92, s67
s_add_u32 s61, s61, 128
v_mfma_f32_16x16x32_bf16 v[40:43], a[248:251], a[196:199], v[40:43]
buffer_load_dwordx4 v245, s[36:39], s62 offen lds
v_mfma_f32_16x16x32_bf16 v[76:79], a[252:255], a[196:199], v[76:79]
ds_read_b128 a[232:235], v246 offset:16384
s_add_u32 s62, s62, 128
v_mfma_f32_16x16x32_bf16 v[8:11], a[240:243], a[208:211], v[8:11]
s_add_u32 m0, s56, 61440
v_exp_f32 v119, v119
v_mfma_f32_16x16x32_bf16 v[44:47], a[248:251], a[208:211], v[44:47]
buffer_load_dwordx4 v236, s[48:51], s60 offen lds
v_mfma_f32_16x16x32_bf16 v[80:83], a[252:255], a[208:211], v[80:83]
buffer_load_dwordx4 v237, s[48:51], s60 offen offset:1024 lds
v_mfma_f32_16x16x32_bf16 v[12:15], a[240:243], a[212:215], v[12:15]
v_sub_f32 v120, v110, v116
v_mfma_f32_16x16x32_bf16 v[48:51], a[248:251], a[212:215], v[48:51]
v_exp_f32 v120, v120
v_mfma_f32_16x16x32_bf16 v[84:87], a[252:255], a[212:215], v[84:87]
v_sub_f32 v121, v111, v116
v_mfma_f32_16x16x32_bf16 v[16:19], a[240:243], a[216:219], v[16:19]
s_waitcnt lgkmcnt(1)
s_waitcnt vmcnt(7) lgkmcnt(0)
s_barrier
v_mfma_f32_16x16x32_bf16 v[52:55], a[248:251], a[216:219], v[52:55]
ds_read_b64_tr_b16 a[192:193], v228 offset:49152
ds_read_b64_tr_b16 a[194:195], v228 offset:49664
v_mfma_f32_16x16x32_bf16 v[88:91], a[252:255], a[216:219], v[88:91]
ds_read_b128 a[200:203], v231 offset:18432
ds_read_b64_tr_b16 a[204:205], v229 offset:49152
v_mfma_f32_16x16x32_bf16 v[20:23], a[240:243], a[220:223], v[20:23]
ds_read_b64_tr_b16 a[206:207], v229 offset:49664
ds_read_b64_tr_b16 a[236:237], v230 offset:49152
v_mfma_f32_16x16x32_bf16 v[56:59], a[248:251], a[220:223], v[56:59]
ds_read_b64_tr_b16 a[238:239], v230 offset:49664
ds_read_b128 a[244:247], v231 offset:20480
v_mfma_f32_16x16x32_bf16 v[92:95], a[252:255], a[220:223], v[92:95]
ds_read_b128 a[196:199], v231 offset:22528
v_mfma_f32_16x16x32_bf16 v[24:27], a[240:243], a[224:227], v[24:27]
buffer_load_dwordx4 v238, s[48:51], s60 offen offset:2048 lds
v_mfma_f32_16x16x32_bf16 v[60:63], a[248:251], a[224:227], v[60:63]
ds_read_b128 a[208:211], v231 offset:24576
s_add_u32 s60, s60, 393216
v_mfma_f32_16x16x32_bf16 v[96:99], a[252:255], a[224:227], v[96:99]
s_add_u32 m0, s56, 0
v_exp_f32 v121, v121
v_mfma_f32_16x16x32_bf16 v[28:31], a[240:243], a[228:231], v[28:31]
buffer_load_dwordx4 v236, s[48:51], s60 offen lds
v_mfma_f32_16x16x32_bf16 v[64:67], a[248:251], a[228:231], v[64:67]
ds_read_b128 a[212:215], v231 offset:26624
v_mul_f32 v117, v118, v112
v_mfma_f32_16x16x32_bf16 v[100:103], a[252:255], a[228:231], v[100:103]
buffer_load_dwordx4 v237, s[48:51], s60 offen offset:1024 lds
v_mfma_f32_16x16x32_bf16 v[32:35], a[240:243], a[232:235], v[32:35]
s_waitcnt lgkmcnt(3)
ds_read_b128 a[216:219], v231 offset:28672
v_mfma_f32_16x16x32_bf16 v[68:71], a[248:251], a[232:235], v[68:71]
ds_read_b128 a[220:223], v231 offset:30720
v_fmac_f32 v117, v119, v113
v_mfma_f32_16x16x32_bf16 v[104:107], a[252:255], a[232:235], v[104:107]
buffer_load_dwordx4 v238, s[48:51], s60 offen offset:2048 lds
v_mfma_f32_16x16x32_bf16 v[0:3], a[192:195], a[200:203], v[0:3]
ds_read_b128 a[224:227], v231 offset:32768
v_fmac_f32 v117, v120, v114
v_mfma_f32_16x16x32_bf16 v[36:39], a[204:207], a[200:203], v[36:39]
s_add_u32 s60, s60, 393216
s_add_u32 m0, s56, 12288
v_fmac_f32 v117, v121, v115
v_mfma_f32_16x16x32_bf16 v[72:75], a[236:239], a[200:203], v[72:75]
buffer_load_dwordx4 v236, s[48:51], s60 offen lds
v_mfma_f32_16x16x32_bf16 v[4:7], a[192:195], a[244:247], v[4:7]
s_waitcnt lgkmcnt(3)
ds_read_b128 a[228:231], v231 offset:34816
v_mfma_f32_16x16x32_bf16 v[40:43], a[204:207], a[244:247], v[40:43]
v_rcp_f32 v117, v117
v_mfma_f32_16x16x32_bf16 v[76:79], a[236:239], a[244:247], v[76:79]
buffer_load_dwordx4 v237, s[48:51], s60 offen offset:1024 lds
v_mfma_f32_16x16x32_bf16 v[8:11], a[192:195], a[196:199], v[8:11]
v_mul_f32 v118, v118, v117
v_mul_f32 v119, v119, v117
v_mfma_f32_16x16x32_bf16 v[44:47], a[204:207], a[196:199], v[44:47]
v_mul_f32 v120, v120, v117
v_mul_f32 v121, v121, v117
v_mfma_f32_16x16x32_bf16 v[80:83], a[236:239], a[196:199], v[80:83]
v_lshlrev_b32 v122, 16, v180
v_and_b32 v123, 4294901760, v180
v_mfma_f32_16x16x32_bf16 v[12:15], a[192:195], a[208:211], v[12:15]
buffer_load_dwordx4 v238, s[48:51], s60 offen offset:2048 lds
v_mfma_f32_16x16x32_bf16 v[48:51], a[204:207], a[208:211], v[48:51]
v_lshlrev_b32 v124, 16, v181
v_and_b32 v125, 4294901760, v181
v_mfma_f32_16x16x32_bf16 v[84:87], a[236:239], a[208:211], v[84:87]
v_mul_f32 v128, v122, v118
v_mul_f32 v129, v123, v118
v_mfma_f32_16x16x32_bf16 v[16:19], a[192:195], a[212:215], v[16:19]
s_waitcnt lgkmcnt(0)
s_waitcnt vmcnt(6) lgkmcnt(0)
s_barrier
v_mfma_f32_16x16x32_bf16 v[52:55], a[204:207], a[212:215], v[52:55]
ds_read_b64_tr_b16 a[240:241], v228 offset:61440
ds_read_b64_tr_b16 a[242:243], v228 offset:61952
v_mfma_f32_16x16x32_bf16 v[88:91], a[236:239], a[212:215], v[88:91]
ds_read_b128 a[248:251], v246 offset:18432
ds_read_b64_tr_b16 a[252:253], v229 offset:61440
v_mfma_f32_16x16x32_bf16 v[20:23], a[192:195], a[216:219], v[20:23]
ds_read_b64_tr_b16 a[254:255], v229 offset:61952
ds_read_b64_tr_b16 a[232:233], v230 offset:61440
v_mfma_f32_16x16x32_bf16 v[56:59], a[204:207], a[216:219], v[56:59]
ds_read_b64_tr_b16 a[234:235], v230 offset:61952
ds_read_b128 a[200:203], v246 offset:20480
v_mfma_f32_16x16x32_bf16 v[92:95], a[236:239], a[216:219], v[92:95]
ds_read_b128 a[244:247], v246 offset:22528
s_add_u32 s60, s60, 393216
v_mfma_f32_16x16x32_bf16 v[24:27], a[192:195], a[220:223], v[24:27]
s_add_u32 m0, s57, 0
s_add_u32 s67, s61, 0
v_mul_f32 v130, v124, v118
v_mfma_f32_16x16x32_bf16 v[60:63], a[204:207], a[220:223], v[60:63]
buffer_load_dwordx4 v239, s[36:39], s67 offen lds
v_mfma_f32_16x16x32_bf16 v[96:99], a[236:239], a[220:223], v[96:99]
ds_read_b128 a[196:199], v246 offset:24576
v_mul_f32 v131, v125, v118
v_mfma_f32_16x16x32_bf16 v[28:31], a[192:195], a[224:227], v[28:31]
buffer_load_dwordx4 v240, s[36:39], s67 offen offset:1024 lds
v_mfma_f32_16x16x32_bf16 v[64:67], a[204:207], a[224:227], v[64:67]
ds_read_b128 a[208:211], v246 offset:26624
v_lshlrev_b32 v122, 16, v186
v_mfma_f32_16x16x32_bf16 v[100:103], a[236:239], a[224:227], v[100:103]
ds_read_b128 a[212:215], v246 offset:28672
s_add_u32 m0, s57, 2048
v_mfma_f32_16x16x32_bf16 v[32:35], a[192:195], a[228:231], v[32:35]
s_waitcnt lgkmcnt(4)
s_add_u32 s67, s61, 20480
v_and_b32 v123, 4294901760, v186
v_mfma_f32_16x16x32_bf16 v[68:71], a[204:207], a[228:231], v[68:71]
buffer_load_dwordx4 v239, s[36:39], s67 offen lds
v_mfma_f32_16x16x32_bf16 v[104:107], a[236:239], a[228:231], v[104:107]
ds_read_b128 a[216:219], v246 offset:30720
v_lshlrev_b32 v124, 16, v187
v_mfma_f32_16x16x32_bf16 v[0:3], a[240:243], a[248:251], v[0:3]
buffer_load_dwordx4 v240, s[36:39], s67 offen offset:1024 lds
v_mfma_f32_16x16x32_bf16 v[36:39], a[252:255], a[248:251], v[36:39]
ds_read_b128 a[220:223], v246 offset:32768
v_and_b32 v125, 4294901760, v187
v_mfma_f32_16x16x32_bf16 v[72:75], a[232:235], a[248:251], v[72:75]
s_and_b32 s67, s72, 0
s_add_u32 m0, s92, s67
s_add_u32 s61, s61, 128
v_mfma_f32_16x16x32_bf16 v[4:7], a[240:243], a[200:203], v[4:7]
s_waitcnt lgkmcnt(3)
v_fmac_f32 v128, v122, v119
v_fmac_f32 v129, v123, v119
v_mfma_f32_16x16x32_bf16 v[40:43], a[252:255], a[200:203], v[40:43]
buffer_load_dwordx4 v245, s[36:39], s62 offen lds
v_mfma_f32_16x16x32_bf16 v[76:79], a[232:235], a[200:203], v[76:79]
ds_read_b128 a[224:227], v246 offset:34816
v_fmac_f32 v130, v124, v119
v_mfma_f32_16x16x32_bf16 v[8:11], a[240:243], a[244:247], v[8:11]
s_add_u32 s62, s62, 128
s_add_u32 m0, s56, 49152
v_fmac_f32 v131, v125, v119
v_mfma_f32_16x16x32_bf16 v[44:47], a[252:255], a[244:247], v[44:47]
buffer_load_dwordx4 v236, s[48:51], s60 offen lds
v_mfma_f32_16x16x32_bf16 v[80:83], a[232:235], a[244:247], v[80:83]
v_lshlrev_b32 v122, 16, v192
v_and_b32 v123, 4294901760, v192
v_mfma_f32_16x16x32_bf16 v[12:15], a[240:243], a[196:199], v[12:15]
v_lshlrev_b32 v124, 16, v193
v_and_b32 v125, 4294901760, v193
v_mfma_f32_16x16x32_bf16 v[48:51], a[252:255], a[196:199], v[48:51]
buffer_load_dwordx4 v237, s[48:51], s60 offen offset:1024 lds
v_mfma_f32_16x16x32_bf16 v[84:87], a[232:235], a[196:199], v[84:87]
v_fmac_f32 v128, v122, v120
v_fmac_f32 v129, v123, v120
v_mfma_f32_16x16x32_bf16 v[16:19], a[240:243], a[208:211], v[16:19]
s_waitcnt lgkmcnt(1)
s_waitcnt vmcnt(10) lgkmcnt(0)
s_barrier
v_mfma_f32_16x16x32_bf16 v[52:55], a[252:255], a[208:211], v[52:55]
ds_read_b64_tr_b16 a[192:193], v228 offset:0
ds_read_b64_tr_b16 a[194:195], v228 offset:512
v_mfma_f32_16x16x32_bf16 v[88:91], a[232:235], a[208:211], v[88:91]
ds_read_b128 a[204:207], v164 offset:0
ds_read_b64_tr_b16 a[228:229], v229 offset:0
v_mfma_f32_16x16x32_bf16 v[20:23], a[240:243], a[212:215], v[20:23]
ds_read_b64_tr_b16 a[230:231], v229 offset:512
ds_read_b64_tr_b16 a[236:237], v230 offset:0
v_mfma_f32_16x16x32_bf16 v[56:59], a[252:255], a[212:215], v[56:59]
ds_read_b64_tr_b16 a[238:239], v230 offset:512
ds_read_b128 a[248:251], v164 offset:2048
v_mfma_f32_16x16x32_bf16 v[92:95], a[232:235], a[212:215], v[92:95]
ds_read_b128 a[200:203], v164 offset:4096
v_fmac_f32 v130, v124, v120
v_mfma_f32_16x16x32_bf16 v[24:27], a[240:243], a[216:219], v[24:27]
buffer_load_dwordx4 v238, s[48:51], s60 offen offset:2048 lds
v_mfma_f32_16x16x32_bf16 v[60:63], a[252:255], a[216:219], v[60:63]
ds_read_b128 a[244:247], v164 offset:6144
v_fmac_f32 v131, v125, v120
v_mfma_f32_16x16x32_bf16 v[96:99], a[232:235], a[216:219], v[96:99]
ds_read_b128 a[196:199], v164 offset:8192
s_add_u32 s60, s60, 393216
v_mfma_f32_16x16x32_bf16 v[28:31], a[240:243], a[220:223], v[28:31]
s_add_u32 m0, s56, 61440
v_lshlrev_b32 v122, 16, v198
v_and_b32 v123, 4294901760, v198
v_mfma_f32_16x16x32_bf16 v[64:67], a[252:255], a[220:223], v[64:67]
buffer_load_dwordx4 v236, s[48:51], s60 offen lds
v_mfma_f32_16x16x32_bf16 v[100:103], a[232:235], a[220:223], v[100:103]
ds_read_b128 a[208:211], v164 offset:10240
v_lshlrev_b32 v124, 16, v199
v_mfma_f32_16x16x32_bf16 v[32:35], a[240:243], a[224:227], v[32:35]
s_waitcnt lgkmcnt(4)
v_and_b32 v125, 4294901760, v199
v_fmac_f32 v128, v122, v121
v_mfma_f32_16x16x32_bf16 v[68:71], a[252:255], a[224:227], v[68:71]
buffer_load_dwordx4 v237, s[48:51], s60 offen offset:1024 lds
v_mfma_f32_16x16x32_bf16 v[104:107], a[232:235], a[224:227], v[104:107]
ds_read_b128 a[212:215], v164 offset:12288
v_fmac_f32 v129, v123, v121
v_mfma_f32_16x16x32_bf16 v[0:3], a[192:195], a[204:207], v[0:3]
buffer_load_dwordx4 v238, s[48:51], s60 offen offset:2048 lds
v_mfma_f32_16x16x32_bf16 v[36:39], a[228:231], a[204:207], v[36:39]
ds_read_b128 a[216:219], v164 offset:14336
v_fmac_f32 v130, v124, v121
v_mfma_f32_16x16x32_bf16 v[72:75], a[236:239], a[204:207], v[72:75]
ds_read_b128 a[220:223], v164 offset:16384
v_fmac_f32 v131, v125, v121
v_mfma_f32_16x16x32_bf16 v[4:7], a[192:195], a[248:251], v[4:7]
s_waitcnt lgkmcnt(4)
s_add_u32 s60, s60, 393216
s_add_u32 m0, s57, 18432
v_mfma_f32_16x16x32_bf16 v[40:43], a[228:231], a[248:251], v[40:43]
s_add_u32 s67, s61, 0
v_cvt_pk_bf16_f32 v156, v128, v129
v_cvt_pk_bf16_f32 v157, v130, v131
v_mfma_f32_16x16x32_bf16 v[76:79], a[236:239], a[248:251], v[76:79]
buffer_load_dwordx4 v239, s[36:39], s67 offen lds
v_mfma_f32_16x16x32_bf16 v[8:11], a[192:195], a[200:203], v[8:11]
v_lshlrev_b32 v122, 16, v182
v_and_b32 v123, 4294901760, v182
v_mfma_f32_16x16x32_bf16 v[44:47], a[228:231], a[200:203], v[44:47]
buffer_load_dwordx4 v240, s[36:39], s67 offen offset:1024 lds
v_mfma_f32_16x16x32_bf16 v[80:83], a[236:239], a[200:203], v[80:83]
v_lshlrev_b32 v124, 16, v183
v_and_b32 v125, 4294901760, v183
v_mfma_f32_16x16x32_bf16 v[12:15], a[192:195], a[244:247], v[12:15]
v_mul_f32 v132, v122, v118
v_mul_f32 v133, v123, v118
v_mfma_f32_16x16x32_bf16 v[48:51], a[228:231], a[244:247], v[48:51]
v_mul_f32 v134, v124, v118
v_mul_f32 v135, v125, v118
v_mfma_f32_16x16x32_bf16 v[84:87], a[236:239], a[244:247], v[84:87]
s_add_u32 m0, s57, 20480
s_add_u32 s67, s61, 20480
v_lshlrev_b32 v122, 16, v188
v_mfma_f32_16x16x32_bf16 v[16:19], a[192:195], a[196:199], v[16:19]
s_waitcnt lgkmcnt(0)
s_waitcnt vmcnt(13) lgkmcnt(0)
s_barrier
v_mfma_f32_16x16x32_bf16 v[52:55], a[228:231], a[196:199], v[52:55]
ds_read_b64_tr_b16 a[240:241], v228 offset:12288
ds_read_b64_tr_b16 a[242:243], v228 offset:12800
v_mfma_f32_16x16x32_bf16 v[88:91], a[236:239], a[196:199], v[88:91]
ds_read_b128 a[252:255], v247 offset:0
ds_read_b64_tr_b16 a[224:225], v229 offset:12288
v_mfma_f32_16x16x32_bf16 v[20:23], a[192:195], a[208:211], v[20:23]
ds_read_b64_tr_b16 a[226:227], v229 offset:12800
ds_read_b64_tr_b16 a[232:233], v230 offset:12288
v_mfma_f32_16x16x32_bf16 v[56:59], a[228:231], a[208:211], v[56:59]
ds_read_b64_tr_b16 a[234:235], v230 offset:12800
ds_read_b128 a[204:207], v247 offset:2048
v_mfma_f32_16x16x32_bf16 v[92:95], a[236:239], a[208:211], v[92:95]
buffer_load_dwordx4 v239, s[36:39], s67 offen lds
v_mfma_f32_16x16x32_bf16 v[24:27], a[192:195], a[212:215], v[24:27]
ds_read_b128 a[248:251], v247 offset:4096
v_and_b32 v123, 4294901760, v188
v_mfma_f32_16x16x32_bf16 v[60:63], a[228:231], a[212:215], v[60:63]
ds_read_b128 a[200:203], v247 offset:6144
v_lshlrev_b32 v124, 16, v189
v_mfma_f32_16x16x32_bf16 v[96:99], a[236:239], a[212:215], v[96:99]
buffer_load_dwordx4 v240, s[36:39], s67 offen offset:1024 lds
v_mfma_f32_16x16x32_bf16 v[28:31], a[192:195], a[216:219], v[28:31]
ds_read_b128 a[244:247], v247 offset:8192
s_and_b32 s67, s72, 18432
v_mfma_f32_16x16x32_bf16 v[64:67], a[228:231], a[216:219], v[64:67]
s_add_u32 m0, s92, s67
s_add_u32 s61, s61, 128
v_and_b32 v125, 4294901760, v189
v_mfma_f32_16x16x32_bf16 v[100:103], a[236:239], a[216:219], v[100:103]
buffer_load_dwordx4 v245, s[36:39], s62 offen lds
v_mfma_f32_16x16x32_bf16 v[32:35], a[192:195], a[220:223], v[32:35]
s_waitcnt lgkmcnt(3)
ds_read_b128 a[196:199], v247 offset:10240
v_mfma_f32_16x16x32_bf16 v[68:71], a[228:231], a[220:223], v[68:71]
s_add_u32 s62, s62, 128
s_add_u32 m0, s56, 0
v_fmac_f32 v132, v122, v119
v_mfma_f32_16x16x32_bf16 v[104:107], a[236:239], a[220:223], v[104:107]
buffer_load_dwordx4 v236, s[48:51], s60 offen lds
v_mfma_f32_16x16x32_bf16 v[0:3], a[240:243], a[252:255], v[0:3]
ds_read_b128 a[208:211], v247 offset:12288
v_fmac_f32 v133, v123, v119
v_mfma_f32_16x16x32_bf16 v[36:39], a[224:227], a[252:255], v[36:39]
ds_read_b128 a[212:215], v247 offset:14336
v_fmac_f32 v134, v124, v119
v_mfma_f32_16x16x32_bf16 v[72:75], a[232:235], a[252:255], v[72:75]
buffer_load_dwordx4 v237, s[48:51], s60 offen offset:1024 lds
v_mfma_f32_16x16x32_bf16 v[4:7], a[240:243], a[204:207], v[4:7]
s_waitcnt lgkmcnt(3)
ds_read_b128 a[216:219], v247 offset:16384
v_mfma_f32_16x16x32_bf16 v[40:43], a[224:227], a[204:207], v[40:43]
buffer_load_dwordx4 v238, s[48:51], s60 offen offset:2048 lds
v_mfma_f32_16x16x32_bf16 v[76:79], a[232:235], a[204:207], v[76:79]
v_fmac_f32 v135, v125, v119
v_lshlrev_b32 v122, 16, v194
v_mfma_f32_16x16x32_bf16 v[8:11], a[240:243], a[248:251], v[8:11]
v_and_b32 v123, 4294901760, v194
v_lshlrev_b32 v124, 16, v195
v_mfma_f32_16x16x32_bf16 v[44:47], a[224:227], a[248:251], v[44:47]
v_and_b32 v125, 4294901760, v195
v_fmac_f32 v132, v122, v120
v_mfma_f32_16x16x32_bf16 v[80:83], a[232:235], a[248:251], v[80:83]
s_add_u32 s60, s60, 393216
v_fmac_f32 v133, v123, v120
v_fmac_f32 v134, v124, v120
v_mfma_f32_16x16x32_bf16 v[12:15], a[240:243], a[200:203], v[12:15]
v_fmac_f32 v135, v125, v120
v_lshlrev_b32 v122, 16, v200
v_mfma_f32_16x16x32_bf16 v[48:51], a[224:227], a[200:203], v[48:51]
v_and_b32 v123, 4294901760, v200
v_lshlrev_b32 v124, 16, v201
v_mfma_f32_16x16x32_bf16 v[84:87], a[232:235], a[200:203], v[84:87]
v_and_b32 v125, 4294901760, v201
v_fmac_f32 v132, v122, v121
v_mfma_f32_16x16x32_bf16 v[16:19], a[240:243], a[244:247], v[16:19]
s_waitcnt lgkmcnt(0)
s_waitcnt vmcnt(11) lgkmcnt(0)
s_barrier
v_mfma_f32_16x16x32_bf16 v[52:55], a[224:227], a[244:247], v[52:55]
ds_read_b64_tr_b16 a[192:193], v228 offset:49152
ds_read_b64_tr_b16 a[194:195], v228 offset:49664
v_mfma_f32_16x16x32_bf16 v[88:91], a[232:235], a[244:247], v[88:91]
ds_read_b128 a[228:231], v231 offset:0
ds_read_b64_tr_b16 a[236:237], v229 offset:49152
v_mfma_f32_16x16x32_bf16 v[20:23], a[240:243], a[196:199], v[20:23]
ds_read_b64_tr_b16 a[238:239], v229 offset:49664
ds_read_b64_tr_b16 a[220:221], v230 offset:49152
v_mfma_f32_16x16x32_bf16 v[56:59], a[224:227], a[196:199], v[56:59]
ds_read_b64_tr_b16 a[222:223], v230 offset:49664
ds_read_b128 a[252:255], v231 offset:2048
v_mfma_f32_16x16x32_bf16 v[92:95], a[232:235], a[196:199], v[92:95]
ds_read_b128 a[204:207], v231 offset:4096
s_add_u32 m0, s56, 12288
v_mfma_f32_16x16x32_bf16 v[24:27], a[240:243], a[208:211], v[24:27]
buffer_load_dwordx4 v236, s[48:51], s60 offen lds
v_mfma_f32_16x16x32_bf16 v[60:63], a[224:227], a[208:211], v[60:63]
ds_read_b128 a[248:251], v231 offset:6144
v_fmac_f32 v133, v123, v121
v_mfma_f32_16x16x32_bf16 v[96:99], a[232:235], a[208:211], v[96:99]
buffer_load_dwordx4 v237, s[48:51], s60 offen offset:1024 lds
v_mfma_f32_16x16x32_bf16 v[28:31], a[240:243], a[212:215], v[28:31]
ds_read_b128 a[200:203], v231 offset:8192
v_fmac_f32 v134, v124, v121
v_mfma_f32_16x16x32_bf16 v[64:67], a[224:227], a[212:215], v[64:67]
ds_read_b128 a[244:247], v231 offset:10240
v_fmac_f32 v135, v125, v121
v_mfma_f32_16x16x32_bf16 v[100:103], a[232:235], a[212:215], v[100:103]
buffer_load_dwordx4 v238, s[48:51], s60 offen offset:2048 lds
v_mfma_f32_16x16x32_bf16 v[32:35], a[240:243], a[216:219], v[32:35]
s_waitcnt lgkmcnt(4)
ds_read_b128 a[196:199], v231 offset:12288
v_mfma_f32_16x16x32_bf16 v[68:71], a[224:227], a[216:219], v[68:71]
s_add_u32 s60, s60, 393216
s_add_u32 m0, s57, 4294868992
s_add_u32 s67, s61, 0
v_mfma_f32_16x16x32_bf16 v[104:107], a[232:235], a[216:219], v[104:107]
buffer_load_dwordx4 v239, s[36:39], s67 offen lds
v_mfma_f32_16x16x32_bf16 v[0:3], a[192:195], a[228:231], v[0:3]
ds_read_b128 a[208:211], v231 offset:14336
v_cvt_pk_bf16_f32 v158, v132, v133
v_mfma_f32_16x16x32_bf16 v[36:39], a[236:239], a[228:231], v[36:39]
ds_read_b128 a[212:215], v231 offset:16384
v_cvt_pk_bf16_f32 v159, v134, v135
v_mfma_f32_16x16x32_bf16 v[72:75], a[220:223], a[228:231], v[72:75]
buffer_load_dwordx4 v240, s[36:39], s67 offen offset:1024 lds
v_mfma_f32_16x16x32_bf16 v[4:7], a[192:195], a[252:255], v[4:7]
s_waitcnt lgkmcnt(3)
v_lshlrev_b32 v122, 16, v184
v_and_b32 v123, 4294901760, v184
v_mfma_f32_16x16x32_bf16 v[40:43], a[236:239], a[252:255], v[40:43]
v_lshlrev_b32 v124, 16, v185
v_and_b32 v125, 4294901760, v185
v_mfma_f32_16x16x32_bf16 v[76:79], a[220:223], a[252:255], v[76:79]
v_mul_f32 v136, v122, v118
v_mul_f32 v137, v123, v118
v_mfma_f32_16x16x32_bf16 v[8:11], a[192:195], a[204:207], v[8:11]
s_add_u32 m0, s57, 4294871040
s_add_u32 s67, s61, 20480
v_mul_f32 v138, v124, v118
v_mfma_f32_16x16x32_bf16 v[44:47], a[236:239], a[204:207], v[44:47]
buffer_load_dwordx4 v239, s[36:39], s67 offen lds
v_mfma_f32_16x16x32_bf16 v[80:83], a[220:223], a[204:207], v[80:83]
v_mul_f32 v139, v125, v118
v_lshlrev_b32 v122, 16, v190
v_mfma_f32_16x16x32_bf16 v[12:15], a[192:195], a[248:251], v[12:15]
v_and_b32 v123, 4294901760, v190
v_lshlrev_b32 v124, 16, v191
v_mfma_f32_16x16x32_bf16 v[48:51], a[236:239], a[248:251], v[48:51]
buffer_load_dwordx4 v240, s[36:39], s67 offen offset:1024 lds
v_mfma_f32_16x16x32_bf16 v[84:87], a[220:223], a[248:251], v[84:87]
v_and_b32 v125, 4294901760, v191
v_fmac_f32 v136, v122, v119
v_mfma_f32_16x16x32_bf16 v[16:19], a[192:195], a[200:203], v[16:19]
s_waitcnt vmcnt(15) lgkmcnt(0)
s_barrier
v_mfma_f32_16x16x32_bf16 v[52:55], a[236:239], a[200:203], v[52:55]
ds_read_b64_tr_b16 a[240:241], v228 offset:61440
ds_read_b64_tr_b16 a[242:243], v228 offset:61952
v_mfma_f32_16x16x32_bf16 v[88:91], a[220:223], a[200:203], v[88:91]
ds_read_b128 a[224:227], v246 offset:0
ds_read_b64_tr_b16 a[232:233], v229 offset:61440
v_mfma_f32_16x16x32_bf16 v[20:23], a[192:195], a[244:247], v[20:23]
ds_read_b64_tr_b16 a[234:235], v229 offset:61952
ds_read_b64_tr_b16 a[216:217], v230 offset:61440
v_mfma_f32_16x16x32_bf16 v[56:59], a[236:239], a[244:247], v[56:59]
ds_read_b64_tr_b16 a[218:219], v230 offset:61952
ds_read_b128 a[228:231], v246 offset:2048
v_mfma_f32_16x16x32_bf16 v[92:95], a[220:223], a[244:247], v[92:95]
ds_read_b128 a[252:255], v246 offset:4096
s_and_b32 s67, s72, 4294868992
v_mfma_f32_16x16x32_bf16 v[24:27], a[192:195], a[196:199], v[24:27]
s_add_u32 m0, s92, s67
s_add_u32 s61, s61, 128
v_fmac_f32 v137, v123, v119
v_mfma_f32_16x16x32_bf16 v[60:63], a[236:239], a[196:199], v[60:63]
buffer_load_dwordx4 v245, s[36:39], s62 offen lds
v_mfma_f32_16x16x32_bf16 v[96:99], a[220:223], a[196:199], v[96:99]
ds_read_b128 a[204:207], v246 offset:6144
v_fmac_f32 v138, v124, v119
v_mfma_f32_16x16x32_bf16 v[28:31], a[192:195], a[208:211], v[28:31]
ds_read_b128 a[248:251], v246 offset:8192
s_add_u32 s62, s62, 128
v_mfma_f32_16x16x32_bf16 v[64:67], a[236:239], a[208:211], v[64:67]
s_add_u32 m0, s56, 49152
v_fmac_f32 v139, v125, v119
v_lshlrev_b32 v122, 16, v196
v_mfma_f32_16x16x32_bf16 v[100:103], a[220:223], a[208:211], v[100:103]
buffer_load_dwordx4 v236, s[48:51], s60 offen lds
v_mfma_f32_16x16x32_bf16 v[32:35], a[192:195], a[212:215], v[32:35]
s_waitcnt lgkmcnt(3)
ds_read_b128 a[200:203], v246 offset:10240
v_mfma_f32_16x16x32_bf16 v[68:71], a[236:239], a[212:215], v[68:71]
buffer_load_dwordx4 v237, s[48:51], s60 offen offset:1024 lds
v_mfma_f32_16x16x32_bf16 v[104:107], a[220:223], a[212:215], v[104:107]
ds_read_b128 a[244:247], v246 offset:12288
v_and_b32 v123, 4294901760, v196
v_mfma_f32_16x16x32_bf16 v[0:3], a[240:243], a[224:227], v[0:3]
buffer_load_dwordx4 v238, s[48:51], s60 offen offset:2048 lds
v_mfma_f32_16x16x32_bf16 v[36:39], a[232:235], a[224:227], v[36:39]
ds_read_b128 a[196:199], v246 offset:14336
v_lshlrev_b32 v124, 16, v197
v_mfma_f32_16x16x32_bf16 v[72:75], a[216:219], a[224:227], v[72:75]
ds_read_b128 a[208:211], v246 offset:16384
v_and_b32 v125, 4294901760, v197
v_mfma_f32_16x16x32_bf16 v[4:7], a[240:243], a[228:231], v[4:7]
s_waitcnt lgkmcnt(4)
s_add_u32 s60, s60, 393216
v_fmac_f32 v136, v122, v120
v_mfma_f32_16x16x32_bf16 v[40:43], a[232:235], a[228:231], v[40:43]
v_fmac_f32 v137, v123, v120
v_fmac_f32 v138, v124, v120
v_mfma_f32_16x16x32_bf16 v[76:79], a[216:219], a[228:231], v[76:79]
v_fmac_f32 v139, v125, v120
v_lshlrev_b32 v122, 16, v202
v_mfma_f32_16x16x32_bf16 v[8:11], a[240:243], a[252:255], v[8:11]
v_and_b32 v123, 4294901760, v202
v_lshlrev_b32 v124, 16, v203
v_mfma_f32_16x16x32_bf16 v[44:47], a[232:235], a[252:255], v[44:47]
v_and_b32 v125, 4294901760, v203
v_fmac_f32 v136, v122, v121
v_mfma_f32_16x16x32_bf16 v[80:83], a[216:219], a[252:255], v[80:83]
v_fmac_f32 v137, v123, v121
v_fmac_f32 v138, v124, v121
v_mfma_f32_16x16x32_bf16 v[12:15], a[240:243], a[204:207], v[12:15]
v_fmac_f32 v139, v125, v121
v_cvt_pk_bf16_f32 v160, v136, v137
v_mfma_f32_16x16x32_bf16 v[48:51], a[232:235], a[204:207], v[48:51]
v_cvt_pk_bf16_f32 v161, v138, v139
v_mfma_f32_16x16x32_bf16 v[84:87], a[216:219], a[204:207], v[84:87]
s_lshr_b32 s68, s33, 10
s_mul_i32 s68, s68, 294912
s_and_b32 s89, s33, 8
s_add_u32 s89, s89, s68
s_add_u32 s89, s89, s85
v_mfma_f32_16x16x32_bf16 v[16:19], a[240:243], a[248:251], v[16:19]
s_waitcnt lgkmcnt(0)
s_waitcnt vmcnt(11) lgkmcnt(0)
s_barrier
v_mfma_f32_16x16x32_bf16 v[52:55], a[232:235], a[248:251], v[52:55]
ds_read_b64_tr_b16 a[192:193], v228 offset:0
ds_read_b64_tr_b16 a[194:195], v228 offset:512
v_mfma_f32_16x16x32_bf16 v[88:91], a[216:219], a[248:251], v[88:91]
ds_read_b128 a[236:239], v231 offset:18432
ds_read_b64_tr_b16 a[212:213], v229 offset:0
v_mfma_f32_16x16x32_bf16 v[20:23], a[240:243], a[200:203], v[20:23]
ds_read_b64_tr_b16 a[214:215], v229 offset:512
ds_read_b64_tr_b16 a[220:221], v230 offset:0
v_mfma_f32_16x16x32_bf16 v[56:59], a[232:235], a[200:203], v[56:59]
ds_read_b64_tr_b16 a[222:223], v230 offset:512
ds_read_b128 a[224:227], v231 offset:20480
v_mfma_f32_16x16x32_bf16 v[92:95], a[216:219], a[200:203], v[92:95]
ds_read_b128 a[228:231], v231 offset:22528
s_add_u32 m0, s56, 61440
v_mfma_f32_16x16x32_bf16 v[24:27], a[240:243], a[244:247], v[24:27]
buffer_load_dwordx4 v236, s[48:51], s60 offen lds
v_mfma_f32_16x16x32_bf16 v[60:63], a[232:235], a[244:247], v[60:63]
ds_read_b128 a[252:255], v231 offset:24576
v_mfma_f32_16x16x32_bf16 v[96:99], a[216:219], a[244:247], v[96:99]
buffer_load_dwordx4 v237, s[48:51], s60 offen offset:1024 lds
v_mfma_f32_16x16x32_bf16 v[28:31], a[240:243], a[196:199], v[28:31]
ds_read_b128 a[204:207], v231 offset:26624
v_mfma_f32_16x16x32_bf16 v[64:67], a[232:235], a[196:199], v[64:67]
ds_read_b128 a[248:251], v231 offset:28672
v_mfma_f32_16x16x32_bf16 v[100:103], a[216:219], a[196:199], v[100:103]
buffer_load_dwordx4 v238, s[48:51], s60 offen offset:2048 lds
v_mfma_f32_16x16x32_bf16 v[32:35], a[240:243], a[208:211], v[32:35]
s_waitcnt lgkmcnt(4)
ds_read_b128 a[200:203], v231 offset:30720
v_mfma_f32_16x16x32_bf16 v[68:71], a[232:235], a[208:211], v[68:71]
s_add_u32 s60, s60, 393216
s_add_u32 m0, s57, 0
s_add_u32 s67, s61, 0
v_mfma_f32_16x16x32_bf16 v[104:107], a[216:219], a[208:211], v[104:107]
buffer_load_dwordx4 v239, s[36:39], s67 offen lds
v_mfma_f32_16x16x32_bf16 v[0:3], a[192:195], a[236:239], v[0:3]
ds_read_b128 a[244:247], v231 offset:32768
v_mfma_f32_16x16x32_bf16 v[36:39], a[212:215], a[236:239], v[36:39]
ds_read_b128 a[196:199], v231 offset:34816
v_mfma_f32_16x16x32_bf16 v[72:75], a[220:223], a[236:239], v[72:75]
buffer_load_dwordx4 v240, s[36:39], s67 offen offset:1024 lds
v_mfma_f32_16x16x32_bf16 v[4:7], a[192:195], a[224:227], v[4:7]
s_waitcnt lgkmcnt(3)
v_mfma_f32_16x16x32_bf16 v[40:43], a[212:215], a[224:227], v[40:43]
buffer_store_dwordx2 v[156:157], v241, s[52:55], s89 offen sc0 sc1
v_mfma_f32_16x16x32_bf16 v[76:79], a[220:223], a[224:227], v[76:79]
s_lshr_b32 s68, s34, 10
s_mul_i32 s68, s68, 294912
s_and_b32 s89, s34, 8
s_add_u32 s89, s89, s68
s_add_u32 s89, s89, s85
v_mfma_f32_16x16x32_bf16 v[8:11], a[192:195], a[228:231], v[8:11]
s_add_u32 m0, s57, 2048
s_add_u32 s67, s61, 20480
v_mfma_f32_16x16x32_bf16 v[44:47], a[212:215], a[228:231], v[44:47]
buffer_load_dwordx4 v239, s[36:39], s67 offen lds
v_mfma_f32_16x16x32_bf16 v[80:83], a[220:223], a[228:231], v[80:83]
buffer_store_dwordx2 v[158:159], v241, s[52:55], s89 offen sc0 sc1
v_mfma_f32_16x16x32_bf16 v[12:15], a[192:195], a[252:255], v[12:15]
s_lshr_b32 s68, s35, 10
s_mul_i32 s68, s68, 294912
s_and_b32 s89, s35, 8
s_add_u32 s89, s89, s68
s_add_u32 s89, s89, s85
v_mfma_f32_16x16x32_bf16 v[48:51], a[212:215], a[252:255], v[48:51]
buffer_load_dwordx4 v240, s[36:39], s67 offen offset:1024 lds
v_mfma_f32_16x16x32_bf16 v[84:87], a[220:223], a[252:255], v[84:87]
buffer_store_dwordx2 v[160:161], v241, s[52:55], s89 offen sc0 sc1
v_mfma_f32_16x16x32_bf16 v[16:19], a[192:195], a[204:207], v[16:19]
s_waitcnt vmcnt(18) lgkmcnt(0)
s_barrier
v_mfma_f32_16x16x32_bf16 v[52:55], a[212:215], a[204:207], v[52:55]
ds_read_b64_tr_b16 a[240:241], v228 offset:12288
ds_read_b64_tr_b16 a[242:243], v228 offset:12800
v_mfma_f32_16x16x32_bf16 v[88:91], a[220:223], a[204:207], v[88:91]
ds_read_b128 a[232:235], v246 offset:18432
ds_read_b64_tr_b16 a[208:209], v229 offset:12288
v_mfma_f32_16x16x32_bf16 v[20:23], a[192:195], a[248:251], v[20:23]
ds_read_b64_tr_b16 a[210:211], v229 offset:12800
ds_read_b64_tr_b16 a[216:217], v230 offset:12288
v_mfma_f32_16x16x32_bf16 v[56:59], a[212:215], a[248:251], v[56:59]
ds_read_b64_tr_b16 a[218:219], v230 offset:12800
ds_read_b128 a[236:239], v246 offset:20480
v_mfma_f32_16x16x32_bf16 v[92:95], a[220:223], a[248:251], v[92:95]
ds_read_b128 a[224:227], v246 offset:22528
s_and_b32 s67, s72, 0
v_mfma_f32_16x16x32_bf16 v[24:27], a[192:195], a[200:203], v[24:27]
s_add_u32 m0, s92, s67
s_add_u32 s61, s61, 128
v_accvgpr_read_b32 v168, a0
v_mfma_f32_16x16x32_bf16 v[60:63], a[212:215], a[200:203], v[60:63]
buffer_load_dwordx4 v245, s[36:39], s62 offen lds
v_mfma_f32_16x16x32_bf16 v[96:99], a[220:223], a[200:203], v[96:99]
ds_read_b128 a[228:231], v246 offset:24576
v_accvgpr_read_b32 v169, a1
v_mfma_f32_16x16x32_bf16 v[28:31], a[192:195], a[244:247], v[28:31]
ds_read_b128 a[252:255], v246 offset:26624
s_add_u32 s62, s62, 128
v_mfma_f32_16x16x32_bf16 v[64:67], a[212:215], a[244:247], v[64:67]
s_add_u32 m0, s56, 0
v_accvgpr_read_b32 v170, a2
v_accvgpr_read_b32 v171, a3
v_mfma_f32_16x16x32_bf16 v[100:103], a[220:223], a[244:247], v[100:103]
buffer_load_dwordx4 v236, s[48:51], s60 offen lds
v_mfma_f32_16x16x32_bf16 v[32:35], a[192:195], a[196:199], v[32:35]
s_waitcnt lgkmcnt(3)
ds_read_b128 a[204:207], v246 offset:28672
v_mfma_f32_16x16x32_bf16 v[68:71], a[212:215], a[196:199], v[68:71]
buffer_load_dwordx4 v237, s[48:51], s60 offen offset:1024 lds
v_mfma_f32_16x16x32_bf16 v[104:107], a[220:223], a[196:199], v[104:107]
ds_read_b128 a[248:251], v246 offset:30720
v_mul_f32 v168, v168, v242
v_mfma_f32_16x16x32_bf16 v[0:3], a[240:243], a[232:235], v[0:3]
buffer_load_dwordx4 v238, s[48:51], s60 offen offset:2048 lds
v_mfma_f32_16x16x32_bf16 v[36:39], a[208:211], a[232:235], v[36:39]
ds_read_b128 a[200:203], v246 offset:32768
v_mul_f32 v169, v169, v242
v_mfma_f32_16x16x32_bf16 v[72:75], a[216:219], a[232:235], v[72:75]
ds_read_b128 a[244:247], v246 offset:34816
v_mul_f32 v170, v170, v242
v_mfma_f32_16x16x32_bf16 v[4:7], a[240:243], a[236:239], v[4:7]
s_waitcnt lgkmcnt(4)
s_add_u32 s60, s60, 393216
v_mul_f32 v171, v171, v242
v_mfma_f32_16x16x32_bf16 v[40:43], a[208:211], a[236:239], v[40:43]
v_cvt_pk_bf16_f32 v108, v168, v169
v_cvt_pk_bf16_f32 v109, v170, v171
v_mfma_f32_16x16x32_bf16 v[76:79], a[216:219], a[236:239], v[76:79]
v_accvgpr_read_b32 v172, a8
v_accvgpr_read_b32 v173, a9
v_mfma_f32_16x16x32_bf16 v[8:11], a[240:243], a[224:227], v[8:11]
v_accvgpr_read_b32 v174, a10
v_accvgpr_read_b32 v175, a11
v_mfma_f32_16x16x32_bf16 v[44:47], a[208:211], a[224:227], v[44:47]
v_mul_f32 v172, v172, v242
v_mul_f32 v173, v173, v242
v_mfma_f32_16x16x32_bf16 v[80:83], a[216:219], a[224:227], v[80:83]
v_mul_f32 v174, v174, v242
v_mul_f32 v175, v175, v242
v_mfma_f32_16x16x32_bf16 v[12:15], a[240:243], a[228:231], v[12:15]
v_cvt_pk_bf16_f32 v110, v172, v173
v_cvt_pk_bf16_f32 v111, v174, v175
v_mfma_f32_16x16x32_bf16 v[48:51], a[208:211], a[228:231], v[48:51]
s_add_u32 s89, s84, 0
v_mfma_f32_16x16x32_bf16 v[84:87], a[216:219], a[228:231], v[84:87]
buffer_store_dwordx4 v[108:111], v241, s[52:55], s89 offen sc0 sc1
v_mfma_f32_16x16x32_bf16 v[16:19], a[240:243], a[252:255], v[16:19]
s_waitcnt lgkmcnt(0)
s_waitcnt vmcnt(15) lgkmcnt(0)
s_barrier
v_mfma_f32_16x16x32_bf16 v[52:55], a[208:211], a[252:255], v[52:55]
ds_read_b64_tr_b16 a[192:193], v228 offset:49152
ds_read_b64_tr_b16 a[194:195], v228 offset:49664
v_mfma_f32_16x16x32_bf16 v[88:91], a[216:219], a[252:255], v[88:91]
ds_read_b128 a[212:215], v164 offset:0
ds_read_b64_tr_b16 a[220:221], v229 offset:49152
v_mfma_f32_16x16x32_bf16 v[20:23], a[240:243], a[204:207], v[20:23]
ds_read_b64_tr_b16 a[222:223], v229 offset:49664
ds_read_b64_tr_b16 a[196:197], v230 offset:49152
v_mfma_f32_16x16x32_bf16 v[56:59], a[208:211], a[204:207], v[56:59]
ds_read_b64_tr_b16 a[198:199], v230 offset:49664
ds_read_b128 a[232:235], v164 offset:2048
v_mfma_f32_16x16x32_bf16 v[92:95], a[216:219], a[204:207], v[92:95]
ds_read_b128 a[236:239], v164 offset:4096
s_add_u32 m0, s56, 12288
v_mfma_f32_16x16x32_bf16 v[24:27], a[240:243], a[248:251], v[24:27]
buffer_load_dwordx4 v236, s[48:51], s60 offen lds
v_mfma_f32_16x16x32_bf16 v[60:63], a[208:211], a[248:251], v[60:63]
ds_read_b128 a[224:227], v164 offset:6144
v_accvgpr_read_b32 v168, a16
v_mfma_f32_16x16x32_bf16 v[96:99], a[216:219], a[248:251], v[96:99]
buffer_load_dwordx4 v237, s[48:51], s60 offen offset:1024 lds
v_mfma_f32_16x16x32_bf16 v[28:31], a[240:243], a[200:203], v[28:31]
ds_read_b128 a[228:231], v164 offset:8192
v_accvgpr_read_b32 v169, a17
v_mfma_f32_16x16x32_bf16 v[64:67], a[208:211], a[200:203], v[64:67]
ds_read_b128 a[252:255], v164 offset:10240
v_accvgpr_read_b32 v170, a18
v_mfma_f32_16x16x32_bf16 v[100:103], a[216:219], a[200:203], v[100:103]
buffer_load_dwordx4 v238, s[48:51], s60 offen offset:2048 lds
v_mfma_f32_16x16x32_bf16 v[32:35], a[240:243], a[244:247], v[32:35]
s_waitcnt lgkmcnt(4)
ds_read_b128 a[204:207], v164 offset:12288
v_mfma_f32_16x16x32_bf16 v[68:71], a[208:211], a[244:247], v[68:71]
s_add_u32 s60, s60, 393216
s_add_u32 m0, s57, 18432
s_add_u32 s67, s61, 0
v_mfma_f32_16x16x32_bf16 v[104:107], a[216:219], a[244:247], v[104:107]
buffer_load_dwordx4 v239, s[36:39], s67 offen lds
v_mfma_f32_16x16x32_bf16 v[0:3], a[192:195], a[212:215], v[0:3]
ds_read_b128 a[248:251], v164 offset:14336
v_accvgpr_read_b32 v171, a19
v_mfma_f32_16x16x32_bf16 v[36:39], a[220:223], a[212:215], v[36:39]
ds_read_b128 a[200:203], v164 offset:16384
v_mul_f32 v168, v168, v242
v_mfma_f32_16x16x32_bf16 v[72:75], a[196:199], a[212:215], v[72:75]
buffer_load_dwordx4 v240, s[36:39], s67 offen offset:1024 lds
v_mfma_f32_16x16x32_bf16 v[4:7], a[192:195], a[232:235], v[4:7]
s_waitcnt lgkmcnt(3)
v_mul_f32 v169, v169, v242
v_mul_f32 v170, v170, v242
v_mfma_f32_16x16x32_bf16 v[40:43], a[220:223], a[232:235], v[40:43]
v_mul_f32 v171, v171, v242
v_cvt_pk_bf16_f32 v112, v168, v169
v_mfma_f32_16x16x32_bf16 v[76:79], a[196:199], a[232:235], v[76:79]
v_cvt_pk_bf16_f32 v113, v170, v171
v_accvgpr_read_b32 v172, a24
v_mfma_f32_16x16x32_bf16 v[8:11], a[192:195], a[236:239], v[8:11]
s_add_u32 m0, s57, 20480
s_add_u32 s67, s61, 20480
v_accvgpr_read_b32 v173, a25
v_mfma_f32_16x16x32_bf16 v[44:47], a[220:223], a[236:239], v[44:47]
buffer_load_dwordx4 v239, s[36:39], s67 offen lds
v_mfma_f32_16x16x32_bf16 v[80:83], a[196:199], a[236:239], v[80:83]
v_accvgpr_read_b32 v174, a26
v_accvgpr_read_b32 v175, a27
v_mfma_f32_16x16x32_bf16 v[12:15], a[192:195], a[224:227], v[12:15]
v_mul_f32 v172, v172, v242
v_mul_f32 v173, v173, v242
v_mfma_f32_16x16x32_bf16 v[48:51], a[220:223], a[224:227], v[48:51]
buffer_load_dwordx4 v240, s[36:39], s67 offen offset:1024 lds
v_mfma_f32_16x16x32_bf16 v[84:87], a[196:199], a[224:227], v[84:87]
v_mul_f32 v174, v174, v242
v_mul_f32 v175, v175, v242
v_mfma_f32_16x16x32_bf16 v[16:19], a[192:195], a[228:231], v[16:19]
s_waitcnt vmcnt(19) lgkmcnt(0)
s_barrier
v_mfma_f32_16x16x32_bf16 v[52:55], a[220:223], a[228:231], v[52:55]
ds_read_b64_tr_b16 a[240:241], v228 offset:61440
ds_read_b64_tr_b16 a[242:243], v228 offset:61952
v_mfma_f32_16x16x32_bf16 v[88:91], a[196:199], a[228:231], v[88:91]
ds_read_b128 a[208:211], v247 offset:0
ds_read_b64_tr_b16 a[216:217], v229 offset:61440
v_mfma_f32_16x16x32_bf16 v[20:23], a[192:195], a[252:255], v[20:23]
ds_read_b64_tr_b16 a[218:219], v229 offset:61952
ds_read_b64_tr_b16 a[244:245], v230 offset:61440
v_mfma_f32_16x16x32_bf16 v[56:59], a[220:223], a[252:255], v[56:59]
ds_read_b64_tr_b16 a[246:247], v230 offset:61952
ds_read_b128 a[212:215], v247 offset:2048
v_mfma_f32_16x16x32_bf16 v[92:95], a[196:199], a[252:255], v[92:95]
ds_read_b128 a[232:235], v247 offset:4096
s_and_b32 s67, s72, 18432
v_mfma_f32_16x16x32_bf16 v[24:27], a[192:195], a[204:207], v[24:27]
s_add_u32 m0, s92, s67
s_add_u32 s61, s61, 128
v_cvt_pk_bf16_f32 v114, v172, v173
v_mfma_f32_16x16x32_bf16 v[60:63], a[220:223], a[204:207], v[60:63]
buffer_load_dwordx4 v245, s[36:39], s62 offen lds
v_mfma_f32_16x16x32_bf16 v[96:99], a[196:199], a[204:207], v[96:99]
ds_read_b128 a[236:239], v247 offset:6144
v_cvt_pk_bf16_f32 v115, v174, v175
v_mfma_f32_16x16x32_bf16 v[28:31], a[192:195], a[248:251], v[28:31]
ds_read_b128 a[224:227], v247 offset:8192
s_add_u32 s62, s62, 128
v_mfma_f32_16x16x32_bf16 v[64:67], a[220:223], a[248:251], v[64:67]
s_add_u32 m0, s56, 49152
v_mfma_f32_16x16x32_bf16 v[100:103], a[196:199], a[248:251], v[100:103]
buffer_load_dwordx4 v236, s[48:51], s60 offen lds
v_mfma_f32_16x16x32_bf16 v[32:35], a[192:195], a[200:203], v[32:35]
s_waitcnt lgkmcnt(3)
ds_read_b128 a[228:231], v247 offset:10240
v_mfma_f32_16x16x32_bf16 v[68:71], a[220:223], a[200:203], v[68:71]
buffer_load_dwordx4 v237, s[48:51], s60 offen offset:1024 lds
v_mfma_f32_16x16x32_bf16 v[104:107], a[196:199], a[200:203], v[104:107]
ds_read_b128 a[252:255], v247 offset:12288
v_mfma_f32_16x16x32_bf16 v[0:3], a[240:243], a[208:211], v[0:3]
buffer_load_dwordx4 v238, s[48:51], s60 offen offset:2048 lds
v_mfma_f32_16x16x32_bf16 v[36:39], a[216:219], a[208:211], v[36:39]
ds_read_b128 a[204:207], v247 offset:14336
v_mfma_f32_16x16x32_bf16 v[72:75], a[244:247], a[208:211], v[72:75]
ds_read_b128 a[248:251], v247 offset:16384
s_add_u32 s89, s84, 294912
v_mfma_f32_16x16x32_bf16 v[4:7], a[240:243], a[212:215], v[4:7]
s_waitcnt lgkmcnt(4)
s_add_u32 s60, s60, 393216
v_mfma_f32_16x16x32_bf16 v[40:43], a[216:219], a[212:215], v[40:43]
buffer_store_dwordx4 v[112:115], v241, s[52:55], s89 offen sc0 sc1
v_mfma_f32_16x16x32_bf16 v[76:79], a[244:247], a[212:215], v[76:79]
v_accvgpr_read_b32 v168, a32
v_accvgpr_read_b32 v169, a33
v_mfma_f32_16x16x32_bf16 v[8:11], a[240:243], a[232:235], v[8:11]
v_accvgpr_read_b32 v170, a34
v_accvgpr_read_b32 v171, a35
v_mfma_f32_16x16x32_bf16 v[44:47], a[216:219], a[232:235], v[44:47]
v_mul_f32 v168, v168, v242
v_mul_f32 v169, v169, v242
v_mfma_f32_16x16x32_bf16 v[80:83], a[244:247], a[232:235], v[80:83]
v_mul_f32 v170, v170, v242
v_mul_f32 v171, v171, v242
v_mfma_f32_16x16x32_bf16 v[12:15], a[240:243], a[236:239], v[12:15]
v_cvt_pk_bf16_f32 v116, v168, v169
v_cvt_pk_bf16_f32 v117, v170, v171
v_mfma_f32_16x16x32_bf16 v[48:51], a[216:219], a[236:239], v[48:51]
v_accvgpr_read_b32 v172, a40
v_accvgpr_read_b32 v173, a41
v_mfma_f32_16x16x32_bf16 v[84:87], a[244:247], a[236:239], v[84:87]
v_accvgpr_read_b32 v174, a42
v_accvgpr_read_b32 v175, a43
v_mfma_f32_16x16x32_bf16 v[16:19], a[240:243], a[224:227], v[16:19]
s_waitcnt lgkmcnt(0)
s_waitcnt vmcnt(13) lgkmcnt(0)
s_barrier
v_mfma_f32_16x16x32_bf16 v[52:55], a[216:219], a[224:227], v[52:55]
ds_read_b64_tr_b16 a[192:193], v228 offset:0
ds_read_b64_tr_b16 a[194:195], v228 offset:512
v_mfma_f32_16x16x32_bf16 v[88:91], a[244:247], a[224:227], v[88:91]
ds_read_b128 a[220:223], v231 offset:0
ds_read_b64_tr_b16 a[196:197], v229 offset:0
v_mfma_f32_16x16x32_bf16 v[20:23], a[240:243], a[228:231], v[20:23]
ds_read_b64_tr_b16 a[198:199], v229 offset:512
ds_read_b64_tr_b16 a[200:201], v230 offset:0
v_mfma_f32_16x16x32_bf16 v[56:59], a[216:219], a[228:231], v[56:59]
ds_read_b64_tr_b16 a[202:203], v230 offset:512
ds_read_b128 a[208:211], v231 offset:2048
v_mfma_f32_16x16x32_bf16 v[92:95], a[244:247], a[228:231], v[92:95]
ds_read_b128 a[212:215], v231 offset:4096
s_add_u32 m0, s56, 61440
v_mfma_f32_16x16x32_bf16 v[24:27], a[240:243], a[252:255], v[24:27]
buffer_load_dwordx4 v236, s[48:51], s60 offen lds
v_mfma_f32_16x16x32_bf16 v[60:63], a[216:219], a[252:255], v[60:63]
ds_read_b128 a[232:235], v231 offset:6144
v_mul_f32 v172, v172, v242
v_mfma_f32_16x16x32_bf16 v[96:99], a[244:247], a[252:255], v[96:99]
buffer_load_dwordx4 v237, s[48:51], s60 offen offset:1024 lds
v_mfma_f32_16x16x32_bf16 v[28:31], a[240:243], a[204:207], v[28:31]
ds_read_b128 a[236:239], v231 offset:8192
v_mul_f32 v173, v173, v242
v_mfma_f32_16x16x32_bf16 v[64:67], a[216:219], a[204:207], v[64:67]
ds_read_b128 a[224:227], v231 offset:10240
v_mul_f32 v174, v174, v242
v_mfma_f32_16x16x32_bf16 v[100:103], a[244:247], a[204:207], v[100:103]
buffer_load_dwordx4 v238, s[48:51], s60 offen offset:2048 lds
v_mfma_f32_16x16x32_bf16 v[32:35], a[240:243], a[248:251], v[32:35]
s_waitcnt lgkmcnt(4)
ds_read_b128 a[228:231], v231 offset:12288
v_mfma_f32_16x16x32_bf16 v[68:71], a[216:219], a[248:251], v[68:71]
s_add_u32 s60, s60, 393216
s_add_u32 m0, s57, 4294868992
s_add_u32 s67, s61, 0
v_mfma_f32_16x16x32_bf16 v[104:107], a[244:247], a[248:251], v[104:107]
buffer_load_dwordx4 v239, s[36:39], s67 offen lds
v_mfma_f32_16x16x32_bf16 v[0:3], a[192:195], a[220:223], v[0:3]
ds_read_b128 a[252:255], v231 offset:14336
v_mul_f32 v175, v175, v242
v_mfma_f32_16x16x32_bf16 v[36:39], a[196:199], a[220:223], v[36:39]
ds_read_b128 a[204:207], v231 offset:16384
v_cvt_pk_bf16_f32 v118, v172, v173
v_mfma_f32_16x16x32_bf16 v[72:75], a[200:203], a[220:223], v[72:75]
buffer_load_dwordx4 v240, s[36:39], s67 offen offset:1024 lds
v_mfma_f32_16x16x32_bf16 v[4:7], a[192:195], a[208:211], v[4:7]
s_waitcnt lgkmcnt(3)
v_cvt_pk_bf16_f32 v119, v174, v175
s_add_u32 s89, s84, 589824
v_mfma_f32_16x16x32_bf16 v[40:43], a[196:199], a[208:211], v[40:43]
buffer_store_dwordx4 v[116:119], v241, s[52:55], s89 offen sc0 sc1
v_mfma_f32_16x16x32_bf16 v[76:79], a[200:203], a[208:211], v[76:79]
v_accvgpr_read_b32 v168, a48
v_accvgpr_read_b32 v169, a49
v_mfma_f32_16x16x32_bf16 v[8:11], a[192:195], a[212:215], v[8:11]
s_add_u32 m0, s57, 4294871040
s_add_u32 s67, s61, 20480
v_accvgpr_read_b32 v170, a50
v_mfma_f32_16x16x32_bf16 v[44:47], a[196:199], a[212:215], v[44:47]
buffer_load_dwordx4 v239, s[36:39], s67 offen lds
v_mfma_f32_16x16x32_bf16 v[80:83], a[200:203], a[212:215], v[80:83]
v_accvgpr_read_b32 v171, a51
v_mul_f32 v168, v168, v242
v_mfma_f32_16x16x32_bf16 v[12:15], a[192:195], a[232:235], v[12:15]
v_mul_f32 v169, v169, v242
v_mul_f32 v170, v170, v242
v_mfma_f32_16x16x32_bf16 v[48:51], a[196:199], a[232:235], v[48:51]
buffer_load_dwordx4 v240, s[36:39], s67 offen offset:1024 lds
v_mfma_f32_16x16x32_bf16 v[84:87], a[200:203], a[232:235], v[84:87]
v_mul_f32 v171, v171, v242
v_cvt_pk_bf16_f32 v120, v168, v169
v_mfma_f32_16x16x32_bf16 v[16:19], a[192:195], a[236:239], v[16:19]
s_waitcnt vmcnt(17) lgkmcnt(0)
s_barrier
v_mfma_f32_16x16x32_bf16 v[52:55], a[196:199], a[236:239], v[52:55]
ds_read_b64_tr_b16 a[240:241], v228 offset:12288
ds_read_b64_tr_b16 a[242:243], v228 offset:12800
v_mfma_f32_16x16x32_bf16 v[88:91], a[200:203], a[236:239], v[88:91]
ds_read_b128 a[216:219], v246 offset:0
ds_read_b64_tr_b16 a[244:245], v229 offset:12288
v_mfma_f32_16x16x32_bf16 v[20:23], a[192:195], a[224:227], v[20:23]
ds_read_b64_tr_b16 a[246:247], v229 offset:12800
ds_read_b64_tr_b16 a[248:249], v230 offset:12288
v_mfma_f32_16x16x32_bf16 v[56:59], a[196:199], a[224:227], v[56:59]
ds_read_b64_tr_b16 a[250:251], v230 offset:12800
ds_read_b128 a[220:223], v246 offset:2048
v_mfma_f32_16x16x32_bf16 v[92:95], a[200:203], a[224:227], v[92:95]
ds_read_b128 a[208:211], v246 offset:4096
s_and_b32 s67, s72, 4294868992
v_mfma_f32_16x16x32_bf16 v[24:27], a[192:195], a[228:231], v[24:27]
s_add_u32 m0, s92, s67
s_add_u32 s61, s61, 128
v_cvt_pk_bf16_f32 v121, v170, v171
v_mfma_f32_16x16x32_bf16 v[60:63], a[196:199], a[228:231], v[60:63]
buffer_load_dwordx4 v245, s[36:39], s62 offen lds
v_mfma_f32_16x16x32_bf16 v[96:99], a[200:203], a[228:231], v[96:99]
ds_read_b128 a[212:215], v246 offset:6144
v_accvgpr_read_b32 v172, a56
v_mfma_f32_16x16x32_bf16 v[28:31], a[192:195], a[252:255], v[28:31]
ds_read_b128 a[232:235], v246 offset:8192
s_add_u32 s62, s62, 128
v_mfma_f32_16x16x32_bf16 v[64:67], a[196:199], a[252:255], v[64:67]
s_add_u32 m0, s56, 0
v_accvgpr_read_b32 v173, a57
v_accvgpr_read_b32 v174, a58
v_mfma_f32_16x16x32_bf16 v[100:103], a[200:203], a[252:255], v[100:103]
buffer_load_dwordx4 v236, s[48:51], s60 offen lds
v_mfma_f32_16x16x32_bf16 v[32:35], a[192:195], a[204:207], v[32:35]
s_waitcnt lgkmcnt(3)
ds_read_b128 a[236:239], v246 offset:10240
v_mfma_f32_16x16x32_bf16 v[68:71], a[196:199], a[204:207], v[68:71]
buffer_load_dwordx4 v237, s[48:51], s60 offen offset:1024 lds
v_mfma_f32_16x16x32_bf16 v[104:107], a[200:203], a[204:207], v[104:107]
ds_read_b128 a[224:227], v246 offset:12288
v_accvgpr_read_b32 v175, a59
v_mfma_f32_16x16x32_bf16 v[0:3], a[240:243], a[216:219], v[0:3]
buffer_load_dwordx4 v238, s[48:51], s60 offen offset:2048 lds
v_mfma_f32_16x16x32_bf16 v[36:39], a[244:247], a[216:219], v[36:39]
ds_read_b128 a[228:231], v246 offset:14336
v_mul_f32 v172, v172, v242
v_mfma_f32_16x16x32_bf16 v[72:75], a[248:251], a[216:219], v[72:75]
ds_read_b128 a[252:255], v246 offset:16384
v_mul_f32 v173, v173, v242
v_mfma_f32_16x16x32_bf16 v[4:7], a[240:243], a[220:223], v[4:7]
s_waitcnt lgkmcnt(4)
s_add_u32 s60, s60, 393216
v_mul_f32 v174, v174, v242
v_mfma_f32_16x16x32_bf16 v[40:43], a[244:247], a[220:223], v[40:43]
v_mul_f32 v175, v175, v242
v_cvt_pk_bf16_f32 v122, v172, v173
v_mfma_f32_16x16x32_bf16 v[76:79], a[248:251], a[220:223], v[76:79]
v_cvt_pk_bf16_f32 v123, v174, v175
s_add_u32 s89, s84, 884736
v_mfma_f32_16x16x32_bf16 v[8:11], a[240:243], a[208:211], v[8:11]
buffer_store_dwordx4 v[120:123], v241, s[52:55], s89 offen sc0 sc1
v_mfma_f32_16x16x32_bf16 v[44:47], a[244:247], a[208:211], v[44:47]
v_accvgpr_read_b32 v168, a64
v_accvgpr_read_b32 v169, a65
v_mfma_f32_16x16x32_bf16 v[80:83], a[248:251], a[208:211], v[80:83]
v_accvgpr_read_b32 v170, a66
v_accvgpr_read_b32 v171, a67
v_mfma_f32_16x16x32_bf16 v[12:15], a[240:243], a[212:215], v[12:15]
v_mul_f32 v168, v168, v242
v_mul_f32 v169, v169, v242
v_mfma_f32_16x16x32_bf16 v[48:51], a[244:247], a[212:215], v[48:51]
v_mul_f32 v170, v170, v242
v_mul_f32 v171, v171, v242
v_mfma_f32_16x16x32_bf16 v[84:87], a[248:251], a[212:215], v[84:87]
v_cvt_pk_bf16_f32 v124, v168, v169
v_cvt_pk_bf16_f32 v125, v170, v171
v_mfma_f32_16x16x32_bf16 v[16:19], a[240:243], a[232:235], v[16:19]
s_waitcnt lgkmcnt(0)
s_waitcnt vmcnt(14) lgkmcnt(0)
s_barrier
v_mfma_f32_16x16x32_bf16 v[52:55], a[244:247], a[232:235], v[52:55]
ds_read_b64_tr_b16 a[192:193], v228 offset:49152
ds_read_b64_tr_b16 a[194:195], v228 offset:49664
v_mfma_f32_16x16x32_bf16 v[88:91], a[248:251], a[232:235], v[88:91]
ds_read_b128 a[196:199], v231 offset:18432
ds_read_b64_tr_b16 a[200:201], v229 offset:49152
v_mfma_f32_16x16x32_bf16 v[20:23], a[240:243], a[236:239], v[20:23]
ds_read_b64_tr_b16 a[202:203], v229 offset:49664
ds_read_b64_tr_b16 a[204:205], v230 offset:49152
v_mfma_f32_16x16x32_bf16 v[56:59], a[244:247], a[236:239], v[56:59]
ds_read_b64_tr_b16 a[206:207], v230 offset:49664
ds_read_b128 a[216:219], v231 offset:20480
v_mfma_f32_16x16x32_bf16 v[92:95], a[248:251], a[236:239], v[92:95]
ds_read_b128 a[220:223], v231 offset:22528
s_add_u32 m0, s56, 12288
v_mfma_f32_16x16x32_bf16 v[24:27], a[240:243], a[224:227], v[24:27]
buffer_load_dwordx4 v236, s[48:51], s60 offen lds
v_mfma_f32_16x16x32_bf16 v[60:63], a[244:247], a[224:227], v[60:63]
ds_read_b128 a[208:211], v231 offset:24576
v_accvgpr_read_b32 v172, a72
v_mfma_f32_16x16x32_bf16 v[96:99], a[248:251], a[224:227], v[96:99]
buffer_load_dwordx4 v237, s[48:51], s60 offen offset:1024 lds
v_mfma_f32_16x16x32_bf16 v[28:31], a[240:243], a[228:231], v[28:31]
ds_read_b128 a[212:215], v231 offset:26624
v_accvgpr_read_b32 v173, a73
v_mfma_f32_16x16x32_bf16 v[64:67], a[244:247], a[228:231], v[64:67]
ds_read_b128 a[232:235], v231 offset:28672
v_accvgpr_read_b32 v174, a74
v_mfma_f32_16x16x32_bf16 v[100:103], a[248:251], a[228:231], v[100:103]
buffer_load_dwordx4 v238, s[48:51], s60 offen offset:2048 lds
v_mfma_f32_16x16x32_bf16 v[32:35], a[240:243], a[252:255], v[32:35]
s_waitcnt lgkmcnt(4)
ds_read_b128 a[236:239], v231 offset:30720
v_mfma_f32_16x16x32_bf16 v[68:71], a[244:247], a[252:255], v[68:71]
s_add_u32 s60, s60, 393216
s_add_u32 m0, s57, 0
s_add_u32 s67, s61, 0
v_mfma_f32_16x16x32_bf16 v[104:107], a[248:251], a[252:255], v[104:107]
buffer_load_dwordx4 v239, s[36:39], s67 offen lds
v_mfma_f32_16x16x32_bf16 v[0:3], a[192:195], a[196:199], v[0:3]
ds_read_b128 a[224:227], v231 offset:32768
v_accvgpr_read_b32 v175, a75
v_mfma_f32_16x16x32_bf16 v[36:39], a[200:203], a[196:199], v[36:39]
ds_read_b128 a[228:231], v231 offset:34816
v_mul_f32 v172, v172, v242
v_mfma_f32_16x16x32_bf16 v[72:75], a[204:207], a[196:199], v[72:75]
buffer_load_dwordx4 v240, s[36:39], s67 offen offset:1024 lds
v_mfma_f32_16x16x32_bf16 v[4:7], a[192:195], a[216:219], v[4:7]
s_waitcnt lgkmcnt(3)
v_mul_f32 v173, v173, v242
v_mul_f32 v174, v174, v242
v_mfma_f32_16x16x32_bf16 v[40:43], a[200:203], a[216:219], v[40:43]
v_mul_f32 v175, v175, v242
v_cvt_pk_bf16_f32 v126, v172, v173
v_mfma_f32_16x16x32_bf16 v[76:79], a[204:207], a[216:219], v[76:79]
v_cvt_pk_bf16_f32 v127, v174, v175
s_add_u32 s89, s84, 1179648
v_mfma_f32_16x16x32_bf16 v[8:11], a[192:195], a[220:223], v[8:11]
s_add_u32 m0, s57, 2048
s_add_u32 s67, s61, 20480
v_mfma_f32_16x16x32_bf16 v[44:47], a[200:203], a[220:223], v[44:47]
buffer_load_dwordx4 v239, s[36:39], s67 offen lds
v_mfma_f32_16x16x32_bf16 v[80:83], a[204:207], a[220:223], v[80:83]
buffer_store_dwordx4 v[124:127], v241, s[52:55], s89 offen sc0 sc1
v_mfma_f32_16x16x32_bf16 v[12:15], a[192:195], a[208:211], v[12:15]
v_accvgpr_read_b32 v168, a80
v_accvgpr_read_b32 v169, a81
v_mfma_f32_16x16x32_bf16 v[48:51], a[200:203], a[208:211], v[48:51]
buffer_load_dwordx4 v240, s[36:39], s67 offen offset:1024 lds
v_mfma_f32_16x16x32_bf16 v[84:87], a[204:207], a[208:211], v[84:87]
v_accvgpr_read_b32 v170, a82
v_accvgpr_read_b32 v171, a83
v_mfma_f32_16x16x32_bf16 v[16:19], a[192:195], a[212:215], v[16:19]
s_waitcnt vmcnt(18) lgkmcnt(0)
s_barrier
v_mfma_f32_16x16x32_bf16 v[52:55], a[200:203], a[212:215], v[52:55]
ds_read_b64_tr_b16 a[240:241], v228 offset:61440
ds_read_b64_tr_b16 a[242:243], v228 offset:61952
v_mfma_f32_16x16x32_bf16 v[88:91], a[204:207], a[212:215], v[88:91]
ds_read_b128 a[244:247], v246 offset:18432
ds_read_b64_tr_b16 a[248:249], v229 offset:61440
v_mfma_f32_16x16x32_bf16 v[20:23], a[192:195], a[232:235], v[20:23]
ds_read_b64_tr_b16 a[250:251], v229 offset:61952
ds_read_b64_tr_b16 a[252:253], v230 offset:61440
v_mfma_f32_16x16x32_bf16 v[56:59], a[200:203], a[232:235], v[56:59]
ds_read_b64_tr_b16 a[254:255], v230 offset:61952
ds_read_b128 a[196:199], v246 offset:20480
v_mfma_f32_16x16x32_bf16 v[92:95], a[204:207], a[232:235], v[92:95]
ds_read_b128 a[216:219], v246 offset:22528
s_and_b32 s67, s72, 0
v_mfma_f32_16x16x32_bf16 v[24:27], a[192:195], a[236:239], v[24:27]
s_add_u32 m0, s92, s67
s_add_u32 s61, s61, 128
v_mul_f32 v168, v168, v242
v_mfma_f32_16x16x32_bf16 v[60:63], a[200:203], a[236:239], v[60:63]
buffer_load_dwordx4 v245, s[36:39], s62 offen lds
v_mfma_f32_16x16x32_bf16 v[96:99], a[204:207], a[236:239], v[96:99]
ds_read_b128 a[220:223], v246 offset:24576
v_mul_f32 v169, v169, v242
v_mfma_f32_16x16x32_bf16 v[28:31], a[192:195], a[224:227], v[28:31]
ds_read_b128 a[208:211], v246 offset:26624
s_add_u32 s62, s62, 128
v_mfma_f32_16x16x32_bf16 v[64:67], a[200:203], a[224:227], v[64:67]
s_add_u32 m0, s56, 49152
v_mul_f32 v170, v170, v242
v_mul_f32 v171, v171, v242
v_mfma_f32_16x16x32_bf16 v[100:103], a[204:207], a[224:227], v[100:103]
buffer_load_dwordx4 v236, s[48:51], s60 offen lds
v_mfma_f32_16x16x32_bf16 v[32:35], a[192:195], a[228:231], v[32:35]
s_waitcnt lgkmcnt(3)
ds_read_b128 a[212:215], v246 offset:28672
v_mfma_f32_16x16x32_bf16 v[68:71], a[200:203], a[228:231], v[68:71]
buffer_load_dwordx4 v237, s[48:51], s60 offen offset:1024 lds
v_mfma_f32_16x16x32_bf16 v[104:107], a[204:207], a[228:231], v[104:107]
ds_read_b128 a[232:235], v246 offset:30720
v_cvt_pk_bf16_f32 v128, v168, v169
v_mfma_f32_16x16x32_bf16 v[0:3], a[240:243], a[244:247], v[0:3]
buffer_load_dwordx4 v238, s[48:51], s60 offen offset:2048 lds
v_mfma_f32_16x16x32_bf16 v[36:39], a[248:251], a[244:247], v[36:39]
ds_read_b128 a[236:239], v246 offset:32768
v_cvt_pk_bf16_f32 v129, v170, v171
v_mfma_f32_16x16x32_bf16 v[72:75], a[252:255], a[244:247], v[72:75]
ds_read_b128 a[224:227], v246 offset:34816
v_accvgpr_read_b32 v172, a88
v_mfma_f32_16x16x32_bf16 v[4:7], a[240:243], a[196:199], v[4:7]
s_waitcnt lgkmcnt(4)
s_add_u32 s60, s60, 393216
v_accvgpr_read_b32 v173, a89
v_mfma_f32_16x16x32_bf16 v[40:43], a[248:251], a[196:199], v[40:43]
v_accvgpr_read_b32 v174, a90
v_accvgpr_read_b32 v175, a91
v_mfma_f32_16x16x32_bf16 v[76:79], a[252:255], a[196:199], v[76:79]
v_mul_f32 v172, v172, v242
v_mul_f32 v173, v173, v242
v_mfma_f32_16x16x32_bf16 v[8:11], a[240:243], a[216:219], v[8:11]
v_mul_f32 v174, v174, v242
v_mul_f32 v175, v175, v242
v_mfma_f32_16x16x32_bf16 v[44:47], a[248:251], a[216:219], v[44:47]
v_cvt_pk_bf16_f32 v130, v172, v173
v_cvt_pk_bf16_f32 v131, v174, v175
v_mfma_f32_16x16x32_bf16 v[80:83], a[252:255], a[216:219], v[80:83]
s_add_u32 s89, s84, 1474560
v_mfma_f32_16x16x32_bf16 v[12:15], a[240:243], a[220:223], v[12:15]
buffer_store_dwordx4 v[128:131], v241, s[52:55], s89 offen sc0 sc1
v_mfma_f32_16x16x32_bf16 v[48:51], a[248:251], a[220:223], v[48:51]
v_accvgpr_read_b32 v168, a4
v_accvgpr_read_b32 v169, a5
v_mfma_f32_16x16x32_bf16 v[84:87], a[252:255], a[220:223], v[84:87]
v_accvgpr_read_b32 v170, a6
v_accvgpr_read_b32 v171, a7
v_mfma_f32_16x16x32_bf16 v[16:19], a[240:243], a[208:211], v[16:19]
s_waitcnt lgkmcnt(0)
s_waitcnt vmcnt(14) lgkmcnt(0)
s_barrier
v_mfma_f32_16x16x32_bf16 v[52:55], a[248:251], a[208:211], v[52:55]
ds_read_b64_tr_b16 a[192:193], v228 offset:0
ds_read_b64_tr_b16 a[194:195], v228 offset:512
v_mfma_f32_16x16x32_bf16 v[88:91], a[252:255], a[208:211], v[88:91]
ds_read_b128 a[200:203], v164 offset:0
ds_read_b64_tr_b16 a[204:205], v229 offset:0
v_mfma_f32_16x16x32_bf16 v[20:23], a[240:243], a[212:215], v[20:23]
ds_read_b64_tr_b16 a[206:207], v229 offset:512
ds_read_b64_tr_b16 a[228:229], v230 offset:0
v_mfma_f32_16x16x32_bf16 v[56:59], a[248:251], a[212:215], v[56:59]
ds_read_b64_tr_b16 a[230:231], v230 offset:512
ds_read_b128 a[244:247], v164 offset:2048
v_mfma_f32_16x16x32_bf16 v[92:95], a[252:255], a[212:215], v[92:95]
ds_read_b128 a[196:199], v164 offset:4096
s_add_u32 m0, s56, 61440
v_mfma_f32_16x16x32_bf16 v[24:27], a[240:243], a[232:235], v[24:27]
buffer_load_dwordx4 v236, s[48:51], s60 offen lds
v_mfma_f32_16x16x32_bf16 v[60:63], a[248:251], a[232:235], v[60:63]
ds_read_b128 a[216:219], v164 offset:6144
v_mul_f32 v168, v168, v243
v_mfma_f32_16x16x32_bf16 v[96:99], a[252:255], a[232:235], v[96:99]
buffer_load_dwordx4 v237, s[48:51], s60 offen offset:1024 lds
v_mfma_f32_16x16x32_bf16 v[28:31], a[240:243], a[236:239], v[28:31]
ds_read_b128 a[220:223], v164 offset:8192
v_mul_f32 v169, v169, v243
v_mfma_f32_16x16x32_bf16 v[64:67], a[248:251], a[236:239], v[64:67]
ds_read_b128 a[208:211], v164 offset:10240
v_mul_f32 v170, v170, v243
v_mfma_f32_16x16x32_bf16 v[100:103], a[252:255], a[236:239], v[100:103]
buffer_load_dwordx4 v238, s[48:51], s60 offen offset:2048 lds
v_mfma_f32_16x16x32_bf16 v[32:35], a[240:243], a[224:227], v[32:35]
s_waitcnt lgkmcnt(4)
ds_read_b128 a[212:215], v164 offset:12288
v_mfma_f32_16x16x32_bf16 v[68:71], a[248:251], a[224:227], v[68:71]
s_add_u32 s60, s60, 393216
ds_read_b128 a[232:235], v164 offset:14336
v_mfma_f32_16x16x32_bf16 v[104:107], a[252:255], a[224:227], v[104:107]
v_mul_f32 v171, v171, v243
v_cvt_pk_bf16_f32 v132, v168, v169
v_mfma_f32_16x16x32_bf16 v[0:3], a[192:195], a[200:203], v[0:3]
v_cvt_pk_bf16_f32 v133, v170, v171
v_accvgpr_read_b32 v172, a12
v_mfma_f32_16x16x32_bf16 v[36:39], a[204:207], a[200:203], v[36:39]
ds_read_b128 a[236:239], v164 offset:16384
v_accvgpr_read_b32 v173, a13
v_mfma_f32_16x16x32_bf16 v[72:75], a[228:231], a[200:203], v[72:75]
v_accvgpr_read_b32 v174, a14
v_accvgpr_read_b32 v175, a15
v_mfma_f32_16x16x32_bf16 v[4:7], a[192:195], a[244:247], v[4:7]
s_waitcnt lgkmcnt(3)
v_mul_f32 v172, v172, v243
v_mul_f32 v173, v173, v243
v_mfma_f32_16x16x32_bf16 v[40:43], a[204:207], a[244:247], v[40:43]
v_mul_f32 v174, v174, v243
v_mul_f32 v175, v175, v243
v_mfma_f32_16x16x32_bf16 v[76:79], a[228:231], a[244:247], v[76:79]
v_cvt_pk_bf16_f32 v134, v172, v173
v_cvt_pk_bf16_f32 v135, v174, v175
v_mfma_f32_16x16x32_bf16 v[8:11], a[192:195], a[196:199], v[8:11]
s_add_u32 s89, s84, 0
v_mfma_f32_16x16x32_bf16 v[44:47], a[204:207], a[196:199], v[44:47]
buffer_store_dwordx4 v[132:135], v241, s[52:55], s89 offen offset:1024 sc0 sc1
v_mfma_f32_16x16x32_bf16 v[80:83], a[228:231], a[196:199], v[80:83]
v_accvgpr_read_b32 v168, a20
v_accvgpr_read_b32 v169, a21
v_mfma_f32_16x16x32_bf16 v[12:15], a[192:195], a[216:219], v[12:15]
v_accvgpr_read_b32 v170, a22
v_accvgpr_read_b32 v171, a23
v_mfma_f32_16x16x32_bf16 v[48:51], a[204:207], a[216:219], v[48:51]
v_mul_f32 v168, v168, v243
v_mul_f32 v169, v169, v243
v_mfma_f32_16x16x32_bf16 v[84:87], a[228:231], a[216:219], v[84:87]
v_mul_f32 v170, v170, v243
v_mul_f32 v171, v171, v243
v_mfma_f32_16x16x32_bf16 v[16:19], a[192:195], a[220:223], v[16:19]
s_waitcnt vmcnt(14) lgkmcnt(0)
s_barrier
v_mfma_f32_16x16x32_bf16 v[52:55], a[204:207], a[220:223], v[52:55]
ds_read_b64_tr_b16 a[240:241], v228 offset:12288
ds_read_b64_tr_b16 a[242:243], v228 offset:12800
v_mfma_f32_16x16x32_bf16 v[88:91], a[228:231], a[220:223], v[88:91]
ds_read_b128 a[248:251], v247 offset:0
ds_read_b64_tr_b16 a[252:253], v229 offset:12288
v_mfma_f32_16x16x32_bf16 v[20:23], a[192:195], a[208:211], v[20:23]
ds_read_b64_tr_b16 a[254:255], v229 offset:12800
ds_read_b64_tr_b16 a[224:225], v230 offset:12288
v_mfma_f32_16x16x32_bf16 v[56:59], a[204:207], a[208:211], v[56:59]
ds_read_b64_tr_b16 a[226:227], v230 offset:12800
ds_read_b128 a[200:203], v247 offset:2048
v_mfma_f32_16x16x32_bf16 v[92:95], a[228:231], a[208:211], v[92:95]
ds_read_b128 a[244:247], v247 offset:4096
v_cvt_pk_bf16_f32 v136, v168, v169
v_mfma_f32_16x16x32_bf16 v[24:27], a[192:195], a[212:215], v[24:27]
ds_read_b128 a[196:199], v247 offset:6144
v_cvt_pk_bf16_f32 v137, v170, v171
v_mfma_f32_16x16x32_bf16 v[60:63], a[204:207], a[212:215], v[60:63]
ds_read_b128 a[216:219], v247 offset:8192
v_accvgpr_read_b32 v172, a28
v_mfma_f32_16x16x32_bf16 v[96:99], a[228:231], a[212:215], v[96:99]
ds_read_b128 a[220:223], v247 offset:10240
v_accvgpr_read_b32 v173, a29
v_mfma_f32_16x16x32_bf16 v[28:31], a[192:195], a[232:235], v[28:31]
v_accvgpr_read_b32 v174, a30
v_accvgpr_read_b32 v175, a31
v_mfma_f32_16x16x32_bf16 v[64:67], a[204:207], a[232:235], v[64:67]
v_mul_f32 v172, v172, v243
v_mul_f32 v173, v173, v243
v_mfma_f32_16x16x32_bf16 v[100:103], a[228:231], a[232:235], v[100:103]
ds_read_b128 a[208:211], v247 offset:12288
v_mul_f32 v174, v174, v243
v_mfma_f32_16x16x32_bf16 v[32:35], a[192:195], a[236:239], v[32:35]
s_waitcnt lgkmcnt(5)
v_mul_f32 v175, v175, v243
v_cvt_pk_bf16_f32 v138, v172, v173
v_mfma_f32_16x16x32_bf16 v[68:71], a[204:207], a[236:239], v[68:71]
ds_read_b128 a[212:215], v247 offset:14336
v_cvt_pk_bf16_f32 v139, v174, v175
v_mfma_f32_16x16x32_bf16 v[104:107], a[228:231], a[236:239], v[104:107]
s_add_u32 s89, s84, 294912
v_mfma_f32_16x16x32_bf16 v[0:3], a[240:243], a[248:251], v[0:3]
buffer_store_dwordx4 v[136:139], v241, s[52:55], s89 offen offset:1024 sc0 sc1
v_mfma_f32_16x16x32_bf16 v[36:39], a[252:255], a[248:251], v[36:39]
ds_read_b128 a[232:235], v247 offset:16384
v_accvgpr_read_b32 v168, a36
v_mfma_f32_16x16x32_bf16 v[72:75], a[224:227], a[248:251], v[72:75]
v_accvgpr_read_b32 v169, a37
v_accvgpr_read_b32 v170, a38
v_mfma_f32_16x16x32_bf16 v[4:7], a[240:243], a[200:203], v[4:7]
s_waitcnt lgkmcnt(3)
v_accvgpr_read_b32 v171, a39
v_mul_f32 v168, v168, v243
v_mfma_f32_16x16x32_bf16 v[40:43], a[252:255], a[200:203], v[40:43]
v_mul_f32 v169, v169, v243
v_mul_f32 v170, v170, v243
v_mfma_f32_16x16x32_bf16 v[76:79], a[224:227], a[200:203], v[76:79]
v_mul_f32 v171, v171, v243
v_cvt_pk_bf16_f32 v140, v168, v169
v_mfma_f32_16x16x32_bf16 v[8:11], a[240:243], a[244:247], v[8:11]
v_cvt_pk_bf16_f32 v141, v170, v171
v_accvgpr_read_b32 v172, a44
v_mfma_f32_16x16x32_bf16 v[44:47], a[252:255], a[244:247], v[44:47]
v_accvgpr_read_b32 v173, a45
v_accvgpr_read_b32 v174, a46
v_mfma_f32_16x16x32_bf16 v[80:83], a[224:227], a[244:247], v[80:83]
v_accvgpr_read_b32 v175, a47
v_mul_f32 v172, v172, v243
v_mfma_f32_16x16x32_bf16 v[12:15], a[240:243], a[196:199], v[12:15]
v_mul_f32 v173, v173, v243
v_mul_f32 v174, v174, v243
v_mfma_f32_16x16x32_bf16 v[48:51], a[252:255], a[196:199], v[48:51]
v_mul_f32 v175, v175, v243
v_cvt_pk_bf16_f32 v142, v172, v173
v_mfma_f32_16x16x32_bf16 v[84:87], a[224:227], a[196:199], v[84:87]
v_cvt_pk_bf16_f32 v143, v174, v175
s_add_u32 s89, s84, 589824
v_mfma_f32_16x16x32_bf16 v[16:19], a[240:243], a[216:219], v[16:19]
s_waitcnt vmcnt(6) lgkmcnt(0)
s_barrier
v_mfma_f32_16x16x32_bf16 v[52:55], a[252:255], a[216:219], v[52:55]
ds_read_b64_tr_b16 a[192:193], v228 offset:49152
ds_read_b64_tr_b16 a[194:195], v228 offset:49664
v_mfma_f32_16x16x32_bf16 v[88:91], a[224:227], a[216:219], v[88:91]
ds_read_b128 a[204:207], v231 offset:0
ds_read_b64_tr_b16 a[228:229], v229 offset:49152
v_mfma_f32_16x16x32_bf16 v[20:23], a[240:243], a[220:223], v[20:23]
ds_read_b64_tr_b16 a[230:231], v229 offset:49664
ds_read_b64_tr_b16 a[236:237], v230 offset:49152
v_mfma_f32_16x16x32_bf16 v[56:59], a[252:255], a[220:223], v[56:59]
ds_read_b64_tr_b16 a[238:239], v230 offset:49664
ds_read_b128 a[248:251], v231 offset:2048
v_mfma_f32_16x16x32_bf16 v[92:95], a[224:227], a[220:223], v[92:95]
ds_read_b128 a[200:203], v231 offset:4096
s_add_u32 m0, s58, 24576
v_mfma_f32_16x16x32_bf16 v[24:27], a[240:243], a[208:211], v[24:27]
s_add_u32 s67, s63, 24576
v_mfma_f32_16x16x32_bf16 v[60:63], a[252:255], a[208:211], v[60:63]
buffer_load_dwordx4 v232, s[40:43], s67 offen nt lds
v_mfma_f32_16x16x32_bf16 v[96:99], a[224:227], a[208:211], v[96:99]
ds_read_b128 a[244:247], v231 offset:6144
v_mfma_f32_16x16x32_bf16 v[28:31], a[240:243], a[212:215], v[28:31]
buffer_load_dwordx4 v233, s[40:43], s67 offen offset:1024 nt lds
v_mfma_f32_16x16x32_bf16 v[64:67], a[252:255], a[212:215], v[64:67]
ds_read_b128 a[196:199], v231 offset:8192
v_mfma_f32_16x16x32_bf16 v[100:103], a[224:227], a[212:215], v[100:103]
ds_read_b128 a[216:219], v231 offset:10240
s_add_u32 m0, s58, 26624
v_mfma_f32_16x16x32_bf16 v[32:35], a[240:243], a[232:235], v[32:35]
s_waitcnt lgkmcnt(4)
s_add_u32 s67, s63, 24704
v_mfma_f32_16x16x32_bf16 v[68:71], a[252:255], a[232:235], v[68:71]
buffer_load_dwordx4 v232, s[40:43], s67 offen nt lds
v_mfma_f32_16x16x32_bf16 v[104:107], a[224:227], a[232:235], v[104:107]
ds_read_b128 a[220:223], v231 offset:12288
v_mfma_f32_16x16x32_bf16 v[0:3], a[192:195], a[204:207], v[0:3]
buffer_load_dwordx4 v233, s[40:43], s67 offen offset:1024 nt lds
v_mfma_f32_16x16x32_bf16 v[36:39], a[228:231], a[204:207], v[36:39]
ds_read_b128 a[208:211], v231 offset:14336
v_mfma_f32_16x16x32_bf16 v[72:75], a[236:239], a[204:207], v[72:75]
s_add_u32 m0, s58, 28672
s_add_u32 s67, s63, 24832
v_mfma_f32_16x16x32_bf16 v[4:7], a[192:195], a[248:251], v[4:7]
s_waitcnt lgkmcnt(3)
v_mfma_f32_16x16x32_bf16 v[40:43], a[228:231], a[248:251], v[40:43]
buffer_load_dwordx4 v232, s[40:43], s67 offen nt lds
v_mfma_f32_16x16x32_bf16 v[76:79], a[236:239], a[248:251], v[76:79]
ds_read_b128 a[212:215], v231 offset:16384
v_mfma_f32_16x16x32_bf16 v[8:11], a[192:195], a[200:203], v[8:11]
buffer_load_dwordx4 v233, s[40:43], s67 offen offset:1024 nt lds
v_mfma_f32_16x16x32_bf16 v[44:47], a[228:231], a[200:203], v[44:47]
buffer_store_dwordx4 v[140:143], v241, s[52:55], s89 offen offset:1024 sc0 sc1
v_mfma_f32_16x16x32_bf16 v[80:83], a[236:239], a[200:203], v[80:83]
v_accvgpr_read_b32 v168, a52
v_accvgpr_read_b32 v169, a53
v_mfma_f32_16x16x32_bf16 v[12:15], a[192:195], a[244:247], v[12:15]
v_accvgpr_read_b32 v170, a54
v_accvgpr_read_b32 v171, a55
v_mfma_f32_16x16x32_bf16 v[48:51], a[228:231], a[244:247], v[48:51]
s_add_u32 m0, s58, 49152
v_mul_f32 v168, v168, v243
v_mul_f32 v169, v169, v243
v_mfma_f32_16x16x32_bf16 v[84:87], a[236:239], a[244:247], v[84:87]
s_add_u32 s67, s63, 49152
v_mul_f32 v170, v170, v243
v_mul_f32 v171, v171, v243
v_mfma_f32_16x16x32_bf16 v[16:19], a[192:195], a[196:199], v[16:19]
s_waitcnt lgkmcnt(1)
s_waitcnt vmcnt(9) lgkmcnt(0)
s_barrier
v_mfma_f32_16x16x32_bf16 v[52:55], a[228:231], a[196:199], v[52:55]
ds_read_b64_tr_b16 a[240:241], v228 offset:61440
v_cvt_pk_bf16_f32 v144, v168, v169
v_mfma_f32_16x16x32_bf16 v[88:91], a[236:239], a[196:199], v[88:91]
ds_read_b64_tr_b16 a[242:243], v228 offset:61952
v_cvt_pk_bf16_f32 v145, v170, v171
v_mfma_f32_16x16x32_bf16 v[20:23], a[192:195], a[216:219], v[20:23]
ds_read_b128 a[252:255], v246 offset:0
v_accvgpr_read_b32 v172, a60
v_mfma_f32_16x16x32_bf16 v[56:59], a[228:231], a[216:219], v[56:59]
ds_read_b64_tr_b16 a[224:225], v229 offset:61440
v_accvgpr_read_b32 v173, a61
v_mfma_f32_16x16x32_bf16 v[92:95], a[236:239], a[216:219], v[92:95]
ds_read_b64_tr_b16 a[226:227], v229 offset:61952
v_accvgpr_read_b32 v174, a62
v_mfma_f32_16x16x32_bf16 v[24:27], a[192:195], a[220:223], v[24:27]
ds_read_b64_tr_b16 a[232:233], v230 offset:61440
v_accvgpr_read_b32 v175, a63
v_mfma_f32_16x16x32_bf16 v[60:63], a[228:231], a[220:223], v[60:63]
ds_read_b64_tr_b16 a[234:235], v230 offset:61952
v_mul_f32 v172, v172, v243
v_mfma_f32_16x16x32_bf16 v[96:99], a[236:239], a[220:223], v[96:99]
ds_read_b128 a[204:207], v246 offset:2048
v_mul_f32 v173, v173, v243
v_mfma_f32_16x16x32_bf16 v[28:31], a[192:195], a[208:211], v[28:31]
buffer_load_dwordx4 v232, s[40:43], s67 offen nt lds
v_mfma_f32_16x16x32_bf16 v[64:67], a[228:231], a[208:211], v[64:67]
ds_read_b128 a[248:251], v246 offset:4096
v_mul_f32 v174, v174, v243
v_mfma_f32_16x16x32_bf16 v[100:103], a[236:239], a[208:211], v[100:103]
buffer_load_dwordx4 v233, s[40:43], s67 offen offset:1024 nt lds
v_mfma_f32_16x16x32_bf16 v[32:35], a[192:195], a[212:215], v[32:35]
s_waitcnt lgkmcnt(5)
v_mul_f32 v175, v175, v243
v_cvt_pk_bf16_f32 v146, v172, v173
v_mfma_f32_16x16x32_bf16 v[68:71], a[228:231], a[212:215], v[68:71]
s_waitcnt lgkmcnt(4)
v_cvt_pk_bf16_f32 v147, v174, v175
s_add_u32 s89, s84, 884736
v_mfma_f32_16x16x32_bf16 v[104:107], a[236:239], a[212:215], v[104:107]
s_waitcnt lgkmcnt(3)
v_mfma_f32_16x16x32_bf16 v[0:3], a[240:243], a[252:255], v[0:3]
s_waitcnt lgkmcnt(2)
v_mfma_f32_16x16x32_bf16 v[36:39], a[224:227], a[252:255], v[36:39]
s_waitcnt lgkmcnt(1)
v_mfma_f32_16x16x32_bf16 v[72:75], a[232:235], a[252:255], v[72:75]
buffer_store_dwordx4 v[144:147], v241, s[52:55], s89 offen offset:1024 sc0 sc1
v_mfma_f32_16x16x32_bf16 v[4:7], a[240:243], a[204:207], v[4:7]
s_waitcnt lgkmcnt(0)
v_accvgpr_read_b32 v168, a68
v_accvgpr_read_b32 v169, a69
v_mfma_f32_16x16x32_bf16 v[40:43], a[224:227], a[204:207], v[40:43]
ds_read_b128 a[200:203], v246 offset:6144
v_accvgpr_read_b32 v170, a70
v_mfma_f32_16x16x32_bf16 v[76:79], a[232:235], a[204:207], v[76:79]
ds_read_b128 a[244:247], v246 offset:8192
v_accvgpr_read_b32 v171, a71
v_mfma_f32_16x16x32_bf16 v[8:11], a[240:243], a[248:251], v[8:11]
s_add_u32 m0, s58, 51200
v_mul_f32 v168, v168, v243
v_mul_f32 v169, v169, v243
v_mul_f32 v170, v170, v243
v_mul_f32 v171, v171, v243
v_cvt_pk_bf16_f32 v148, v168, v169
v_cvt_pk_bf16_f32 v149, v170, v171
v_accvgpr_read_b32 v172, a76
v_accvgpr_read_b32 v173, a77
v_accvgpr_read_b32 v174, a78
v_accvgpr_read_b32 v175, a79
v_mul_f32 v172, v172, v243
v_mul_f32 v173, v173, v243
v_mul_f32 v174, v174, v243
v_mul_f32 v175, v175, v243
v_cvt_pk_bf16_f32 v150, v172, v173
v_cvt_pk_bf16_f32 v151, v174, v175
s_add_u32 s89, s84, 1179648
buffer_store_dwordx4 v[148:151], v241, s[52:55], s89 offen offset:1024 sc0 sc1
v_accvgpr_read_b32 v168, a84
v_accvgpr_read_b32 v169, a85
v_accvgpr_read_b32 v170, a86
v_accvgpr_read_b32 v171, a87
v_mul_f32 v168, v168, v243
v_mul_f32 v169, v169, v243
v_mul_f32 v170, v170, v243
v_mul_f32 v171, v171, v243
v_cvt_pk_bf16_f32 v152, v168, v169
v_cvt_pk_bf16_f32 v153, v170, v171
v_accvgpr_read_b32 v172, a92
v_accvgpr_read_b32 v173, a93
v_accvgpr_read_b32 v174, a94
v_accvgpr_read_b32 v175, a95
v_mul_f32 v172, v172, v243
v_mul_f32 v173, v173, v243
v_mul_f32 v174, v174, v243
v_mul_f32 v175, v175, v243
v_cvt_pk_bf16_f32 v154, v172, v173
v_cvt_pk_bf16_f32 v155, v174, v175
s_add_u32 s89, s84, 1474560
buffer_store_dwordx4 v[152:155], v241, s[52:55], s89 offen offset:1024 sc0 sc1
v_mfma_f32_16x16x32_bf16 v[44:47], a[224:227], a[248:251], v[44:47]
s_add_u32 s67, s63, 49280
v_mfma_f32_16x16x32_bf16 v[80:83], a[232:235], a[248:251], v[80:83]
buffer_load_dwordx4 v232, s[40:43], s67 offen nt lds
s_waitcnt lgkmcnt(1)
v_mfma_f32_16x16x32_bf16 v[12:15], a[240:243], a[200:203], v[12:15]
ds_read_b128 a[196:199], v246 offset:10240
v_mfma_f32_16x16x32_bf16 v[48:51], a[224:227], a[200:203], v[48:51]
buffer_load_dwordx4 v233, s[40:43], s67 offen offset:1024 nt lds
v_mfma_f32_16x16x32_bf16 v[84:87], a[232:235], a[200:203], v[84:87]
ds_read_b128 a[216:219], v246 offset:12288
s_add_u32 m0, s58, 53248
s_waitcnt lgkmcnt(2)
v_mfma_f32_16x16x32_bf16 v[16:19], a[240:243], a[244:247], v[16:19]
s_add_u32 s67, s63, 49408
v_mfma_f32_16x16x32_bf16 v[52:55], a[224:227], a[244:247], v[52:55]
buffer_load_dwordx4 v232, s[40:43], s67 offen nt lds
v_mfma_f32_16x16x32_bf16 v[88:91], a[232:235], a[244:247], v[88:91]
ds_read_b128 a[220:223], v246 offset:14336
ds_read_b128 a[208:211], v246 offset:16384
s_waitcnt lgkmcnt(3)
v_mfma_f32_16x16x32_bf16 v[20:23], a[240:243], a[196:199], v[20:23]
buffer_load_dwordx4 v233, s[40:43], s67 offen offset:1024 nt lds
v_mfma_f32_16x16x32_bf16 v[56:59], a[224:227], a[196:199], v[56:59]
v_mfma_f32_16x16x32_bf16 v[92:95], a[232:235], a[196:199], v[92:95]
s_waitcnt lgkmcnt(2)
v_mfma_f32_16x16x32_bf16 v[24:27], a[240:243], a[216:219], v[24:27]
v_mfma_f32_16x16x32_bf16 v[60:63], a[224:227], a[216:219], v[60:63]
s_waitcnt lgkmcnt(0)
s_barrier
v_mfma_f32_16x16x32_bf16 v[96:99], a[232:235], a[216:219], v[96:99]
v_mfma_f32_16x16x32_bf16 v[28:31], a[240:243], a[220:223], v[28:31]
v_mfma_f32_16x16x32_bf16 v[64:67], a[224:227], a[220:223], v[64:67]
v_mfma_f32_16x16x32_bf16 v[100:103], a[232:235], a[220:223], v[100:103]
v_mfma_f32_16x16x32_bf16 v[32:35], a[240:243], a[208:211], v[32:35]
v_mfma_f32_16x16x32_bf16 v[68:71], a[224:227], a[208:211], v[68:71]
v_mfma_f32_16x16x32_bf16 v[104:107], a[232:235], a[208:211], v[104:107]
s_nop 7
v_lshrrev_b32 v248, 4, v241
v_and_b32 v249, 15, v248
v_lshrrev_b32 v250, 4, v248
v_lshrrev_b32 v251, 1, v250
v_lshl_add_u32 v251, v251, 4, v249
v_lshlrev_b32 v251, 4, v251
v_and_b32 v252, 1, v250
v_lshl_add_u32 v251, v252, 3, v251
s_mul_i32 s68, s19, 1536
s_add_u32 s68, s68, 98304
v_add_u32 v253, s68, v251
v_pk_mul_f32 v[0:1], v[0:1], s[70:71]
v_pk_mul_f32 v[2:3], v[2:3], s[70:71]
v_cvt_pk_bf16_f32 v180, v0, v1
v_cvt_pk_bf16_f32 v181, v2, v3
ds_write_b64 v253, v[180:181] offset:0
v_pk_mul_f32 v[36:37], v[36:37], s[70:71]
v_pk_mul_f32 v[38:39], v[38:39], s[70:71]
v_cvt_pk_bf16_f32 v182, v36, v37
v_cvt_pk_bf16_f32 v183, v38, v39
ds_write_b64 v253, v[182:183] offset:512
v_pk_mul_f32 v[72:73], v[72:73], s[70:71]
v_pk_mul_f32 v[74:75], v[74:75], s[70:71]
v_cvt_pk_bf16_f32 v184, v72, v73
v_cvt_pk_bf16_f32 v185, v74, v75
ds_write_b64 v253, v[184:185] offset:1024
v_pk_mul_f32 v[4:5], v[4:5], s[70:71]
v_pk_mul_f32 v[6:7], v[6:7], s[70:71]
v_cvt_pk_bf16_f32 v186, v4, v5
v_cvt_pk_bf16_f32 v187, v6, v7
ds_write_b64 v253, v[186:187] offset:6144
v_pk_mul_f32 v[40:41], v[40:41], s[70:71]
v_pk_mul_f32 v[42:43], v[42:43], s[70:71]
v_cvt_pk_bf16_f32 v188, v40, v41
v_cvt_pk_bf16_f32 v189, v42, v43
ds_write_b64 v253, v[188:189] offset:6656
v_pk_mul_f32 v[76:77], v[76:77], s[70:71]
v_pk_mul_f32 v[78:79], v[78:79], s[70:71]
v_cvt_pk_bf16_f32 v190, v76, v77
v_cvt_pk_bf16_f32 v191, v78, v79
ds_write_b64 v253, v[190:191] offset:7168
v_pk_mul_f32 v[8:9], v[8:9], s[70:71]
v_pk_mul_f32 v[10:11], v[10:11], s[70:71]
v_cvt_pk_bf16_f32 v192, v8, v9
v_cvt_pk_bf16_f32 v193, v10, v11
ds_write_b64 v253, v[192:193] offset:12288
v_pk_mul_f32 v[44:45], v[44:45], s[70:71]
v_pk_mul_f32 v[46:47], v[46:47], s[70:71]
v_cvt_pk_bf16_f32 v194, v44, v45
v_cvt_pk_bf16_f32 v195, v46, v47
ds_write_b64 v253, v[194:195] offset:12800
v_pk_mul_f32 v[80:81], v[80:81], s[70:71]
v_pk_mul_f32 v[82:83], v[82:83], s[70:71]
v_cvt_pk_bf16_f32 v180, v80, v81
v_cvt_pk_bf16_f32 v181, v82, v83
ds_write_b64 v253, v[180:181] offset:13312
v_pk_mul_f32 v[12:13], v[12:13], s[70:71]
v_pk_mul_f32 v[14:15], v[14:15], s[70:71]
v_cvt_pk_bf16_f32 v182, v12, v13
v_cvt_pk_bf16_f32 v183, v14, v15
ds_write_b64 v253, v[182:183] offset:18432
v_pk_mul_f32 v[48:49], v[48:49], s[70:71]
v_pk_mul_f32 v[50:51], v[50:51], s[70:71]
v_cvt_pk_bf16_f32 v184, v48, v49
v_cvt_pk_bf16_f32 v185, v50, v51
ds_write_b64 v253, v[184:185] offset:18944
v_pk_mul_f32 v[84:85], v[84:85], s[70:71]
v_pk_mul_f32 v[86:87], v[86:87], s[70:71]
v_cvt_pk_bf16_f32 v186, v84, v85
v_cvt_pk_bf16_f32 v187, v86, v87
ds_write_b64 v253, v[186:187] offset:19456
v_pk_mul_f32 v[16:17], v[16:17], s[70:71]
v_pk_mul_f32 v[18:19], v[18:19], s[70:71]
v_cvt_pk_bf16_f32 v188, v16, v17
v_cvt_pk_bf16_f32 v189, v18, v19
ds_write_b64 v253, v[188:189] offset:24576
v_pk_mul_f32 v[52:53], v[52:53], s[70:71]
v_pk_mul_f32 v[54:55], v[54:55], s[70:71]
v_cvt_pk_bf16_f32 v190, v52, v53
v_cvt_pk_bf16_f32 v191, v54, v55
ds_write_b64 v253, v[190:191] offset:25088
v_pk_mul_f32 v[88:89], v[88:89], s[70:71]
v_pk_mul_f32 v[90:91], v[90:91], s[70:71]
v_cvt_pk_bf16_f32 v192, v88, v89
v_cvt_pk_bf16_f32 v193, v90, v91
ds_write_b64 v253, v[192:193] offset:25600
v_pk_mul_f32 v[20:21], v[20:21], s[70:71]
v_pk_mul_f32 v[22:23], v[22:23], s[70:71]
v_cvt_pk_bf16_f32 v194, v20, v21
v_cvt_pk_bf16_f32 v195, v22, v23
ds_write_b64 v253, v[194:195] offset:30720
s_waitcnt lgkmcnt(14)
v_pk_mul_f32 v[56:57], v[56:57], s[70:71]
v_pk_mul_f32 v[58:59], v[58:59], s[70:71]
v_cvt_pk_bf16_f32 v180, v56, v57
v_cvt_pk_bf16_f32 v181, v58, v59
ds_write_b64 v253, v[180:181] offset:31232
s_waitcnt lgkmcnt(14)
v_pk_mul_f32 v[92:93], v[92:93], s[70:71]
v_pk_mul_f32 v[94:95], v[94:95], s[70:71]
v_cvt_pk_bf16_f32 v182, v92, v93
v_cvt_pk_bf16_f32 v183, v94, v95
ds_write_b64 v253, v[182:183] offset:31744
s_waitcnt lgkmcnt(14)
v_pk_mul_f32 v[24:25], v[24:25], s[70:71]
v_pk_mul_f32 v[26:27], v[26:27], s[70:71]
v_cvt_pk_bf16_f32 v184, v24, v25
v_cvt_pk_bf16_f32 v185, v26, v27
ds_write_b64 v253, v[184:185] offset:36864
s_waitcnt lgkmcnt(14)
v_pk_mul_f32 v[60:61], v[60:61], s[70:71]
v_pk_mul_f32 v[62:63], v[62:63], s[70:71]
v_cvt_pk_bf16_f32 v186, v60, v61
v_cvt_pk_bf16_f32 v187, v62, v63
ds_write_b64 v253, v[186:187] offset:37376
s_waitcnt lgkmcnt(14)
v_pk_mul_f32 v[96:97], v[96:97], s[70:71]
v_pk_mul_f32 v[98:99], v[98:99], s[70:71]
v_cvt_pk_bf16_f32 v188, v96, v97
v_cvt_pk_bf16_f32 v189, v98, v99
ds_write_b64 v253, v[188:189] offset:37888
s_waitcnt lgkmcnt(14)
v_pk_mul_f32 v[28:29], v[28:29], s[70:71]
v_pk_mul_f32 v[30:31], v[30:31], s[70:71]
v_cvt_pk_bf16_f32 v190, v28, v29
v_cvt_pk_bf16_f32 v191, v30, v31
ds_write_b64 v253, v[190:191] offset:43008
s_waitcnt lgkmcnt(14)
v_pk_mul_f32 v[64:65], v[64:65], s[70:71]
v_pk_mul_f32 v[66:67], v[66:67], s[70:71]
v_cvt_pk_bf16_f32 v192, v64, v65
v_cvt_pk_bf16_f32 v193, v66, v67
ds_write_b64 v253, v[192:193] offset:43520
s_waitcnt lgkmcnt(14)
v_pk_mul_f32 v[100:101], v[100:101], s[70:71]
v_pk_mul_f32 v[102:103], v[102:103], s[70:71]
v_cvt_pk_bf16_f32 v194, v100, v101
v_cvt_pk_bf16_f32 v195, v102, v103
ds_write_b64 v253, v[194:195] offset:44032
s_waitcnt lgkmcnt(14)
v_pk_mul_f32 v[32:33], v[32:33], s[70:71]
v_pk_mul_f32 v[34:35], v[34:35], s[70:71]
v_cvt_pk_bf16_f32 v180, v32, v33
v_cvt_pk_bf16_f32 v181, v34, v35
ds_write_b64 v253, v[180:181] offset:49152
s_waitcnt lgkmcnt(14)
v_pk_mul_f32 v[68:69], v[68:69], s[70:71]
v_pk_mul_f32 v[70:71], v[70:71], s[70:71]
v_cvt_pk_bf16_f32 v182, v68, v69
v_cvt_pk_bf16_f32 v183, v70, v71
ds_write_b64 v253, v[182:183] offset:49664
s_waitcnt lgkmcnt(14)
v_pk_mul_f32 v[104:105], v[104:105], s[70:71]
v_pk_mul_f32 v[106:107], v[106:107], s[70:71]
v_cvt_pk_bf16_f32 v184, v104, v105
v_cvt_pk_bf16_f32 v185, v106, v107
ds_write_b64 v253, v[184:185] offset:50176
s_waitcnt lgkmcnt(14)
s_mov_b32 s83, 0
v_mov_b32 v168, 0
v_mov_b32 v169, 0
v_mov_b32 v170, 0
v_mov_b32 v171, 0
v_mov_b32 v172, 0
v_mov_b32 v173, 0
v_mov_b32 v174, 0
v_mov_b32 v175, 0
v_mov_b32 v176, 0
v_mov_b32 v177, 0
v_mov_b32 v178, 0
v_mov_b32 v179, 0
v_mov_b32 v12, 1065369472
v_mov_b32 v13, 1065369472
v_mov_b32 v14, 1065369472
v_mov_b32 v15, 1065369472
s_waitcnt vmcnt(63) lgkmcnt(0)
s_barrier
s_mul_i32 s68, s19, 12288
s_add_u32 s68, s68, 98304
v_add_u32 v254, s68, v241
v_add_u32 v255, 147456, v241
ds_read_b128 a[144:147], v254 offset:0
ds_read_b128 a[148:151], v254 offset:1024
ds_read_b128 a[152:155], v254 offset:2048
ds_read_b128 a[156:159], v254 offset:3072
ds_read_b128 a[160:163], v254 offset:4096
ds_read_b128 a[164:167], v254 offset:5120
ds_read_b128 a[168:171], v254 offset:6144
ds_read_b128 a[172:175], v254 offset:7168
ds_read_b128 a[176:179], v254 offset:8192
ds_read_b128 a[180:183], v254 offset:9216
ds_read_b128 a[184:187], v254 offset:10240
ds_read_b128 a[188:191], v254 offset:11264
ds_read_b128 v[180:183], v255 offset:0
ds_read_b128 v[184:187], v255 offset:1024
ds_read_b128 v[188:191], v255 offset:2048
ds_read_b128 v[192:195], v255 offset:3072
s_waitcnt lgkmcnt(14)
ds_read_b128 v[196:199], v255 offset:4096
s_waitcnt lgkmcnt(14)
ds_read_b128 v[200:203], v255 offset:5120
s_waitcnt lgkmcnt(14)
ds_read_b128 a[192:195], v204 offset:0
s_waitcnt lgkmcnt(14)
s_waitcnt lgkmcnt(0)
v_mfma_f32_16x16x32_bf16 v[108:111], a[192:195], a[144:147], 0
ds_read_b128 a[196:199], v205 offset:0
ds_read_b128 a[200:203], v208 offset:0
v_mfma_f32_16x16x32_bf16 v[112:115], a[192:195], a[168:171], 0
ds_read_b128 a[204:207], v209 offset:0
ds_read_b128 a[208:211], v204 offset:2048
v_mfma_f32_16x16x32_bf16 v[140:143], a[192:195], v[180:183], 0
ds_read_b128 a[212:215], v205 offset:2048
ds_read_b128 a[216:219], v208 offset:2048
s_waitcnt lgkmcnt(5)
v_mfma_f32_16x16x32_bf16 v[116:119], a[196:199], a[144:147], 0
ds_read_b128 a[220:223], v209 offset:2048
v_mfma_f32_16x16x32_bf16 v[120:123], a[196:199], a[168:171], 0
ds_read_b128 a[224:227], v204 offset:4096
s_waitcnt lgkmcnt(6)
v_mfma_f32_16x16x32_bf16 v[108:111], a[200:203], a[148:151], v[108:111]
ds_read_b128 a[228:231], v205 offset:4096
v_mfma_f32_16x16x32_bf16 v[112:115], a[200:203], a[172:175], v[112:115]
ds_read_b128 a[232:235], v208 offset:4096
v_mfma_f32_16x16x32_bf16 v[140:143], a[200:203], v[184:187], v[140:143]
ds_read_b128 a[236:239], v209 offset:4096
s_waitcnt lgkmcnt(8)
v_mfma_f32_16x16x32_bf16 v[116:119], a[204:207], a[148:151], v[116:119]
ds_read_b128 a[240:243], v206 offset:0
v_mfma_f32_16x16x32_bf16 v[120:123], a[204:207], a[172:175], v[120:123]
s_waitcnt lgkmcnt(8)
v_mfma_f32_16x16x32_bf16 v[108:111], a[208:211], a[152:155], v[108:111]
s_waitcnt lgkmcnt(6)
ds_read_b128 a[244:247], v207 offset:0
v_mfma_f32_16x16x32_bf16 v[112:115], a[208:211], a[176:179], v[112:115]
v_mfma_f32_16x16x32_bf16 v[140:143], a[208:211], v[188:191], v[140:143]
ds_read_b128 a[248:251], v210 offset:0
v_mfma_f32_16x16x32_bf16 v[116:119], a[212:215], a[152:155], v[116:119]
v_mfma_f32_16x16x32_bf16 v[120:123], a[212:215], a[176:179], v[120:123]
ds_read_b128 a[252:255], v211 offset:0
v_mfma_f32_16x16x32_bf16 v[108:111], a[216:219], a[156:159], v[108:111]
s_waitcnt lgkmcnt(4)
v_mfma_f32_16x16x32_bf16 v[112:115], a[216:219], a[180:183], v[112:115]
ds_read_b128 a[192:195], v206 offset:2048
v_mfma_f32_16x16x32_bf16 v[140:143], a[216:219], v[192:195], v[140:143]
v_mfma_f32_16x16x32_bf16 v[116:119], a[220:223], a[156:159], v[116:119]
ds_read_b128 a[196:199], v207 offset:2048
v_mfma_f32_16x16x32_bf16 v[120:123], a[220:223], a[180:183], v[120:123]
v_mfma_f32_16x16x32_bf16 v[108:111], a[224:227], a[160:163], v[108:111]
ds_read_b128 a[200:203], v210 offset:2048
v_mfma_f32_16x16x32_bf16 v[112:115], a[224:227], a[184:187], v[112:115]
ds_read_b128 a[204:207], v211 offset:2048
v_mfma_f32_16x16x32_bf16 v[140:143], a[224:227], v[196:199], v[140:143]
v_mfma_f32_16x16x32_bf16 v[116:119], a[228:231], a[160:163], v[116:119]
ds_read_b128 a[208:211], v206 offset:4096
v_mfma_f32_16x16x32_bf16 v[120:123], a[228:231], a[184:187], v[120:123]
v_mfma_f32_16x16x32_bf16 v[108:111], a[232:235], a[164:167], v[108:111]
ds_read_b128 a[212:215], v207 offset:4096
v_mfma_f32_16x16x32_bf16 v[112:115], a[232:235], a[188:191], v[112:115]
v_mfma_f32_16x16x32_bf16 v[140:143], a[232:235], v[200:203], v[140:143]
s_waitcnt lgkmcnt(4)
ds_read_b128 a[216:219], v210 offset:4096
v_mfma_f32_16x16x32_bf16 v[116:119], a[236:239], a[164:167], v[116:119]
v_mfma_f32_16x16x32_bf16 v[120:123], a[236:239], a[188:191], v[120:123]
ds_read_b128 a[220:223], v211 offset:4096
s_mov_b32 s82, 4284592614
s_swappc_b64 s[74:75], s[76:77]
s_mov_b32 s83, 0
s_mov_b32 s82, 0
v_mfma_f32_16x16x32_bf16 v[124:127], a[240:243], a[144:147], v[168:171]
v_exp_f32 v108, v108
v_mfma_f32_16x16x32_bf16 v[128:131], a[240:243], a[168:171], v[172:175]
v_exp_f32 v109, v109
v_mfma_f32_16x16x32_bf16 v[132:135], a[244:247], a[144:147], v[168:171]
v_exp_f32 v110, v110
v_mfma_f32_16x16x32_bf16 v[136:139], a[244:247], a[168:171], v[172:175]
v_exp_f32 v111, v111
v_mfma_f32_16x16x32_bf16 v[124:127], a[248:251], a[148:151], v[124:127]
v_cvt_pk_bf16_f32 v144, v108, v109
v_cvt_pk_bf16_f32 v145, v110, v111
v_mfma_f32_16x16x32_bf16 v[128:131], a[248:251], a[172:175], v[128:131]
v_exp_f32 v116, v116
v_mfma_f32_16x16x32_bf16 v[132:135], a[252:255], a[148:151], v[132:135]
v_exp_f32 v117, v117
v_mfma_f32_16x16x32_bf16 v[136:139], a[252:255], a[172:175], v[136:139]
v_exp_f32 v118, v118
v_mfma_f32_16x16x32_bf16 v[124:127], a[192:195], a[152:155], v[124:127]
v_exp_f32 v119, v119
v_mfma_f32_16x16x32_bf16 v[128:131], a[192:195], a[176:179], v[128:131]
s_waitcnt lgkmcnt(0)
s_waitcnt vmcnt(10) lgkmcnt(0)
s_barrier
v_mfma_f32_16x16x32_bf16 v[132:135], a[196:199], a[152:155], v[132:135]
ds_read_b64_tr_b16 a[224:225], v212 offset:0
v_cvt_pk_bf16_f32 v146, v116, v117
v_mfma_f32_16x16x32_bf16 v[136:139], a[196:199], a[176:179], v[136:139]
ds_read_b64_tr_b16 a[226:227], v216 offset:0
v_cvt_pk_bf16_f32 v147, v118, v119
v_mfma_f32_16x16x32_bf16 v[124:127], a[200:203], a[156:159], v[124:127]
ds_read_b64_tr_b16 a[228:229], v213 offset:0
v_mfma_f32_16x16x32_bf16 v[128:131], a[200:203], a[180:183], v[128:131]
ds_read_b64_tr_b16 a[230:231], v217 offset:0
s_add_u32 m0, s59, 24576
v_mfma_f32_16x16x32_bf16 v[132:135], a[204:207], a[156:159], v[132:135]
s_add_u32 s67, s63, 24576
v_exp_f32 v112, v112
v_mfma_f32_16x16x32_bf16 v[136:139], a[204:207], a[180:183], v[136:139]
buffer_load_dwordx4 v234, s[44:47], s67 offen nt lds
v_mfma_f32_16x16x32_bf16 v[124:127], a[208:211], a[160:163], v[124:127]
v_exp_f32 v113, v113
v_mfma_f32_16x16x32_bf16 v[128:131], a[208:211], a[184:187], v[128:131]
v_exp_f32 v114, v114
v_mfma_f32_16x16x32_bf16 v[132:135], a[212:215], a[160:163], v[132:135]
v_exp_f32 v115, v115
v_mfma_f32_16x16x32_bf16 v[136:139], a[212:215], a[184:187], v[136:139]
ds_read_b64_tr_b16 a[232:233], v214 offset:0
v_cvt_pk_bf16_f32 v148, v112, v113
v_mfma_f32_16x16x32_bf16 v[124:127], a[216:219], a[164:167], v[124:127]
ds_read_b64_tr_b16 a[234:235], v218 offset:0
v_cvt_pk_bf16_f32 v149, v114, v115
v_mfma_f32_16x16x32_bf16 v[128:131], a[216:219], a[188:191], v[128:131]
v_exp_f32 v120, v120
v_mfma_f32_16x16x32_bf16 v[132:135], a[220:223], a[164:167], v[132:135]
v_exp_f32 v121, v121
v_mfma_f32_16x16x32_bf16 v[136:139], a[220:223], a[188:191], v[136:139]
v_exp_f32 v122, v122
v_exp_f32 v123, v123
v_cvt_pk_bf16_f32 v150, v120, v121
v_cvt_pk_bf16_f32 v151, v122, v123
v_exp_f32 v140, v140
v_exp_f32 v141, v141
v_exp_f32 v142, v142
v_exp_f32 v143, v143
v_cvt_pk_bf16_f32 v160, v140, v141
v_cvt_pk_bf16_f32 v161, v142, v143
v_mfma_f32_16x16x32_bf16 v[0:3], v[12:15], v[144:147], 0
s_waitcnt lgkmcnt(2)
v_max3_f32 v165, v124, v125, v126
v_max3_f32 v165, v165, v127, v128
v_mfma_f32_16x16x32_bf16 v[4:7], v[12:15], v[148:151], 0
ds_read_b64_tr_b16 a[236:237], v215 offset:0
v_max3_f32 v165, v165, v129, v130
v_mfma_f32_16x16x16_bf16 v[8:11], v[12:13], v[160:161], 0
ds_read_b64_tr_b16 a[238:239], v219 offset:0
v_max3_f32 v165, v165, v131, v132
v_mfma_f32_16x16x32_bf16 a[0:3], a[224:227], v[144:147], 0
buffer_load_dwordx4 v235, s[44:47], s67 offen offset:1024 nt lds
v_mfma_f32_16x16x32_bf16 a[4:7], a[224:227], v[148:151], 0
ds_read_b64_tr_b16 a[240:241], v212 offset:2048
v_max3_f32 v165, v165, v133, v134
v_mfma_f32_16x16x16_bf16 a[96:99], a[224:225], v[160:161], 0
ds_read_b64_tr_b16 a[242:243], v216 offset:2048
v_max3_f32 v165, v165, v135, v136
v_mfma_f32_16x16x32_bf16 a[8:11], a[228:231], v[144:147], 0
s_waitcnt lgkmcnt(4)
v_max3_f32 v165, v165, v137, v138
v_max_f32 v165, v165, v139
v_mfma_f32_16x16x32_bf16 a[12:15], a[228:231], v[148:151], 0
ds_read_b64_tr_b16 a[244:245], v213 offset:2048
v_cmp_lt_f32 vcc, s69, v165
s_cbranch_vccnz K1_l2_t
K1_l2:
v_mfma_f32_16x16x16_bf16 a[100:103], a[228:229], v[160:161], 0
ds_read_b64_tr_b16 a[246:247], v217 offset:2048
s_add_u32 m0, s59, 26624
v_mfma_f32_16x16x32_bf16 a[16:19], a[232:235], v[144:147], 0
s_waitcnt lgkmcnt(5)
v_exp_f32 v124, v124
v_mfma_f32_16x16x32_bf16 a[20:23], a[232:235], v[148:151], 0
s_waitcnt lgkmcnt(4)
v_exp_f32 v125, v125
v_mfma_f32_16x16x16_bf16 a[104:107], a[232:233], v[160:161], 0
s_add_u32 s67, s63, 24704
v_exp_f32 v126, v126
v_mfma_f32_16x16x32_bf16 a[24:27], a[236:239], v[144:147], 0
s_waitcnt lgkmcnt(3)
v_exp_f32 v127, v127
v_mfma_f32_16x16x32_bf16 a[28:31], a[236:239], v[148:151], 0
s_waitcnt lgkmcnt(2)
v_cvt_pk_bf16_f32 v152, v124, v125
v_cvt_pk_bf16_f32 v153, v126, v127
v_mfma_f32_16x16x16_bf16 a[108:111], a[236:237], v[160:161], 0
buffer_load_dwordx4 v234, s[44:47], s67 offen nt lds
v_mfma_f32_16x16x32_bf16 a[32:35], a[240:243], v[144:147], 0
s_waitcnt lgkmcnt(1)
ds_read_b64_tr_b16 a[248:249], v214 offset:2048
v_mfma_f32_16x16x32_bf16 a[36:39], a[240:243], v[148:151], 0
s_waitcnt lgkmcnt(1)
ds_read_b64_tr_b16 a[250:251], v218 offset:2048
v_mfma_f32_16x16x16_bf16 a[112:115], a[240:241], v[160:161], 0
ds_read_b64_tr_b16 a[252:253], v215 offset:2048
ds_read_b64_tr_b16 a[254:255], v219 offset:2048
v_mfma_f32_16x16x32_bf16 a[40:43], a[244:247], v[144:147], 0
buffer_load_dwordx4 v235, s[44:47], s67 offen offset:1024 nt lds
v_mfma_f32_16x16x32_bf16 a[44:47], a[244:247], v[148:151], 0
ds_read_b64_tr_b16 a[192:193], v212 offset:4096
ds_read_b64_tr_b16 a[194:195], v216 offset:4096
v_mfma_f32_16x16x16_bf16 a[116:119], a[244:245], v[160:161], 0
ds_read_b64_tr_b16 a[196:197], v213 offset:4096
ds_read_b64_tr_b16 a[198:199], v217 offset:4096
s_waitcnt lgkmcnt(6)
v_mfma_f32_16x16x32_bf16 a[48:51], a[248:251], v[144:147], 0
s_add_u32 m0, s59, 28672
v_exp_f32 v132, v132
v_mfma_f32_16x16x32_bf16 a[52:55], a[248:251], v[148:151], 0
s_add_u32 s67, s63, 24832
v_exp_f32 v133, v133
v_mfma_f32_16x16x16_bf16 a[120:123], a[248:249], v[160:161], 0
buffer_load_dwordx4 v234, s[44:47], s67 offen nt lds
s_waitcnt lgkmcnt(4)
v_mfma_f32_16x16x32_bf16 a[56:59], a[252:255], v[144:147], 0
ds_read_b64_tr_b16 a[200:201], v214 offset:4096
ds_read_b64_tr_b16 a[202:203], v218 offset:4096
v_mfma_f32_16x16x32_bf16 a[60:63], a[252:255], v[148:151], 0
ds_read_b64_tr_b16 a[204:205], v215 offset:4096
ds_read_b64_tr_b16 a[206:207], v219 offset:4096
v_mfma_f32_16x16x16_bf16 a[124:127], a[252:253], v[160:161], 0
buffer_load_dwordx4 v235, s[44:47], s67 offen offset:1024 nt lds
s_waitcnt lgkmcnt(6)
v_mfma_f32_16x16x32_bf16 a[64:67], a[192:195], v[144:147], 0
ds_read_b128 a[208:211], v204 offset:24576
ds_read_b128 a[212:215], v205 offset:24576
v_mfma_f32_16x16x32_bf16 a[68:71], a[192:195], v[148:151], 0
s_waitcnt lgkmcnt(6)
v_exp_f32 v134, v134
v_mfma_f32_16x16x16_bf16 a[128:131], a[192:193], v[160:161], 0
s_add_u32 m0, s59, 49152
v_exp_f32 v135, v135
v_mfma_f32_16x16x32_bf16 a[72:75], a[196:199], v[144:147], 0
s_add_u32 s67, s63, 49152
v_cvt_pk_bf16_f32 v154, v132, v133
v_cvt_pk_bf16_f32 v155, v134, v135
v_mfma_f32_16x16x32_bf16 a[76:79], a[196:199], v[148:151], 0
buffer_load_dwordx4 v234, s[44:47], s67 offen nt lds
v_mfma_f32_16x16x16_bf16 a[132:135], a[196:197], v[160:161], 0
s_waitcnt lgkmcnt(4)
v_exp_f32 v128, v128
v_mfma_f32_16x16x32_bf16 a[80:83], a[200:203], v[144:147], 0
s_waitcnt lgkmcnt(2)
v_exp_f32 v129, v129
v_mfma_f32_16x16x32_bf16 a[84:87], a[200:203], v[148:151], 0
ds_read_b128 a[216:219], v208 offset:24576
ds_read_b128 a[220:223], v209 offset:24576
v_mfma_f32_16x16x16_bf16 a[136:139], a[200:201], v[160:161], 0
ds_read_b128 a[224:227], v204 offset:26624
v_mfma_f32_16x16x32_bf16 a[88:91], a[204:207], v[144:147], 0
s_waitcnt lgkmcnt(3)
v_exp_f32 v130, v130
v_mfma_f32_16x16x32_bf16 a[92:95], a[204:207], v[148:151], 0
buffer_load_dwordx4 v235, s[44:47], s67 offen offset:1024 nt lds
v_mfma_f32_16x16x16_bf16 a[140:143], a[204:205], v[160:161], 0
ds_read_b128 a[228:231], v205 offset:26624
ds_read_b128 a[232:235], v208 offset:26624
v_mfma_f32_16x16x32_bf16 v[108:111], a[208:211], a[144:147], v[168:171]
s_add_u32 m0, s59, 51200
v_exp_f32 v131, v131
v_mfma_f32_16x16x32_bf16 v[112:115], a[208:211], a[168:171], v[172:175]
s_add_u32 s67, s63, 49280
v_cvt_pk_bf16_f32 v156, v128, v129
v_cvt_pk_bf16_f32 v157, v130, v131
v_mfma_f32_16x16x32_bf16 v[140:143], a[208:211], v[180:183], v[176:179]
buffer_load_dwordx4 v234, s[44:47], s67 offen nt lds
v_mfma_f32_16x16x32_bf16 v[116:119], a[212:215], a[144:147], v[168:171]
s_waitcnt lgkmcnt(3)
v_exp_f32 v136, v136
v_mfma_f32_16x16x32_bf16 v[120:123], a[212:215], a[168:171], v[172:175]
ds_read_b128 a[236:239], v209 offset:26624
ds_read_b128 a[240:243], v204 offset:28672
v_mfma_f32_16x16x32_bf16 v[108:111], a[216:219], a[148:151], v[108:111]
buffer_load_dwordx4 v235, s[44:47], s67 offen offset:1024 nt lds
v_mfma_f32_16x16x32_bf16 v[112:115], a[216:219], a[172:175], v[112:115]
ds_read_b128 a[244:247], v205 offset:28672
v_mfma_f32_16x16x32_bf16 v[140:143], a[216:219], v[184:187], v[140:143]
s_waitcnt lgkmcnt(3)
v_exp_f32 v137, v137
v_mfma_f32_16x16x32_bf16 v[116:119], a[220:223], a[148:151], v[116:119]
ds_read_b128 a[248:251], v208 offset:28672
v_mfma_f32_16x16x32_bf16 v[120:123], a[220:223], a[172:175], v[120:123]
ds_read_b128 a[252:255], v209 offset:28672
s_add_u32 m0, s59, 53248
v_mfma_f32_16x16x32_bf16 v[108:111], a[224:227], a[152:155], v[108:111]
s_add_u32 s67, s63, 49408
v_exp_f32 v138, v138
v_mfma_f32_16x16x32_bf16 v[112:115], a[224:227], a[176:179], v[112:115]
buffer_load_dwordx4 v234, s[44:47], s67 offen nt lds
v_mfma_f32_16x16x32_bf16 v[140:143], a[224:227], v[188:191], v[140:143]
ds_read_b64_tr_b16 a[192:193], v220 offset:0
v_mfma_f32_16x16x32_bf16 v[116:119], a[228:231], a[152:155], v[116:119]
ds_read_b64_tr_b16 a[194:195], v224 offset:0
v_mfma_f32_16x16x32_bf16 v[120:123], a[228:231], a[176:179], v[120:123]
ds_read_b64_tr_b16 a[196:197], v221 offset:0
v_mfma_f32_16x16x32_bf16 v[108:111], a[232:235], a[156:159], v[108:111]
s_waitcnt lgkmcnt(5)
v_exp_f32 v139, v139
v_mfma_f32_16x16x32_bf16 v[112:115], a[232:235], a[180:183], v[112:115]
ds_read_b64_tr_b16 a[198:199], v225 offset:0
v_cvt_pk_bf16_f32 v158, v136, v137
v_mfma_f32_16x16x32_bf16 v[140:143], a[232:235], v[192:195], v[140:143]
buffer_load_dwordx4 v235, s[44:47], s67 offen offset:1024 nt lds
v_mfma_f32_16x16x32_bf16 v[116:119], a[236:239], a[156:159], v[116:119]
ds_read_b64_tr_b16 a[200:201], v222 offset:0
v_cvt_pk_bf16_f32 v159, v138, v139
v_mfma_f32_16x16x32_bf16 v[120:123], a[236:239], a[180:183], v[120:123]
ds_read_b64_tr_b16 a[202:203], v226 offset:0
v_mfma_f32_16x16x32_bf16 v[108:111], a[240:243], a[160:163], v[108:111]
ds_read_b64_tr_b16 a[204:205], v223 offset:0
v_mfma_f32_16x16x32_bf16 v[112:115], a[240:243], a[184:187], v[112:115]
ds_read_b64_tr_b16 a[206:207], v227 offset:0
v_mfma_f32_16x16x32_bf16 v[140:143], a[240:243], v[196:199], v[140:143]
s_waitcnt lgkmcnt(5)
ds_read_b64_tr_b16 a[208:209], v220 offset:2048
v_mfma_f32_16x16x32_bf16 v[116:119], a[244:247], a[160:163], v[116:119]
ds_read_b64_tr_b16 a[210:211], v224 offset:2048
s_add_u32 m0, s58, 0
v_mfma_f32_16x16x32_bf16 v[120:123], a[244:247], a[184:187], v[120:123]
s_add_u32 s67, s63, 73728
v_mfma_f32_16x16x32_bf16 v[108:111], a[248:251], a[164:167], v[108:111]
buffer_load_dwordx4 v232, s[40:43], s67 offen nt lds
v_mfma_f32_16x16x32_bf16 v[112:115], a[248:251], a[188:191], v[112:115]
ds_read_b64_tr_b16 a[212:213], v221 offset:2048
ds_read_b64_tr_b16 a[214:215], v225 offset:2048
v_mfma_f32_16x16x32_bf16 v[140:143], a[248:251], v[200:203], v[140:143]
ds_read_b64_tr_b16 a[216:217], v222 offset:2048
v_mfma_f32_16x16x32_bf16 v[116:119], a[252:255], a[164:167], v[116:119]
ds_read_b64_tr_b16 a[218:219], v226 offset:2048
v_mfma_f32_16x16x32_bf16 v[120:123], a[252:255], a[188:191], v[120:123]
ds_read_b64_tr_b16 a[220:221], v223 offset:2048
s_cmp_lg_u32 s83, 0
s_cbranch_scc1 K1_l3_t
K1_l3:
v_mfma_f32_16x16x32_bf16 v[0:3], v[12:15], v[152:155], v[0:3]
ds_read_b64_tr_b16 a[222:223], v227 offset:2048
v_max3_f32 v165, v108, v109, v110
v_mfma_f32_16x16x32_bf16 v[4:7], v[12:15], v[156:159], v[4:7]
s_waitcnt lgkmcnt(6)
v_max3_f32 v165, v165, v111, v112
v_max3_f32 v165, v165, v113, v114
v_mfma_f32_16x16x32_bf16 a[0:3], a[192:195], v[152:155], a[0:3]
buffer_load_dwordx4 v233, s[40:43], s67 offen offset:1024 nt lds
v_mfma_f32_16x16x32_bf16 a[4:7], a[192:195], v[156:159], a[4:7]
ds_read_b64_tr_b16 a[224:225], v220 offset:4096
v_max3_f32 v165, v165, v115, v116
v_mfma_f32_16x16x32_bf16 a[8:11], a[196:199], v[152:155], a[8:11]
ds_read_b64_tr_b16 a[226:227], v224 offset:4096
v_max3_f32 v165, v165, v117, v118
v_mfma_f32_16x16x32_bf16 a[12:15], a[196:199], v[156:159], a[12:15]
ds_read_b64_tr_b16 a[228:229], v221 offset:4096
v_max3_f32 v165, v165, v119, v120
v_mfma_f32_16x16x32_bf16 a[16:19], a[200:203], v[152:155], a[16:19]
ds_read_b64_tr_b16 a[230:231], v225 offset:4096
v_max3_f32 v165, v165, v121, v122
v_mfma_f32_16x16x32_bf16 a[20:23], a[200:203], v[156:159], a[20:23]
ds_read_b64_tr_b16 a[232:233], v222 offset:4096
v_max3_f32 v165, v165, v123, v140
v_mfma_f32_16x16x32_bf16 a[24:27], a[204:207], v[152:155], a[24:27]
ds_read_b64_tr_b16 a[234:235], v226 offset:4096
v_max3_f32 v165, v165, v141, v142
v_mfma_f32_16x16x32_bf16 a[28:31], a[204:207], v[156:159], a[28:31]
s_waitcnt lgkmcnt(6)
v_max_f32 v165, v165, v143
v_cmp_lt_f32 vcc, s69, v165
s_cbranch_vccnz K1_l4_t
K1_l4:
v_mfma_f32_16x16x32_bf16 a[32:35], a[208:211], v[152:155], a[32:35]
s_add_u32 m0, s58, 2048
v_exp_f32 v108, v108
v_mfma_f32_16x16x32_bf16 a[36:39], a[208:211], v[156:159], a[36:39]
s_add_u32 s67, s63, 73856
v_exp_f32 v109, v109
v_mfma_f32_16x16x32_bf16 a[40:43], a[212:215], v[152:155], a[40:43]
buffer_load_dwordx4 v232, s[40:43], s67 offen nt lds
v_mfma_f32_16x16x32_bf16 a[44:47], a[212:215], v[156:159], a[44:47]
ds_read_b64_tr_b16 a[236:237], v223 offset:4096
ds_read_b64_tr_b16 a[238:239], v227 offset:4096
v_mfma_f32_16x16x32_bf16 a[48:51], a[216:219], v[152:155], a[48:51]
ds_read_b128 a[240:243], v206 offset:24576
ds_read_b128 a[244:247], v207 offset:24576
v_mfma_f32_16x16x32_bf16 a[52:55], a[216:219], v[156:159], a[52:55]
s_waitcnt lgkmcnt(5)
v_exp_f32 v110, v110
v_mfma_f32_16x16x32_bf16 a[56:59], a[220:223], v[152:155], a[56:59]
buffer_load_dwordx4 v233, s[40:43], s67 offen offset:1024 nt lds
v_mfma_f32_16x16x32_bf16 a[60:63], a[220:223], v[156:159], a[60:63]
s_add_u32 m0, s58, 4096
v_exp_f32 v111, v111
v_mfma_f32_16x16x32_bf16 a[64:67], a[224:227], v[152:155], a[64:67]
ds_read_b128 a[248:251], v210 offset:24576
v_cvt_pk_bf16_f32 v144, v108, v109
v_mfma_f32_16x16x32_bf16 a[68:71], a[224:227], v[156:159], a[68:71]
s_waitcnt lgkmcnt(5)
v_cvt_pk_bf16_f32 v145, v110, v111
ds_read_b128 a[252:255], v211 offset:24576
v_mfma_f32_16x16x32_bf16 a[72:75], a[228:231], v[152:155], a[72:75]
s_add_u32 s67, s63, 73984
v_exp_f32 v116, v116
v_mfma_f32_16x16x32_bf16 a[76:79], a[228:231], v[156:159], a[76:79]
s_waitcnt lgkmcnt(4)
v_exp_f32 v117, v117
v_mfma_f32_16x16x32_bf16 a[80:83], a[232:235], v[152:155], a[80:83]
ds_read_b128 a[192:195], v206 offset:26624
v_mfma_f32_16x16x32_bf16 a[84:87], a[232:235], v[156:159], a[84:87]
s_waitcnt lgkmcnt(3)
v_exp_f32 v118, v118
v_mfma_f32_16x16x32_bf16 a[88:91], a[236:239], v[152:155], a[88:91]
buffer_load_dwordx4 v232, s[40:43], s67 offen nt lds
v_mfma_f32_16x16x32_bf16 a[92:95], a[236:239], v[156:159], a[92:95]
ds_read_b128 a[196:199], v207 offset:26624
ds_read_b128 a[200:203], v210 offset:26624
v_mfma_f32_16x16x32_bf16 v[124:127], a[240:243], a[144:147], v[168:171]
v_exp_f32 v119, v119
v_mfma_f32_16x16x32_bf16 v[128:131], a[240:243], a[168:171], v[172:175]
s_waitcnt lgkmcnt(3)
v_cvt_pk_bf16_f32 v146, v116, v117
v_cvt_pk_bf16_f32 v147, v118, v119
v_mfma_f32_16x16x32_bf16 v[132:135], a[244:247], a[144:147], v[168:171]
ds_read_b128 a[204:207], v211 offset:26624
v_mfma_f32_16x16x32_bf16 v[136:139], a[244:247], a[168:171], v[172:175]
buffer_load_dwordx4 v233, s[40:43], s67 offen offset:1024 nt lds
v_mfma_f32_16x16x32_bf16 v[124:127], a[248:251], a[148:151], v[124:127]
v_exp_f32 v112, v112
v_mfma_f32_16x16x32_bf16 v[128:131], a[248:251], a[172:175], v[128:131]
s_waitcnt lgkmcnt(3)
v_exp_f32 v113, v113
v_mfma_f32_16x16x32_bf16 v[132:135], a[252:255], a[148:151], v[132:135]
ds_read_b128 a[208:211], v206 offset:28672
ds_read_b128 a[212:215], v207 offset:28672
v_mfma_f32_16x16x32_bf16 v[136:139], a[252:255], a[172:175], v[136:139]
s_waitcnt lgkmcnt(3)
v_exp_f32 v114, v114
v_mfma_f32_16x16x32_bf16 v[124:127], a[192:195], a[152:155], v[124:127]
v_exp_f32 v115, v115
v_mfma_f32_16x16x32_bf16 v[128:131], a[192:195], a[176:179], v[128:131]
ds_read_b128 a[216:219], v210 offset:28672
v_cvt_pk_bf16_f32 v148, v112, v113
v_mfma_f32_16x16x32_bf16 v[132:135], a[196:199], a[152:155], v[132:135]
ds_read_b128 a[220:223], v211 offset:28672
v_cvt_pk_bf16_f32 v149, v114, v115
v_mfma_f32_16x16x32_bf16 v[136:139], a[196:199], a[176:179], v[136:139]
s_waitcnt lgkmcnt(4)
v_exp_f32 v120, v120
v_mfma_f32_16x16x32_bf16 v[124:127], a[200:203], a[156:159], v[124:127]
v_exp_f32 v121, v121
v_mfma_f32_16x16x32_bf16 v[128:131], a[200:203], a[180:183], v[128:131]
v_exp_f32 v122, v122
v_mfma_f32_16x16x32_bf16 v[132:135], a[204:207], a[156:159], v[132:135]
s_waitcnt lgkmcnt(2)
v_exp_f32 v123, v123
v_mfma_f32_16x16x32_bf16 v[136:139], a[204:207], a[180:183], v[136:139]
s_waitcnt vmcnt(12) lgkmcnt(0)
s_barrier
v_mfma_f32_16x16x32_bf16 v[124:127], a[208:211], a[160:163], v[124:127]
ds_read_b64_tr_b16 a[224:225], v212 offset:24576
v_cvt_pk_bf16_f32 v150, v120, v121
v_mfma_f32_16x16x32_bf16 v[128:131], a[208:211], a[184:187], v[128:131]
ds_read_b64_tr_b16 a[226:227], v216 offset:24576
v_cvt_pk_bf16_f32 v151, v122, v123
v_mfma_f32_16x16x32_bf16 v[132:135], a[212:215], a[160:163], v[132:135]
ds_read_b64_tr_b16 a[228:229], v213 offset:24576
ds_read_b64_tr_b16 a[230:231], v217 offset:24576
v_mfma_f32_16x16x32_bf16 v[136:139], a[212:215], a[184:187], v[136:139]
s_add_u32 m0, s59, 0
v_exp_f32 v140, v140
v_mfma_f32_16x16x32_bf16 v[124:127], a[216:219], a[164:167], v[124:127]
s_add_u32 s67, s63, 73728
v_exp_f32 v141, v141
v_mfma_f32_16x16x32_bf16 v[128:131], a[216:219], a[188:191], v[128:131]
buffer_load_dwordx4 v234, s[44:47], s67 offen nt lds
v_mfma_f32_16x16x32_bf16 v[132:135], a[220:223], a[164:167], v[132:135]
ds_read_b64_tr_b16 a[232:233], v214 offset:24576
ds_read_b64_tr_b16 a[234:235], v218 offset:24576
v_mfma_f32_16x16x32_bf16 v[136:139], a[220:223], a[188:191], v[136:139]
v_exp_f32 v142, v142
v_exp_f32 v143, v143
v_cvt_pk_bf16_f32 v160, v140, v141
v_cvt_pk_bf16_f32 v161, v142, v143
s_cmp_lg_u32 s83, 0
s_cbranch_scc1 K1_l5_t
K1_l5:
v_mfma_f32_16x16x32_bf16 v[0:3], v[12:15], v[144:147], v[0:3]
s_waitcnt lgkmcnt(5)
v_max3_f32 v165, v124, v125, v126
v_max3_f32 v165, v165, v127, v128
v_mfma_f32_16x16x32_bf16 v[4:7], v[12:15], v[148:151], v[4:7]
s_waitcnt lgkmcnt(4)
v_max3_f32 v165, v165, v129, v130
v_max3_f32 v165, v165, v131, v132
v_mfma_f32_16x16x16_bf16 v[8:11], v[12:13], v[160:161], v[8:11]
ds_read_b64_tr_b16 a[236:237], v215 offset:24576
v_max3_f32 v165, v165, v133, v134
v_mfma_f32_16x16x32_bf16 a[0:3], a[224:227], v[144:147], a[0:3]
s_waitcnt lgkmcnt(3)
v_max3_f32 v165, v165, v135, v136
v_max3_f32 v165, v165, v137, v138
v_mfma_f32_16x16x32_bf16 a[4:7], a[224:227], v[148:151], a[4:7]
ds_read_b64_tr_b16 a[238:239], v219 offset:24576
v_max_f32 v165, v165, v139
v_mfma_f32_16x16x16_bf16 a[96:99], a[224:225], v[160:161], a[96:99]
buffer_load_dwordx4 v235, s[44:47], s67 offen offset:1024 nt lds
v_mfma_f32_16x16x32_bf16 a[8:11], a[228:231], v[144:147], a[8:11]
s_waitcnt lgkmcnt(2)
v_cmp_lt_f32 vcc, s69, v165
s_cbranch_vccnz K1_l6_t
K1_l6:
v_mfma_f32_16x16x32_bf16 a[12:15], a[228:231], v[148:151], a[12:15]
ds_read_b64_tr_b16 a[240:241], v212 offset:26624
ds_read_b64_tr_b16 a[242:243], v216 offset:26624
v_mfma_f32_16x16x16_bf16 a[100:103], a[228:229], v[160:161], a[100:103]
ds_read_b64_tr_b16 a[244:245], v213 offset:26624
ds_read_b64_tr_b16 a[246:247], v217 offset:26624
v_mfma_f32_16x16x32_bf16 a[16:19], a[232:235], v[144:147], a[16:19]
s_add_u32 m0, s59, 2048
v_exp_f32 v124, v124
v_mfma_f32_16x16x32_bf16 a[20:23], a[232:235], v[148:151], a[20:23]
s_waitcnt lgkmcnt(5)
v_exp_f32 v125, v125
v_mfma_f32_16x16x16_bf16 a[104:107], a[232:233], v[160:161], a[104:107]
s_add_u32 s67, s63, 73856
v_exp_f32 v126, v126
s_waitcnt lgkmcnt(4)
v_mfma_f32_16x16x32_bf16 a[24:27], a[236:239], v[144:147], a[24:27]
buffer_load_dwordx4 v234, s[44:47], s67 offen nt lds
v_mfma_f32_16x16x32_bf16 a[28:31], a[236:239], v[148:151], a[28:31]
ds_read_b64_tr_b16 a[248:249], v214 offset:26624
ds_read_b64_tr_b16 a[250:251], v218 offset:26624
v_mfma_f32_16x16x16_bf16 a[108:111], a[236:237], v[160:161], a[108:111]
ds_read_b64_tr_b16 a[252:253], v215 offset:26624
ds_read_b64_tr_b16 a[254:255], v219 offset:26624
s_waitcnt lgkmcnt(6)
v_mfma_f32_16x16x32_bf16 a[32:35], a[240:243], v[144:147], a[32:35]
buffer_load_dwordx4 v235, s[44:47], s67 offen offset:1024 nt lds
v_mfma_f32_16x16x32_bf16 a[36:39], a[240:243], v[148:151], a[36:39]
s_waitcnt lgkmcnt(4)
v_exp_f32 v127, v127
v_mfma_f32_16x16x16_bf16 a[112:115], a[240:241], v[160:161], a[112:115]
ds_read_b64_tr_b16 a[192:193], v212 offset:28672
v_cvt_pk_bf16_f32 v152, v124, v125
v_mfma_f32_16x16x32_bf16 a[40:43], a[244:247], v[144:147], a[40:43]
ds_read_b64_tr_b16 a[194:195], v216 offset:28672
v_cvt_pk_bf16_f32 v153, v126, v127
v_mfma_f32_16x16x32_bf16 a[44:47], a[244:247], v[148:151], a[44:47]
ds_read_b64_tr_b16 a[196:197], v213 offset:28672
ds_read_b64_tr_b16 a[198:199], v217 offset:28672
v_mfma_f32_16x16x16_bf16 a[116:119], a[244:245], v[160:161], a[116:119]
s_add_u32 m0, s59, 4096
v_exp_f32 v132, v132
s_waitcnt lgkmcnt(6)
v_mfma_f32_16x16x32_bf16 a[48:51], a[248:251], v[144:147], a[48:51]
s_add_u32 s67, s63, 73984
v_exp_f32 v133, v133
v_mfma_f32_16x16x32_bf16 a[52:55], a[248:251], v[148:151], a[52:55]
s_waitcnt lgkmcnt(4)
v_exp_f32 v134, v134
v_mfma_f32_16x16x16_bf16 a[120:123], a[248:249], v[160:161], a[120:123]
buffer_load_dwordx4 v234, s[44:47], s67 offen nt lds
v_mfma_f32_16x16x32_bf16 a[56:59], a[252:255], v[144:147], a[56:59]
ds_read_b64_tr_b16 a[200:201], v214 offset:28672
ds_read_b64_tr_b16 a[202:203], v218 offset:28672
v_mfma_f32_16x16x32_bf16 a[60:63], a[252:255], v[148:151], a[60:63]
s_waitcnt lgkmcnt(5)
v_exp_f32 v135, v135
v_mfma_f32_16x16x16_bf16 a[124:127], a[252:253], v[160:161], a[124:127]
s_waitcnt lgkmcnt(4)
ds_read_b64_tr_b16 a[204:205], v215 offset:28672
v_mfma_f32_16x16x32_bf16 a[64:67], a[192:195], v[144:147], a[64:67]
s_waitcnt lgkmcnt(3)
ds_read_b64_tr_b16 a[206:207], v219 offset:28672
v_mfma_f32_16x16x32_bf16 a[68:71], a[192:195], v[148:151], a[68:71]
buffer_load_dwordx4 v235, s[44:47], s67 offen offset:1024 nt lds
v_mfma_f32_16x16x16_bf16 a[128:131], a[192:193], v[160:161], a[128:131]
ds_read_b128 a[208:211], v204 offset:49152
v_cvt_pk_bf16_f32 v154, v132, v133
v_mfma_f32_16x16x32_bf16 a[72:75], a[196:199], v[144:147], a[72:75]
ds_read_b128 a[212:215], v205 offset:49152
v_cvt_pk_bf16_f32 v155, v134, v135
v_mfma_f32_16x16x32_bf16 a[76:79], a[196:199], v[148:151], a[76:79]
s_add_u32 m0, s58, 24576
v_exp_f32 v128, v128
v_mfma_f32_16x16x16_bf16 a[132:135], a[196:197], v[160:161], a[132:135]
s_waitcnt lgkmcnt(4)
s_add_u32 s67, s63, 98304
v_mfma_f32_16x16x32_bf16 a[80:83], a[200:203], v[144:147], a[80:83]
buffer_load_dwordx4 v232, s[40:43], s67 offen nt lds
v_mfma_f32_16x16x32_bf16 a[84:87], a[200:203], v[148:151], a[84:87]
s_waitcnt lgkmcnt(3)
v_exp_f32 v129, v129
v_mfma_f32_16x16x16_bf16 a[136:139], a[200:201], v[160:161], a[136:139]
s_waitcnt lgkmcnt(2)
ds_read_b128 a[216:219], v208 offset:49152
v_mfma_f32_16x16x32_bf16 a[88:91], a[204:207], v[144:147], a[88:91]
ds_read_b128 a[220:223], v209 offset:49152
ds_read_b128 a[224:227], v204 offset:51200
v_mfma_f32_16x16x32_bf16 a[92:95], a[204:207], v[148:151], a[92:95]
s_waitcnt lgkmcnt(4)
v_exp_f32 v130, v130
v_mfma_f32_16x16x16_bf16 a[140:143], a[204:205], v[160:161], a[140:143]
buffer_load_dwordx4 v233, s[40:43], s67 offen offset:1024 nt lds
v_mfma_f32_16x16x32_bf16 v[108:111], a[208:211], a[144:147], v[168:171]
s_waitcnt lgkmcnt(3)
v_exp_f32 v131, v131
v_mfma_f32_16x16x32_bf16 v[112:115], a[208:211], a[168:171], v[172:175]
ds_read_b128 a[228:231], v205 offset:51200
v_cvt_pk_bf16_f32 v156, v128, v129
v_mfma_f32_16x16x32_bf16 v[140:143], a[208:211], v[180:183], v[176:179]
ds_read_b128 a[232:235], v208 offset:51200
v_cvt_pk_bf16_f32 v157, v130, v131
v_mfma_f32_16x16x32_bf16 v[116:119], a[212:215], a[144:147], v[168:171]
s_add_u32 m0, s58, 26624
s_add_u32 s67, s63, 98432
v_mfma_f32_16x16x32_bf16 v[120:123], a[212:215], a[168:171], v[172:175]
s_waitcnt lgkmcnt(4)
v_exp_f32 v136, v136
v_mfma_f32_16x16x32_bf16 v[108:111], a[216:219], a[148:151], v[108:111]
s_waitcnt lgkmcnt(2)
v_exp_f32 v137, v137
v_mfma_f32_16x16x32_bf16 v[112:115], a[216:219], a[172:175], v[112:115]
buffer_load_dwordx4 v232, s[40:43], s67 offen nt lds
v_mfma_f32_16x16x32_bf16 v[140:143], a[216:219], v[184:187], v[140:143]
ds_read_b128 a[236:239], v209 offset:51200
ds_read_b128 a[240:243], v204 offset:53248
v_mfma_f32_16x16x32_bf16 v[116:119], a[220:223], a[148:151], v[116:119]
buffer_load_dwordx4 v233, s[40:43], s67 offen offset:1024 nt lds
v_mfma_f32_16x16x32_bf16 v[120:123], a[220:223], a[172:175], v[120:123]
ds_read_b128 a[244:247], v205 offset:53248
ds_read_b128 a[248:251], v208 offset:53248
v_mfma_f32_16x16x32_bf16 v[108:111], a[224:227], a[152:155], v[108:111]
s_waitcnt lgkmcnt(4)
v_exp_f32 v138, v138
v_mfma_f32_16x16x32_bf16 v[112:115], a[224:227], a[176:179], v[112:115]
ds_read_b128 a[252:255], v209 offset:53248
s_add_u32 m0, s58, 28672
v_mfma_f32_16x16x32_bf16 v[140:143], a[224:227], v[188:191], v[140:143]
s_add_u32 s67, s63, 98560
v_exp_f32 v139, v139
v_mfma_f32_16x16x32_bf16 v[116:119], a[228:231], a[152:155], v[116:119]
buffer_load_dwordx4 v232, s[40:43], s67 offen nt lds
v_mfma_f32_16x16x32_bf16 v[120:123], a[228:231], a[176:179], v[120:123]
ds_read_b64_tr_b16 a[192:193], v220 offset:24576
v_cvt_pk_bf16_f32 v158, v136, v137
v_mfma_f32_16x16x32_bf16 v[108:111], a[232:235], a[156:159], v[108:111]
s_waitcnt lgkmcnt(4)
v_cvt_pk_bf16_f32 v159, v138, v139
v_mfma_f32_16x16x32_bf16 v[112:115], a[232:235], a[180:183], v[112:115]
ds_read_b64_tr_b16 a[194:195], v224 offset:24576
v_mfma_f32_16x16x32_bf16 v[140:143], a[232:235], v[192:195], v[140:143]
ds_read_b64_tr_b16 a[196:197], v221 offset:24576
v_mfma_f32_16x16x32_bf16 v[116:119], a[236:239], a[156:159], v[116:119]
ds_read_b64_tr_b16 a[198:199], v225 offset:24576
v_mfma_f32_16x16x32_bf16 v[120:123], a[236:239], a[180:183], v[120:123]
buffer_load_dwordx4 v233, s[40:43], s67 offen offset:1024 nt lds
v_mfma_f32_16x16x32_bf16 v[108:111], a[240:243], a[160:163], v[108:111]
s_waitcnt lgkmcnt(4)
ds_read_b64_tr_b16 a[200:201], v222 offset:24576
v_mfma_f32_16x16x32_bf16 v[112:115], a[240:243], a[184:187], v[112:115]
ds_read_b64_tr_b16 a[202:203], v226 offset:24576
v_mfma_f32_16x16x32_bf16 v[140:143], a[240:243], v[196:199], v[140:143]
ds_read_b64_tr_b16 a[204:205], v223 offset:24576
ds_read_b64_tr_b16 a[206:207], v227 offset:24576
v_mfma_f32_16x16x32_bf16 v[116:119], a[244:247], a[160:163], v[116:119]
ds_read_b64_tr_b16 a[208:209], v220 offset:26624
v_mfma_f32_16x16x32_bf16 v[120:123], a[244:247], a[184:187], v[120:123]
ds_read_b64_tr_b16 a[210:211], v224 offset:26624
v_mfma_f32_16x16x32_bf16 v[108:111], a[248:251], a[164:167], v[108:111]
ds_read_b64_tr_b16 a[212:213], v221 offset:26624
v_mfma_f32_16x16x32_bf16 v[112:115], a[248:251], a[188:191], v[112:115]
ds_read_b64_tr_b16 a[214:215], v225 offset:26624
v_mfma_f32_16x16x32_bf16 v[140:143], a[248:251], v[200:203], v[140:143]
ds_read_b64_tr_b16 a[216:217], v222 offset:26624
v_mfma_f32_16x16x32_bf16 v[116:119], a[252:255], a[164:167], v[116:119]
ds_read_b64_tr_b16 a[218:219], v226 offset:26624
v_mfma_f32_16x16x32_bf16 v[120:123], a[252:255], a[188:191], v[120:123]
s_waitcnt lgkmcnt(8)
ds_read_b64_tr_b16 a[220:221], v223 offset:26624
s_cmp_lg_u32 s83, 0
s_cbranch_scc1 K1_l7_t
K1_l7:
v_mfma_f32_16x16x32_bf16 v[0:3], v[12:15], v[152:155], v[0:3]
ds_read_b64_tr_b16 a[222:223], v227 offset:26624
v_max3_f32 v165, v108, v109, v110
v_mfma_f32_16x16x32_bf16 v[4:7], v[12:15], v[156:159], v[4:7]
ds_read_b64_tr_b16 a[224:225], v220 offset:28672
v_max3_f32 v165, v165, v111, v112
v_mfma_f32_16x16x32_bf16 a[0:3], a[192:195], v[152:155], a[0:3]
ds_read_b64_tr_b16 a[226:227], v224 offset:28672
v_max3_f32 v165, v165, v113, v114
v_mfma_f32_16x16x32_bf16 a[4:7], a[192:195], v[156:159], a[4:7]
ds_read_b64_tr_b16 a[228:229], v221 offset:28672
v_max3_f32 v165, v165, v115, v116
v_mfma_f32_16x16x32_bf16 a[8:11], a[196:199], v[152:155], a[8:11]
ds_read_b64_tr_b16 a[230:231], v225 offset:28672
v_max3_f32 v165, v165, v117, v118
v_mfma_f32_16x16x32_bf16 a[12:15], a[196:199], v[156:159], a[12:15]
s_waitcnt lgkmcnt(7)
v_max3_f32 v165, v165, v119, v120
v_max3_f32 v165, v165, v121, v122
v_mfma_f32_16x16x32_bf16 a[16:19], a[200:203], v[152:155], a[16:19]
ds_read_b64_tr_b16 a[232:233], v222 offset:28672
v_max3_f32 v165, v165, v123, v140
v_mfma_f32_16x16x32_bf16 a[20:23], a[200:203], v[156:159], a[20:23]
ds_read_b64_tr_b16 a[234:235], v226 offset:28672
v_max3_f32 v165, v165, v141, v142
v_mfma_f32_16x16x32_bf16 a[24:27], a[204:207], v[152:155], a[24:27]
ds_read_b64_tr_b16 a[236:237], v223 offset:28672
v_max_f32 v165, v165, v143
v_mfma_f32_16x16x32_bf16 a[28:31], a[204:207], v[156:159], a[28:31]
ds_read_b64_tr_b16 a[238:239], v227 offset:28672
v_cmp_lt_f32 vcc, s69, v165
s_cbranch_vccnz K1_l8_t
K1_l8:
v_mfma_f32_16x16x32_bf16 a[32:35], a[208:211], v[152:155], a[32:35]
v_exp_f32 v108, v108
v_mfma_f32_16x16x32_bf16 a[36:39], a[208:211], v[156:159], a[36:39]
s_waitcnt lgkmcnt(5)
v_exp_f32 v109, v109
v_mfma_f32_16x16x32_bf16 a[40:43], a[212:215], v[152:155], a[40:43]
ds_read_b128 a[240:243], v206 offset:49152
ds_read_b128 a[244:247], v207 offset:49152
v_mfma_f32_16x16x32_bf16 a[44:47], a[212:215], v[156:159], a[44:47]
v_exp_f32 v110, v110
v_mfma_f32_16x16x32_bf16 a[48:51], a[216:219], v[152:155], a[48:51]
v_exp_f32 v111, v111
v_mfma_f32_16x16x32_bf16 a[52:55], a[216:219], v[156:159], a[52:55]
ds_read_b128 a[248:251], v210 offset:49152
v_cvt_pk_bf16_f32 v144, v108, v109
v_mfma_f32_16x16x32_bf16 a[56:59], a[220:223], v[152:155], a[56:59]
ds_read_b128 a[252:255], v211 offset:49152
v_cvt_pk_bf16_f32 v145, v110, v111
v_mfma_f32_16x16x32_bf16 a[60:63], a[220:223], v[156:159], a[60:63]
s_waitcnt lgkmcnt(4)
v_exp_f32 v116, v116
v_mfma_f32_16x16x32_bf16 a[64:67], a[224:227], v[152:155], a[64:67]
ds_read_b128 a[192:195], v206 offset:51200
v_mfma_f32_16x16x32_bf16 a[68:71], a[224:227], v[156:159], a[68:71]
v_exp_f32 v117, v117
v_mfma_f32_16x16x32_bf16 a[72:75], a[228:231], v[152:155], a[72:75]
v_exp_f32 v118, v118
v_mfma_f32_16x16x32_bf16 a[76:79], a[228:231], v[156:159], a[76:79]
ds_read_b128 a[196:199], v207 offset:51200
v_mfma_f32_16x16x32_bf16 a[80:83], a[232:235], v[152:155], a[80:83]
v_exp_f32 v119, v119
v_mfma_f32_16x16x32_bf16 a[84:87], a[232:235], v[156:159], a[84:87]
s_waitcnt lgkmcnt(3)
v_cvt_pk_bf16_f32 v146, v116, v117
v_cvt_pk_bf16_f32 v147, v118, v119
v_mfma_f32_16x16x32_bf16 a[88:91], a[236:239], v[152:155], a[88:91]
ds_read_b128 a[200:203], v210 offset:51200
ds_read_b128 a[204:207], v211 offset:51200
v_mfma_f32_16x16x32_bf16 a[92:95], a[236:239], v[156:159], a[92:95]
v_exp_f32 v112, v112
v_mfma_f32_16x16x32_bf16 v[124:127], a[240:243], a[144:147], v[168:171]
ds_read_b128 a[208:211], v206 offset:53248
v_mfma_f32_16x16x32_bf16 v[128:131], a[240:243], a[168:171], v[172:175]
v_exp_f32 v113, v113
v_mfma_f32_16x16x32_bf16 v[132:135], a[244:247], a[144:147], v[168:171]
v_exp_f32 v114, v114
v_mfma_f32_16x16x32_bf16 v[136:139], a[244:247], a[168:171], v[172:175]
s_waitcnt lgkmcnt(3)
v_exp_f32 v115, v115
v_mfma_f32_16x16x32_bf16 v[124:127], a[248:251], a[148:151], v[124:127]
ds_read_b128 a[212:215], v207 offset:53248
v_cvt_pk_bf16_f32 v148, v112, v113
v_mfma_f32_16x16x32_bf16 v[128:131], a[248:251], a[172:175], v[128:131]
ds_read_b128 a[216:219], v210 offset:53248
v_cvt_pk_bf16_f32 v149, v114, v115
v_mfma_f32_16x16x32_bf16 v[132:135], a[252:255], a[148:151], v[132:135]
ds_read_b128 a[220:223], v211 offset:53248
v_mfma_f32_16x16x32_bf16 v[136:139], a[252:255], a[172:175], v[136:139]
v_exp_f32 v120, v120
v_mfma_f32_16x16x32_bf16 v[124:127], a[192:195], a[152:155], v[124:127]
v_exp_f32 v121, v121
v_mfma_f32_16x16x32_bf16 v[128:131], a[192:195], a[176:179], v[128:131]
s_waitcnt lgkmcnt(3)
v_exp_f32 v122, v122
v_mfma_f32_16x16x32_bf16 v[132:135], a[196:199], a[152:155], v[132:135]
v_exp_f32 v123, v123
v_mfma_f32_16x16x32_bf16 v[136:139], a[196:199], a[176:179], v[136:139]
s_waitcnt vmcnt(12) lgkmcnt(0)
s_barrier
v_mfma_f32_16x16x32_bf16 v[124:127], a[200:203], a[156:159], v[124:127]
ds_read_b64_tr_b16 a[224:225], v212 offset:49152
v_cvt_pk_bf16_f32 v150, v120, v121
v_mfma_f32_16x16x32_bf16 v[128:131], a[200:203], a[180:183], v[128:131]
ds_read_b64_tr_b16 a[226:227], v216 offset:49152
v_cvt_pk_bf16_f32 v151, v122, v123
v_mfma_f32_16x16x32_bf16 v[132:135], a[204:207], a[156:159], v[132:135]
ds_read_b64_tr_b16 a[228:229], v213 offset:49152
ds_read_b64_tr_b16 a[230:231], v217 offset:49152
v_mfma_f32_16x16x32_bf16 v[136:139], a[204:207], a[180:183], v[136:139]
s_add_u32 m0, s59, 24576
v_exp_f32 v140, v140
v_mfma_f32_16x16x32_bf16 v[124:127], a[208:211], a[160:163], v[124:127]
s_add_u32 s67, s63, 98304
v_exp_f32 v141, v141
v_mfma_f32_16x16x32_bf16 v[128:131], a[208:211], a[184:187], v[128:131]
buffer_load_dwordx4 v234, s[44:47], s67 offen nt lds
v_mfma_f32_16x16x32_bf16 v[132:135], a[212:215], a[160:163], v[132:135]
ds_read_b64_tr_b16 a[232:233], v214 offset:49152
ds_read_b64_tr_b16 a[234:235], v218 offset:49152
v_mfma_f32_16x16x32_bf16 v[136:139], a[212:215], a[184:187], v[136:139]
v_exp_f32 v142, v142
v_mfma_f32_16x16x32_bf16 v[124:127], a[216:219], a[164:167], v[124:127]
v_exp_f32 v143, v143
v_mfma_f32_16x16x32_bf16 v[128:131], a[216:219], a[188:191], v[128:131]
ds_read_b64_tr_b16 a[236:237], v215 offset:49152
v_cvt_pk_bf16_f32 v160, v140, v141
v_mfma_f32_16x16x32_bf16 v[132:135], a[220:223], a[164:167], v[132:135]
ds_read_b64_tr_b16 a[238:239], v219 offset:49152
v_cvt_pk_bf16_f32 v161, v142, v143
v_mfma_f32_16x16x32_bf16 v[136:139], a[220:223], a[188:191], v[136:139]
buffer_load_dwordx4 v235, s[44:47], s67 offen offset:1024 nt lds
s_cmp_lg_u32 s83, 0
s_cbranch_scc1 K1_l9_t
K1_l9:
v_mfma_f32_16x16x32_bf16 v[0:3], v[12:15], v[144:147], v[0:3]
s_waitcnt lgkmcnt(4)
v_max3_f32 v165, v124, v125, v126
v_max3_f32 v165, v165, v127, v128
v_mfma_f32_16x16x32_bf16 v[4:7], v[12:15], v[148:151], v[4:7]
ds_read_b64_tr_b16 a[240:241], v212 offset:51200
v_max3_f32 v165, v165, v129, v130
v_mfma_f32_16x16x16_bf16 v[8:11], v[12:13], v[160:161], v[8:11]
ds_read_b64_tr_b16 a[242:243], v216 offset:51200
v_max3_f32 v165, v165, v131, v132
v_mfma_f32_16x16x32_bf16 a[0:3], a[224:227], v[144:147], a[0:3]
ds_read_b64_tr_b16 a[244:245], v213 offset:51200
v_max3_f32 v165, v165, v133, v134
v_mfma_f32_16x16x32_bf16 a[4:7], a[224:227], v[148:151], a[4:7]
ds_read_b64_tr_b16 a[246:247], v217 offset:51200
v_max3_f32 v165, v165, v135, v136
v_mfma_f32_16x16x16_bf16 a[96:99], a[224:225], v[160:161], a[96:99]
s_add_u32 m0, s59, 26624
v_max3_f32 v165, v165, v137, v138
v_max_f32 v165, v165, v139
v_mfma_f32_16x16x32_bf16 a[8:11], a[228:231], v[144:147], a[8:11]
s_waitcnt lgkmcnt(4)
v_cmp_lt_f32 vcc, s69, v165
s_cbranch_vccnz K1_l10_t
K1_l10:
v_mfma_f32_16x16x32_bf16 a[12:15], a[228:231], v[148:151], a[12:15]
s_add_u32 s67, s63, 98432
v_exp_f32 v124, v124
v_mfma_f32_16x16x16_bf16 a[100:103], a[228:229], v[160:161], a[100:103]
buffer_load_dwordx4 v234, s[44:47], s67 offen nt lds
v_mfma_f32_16x16x32_bf16 a[16:19], a[232:235], v[144:147], a[16:19]
ds_read_b64_tr_b16 a[248:249], v214 offset:51200
ds_read_b64_tr_b16 a[250:251], v218 offset:51200
v_mfma_f32_16x16x32_bf16 a[20:23], a[232:235], v[148:151], a[20:23]
ds_read_b64_tr_b16 a[252:253], v215 offset:51200
ds_read_b64_tr_b16 a[254:255], v219 offset:51200
v_mfma_f32_16x16x16_bf16 a[104:107], a[232:233], v[160:161], a[104:107]
buffer_load_dwordx4 v235, s[44:47], s67 offen offset:1024 nt lds
v_mfma_f32_16x16x32_bf16 a[24:27], a[236:239], v[144:147], a[24:27]
s_waitcnt lgkmcnt(4)
v_exp_f32 v125, v125
v_mfma_f32_16x16x32_bf16 a[28:31], a[236:239], v[148:151], a[28:31]
ds_read_b64_tr_b16 a[192:193], v212 offset:53248
ds_read_b64_tr_b16 a[194:195], v216 offset:53248
v_mfma_f32_16x16x16_bf16 a[108:111], a[236:237], v[160:161], a[108:111]
ds_read_b64_tr_b16 a[196:197], v213 offset:53248
ds_read_b64_tr_b16 a[198:199], v217 offset:53248
v_mfma_f32_16x16x32_bf16 a[32:35], a[240:243], v[144:147], a[32:35]
s_add_u32 m0, s59, 28672
v_exp_f32 v126, v126
v_mfma_f32_16x16x32_bf16 a[36:39], a[240:243], v[148:151], a[36:39]
s_add_u32 s67, s63, 98560
v_exp_f32 v127, v127
v_mfma_f32_16x16x16_bf16 a[112:115], a[240:241], v[160:161], a[112:115]
buffer_load_dwordx4 v234, s[44:47], s67 offen nt lds
v_mfma_f32_16x16x32_bf16 a[40:43], a[244:247], v[144:147], a[40:43]
s_waitcnt lgkmcnt(4)
v_cvt_pk_bf16_f32 v152, v124, v125
v_cvt_pk_bf16_f32 v153, v126, v127
v_mfma_f32_16x16x32_bf16 a[44:47], a[244:247], v[148:151], a[44:47]
ds_read_b64_tr_b16 a[200:201], v214 offset:53248
ds_read_b64_tr_b16 a[202:203], v218 offset:53248
v_mfma_f32_16x16x16_bf16 a[116:119], a[244:245], v[160:161], a[116:119]
ds_read_b64_tr_b16 a[204:205], v215 offset:53248
ds_read_b64_tr_b16 a[206:207], v219 offset:53248
v_mfma_f32_16x16x32_bf16 a[48:51], a[248:251], v[144:147], a[48:51]
buffer_load_dwordx4 v235, s[44:47], s67 offen offset:1024 nt lds
v_mfma_f32_16x16x32_bf16 a[52:55], a[248:251], v[148:151], a[52:55]
ds_read_b128 a[208:211], v204 offset:0
v_mfma_f32_16x16x16_bf16 a[120:123], a[248:249], v[160:161], a[120:123]
s_add_u32 m0, s58, 49152
v_exp_f32 v132, v132
v_mfma_f32_16x16x32_bf16 a[56:59], a[252:255], v[144:147], a[56:59]
s_waitcnt lgkmcnt(5)
v_exp_f32 v133, v133
v_mfma_f32_16x16x32_bf16 a[60:63], a[252:255], v[148:151], a[60:63]
ds_read_b128 a[212:215], v205 offset:0
s_add_u32 s67, s63, 122880
v_mfma_f32_16x16x16_bf16 a[124:127], a[252:253], v[160:161], a[124:127]
buffer_load_dwordx4 v232, s[40:43], s67 offen nt lds
v_mfma_f32_16x16x32_bf16 a[64:67], a[192:195], v[144:147], a[64:67]
ds_read_b128 a[216:219], v208 offset:0
v_mfma_f32_16x16x32_bf16 a[68:71], a[192:195], v[148:151], a[68:71]
v_exp_f32 v134, v134
v_mfma_f32_16x16x16_bf16 a[128:131], a[192:193], v[160:161], a[128:131]
ds_read_b128 a[220:223], v209 offset:0
v_mfma_f32_16x16x32_bf16 a[72:75], a[196:199], v[144:147], a[72:75]
s_waitcnt lgkmcnt(3)
v_exp_f32 v135, v135
v_mfma_f32_16x16x32_bf16 a[76:79], a[196:199], v[148:151], a[76:79]
ds_read_b128 a[224:227], v204 offset:2048
v_cvt_pk_bf16_f32 v154, v132, v133
v_mfma_f32_16x16x16_bf16 a[132:135], a[196:197], v[160:161], a[132:135]
buffer_load_dwordx4 v233, s[40:43], s67 offen offset:1024 nt lds
v_mfma_f32_16x16x32_bf16 a[80:83], a[200:203], v[144:147], a[80:83]
ds_read_b128 a[228:231], v205 offset:2048
v_cvt_pk_bf16_f32 v155, v134, v135
v_mfma_f32_16x16x32_bf16 a[84:87], a[200:203], v[148:151], a[84:87]
ds_read_b128 a[232:235], v208 offset:2048
s_add_u32 m0, s58, 51200
v_mfma_f32_16x16x16_bf16 a[136:139], a[200:201], v[160:161], a[136:139]
s_add_u32 s67, s63, 123008
v_exp_f32 v128, v128
v_mfma_f32_16x16x32_bf16 a[88:91], a[204:207], v[144:147], a[88:91]
buffer_load_dwordx4 v232, s[40:43], s67 offen nt lds
v_mfma_f32_16x16x32_bf16 a[92:95], a[204:207], v[148:151], a[92:95]
v_exp_f32 v129, v129
v_mfma_f32_16x16x16_bf16 a[140:143], a[204:205], v[160:161], a[140:143]
ds_read_b128 a[236:239], v209 offset:2048
v_mfma_f32_16x16x32_bf16 v[108:111], a[208:211], a[144:147], v[168:171]
s_waitcnt lgkmcnt(3)
v_exp_f32 v130, v130
v_mfma_f32_16x16x32_bf16 v[112:115], a[208:211], a[168:171], v[172:175]
ds_read_b128 a[240:243], v204 offset:4096
v_mfma_f32_16x16x32_bf16 v[140:143], a[208:211], v[180:183], v[176:179]
buffer_load_dwordx4 v233, s[40:43], s67 offen offset:1024 nt lds
v_mfma_f32_16x16x32_bf16 v[116:119], a[212:215], a[144:147], v[168:171]
ds_read_b128 a[244:247], v205 offset:4096
v_mfma_f32_16x16x32_bf16 v[120:123], a[212:215], a[168:171], v[172:175]
v_exp_f32 v131, v131
v_mfma_f32_16x16x32_bf16 v[108:111], a[216:219], a[148:151], v[108:111]
ds_read_b128 a[248:251], v208 offset:4096
v_cvt_pk_bf16_f32 v156, v128, v129
v_mfma_f32_16x16x32_bf16 v[112:115], a[216:219], a[172:175], v[112:115]
ds_read_b128 a[252:255], v209 offset:4096
v_cvt_pk_bf16_f32 v157, v130, v131
v_mfma_f32_16x16x32_bf16 v[140:143], a[216:219], v[184:187], v[140:143]
s_add_u32 m0, s58, 53248
v_exp_f32 v136, v136
v_mfma_f32_16x16x32_bf16 v[116:119], a[220:223], a[148:151], v[116:119]
s_add_u32 s67, s63, 123136
v_exp_f32 v137, v137
v_mfma_f32_16x16x32_bf16 v[120:123], a[220:223], a[172:175], v[120:123]
buffer_load_dwordx4 v232, s[40:43], s67 offen nt lds
v_mfma_f32_16x16x32_bf16 v[108:111], a[224:227], a[152:155], v[108:111]
s_waitcnt lgkmcnt(3)
v_exp_f32 v138, v138
v_mfma_f32_16x16x32_bf16 v[112:115], a[224:227], a[176:179], v[112:115]
ds_read_b64_tr_b16 a[192:193], v220 offset:49152
v_mfma_f32_16x16x32_bf16 v[140:143], a[224:227], v[188:191], v[140:143]
ds_read_b64_tr_b16 a[194:195], v224 offset:49152
v_mfma_f32_16x16x32_bf16 v[116:119], a[228:231], a[152:155], v[116:119]
ds_read_b64_tr_b16 a[196:197], v221 offset:49152
v_mfma_f32_16x16x32_bf16 v[120:123], a[228:231], a[176:179], v[120:123]
ds_read_b64_tr_b16 a[198:199], v225 offset:49152
v_mfma_f32_16x16x32_bf16 v[108:111], a[232:235], a[156:159], v[108:111]
buffer_load_dwordx4 v233, s[40:43], s67 offen offset:1024 nt lds
v_mfma_f32_16x16x32_bf16 v[112:115], a[232:235], a[180:183], v[112:115]
ds_read_b64_tr_b16 a[200:201], v222 offset:49152
v_mfma_f32_16x16x32_bf16 v[140:143], a[232:235], v[192:195], v[140:143]
ds_read_b64_tr_b16 a[202:203], v226 offset:49152
v_mfma_f32_16x16x32_bf16 v[116:119], a[236:239], a[156:159], v[116:119]
ds_read_b64_tr_b16 a[204:205], v223 offset:49152
v_mfma_f32_16x16x32_bf16 v[120:123], a[236:239], a[180:183], v[120:123]
ds_read_b64_tr_b16 a[206:207], v227 offset:49152
v_mfma_f32_16x16x32_bf16 v[108:111], a[240:243], a[160:163], v[108:111]
s_waitcnt lgkmcnt(6)
v_exp_f32 v139, v139
v_mfma_f32_16x16x32_bf16 v[112:115], a[240:243], a[184:187], v[112:115]
ds_read_b64_tr_b16 a[208:209], v220 offset:51200
v_cvt_pk_bf16_f32 v158, v136, v137
v_mfma_f32_16x16x32_bf16 v[140:143], a[240:243], v[196:199], v[140:143]
ds_read_b64_tr_b16 a[210:211], v224 offset:51200
v_cvt_pk_bf16_f32 v159, v138, v139
v_mfma_f32_16x16x32_bf16 v[116:119], a[244:247], a[160:163], v[116:119]
ds_read_b64_tr_b16 a[212:213], v221 offset:51200
v_mfma_f32_16x16x32_bf16 v[120:123], a[244:247], a[184:187], v[120:123]
ds_read_b64_tr_b16 a[214:215], v225 offset:51200
v_mfma_f32_16x16x32_bf16 v[108:111], a[248:251], a[164:167], v[108:111]
ds_read_b64_tr_b16 a[216:217], v222 offset:51200
v_mfma_f32_16x16x32_bf16 v[112:115], a[248:251], a[188:191], v[112:115]
ds_read_b64_tr_b16 a[218:219], v226 offset:51200
v_mfma_f32_16x16x32_bf16 v[140:143], a[248:251], v[200:203], v[140:143]
ds_read_b64_tr_b16 a[220:221], v223 offset:51200
v_mfma_f32_16x16x32_bf16 v[116:119], a[252:255], a[164:167], v[116:119]
ds_read_b64_tr_b16 a[222:223], v227 offset:51200
v_mfma_f32_16x16x32_bf16 v[120:123], a[252:255], a[188:191], v[120:123]
ds_read_b64_tr_b16 a[224:225], v220 offset:53248
s_cmp_lg_u32 s83, 0
s_cbranch_scc1 K1_l11_t
K1_l11:
v_mfma_f32_16x16x32_bf16 v[0:3], v[12:15], v[152:155], v[0:3]
ds_read_b64_tr_b16 a[226:227], v224 offset:53248
s_waitcnt lgkmcnt(7)
v_max3_f32 v165, v108, v109, v110
v_mfma_f32_16x16x32_bf16 v[4:7], v[12:15], v[156:159], v[4:7]
ds_read_b64_tr_b16 a[228:229], v221 offset:53248
v_max3_f32 v165, v165, v111, v112
v_mfma_f32_16x16x32_bf16 a[0:3], a[192:195], v[152:155], a[0:3]
ds_read_b64_tr_b16 a[230:231], v225 offset:53248
v_max3_f32 v165, v165, v113, v114
v_mfma_f32_16x16x32_bf16 a[4:7], a[192:195], v[156:159], a[4:7]
ds_read_b64_tr_b16 a[232:233], v222 offset:53248
v_max3_f32 v165, v165, v115, v116
v_mfma_f32_16x16x32_bf16 a[8:11], a[196:199], v[152:155], a[8:11]
ds_read_b64_tr_b16 a[234:235], v226 offset:53248
v_max3_f32 v165, v165, v117, v118
v_mfma_f32_16x16x32_bf16 a[12:15], a[196:199], v[156:159], a[12:15]
ds_read_b64_tr_b16 a[236:237], v223 offset:53248
v_max3_f32 v165, v165, v119, v120
v_mfma_f32_16x16x32_bf16 a[16:19], a[200:203], v[152:155], a[16:19]
ds_read_b64_tr_b16 a[238:239], v227 offset:53248
v_max3_f32 v165, v165, v121, v122
v_mfma_f32_16x16x32_bf16 a[20:23], a[200:203], v[156:159], a[20:23]
ds_read_b128 a[240:243], v206 offset:0
v_max3_f32 v165, v165, v123, v140
v_mfma_f32_16x16x32_bf16 a[24:27], a[204:207], v[152:155], a[24:27]
ds_read_b128 a[244:247], v207 offset:0
s_waitcnt lgkmcnt(7)
v_max3_f32 v165, v165, v141, v142
v_mfma_f32_16x16x32_bf16 a[28:31], a[204:207], v[156:159], a[28:31]
ds_read_b128 a[248:251], v210 offset:0
v_max_f32 v165, v165, v143
v_mfma_f32_16x16x32_bf16 a[32:35], a[208:211], v[152:155], a[32:35]
ds_read_b128 a[252:255], v211 offset:0
v_cmp_lt_f32 vcc, s69, v165
s_cbranch_vccnz K1_l12_t
K1_l12:
v_mfma_f32_16x16x32_bf16 a[36:39], a[208:211], v[156:159], a[36:39]
v_exp_f32 v108, v108
v_mfma_f32_16x16x32_bf16 a[40:43], a[212:215], v[152:155], a[40:43]
v_exp_f32 v109, v109
v_mfma_f32_16x16x32_bf16 a[44:47], a[212:215], v[156:159], a[44:47]
v_exp_f32 v110, v110
v_mfma_f32_16x16x32_bf16 a[48:51], a[216:219], v[152:155], a[48:51]
v_exp_f32 v111, v111
v_mfma_f32_16x16x32_bf16 a[52:55], a[216:219], v[156:159], a[52:55]
ds_read_b128 a[192:195], v206 offset:2048
v_cvt_pk_bf16_f32 v144, v108, v109
v_mfma_f32_16x16x32_bf16 a[56:59], a[220:223], v[152:155], a[56:59]
ds_read_b128 a[196:199], v207 offset:2048
v_cvt_pk_bf16_f32 v145, v110, v111
v_mfma_f32_16x16x32_bf16 a[60:63], a[220:223], v[156:159], a[60:63]
s_waitcnt lgkmcnt(3)
v_exp_f32 v116, v116
v_mfma_f32_16x16x32_bf16 a[64:67], a[224:227], v[152:155], a[64:67]
v_exp_f32 v117, v117
v_mfma_f32_16x16x32_bf16 a[68:71], a[224:227], v[156:159], a[68:71]
v_exp_f32 v118, v118
v_mfma_f32_16x16x32_bf16 a[72:75], a[228:231], v[152:155], a[72:75]
v_exp_f32 v119, v119
v_mfma_f32_16x16x32_bf16 a[76:79], a[228:231], v[156:159], a[76:79]
ds_read_b128 a[200:203], v210 offset:2048
v_cvt_pk_bf16_f32 v146, v116, v117
v_mfma_f32_16x16x32_bf16 a[80:83], a[232:235], v[152:155], a[80:83]
ds_read_b128 a[204:207], v211 offset:2048
v_cvt_pk_bf16_f32 v147, v118, v119
v_mfma_f32_16x16x32_bf16 a[84:87], a[232:235], v[156:159], a[84:87]
v_exp_f32 v112, v112
v_mfma_f32_16x16x32_bf16 a[88:91], a[236:239], v[152:155], a[88:91]
ds_read_b128 a[208:211], v206 offset:4096
v_mfma_f32_16x16x32_bf16 a[92:95], a[236:239], v[156:159], a[92:95]
v_exp_f32 v113, v113
v_mfma_f32_16x16x32_bf16 v[124:127], a[240:243], a[144:147], v[168:171]
v_exp_f32 v114, v114
v_mfma_f32_16x16x32_bf16 v[128:131], a[240:243], a[168:171], v[172:175]
ds_read_b128 a[212:215], v207 offset:4096
v_mfma_f32_16x16x32_bf16 v[132:135], a[244:247], a[144:147], v[168:171]
v_exp_f32 v115, v115
v_mfma_f32_16x16x32_bf16 v[136:139], a[244:247], a[168:171], v[172:175]
s_waitcnt lgkmcnt(3)
v_cvt_pk_bf16_f32 v148, v112, v113
v_cvt_pk_bf16_f32 v149, v114, v115
v_mfma_f32_16x16x32_bf16 v[124:127], a[248:251], a[148:151], v[124:127]
ds_read_b128 a[216:219], v210 offset:4096
v_mfma_f32_16x16x32_bf16 v[128:131], a[248:251], a[172:175], v[128:131]
ds_read_b128 a[220:223], v211 offset:4096
v_mfma_f32_16x16x32_bf16 v[132:135], a[252:255], a[148:151], v[132:135]
v_exp_f32 v120, v120
v_mfma_f32_16x16x32_bf16 v[136:139], a[252:255], a[172:175], v[136:139]
v_exp_f32 v121, v121
v_mfma_f32_16x16x32_bf16 v[124:127], a[192:195], a[152:155], v[124:127]
v_exp_f32 v122, v122
v_mfma_f32_16x16x32_bf16 v[128:131], a[192:195], a[176:179], v[128:131]
v_exp_f32 v123, v123
v_mfma_f32_16x16x32_bf16 v[132:135], a[196:199], a[152:155], v[132:135]
s_waitcnt vmcnt(12) lgkmcnt(0)
s_barrier
v_mfma_f32_16x16x32_bf16 v[136:139], a[196:199], a[176:179], v[136:139]
ds_read_b64_tr_b16 a[224:225], v212 offset:0
v_cvt_pk_bf16_f32 v150, v120, v121
v_mfma_f32_16x16x32_bf16 v[124:127], a[200:203], a[156:159], v[124:127]
ds_read_b64_tr_b16 a[226:227], v216 offset:0
v_cvt_pk_bf16_f32 v151, v122, v123
v_mfma_f32_16x16x32_bf16 v[128:131], a[200:203], a[180:183], v[128:131]
ds_read_b64_tr_b16 a[228:229], v213 offset:0
v_mfma_f32_16x16x32_bf16 v[132:135], a[204:207], a[156:159], v[132:135]
ds_read_b64_tr_b16 a[230:231], v217 offset:0
s_add_u32 m0, s59, 49152
v_mfma_f32_16x16x32_bf16 v[136:139], a[204:207], a[180:183], v[136:139]
s_add_u32 s67, s63, 122880
v_exp_f32 v140, v140
v_mfma_f32_16x16x32_bf16 v[124:127], a[208:211], a[160:163], v[124:127]
buffer_load_dwordx4 v234, s[44:47], s67 offen nt lds
v_mfma_f32_16x16x32_bf16 v[128:131], a[208:211], a[184:187], v[128:131]
v_exp_f32 v141, v141
v_mfma_f32_16x16x32_bf16 v[132:135], a[212:215], a[160:163], v[132:135]
ds_read_b64_tr_b16 a[232:233], v214 offset:0
ds_read_b64_tr_b16 a[234:235], v218 offset:0
v_mfma_f32_16x16x32_bf16 v[136:139], a[212:215], a[184:187], v[136:139]
v_exp_f32 v142, v142
v_mfma_f32_16x16x32_bf16 v[124:127], a[216:219], a[164:167], v[124:127]
v_exp_f32 v143, v143
v_mfma_f32_16x16x32_bf16 v[128:131], a[216:219], a[188:191], v[128:131]
ds_read_b64_tr_b16 a[236:237], v215 offset:0
v_cvt_pk_bf16_f32 v160, v140, v141
v_mfma_f32_16x16x32_bf16 v[132:135], a[220:223], a[164:167], v[132:135]
ds_read_b64_tr_b16 a[238:239], v219 offset:0
v_cvt_pk_bf16_f32 v161, v142, v143
v_mfma_f32_16x16x32_bf16 v[136:139], a[220:223], a[188:191], v[136:139]
buffer_load_dwordx4 v235, s[44:47], s67 offen offset:1024 nt lds
s_cmp_lg_u32 s83, 0
s_cbranch_scc1 K1_l13_t
K1_l13:
v_mfma_f32_16x16x32_bf16 v[0:3], v[12:15], v[144:147], v[0:3]
s_waitcnt lgkmcnt(4)
v_max3_f32 v165, v124, v125, v126
v_max3_f32 v165, v165, v127, v128
v_mfma_f32_16x16x32_bf16 v[4:7], v[12:15], v[148:151], v[4:7]
ds_read_b64_tr_b16 a[240:241], v212 offset:2048
v_max3_f32 v165, v165, v129, v130
v_mfma_f32_16x16x16_bf16 v[8:11], v[12:13], v[160:161], v[8:11]
ds_read_b64_tr_b16 a[242:243], v216 offset:2048
v_max3_f32 v165, v165, v131, v132
v_mfma_f32_16x16x32_bf16 a[0:3], a[224:227], v[144:147], a[0:3]
ds_read_b64_tr_b16 a[244:245], v213 offset:2048
v_max3_f32 v165, v165, v133, v134
v_mfma_f32_16x16x32_bf16 a[4:7], a[224:227], v[148:151], a[4:7]
ds_read_b64_tr_b16 a[246:247], v217 offset:2048
v_max3_f32 v165, v165, v135, v136
v_mfma_f32_16x16x16_bf16 a[96:99], a[224:225], v[160:161], a[96:99]
s_add_u32 m0, s59, 51200
v_max3_f32 v165, v165, v137, v138
v_max_f32 v165, v165, v139
v_mfma_f32_16x16x32_bf16 a[8:11], a[228:231], v[144:147], a[8:11]
s_waitcnt lgkmcnt(4)
v_cmp_lt_f32 vcc, s69, v165
s_cbranch_vccnz K1_l14_t
K1_l14:
v_mfma_f32_16x16x32_bf16 a[12:15], a[228:231], v[148:151], a[12:15]
s_add_u32 s67, s63, 123008
v_exp_f32 v124, v124
v_mfma_f32_16x16x16_bf16 a[100:103], a[228:229], v[160:161], a[100:103]
buffer_load_dwordx4 v234, s[44:47], s67 offen nt lds
v_mfma_f32_16x16x32_bf16 a[16:19], a[232:235], v[144:147], a[16:19]
ds_read_b64_tr_b16 a[248:249], v214 offset:2048
ds_read_b64_tr_b16 a[250:251], v218 offset:2048
v_mfma_f32_16x16x32_bf16 a[20:23], a[232:235], v[148:151], a[20:23]
ds_read_b64_tr_b16 a[252:253], v215 offset:2048
ds_read_b64_tr_b16 a[254:255], v219 offset:2048
v_mfma_f32_16x16x16_bf16 a[104:107], a[232:233], v[160:161], a[104:107]
buffer_load_dwordx4 v235, s[44:47], s67 offen offset:1024 nt lds
v_mfma_f32_16x16x32_bf16 a[24:27], a[236:239], v[144:147], a[24:27]
s_waitcnt lgkmcnt(4)
v_exp_f32 v125, v125
v_mfma_f32_16x16x32_bf16 a[28:31], a[236:239], v[148:151], a[28:31]
ds_read_b64_tr_b16 a[192:193], v212 offset:4096
ds_read_b64_tr_b16 a[194:195], v216 offset:4096
v_mfma_f32_16x16x16_bf16 a[108:111], a[236:237], v[160:161], a[108:111]
ds_read_b64_tr_b16 a[196:197], v213 offset:4096
ds_read_b64_tr_b16 a[198:199], v217 offset:4096
v_mfma_f32_16x16x32_bf16 a[32:35], a[240:243], v[144:147], a[32:35]
s_add_u32 m0, s59, 53248
v_exp_f32 v126, v126
v_mfma_f32_16x16x32_bf16 a[36:39], a[240:243], v[148:151], a[36:39]
s_add_u32 s67, s63, 123136
v_exp_f32 v127, v127
v_mfma_f32_16x16x16_bf16 a[112:115], a[240:241], v[160:161], a[112:115]
buffer_load_dwordx4 v234, s[44:47], s67 offen nt lds
v_mfma_f32_16x16x32_bf16 a[40:43], a[244:247], v[144:147], a[40:43]
s_waitcnt lgkmcnt(4)
v_cvt_pk_bf16_f32 v152, v124, v125
v_cvt_pk_bf16_f32 v153, v126, v127
v_mfma_f32_16x16x32_bf16 a[44:47], a[244:247], v[148:151], a[44:47]
ds_read_b64_tr_b16 a[200:201], v214 offset:4096
ds_read_b64_tr_b16 a[202:203], v218 offset:4096
v_mfma_f32_16x16x16_bf16 a[116:119], a[244:245], v[160:161], a[116:119]
ds_read_b64_tr_b16 a[204:205], v215 offset:4096
ds_read_b64_tr_b16 a[206:207], v219 offset:4096
v_mfma_f32_16x16x32_bf16 a[48:51], a[248:251], v[144:147], a[48:51]
buffer_load_dwordx4 v235, s[44:47], s67 offen offset:1024 nt lds
v_mfma_f32_16x16x32_bf16 a[52:55], a[248:251], v[148:151], a[52:55]
ds_read_b128 a[208:211], v204 offset:24576
v_mfma_f32_16x16x16_bf16 a[120:123], a[248:249], v[160:161], a[120:123]
s_add_u32 m0, s58, 0
v_exp_f32 v132, v132
v_mfma_f32_16x16x32_bf16 a[56:59], a[252:255], v[144:147], a[56:59]
s_waitcnt lgkmcnt(5)
v_exp_f32 v133, v133
v_mfma_f32_16x16x32_bf16 a[60:63], a[252:255], v[148:151], a[60:63]
ds_read_b128 a[212:215], v205 offset:24576
s_add_u32 s67, s63, 147456
v_mfma_f32_16x16x16_bf16 a[124:127], a[252:253], v[160:161], a[124:127]
buffer_load_dwordx4 v232, s[40:43], s67 offen nt lds
v_mfma_f32_16x16x32_bf16 a[64:67], a[192:195], v[144:147], a[64:67]
ds_read_b128 a[216:219], v208 offset:24576
v_mfma_f32_16x16x32_bf16 a[68:71], a[192:195], v[148:151], a[68:71]
v_exp_f32 v134, v134
v_mfma_f32_16x16x16_bf16 a[128:131], a[192:193], v[160:161], a[128:131]
ds_read_b128 a[220:223], v209 offset:24576
v_mfma_f32_16x16x32_bf16 a[72:75], a[196:199], v[144:147], a[72:75]
s_waitcnt lgkmcnt(3)
v_exp_f32 v135, v135
v_mfma_f32_16x16x32_bf16 a[76:79], a[196:199], v[148:151], a[76:79]
ds_read_b128 a[224:227], v204 offset:26624
v_cvt_pk_bf16_f32 v154, v132, v133
v_mfma_f32_16x16x16_bf16 a[132:135], a[196:197], v[160:161], a[132:135]
buffer_load_dwordx4 v233, s[40:43], s67 offen offset:1024 nt lds
v_mfma_f32_16x16x32_bf16 a[80:83], a[200:203], v[144:147], a[80:83]
ds_read_b128 a[228:231], v205 offset:26624
v_cvt_pk_bf16_f32 v155, v134, v135
v_mfma_f32_16x16x32_bf16 a[84:87], a[200:203], v[148:151], a[84:87]
ds_read_b128 a[232:235], v208 offset:26624
s_add_u32 m0, s58, 2048
v_mfma_f32_16x16x16_bf16 a[136:139], a[200:201], v[160:161], a[136:139]
s_add_u32 s67, s63, 147584
v_exp_f32 v128, v128
v_mfma_f32_16x16x32_bf16 a[88:91], a[204:207], v[144:147], a[88:91]
buffer_load_dwordx4 v232, s[40:43], s67 offen nt lds
v_mfma_f32_16x16x32_bf16 a[92:95], a[204:207], v[148:151], a[92:95]
v_exp_f32 v129, v129
v_mfma_f32_16x16x16_bf16 a[140:143], a[204:205], v[160:161], a[140:143]
ds_read_b128 a[236:239], v209 offset:26624
v_mfma_f32_16x16x32_bf16 v[108:111], a[208:211], a[144:147], v[168:171]
s_waitcnt lgkmcnt(3)
v_exp_f32 v130, v130
v_mfma_f32_16x16x32_bf16 v[112:115], a[208:211], a[168:171], v[172:175]
ds_read_b128 a[240:243], v204 offset:28672
v_mfma_f32_16x16x32_bf16 v[140:143], a[208:211], v[180:183], v[176:179]
buffer_load_dwordx4 v233, s[40:43], s67 offen offset:1024 nt lds
v_mfma_f32_16x16x32_bf16 v[116:119], a[212:215], a[144:147], v[168:171]
ds_read_b128 a[244:247], v205 offset:28672
v_mfma_f32_16x16x32_bf16 v[120:123], a[212:215], a[168:171], v[172:175]
v_exp_f32 v131, v131
v_mfma_f32_16x16x32_bf16 v[108:111], a[216:219], a[148:151], v[108:111]
ds_read_b128 a[248:251], v208 offset:28672
v_cvt_pk_bf16_f32 v156, v128, v129
v_mfma_f32_16x16x32_bf16 v[112:115], a[216:219], a[172:175], v[112:115]
ds_read_b128 a[252:255], v209 offset:28672
v_cvt_pk_bf16_f32 v157, v130, v131
v_mfma_f32_16x16x32_bf16 v[140:143], a[216:219], v[184:187], v[140:143]
s_add_u32 m0, s58, 4096
v_exp_f32 v136, v136
v_mfma_f32_16x16x32_bf16 v[116:119], a[220:223], a[148:151], v[116:119]
s_add_u32 s67, s63, 147712
v_exp_f32 v137, v137
v_mfma_f32_16x16x32_bf16 v[120:123], a[220:223], a[172:175], v[120:123]
buffer_load_dwordx4 v232, s[40:43], s67 offen nt lds
v_mfma_f32_16x16x32_bf16 v[108:111], a[224:227], a[152:155], v[108:111]
s_waitcnt lgkmcnt(3)
v_exp_f32 v138, v138
v_mfma_f32_16x16x32_bf16 v[112:115], a[224:227], a[176:179], v[112:115]
ds_read_b64_tr_b16 a[192:193], v220 offset:0
v_mfma_f32_16x16x32_bf16 v[140:143], a[224:227], v[188:191], v[140:143]
ds_read_b64_tr_b16 a[194:195], v224 offset:0
v_mfma_f32_16x16x32_bf16 v[116:119], a[228:231], a[152:155], v[116:119]
ds_read_b64_tr_b16 a[196:197], v221 offset:0
v_mfma_f32_16x16x32_bf16 v[120:123], a[228:231], a[176:179], v[120:123]
ds_read_b64_tr_b16 a[198:199], v225 offset:0
v_mfma_f32_16x16x32_bf16 v[108:111], a[232:235], a[156:159], v[108:111]
buffer_load_dwordx4 v233, s[40:43], s67 offen offset:1024 nt lds
v_mfma_f32_16x16x32_bf16 v[112:115], a[232:235], a[180:183], v[112:115]
ds_read_b64_tr_b16 a[200:201], v222 offset:0
v_mfma_f32_16x16x32_bf16 v[140:143], a[232:235], v[192:195], v[140:143]
ds_read_b64_tr_b16 a[202:203], v226 offset:0
v_mfma_f32_16x16x32_bf16 v[116:119], a[236:239], a[156:159], v[116:119]
ds_read_b64_tr_b16 a[204:205], v223 offset:0
v_mfma_f32_16x16x32_bf16 v[120:123], a[236:239], a[180:183], v[120:123]
ds_read_b64_tr_b16 a[206:207], v227 offset:0
v_mfma_f32_16x16x32_bf16 v[108:111], a[240:243], a[160:163], v[108:111]
s_waitcnt lgkmcnt(6)
v_exp_f32 v139, v139
v_mfma_f32_16x16x32_bf16 v[112:115], a[240:243], a[184:187], v[112:115]
ds_read_b64_tr_b16 a[208:209], v220 offset:2048
v_cvt_pk_bf16_f32 v158, v136, v137
v_mfma_f32_16x16x32_bf16 v[140:143], a[240:243], v[196:199], v[140:143]
ds_read_b64_tr_b16 a[210:211], v224 offset:2048
v_cvt_pk_bf16_f32 v159, v138, v139
v_mfma_f32_16x16x32_bf16 v[116:119], a[244:247], a[160:163], v[116:119]
ds_read_b64_tr_b16 a[212:213], v221 offset:2048
v_mfma_f32_16x16x32_bf16 v[120:123], a[244:247], a[184:187], v[120:123]
ds_read_b64_tr_b16 a[214:215], v225 offset:2048
v_mfma_f32_16x16x32_bf16 v[108:111], a[248:251], a[164:167], v[108:111]
ds_read_b64_tr_b16 a[216:217], v222 offset:2048
v_mfma_f32_16x16x32_bf16 v[112:115], a[248:251], a[188:191], v[112:115]
ds_read_b64_tr_b16 a[218:219], v226 offset:2048
v_mfma_f32_16x16x32_bf16 v[140:143], a[248:251], v[200:203], v[140:143]
ds_read_b64_tr_b16 a[220:221], v223 offset:2048
v_mfma_f32_16x16x32_bf16 v[116:119], a[252:255], a[164:167], v[116:119]
ds_read_b64_tr_b16 a[222:223], v227 offset:2048
v_mfma_f32_16x16x32_bf16 v[120:123], a[252:255], a[188:191], v[120:123]
ds_read_b64_tr_b16 a[224:225], v220 offset:4096
s_cmp_lg_u32 s83, 0
s_cbranch_scc1 K1_l15_t
K1_l15:
v_mfma_f32_16x16x32_bf16 v[0:3], v[12:15], v[152:155], v[0:3]
ds_read_b64_tr_b16 a[226:227], v224 offset:4096
s_waitcnt lgkmcnt(7)
v_max3_f32 v165, v108, v109, v110
v_mfma_f32_16x16x32_bf16 v[4:7], v[12:15], v[156:159], v[4:7]
ds_read_b64_tr_b16 a[228:229], v221 offset:4096
v_max3_f32 v165, v165, v111, v112
v_mfma_f32_16x16x32_bf16 a[0:3], a[192:195], v[152:155], a[0:3]
ds_read_b64_tr_b16 a[230:231], v225 offset:4096
v_max3_f32 v165, v165, v113, v114
v_mfma_f32_16x16x32_bf16 a[4:7], a[192:195], v[156:159], a[4:7]
ds_read_b64_tr_b16 a[232:233], v222 offset:4096
v_max3_f32 v165, v165, v115, v116
v_mfma_f32_16x16x32_bf16 a[8:11], a[196:199], v[152:155], a[8:11]
ds_read_b64_tr_b16 a[234:235], v226 offset:4096
v_max3_f32 v165, v165, v117, v118
v_mfma_f32_16x16x32_bf16 a[12:15], a[196:199], v[156:159], a[12:15]
ds_read_b64_tr_b16 a[236:237], v223 offset:4096
v_max3_f32 v165, v165, v119, v120
v_mfma_f32_16x16x32_bf16 a[16:19], a[200:203], v[152:155], a[16:19]
ds_read_b64_tr_b16 a[238:239], v227 offset:4096
v_max3_f32 v165, v165, v121, v122
v_mfma_f32_16x16x32_bf16 a[20:23], a[200:203], v[156:159], a[20:23]
ds_read_b128 a[240:243], v206 offset:24576
v_max3_f32 v165, v165, v123, v140
v_mfma_f32_16x16x32_bf16 a[24:27], a[204:207], v[152:155], a[24:27]
ds_read_b128 a[244:247], v207 offset:24576
s_waitcnt lgkmcnt(7)
v_max3_f32 v165, v165, v141, v142
v_mfma_f32_16x16x32_bf16 a[28:31], a[204:207], v[156:159], a[28:31]
ds_read_b128 a[248:251], v210 offset:24576
v_max_f32 v165, v165, v143
v_mfma_f32_16x16x32_bf16 a[32:35], a[208:211], v[152:155], a[32:35]
ds_read_b128 a[252:255], v211 offset:24576
v_cmp_lt_f32 vcc, s69, v165
s_cbranch_vccnz K1_l16_t
K1_l16:
v_mfma_f32_16x16x32_bf16 a[36:39], a[208:211], v[156:159], a[36:39]
v_exp_f32 v108, v108
v_mfma_f32_16x16x32_bf16 a[40:43], a[212:215], v[152:155], a[40:43]
v_exp_f32 v109, v109
v_mfma_f32_16x16x32_bf16 a[44:47], a[212:215], v[156:159], a[44:47]
v_exp_f32 v110, v110
v_mfma_f32_16x16x32_bf16 a[48:51], a[216:219], v[152:155], a[48:51]
v_exp_f32 v111, v111
v_mfma_f32_16x16x32_bf16 a[52:55], a[216:219], v[156:159], a[52:55]
ds_read_b128 a[192:195], v206 offset:26624
v_cvt_pk_bf16_f32 v144, v108, v109
v_mfma_f32_16x16x32_bf16 a[56:59], a[220:223], v[152:155], a[56:59]
ds_read_b128 a[196:199], v207 offset:26624
v_cvt_pk_bf16_f32 v145, v110, v111
v_mfma_f32_16x16x32_bf16 a[60:63], a[220:223], v[156:159], a[60:63]
s_waitcnt lgkmcnt(3)
v_exp_f32 v116, v116
v_mfma_f32_16x16x32_bf16 a[64:67], a[224:227], v[152:155], a[64:67]
v_exp_f32 v117, v117
v_mfma_f32_16x16x32_bf16 a[68:71], a[224:227], v[156:159], a[68:71]
v_exp_f32 v118, v118
v_mfma_f32_16x16x32_bf16 a[72:75], a[228:231], v[152:155], a[72:75]
v_exp_f32 v119, v119
v_mfma_f32_16x16x32_bf16 a[76:79], a[228:231], v[156:159], a[76:79]
ds_read_b128 a[200:203], v210 offset:26624
v_cvt_pk_bf16_f32 v146, v116, v117
v_mfma_f32_16x16x32_bf16 a[80:83], a[232:235], v[152:155], a[80:83]
ds_read_b128 a[204:207], v211 offset:26624
v_cvt_pk_bf16_f32 v147, v118, v119
v_mfma_f32_16x16x32_bf16 a[84:87], a[232:235], v[156:159], a[84:87]
v_exp_f32 v112, v112
v_mfma_f32_16x16x32_bf16 a[88:91], a[236:239], v[152:155], a[88:91]
ds_read_b128 a[208:211], v206 offset:28672
v_mfma_f32_16x16x32_bf16 a[92:95], a[236:239], v[156:159], a[92:95]
v_exp_f32 v113, v113
v_mfma_f32_16x16x32_bf16 v[124:127], a[240:243], a[144:147], v[168:171]
v_exp_f32 v114, v114
v_mfma_f32_16x16x32_bf16 v[128:131], a[240:243], a[168:171], v[172:175]
ds_read_b128 a[212:215], v207 offset:28672
v_mfma_f32_16x16x32_bf16 v[132:135], a[244:247], a[144:147], v[168:171]
v_exp_f32 v115, v115
v_mfma_f32_16x16x32_bf16 v[136:139], a[244:247], a[168:171], v[172:175]
s_waitcnt lgkmcnt(3)
v_cvt_pk_bf16_f32 v148, v112, v113
v_cvt_pk_bf16_f32 v149, v114, v115
v_mfma_f32_16x16x32_bf16 v[124:127], a[248:251], a[148:151], v[124:127]
ds_read_b128 a[216:219], v210 offset:28672
v_mfma_f32_16x16x32_bf16 v[128:131], a[248:251], a[172:175], v[128:131]
ds_read_b128 a[220:223], v211 offset:28672
v_mfma_f32_16x16x32_bf16 v[132:135], a[252:255], a[148:151], v[132:135]
v_exp_f32 v120, v120
v_mfma_f32_16x16x32_bf16 v[136:139], a[252:255], a[172:175], v[136:139]
v_exp_f32 v121, v121
v_mfma_f32_16x16x32_bf16 v[124:127], a[192:195], a[152:155], v[124:127]
v_exp_f32 v122, v122
v_mfma_f32_16x16x32_bf16 v[128:131], a[192:195], a[176:179], v[128:131]
v_exp_f32 v123, v123
v_mfma_f32_16x16x32_bf16 v[132:135], a[196:199], a[152:155], v[132:135]
s_waitcnt vmcnt(12) lgkmcnt(0)
s_barrier
v_mfma_f32_16x16x32_bf16 v[136:139], a[196:199], a[176:179], v[136:139]
ds_read_b64_tr_b16 a[224:225], v212 offset:24576
v_cvt_pk_bf16_f32 v150, v120, v121
v_mfma_f32_16x16x32_bf16 v[124:127], a[200:203], a[156:159], v[124:127]
ds_read_b64_tr_b16 a[226:227], v216 offset:24576
v_cvt_pk_bf16_f32 v151, v122, v123
v_mfma_f32_16x16x32_bf16 v[128:131], a[200:203], a[180:183], v[128:131]
ds_read_b64_tr_b16 a[228:229], v213 offset:24576
v_mfma_f32_16x16x32_bf16 v[132:135], a[204:207], a[156:159], v[132:135]
ds_read_b64_tr_b16 a[230:231], v217 offset:24576
s_add_u32 m0, s59, 0
v_mfma_f32_16x16x32_bf16 v[136:139], a[204:207], a[180:183], v[136:139]
s_add_u32 s67, s63, 147456
v_exp_f32 v140, v140
v_mfma_f32_16x16x32_bf16 v[124:127], a[208:211], a[160:163], v[124:127]
buffer_load_dwordx4 v234, s[44:47], s67 offen nt lds
v_mfma_f32_16x16x32_bf16 v[128:131], a[208:211], a[184:187], v[128:131]
v_exp_f32 v141, v141
v_mfma_f32_16x16x32_bf16 v[132:135], a[212:215], a[160:163], v[132:135]
ds_read_b64_tr_b16 a[232:233], v214 offset:24576
ds_read_b64_tr_b16 a[234:235], v218 offset:24576
v_mfma_f32_16x16x32_bf16 v[136:139], a[212:215], a[184:187], v[136:139]
v_exp_f32 v142, v142
v_mfma_f32_16x16x32_bf16 v[124:127], a[216:219], a[164:167], v[124:127]
v_exp_f32 v143, v143
v_mfma_f32_16x16x32_bf16 v[128:131], a[216:219], a[188:191], v[128:131]
ds_read_b64_tr_b16 a[236:237], v215 offset:24576
v_cvt_pk_bf16_f32 v160, v140, v141
v_mfma_f32_16x16x32_bf16 v[132:135], a[220:223], a[164:167], v[132:135]
ds_read_b64_tr_b16 a[238:239], v219 offset:24576
v_cvt_pk_bf16_f32 v161, v142, v143
v_mfma_f32_16x16x32_bf16 v[136:139], a[220:223], a[188:191], v[136:139]
buffer_load_dwordx4 v235, s[44:47], s67 offen offset:1024 nt lds
s_cmp_lg_u32 s83, 0
s_cbranch_scc1 K1_l17_t
K1_l17:
v_mfma_f32_16x16x32_bf16 v[0:3], v[12:15], v[144:147], v[0:3]
s_waitcnt lgkmcnt(4)
v_max3_f32 v165, v124, v125, v126
v_max3_f32 v165, v165, v127, v128
v_mfma_f32_16x16x32_bf16 v[4:7], v[12:15], v[148:151], v[4:7]
ds_read_b64_tr_b16 a[240:241], v212 offset:26624
v_max3_f32 v165, v165, v129, v130
v_mfma_f32_16x16x16_bf16 v[8:11], v[12:13], v[160:161], v[8:11]
ds_read_b64_tr_b16 a[242:243], v216 offset:26624
v_max3_f32 v165, v165, v131, v132
v_mfma_f32_16x16x32_bf16 a[0:3], a[224:227], v[144:147], a[0:3]
ds_read_b64_tr_b16 a[244:245], v213 offset:26624
v_max3_f32 v165, v165, v133, v134
v_mfma_f32_16x16x32_bf16 a[4:7], a[224:227], v[148:151], a[4:7]
ds_read_b64_tr_b16 a[246:247], v217 offset:26624
v_max3_f32 v165, v165, v135, v136
v_mfma_f32_16x16x16_bf16 a[96:99], a[224:225], v[160:161], a[96:99]
s_add_u32 m0, s59, 2048
v_max3_f32 v165, v165, v137, v138
v_max_f32 v165, v165, v139
v_mfma_f32_16x16x32_bf16 a[8:11], a[228:231], v[144:147], a[8:11]
s_waitcnt lgkmcnt(4)
v_cmp_lt_f32 vcc, s69, v165
s_cbranch_vccnz K1_l18_t
K1_l18:
v_mfma_f32_16x16x32_bf16 a[12:15], a[228:231], v[148:151], a[12:15]
s_add_u32 s67, s63, 147584
v_exp_f32 v124, v124
v_mfma_f32_16x16x16_bf16 a[100:103], a[228:229], v[160:161], a[100:103]
buffer_load_dwordx4 v234, s[44:47], s67 offen nt lds
v_mfma_f32_16x16x32_bf16 a[16:19], a[232:235], v[144:147], a[16:19]
ds_read_b64_tr_b16 a[248:249], v214 offset:26624
ds_read_b64_tr_b16 a[250:251], v218 offset:26624
v_mfma_f32_16x16x32_bf16 a[20:23], a[232:235], v[148:151], a[20:23]
ds_read_b64_tr_b16 a[252:253], v215 offset:26624
ds_read_b64_tr_b16 a[254:255], v219 offset:26624
v_mfma_f32_16x16x16_bf16 a[104:107], a[232:233], v[160:161], a[104:107]
buffer_load_dwordx4 v235, s[44:47], s67 offen offset:1024 nt lds
v_mfma_f32_16x16x32_bf16 a[24:27], a[236:239], v[144:147], a[24:27]
s_waitcnt lgkmcnt(4)
v_exp_f32 v125, v125
v_mfma_f32_16x16x32_bf16 a[28:31], a[236:239], v[148:151], a[28:31]
ds_read_b64_tr_b16 a[192:193], v212 offset:28672
ds_read_b64_tr_b16 a[194:195], v216 offset:28672
v_mfma_f32_16x16x16_bf16 a[108:111], a[236:237], v[160:161], a[108:111]
ds_read_b64_tr_b16 a[196:197], v213 offset:28672
ds_read_b64_tr_b16 a[198:199], v217 offset:28672
v_mfma_f32_16x16x32_bf16 a[32:35], a[240:243], v[144:147], a[32:35]
s_add_u32 m0, s59, 4096
v_exp_f32 v126, v126
v_mfma_f32_16x16x32_bf16 a[36:39], a[240:243], v[148:151], a[36:39]
s_add_u32 s67, s63, 147712
v_exp_f32 v127, v127
v_mfma_f32_16x16x16_bf16 a[112:115], a[240:241], v[160:161], a[112:115]
buffer_load_dwordx4 v234, s[44:47], s67 offen nt lds
v_mfma_f32_16x16x32_bf16 a[40:43], a[244:247], v[144:147], a[40:43]
s_waitcnt lgkmcnt(4)
v_cvt_pk_bf16_f32 v152, v124, v125
v_cvt_pk_bf16_f32 v153, v126, v127
v_mfma_f32_16x16x32_bf16 a[44:47], a[244:247], v[148:151], a[44:47]
ds_read_b64_tr_b16 a[200:201], v214 offset:28672
ds_read_b64_tr_b16 a[202:203], v218 offset:28672
v_mfma_f32_16x16x16_bf16 a[116:119], a[244:245], v[160:161], a[116:119]
ds_read_b64_tr_b16 a[204:205], v215 offset:28672
ds_read_b64_tr_b16 a[206:207], v219 offset:28672
v_mfma_f32_16x16x32_bf16 a[48:51], a[248:251], v[144:147], a[48:51]
buffer_load_dwordx4 v235, s[44:47], s67 offen offset:1024 nt lds
v_mfma_f32_16x16x32_bf16 a[52:55], a[248:251], v[148:151], a[52:55]
ds_read_b128 a[208:211], v204 offset:49152
v_mfma_f32_16x16x16_bf16 a[120:123], a[248:249], v[160:161], a[120:123]
s_add_u32 m0, s58, 24576
v_exp_f32 v132, v132
v_mfma_f32_16x16x32_bf16 a[56:59], a[252:255], v[144:147], a[56:59]
s_waitcnt lgkmcnt(5)
v_exp_f32 v133, v133
v_mfma_f32_16x16x32_bf16 a[60:63], a[252:255], v[148:151], a[60:63]
ds_read_b128 a[212:215], v205 offset:49152
s_add_u32 s67, s63, 172032
v_mfma_f32_16x16x16_bf16 a[124:127], a[252:253], v[160:161], a[124:127]
buffer_load_dwordx4 v232, s[40:43], s67 offen nt lds
v_mfma_f32_16x16x32_bf16 a[64:67], a[192:195], v[144:147], a[64:67]
ds_read_b128 a[216:219], v208 offset:49152
v_mfma_f32_16x16x32_bf16 a[68:71], a[192:195], v[148:151], a[68:71]
v_exp_f32 v134, v134
v_mfma_f32_16x16x16_bf16 a[128:131], a[192:193], v[160:161], a[128:131]
ds_read_b128 a[220:223], v209 offset:49152
v_mfma_f32_16x16x32_bf16 a[72:75], a[196:199], v[144:147], a[72:75]
s_waitcnt lgkmcnt(3)
v_exp_f32 v135, v135
v_mfma_f32_16x16x32_bf16 a[76:79], a[196:199], v[148:151], a[76:79]
ds_read_b128 a[224:227], v204 offset:51200
v_cvt_pk_bf16_f32 v154, v132, v133
v_mfma_f32_16x16x16_bf16 a[132:135], a[196:197], v[160:161], a[132:135]
buffer_load_dwordx4 v233, s[40:43], s67 offen offset:1024 nt lds
v_mfma_f32_16x16x32_bf16 a[80:83], a[200:203], v[144:147], a[80:83]
ds_read_b128 a[228:231], v205 offset:51200
v_cvt_pk_bf16_f32 v155, v134, v135
v_mfma_f32_16x16x32_bf16 a[84:87], a[200:203], v[148:151], a[84:87]
ds_read_b128 a[232:235], v208 offset:51200
s_add_u32 m0, s58, 26624
v_mfma_f32_16x16x16_bf16 a[136:139], a[200:201], v[160:161], a[136:139]
s_add_u32 s67, s63, 172160
v_exp_f32 v128, v128
v_mfma_f32_16x16x32_bf16 a[88:91], a[204:207], v[144:147], a[88:91]
buffer_load_dwordx4 v232, s[40:43], s67 offen nt lds
v_mfma_f32_16x16x32_bf16 a[92:95], a[204:207], v[148:151], a[92:95]
v_exp_f32 v129, v129
v_mfma_f32_16x16x16_bf16 a[140:143], a[204:205], v[160:161], a[140:143]
ds_read_b128 a[236:239], v209 offset:51200
v_mfma_f32_16x16x32_bf16 v[108:111], a[208:211], a[144:147], v[168:171]
s_waitcnt lgkmcnt(3)
v_exp_f32 v130, v130
v_mfma_f32_16x16x32_bf16 v[112:115], a[208:211], a[168:171], v[172:175]
ds_read_b128 a[240:243], v204 offset:53248
v_mfma_f32_16x16x32_bf16 v[140:143], a[208:211], v[180:183], v[176:179]
buffer_load_dwordx4 v233, s[40:43], s67 offen offset:1024 nt lds
v_mfma_f32_16x16x32_bf16 v[116:119], a[212:215], a[144:147], v[168:171]
ds_read_b128 a[244:247], v205 offset:53248
v_mfma_f32_16x16x32_bf16 v[120:123], a[212:215], a[168:171], v[172:175]
v_exp_f32 v131, v131
v_mfma_f32_16x16x32_bf16 v[108:111], a[216:219], a[148:151], v[108:111]
ds_read_b128 a[248:251], v208 offset:53248
v_cvt_pk_bf16_f32 v156, v128, v129
v_mfma_f32_16x16x32_bf16 v[112:115], a[216:219], a[172:175], v[112:115]
ds_read_b128 a[252:255], v209 offset:53248
v_cvt_pk_bf16_f32 v157, v130, v131
v_mfma_f32_16x16x32_bf16 v[140:143], a[216:219], v[184:187], v[140:143]
s_add_u32 m0, s58, 28672
v_exp_f32 v136, v136
v_mfma_f32_16x16x32_bf16 v[116:119], a[220:223], a[148:151], v[116:119]
s_add_u32 s67, s63, 172288
v_exp_f32 v137, v137
v_mfma_f32_16x16x32_bf16 v[120:123], a[220:223], a[172:175], v[120:123]
buffer_load_dwordx4 v232, s[40:43], s67 offen nt lds
v_mfma_f32_16x16x32_bf16 v[108:111], a[224:227], a[152:155], v[108:111]
s_waitcnt lgkmcnt(3)
v_exp_f32 v138, v138
v_mfma_f32_16x16x32_bf16 v[112:115], a[224:227], a[176:179], v[112:115]
ds_read_b64_tr_b16 a[192:193], v220 offset:24576
v_mfma_f32_16x16x32_bf16 v[140:143], a[224:227], v[188:191], v[140:143]
ds_read_b64_tr_b16 a[194:195], v224 offset:24576
v_mfma_f32_16x16x32_bf16 v[116:119], a[228:231], a[152:155], v[116:119]
ds_read_b64_tr_b16 a[196:197], v221 offset:24576
v_mfma_f32_16x16x32_bf16 v[120:123], a[228:231], a[176:179], v[120:123]
ds_read_b64_tr_b16 a[198:199], v225 offset:24576
v_mfma_f32_16x16x32_bf16 v[108:111], a[232:235], a[156:159], v[108:111]
buffer_load_dwordx4 v233, s[40:43], s67 offen offset:1024 nt lds
v_mfma_f32_16x16x32_bf16 v[112:115], a[232:235], a[180:183], v[112:115]
ds_read_b64_tr_b16 a[200:201], v222 offset:24576
v_mfma_f32_16x16x32_bf16 v[140:143], a[232:235], v[192:195], v[140:143]
ds_read_b64_tr_b16 a[202:203], v226 offset:24576
v_mfma_f32_16x16x32_bf16 v[116:119], a[236:239], a[156:159], v[116:119]
ds_read_b64_tr_b16 a[204:205], v223 offset:24576
v_mfma_f32_16x16x32_bf16 v[120:123], a[236:239], a[180:183], v[120:123]
ds_read_b64_tr_b16 a[206:207], v227 offset:24576
v_mfma_f32_16x16x32_bf16 v[108:111], a[240:243], a[160:163], v[108:111]
s_waitcnt lgkmcnt(6)
v_exp_f32 v139, v139
v_mfma_f32_16x16x32_bf16 v[112:115], a[240:243], a[184:187], v[112:115]
ds_read_b64_tr_b16 a[208:209], v220 offset:26624
v_cvt_pk_bf16_f32 v158, v136, v137
v_mfma_f32_16x16x32_bf16 v[140:143], a[240:243], v[196:199], v[140:143]
ds_read_b64_tr_b16 a[210:211], v224 offset:26624
v_cvt_pk_bf16_f32 v159, v138, v139
v_mfma_f32_16x16x32_bf16 v[116:119], a[244:247], a[160:163], v[116:119]
ds_read_b64_tr_b16 a[212:213], v221 offset:26624
v_mfma_f32_16x16x32_bf16 v[120:123], a[244:247], a[184:187], v[120:123]
ds_read_b64_tr_b16 a[214:215], v225 offset:26624
v_mfma_f32_16x16x32_bf16 v[108:111], a[248:251], a[164:167], v[108:111]
ds_read_b64_tr_b16 a[216:217], v222 offset:26624
v_mfma_f32_16x16x32_bf16 v[112:115], a[248:251], a[188:191], v[112:115]
ds_read_b64_tr_b16 a[218:219], v226 offset:26624
v_mfma_f32_16x16x32_bf16 v[140:143], a[248:251], v[200:203], v[140:143]
ds_read_b64_tr_b16 a[220:221], v223 offset:26624
v_mfma_f32_16x16x32_bf16 v[116:119], a[252:255], a[164:167], v[116:119]
ds_read_b64_tr_b16 a[222:223], v227 offset:26624
v_mfma_f32_16x16x32_bf16 v[120:123], a[252:255], a[188:191], v[120:123]
ds_read_b64_tr_b16 a[224:225], v220 offset:28672
s_cmp_lg_u32 s83, 0
s_cbranch_scc1 K1_l19_t
K1_l19:
v_mfma_f32_16x16x32_bf16 v[0:3], v[12:15], v[152:155], v[0:3]
ds_read_b64_tr_b16 a[226:227], v224 offset:28672
s_waitcnt lgkmcnt(7)
v_max3_f32 v165, v108, v109, v110
v_mfma_f32_16x16x32_bf16 v[4:7], v[12:15], v[156:159], v[4:7]
ds_read_b64_tr_b16 a[228:229], v221 offset:28672
v_max3_f32 v165, v165, v111, v112
v_mfma_f32_16x16x32_bf16 a[0:3], a[192:195], v[152:155], a[0:3]
ds_read_b64_tr_b16 a[230:231], v225 offset:28672
v_max3_f32 v165, v165, v113, v114
v_mfma_f32_16x16x32_bf16 a[4:7], a[192:195], v[156:159], a[4:7]
ds_read_b64_tr_b16 a[232:233], v222 offset:28672
v_max3_f32 v165, v165, v115, v116
v_mfma_f32_16x16x32_bf16 a[8:11], a[196:199], v[152:155], a[8:11]
ds_read_b64_tr_b16 a[234:235], v226 offset:28672
v_max3_f32 v165, v165, v117, v118
v_mfma_f32_16x16x32_bf16 a[12:15], a[196:199], v[156:159], a[12:15]
ds_read_b64_tr_b16 a[236:237], v223 offset:28672
v_max3_f32 v165, v165, v119, v120
v_mfma_f32_16x16x32_bf16 a[16:19], a[200:203], v[152:155], a[16:19]
ds_read_b64_tr_b16 a[238:239], v227 offset:28672
v_max3_f32 v165, v165, v121, v122
v_mfma_f32_16x16x32_bf16 a[20:23], a[200:203], v[156:159], a[20:23]
ds_read_b128 a[240:243], v206 offset:49152
v_max3_f32 v165, v165, v123, v140
v_mfma_f32_16x16x32_bf16 a[24:27], a[204:207], v[152:155], a[24:27]
ds_read_b128 a[244:247], v207 offset:49152
s_waitcnt lgkmcnt(7)
v_max3_f32 v165, v165, v141, v142
v_mfma_f32_16x16x32_bf16 a[28:31], a[204:207], v[156:159], a[28:31]
ds_read_b128 a[248:251], v210 offset:49152
v_max_f32 v165, v165, v143
v_mfma_f32_16x16x32_bf16 a[32:35], a[208:211], v[152:155], a[32:35]
ds_read_b128 a[252:255], v211 offset:49152
v_cmp_lt_f32 vcc, s69, v165
s_cbranch_vccnz K1_l20_t
K1_l20:
v_mfma_f32_16x16x32_bf16 a[36:39], a[208:211], v[156:159], a[36:39]
v_exp_f32 v108, v108
v_mfma_f32_16x16x32_bf16 a[40:43], a[212:215], v[152:155], a[40:43]
v_exp_f32 v109, v109
v_mfma_f32_16x16x32_bf16 a[44:47], a[212:215], v[156:159], a[44:47]
v_exp_f32 v110, v110
v_mfma_f32_16x16x32_bf16 a[48:51], a[216:219], v[152:155], a[48:51]
v_exp_f32 v111, v111
v_mfma_f32_16x16x32_bf16 a[52:55], a[216:219], v[156:159], a[52:55]
ds_read_b128 a[192:195], v206 offset:51200
v_cvt_pk_bf16_f32 v144, v108, v109
v_mfma_f32_16x16x32_bf16 a[56:59], a[220:223], v[152:155], a[56:59]
ds_read_b128 a[196:199], v207 offset:51200
v_cvt_pk_bf16_f32 v145, v110, v111
v_mfma_f32_16x16x32_bf16 a[60:63], a[220:223], v[156:159], a[60:63]
s_waitcnt lgkmcnt(3)
v_exp_f32 v116, v116
v_mfma_f32_16x16x32_bf16 a[64:67], a[224:227], v[152:155], a[64:67]
v_exp_f32 v117, v117
v_mfma_f32_16x16x32_bf16 a[68:71], a[224:227], v[156:159], a[68:71]
v_exp_f32 v118, v118
v_mfma_f32_16x16x32_bf16 a[72:75], a[228:231], v[152:155], a[72:75]
v_exp_f32 v119, v119
v_mfma_f32_16x16x32_bf16 a[76:79], a[228:231], v[156:159], a[76:79]
ds_read_b128 a[200:203], v210 offset:51200
v_cvt_pk_bf16_f32 v146, v116, v117
v_mfma_f32_16x16x32_bf16 a[80:83], a[232:235], v[152:155], a[80:83]
ds_read_b128 a[204:207], v211 offset:51200
v_cvt_pk_bf16_f32 v147, v118, v119
v_mfma_f32_16x16x32_bf16 a[84:87], a[232:235], v[156:159], a[84:87]
v_exp_f32 v112, v112
v_mfma_f32_16x16x32_bf16 a[88:91], a[236:239], v[152:155], a[88:91]
ds_read_b128 a[208:211], v206 offset:53248
v_mfma_f32_16x16x32_bf16 a[92:95], a[236:239], v[156:159], a[92:95]
v_exp_f32 v113, v113
v_mfma_f32_16x16x32_bf16 v[124:127], a[240:243], a[144:147], v[168:171]
v_exp_f32 v114, v114
v_mfma_f32_16x16x32_bf16 v[128:131], a[240:243], a[168:171], v[172:175]
ds_read_b128 a[212:215], v207 offset:53248
v_mfma_f32_16x16x32_bf16 v[132:135], a[244:247], a[144:147], v[168:171]
v_exp_f32 v115, v115
v_mfma_f32_16x16x32_bf16 v[136:139], a[244:247], a[168:171], v[172:175]
s_waitcnt lgkmcnt(3)
v_cvt_pk_bf16_f32 v148, v112, v113
v_cvt_pk_bf16_f32 v149, v114, v115
v_mfma_f32_16x16x32_bf16 v[124:127], a[248:251], a[148:151], v[124:127]
ds_read_b128 a[216:219], v210 offset:53248
v_mfma_f32_16x16x32_bf16 v[128:131], a[248:251], a[172:175], v[128:131]
ds_read_b128 a[220:223], v211 offset:53248
v_mfma_f32_16x16x32_bf16 v[132:135], a[252:255], a[148:151], v[132:135]
v_exp_f32 v120, v120
v_mfma_f32_16x16x32_bf16 v[136:139], a[252:255], a[172:175], v[136:139]
v_exp_f32 v121, v121
v_mfma_f32_16x16x32_bf16 v[124:127], a[192:195], a[152:155], v[124:127]
v_exp_f32 v122, v122
v_mfma_f32_16x16x32_bf16 v[128:131], a[192:195], a[176:179], v[128:131]
v_exp_f32 v123, v123
v_mfma_f32_16x16x32_bf16 v[132:135], a[196:199], a[152:155], v[132:135]
s_waitcnt vmcnt(12) lgkmcnt(0)
s_barrier
v_mfma_f32_16x16x32_bf16 v[136:139], a[196:199], a[176:179], v[136:139]
ds_read_b64_tr_b16 a[224:225], v212 offset:49152
v_cvt_pk_bf16_f32 v150, v120, v121
v_mfma_f32_16x16x32_bf16 v[124:127], a[200:203], a[156:159], v[124:127]
ds_read_b64_tr_b16 a[226:227], v216 offset:49152
v_cvt_pk_bf16_f32 v151, v122, v123
v_mfma_f32_16x16x32_bf16 v[128:131], a[200:203], a[180:183], v[128:131]
ds_read_b64_tr_b16 a[228:229], v213 offset:49152
v_mfma_f32_16x16x32_bf16 v[132:135], a[204:207], a[156:159], v[132:135]
ds_read_b64_tr_b16 a[230:231], v217 offset:49152
s_add_u32 m0, s59, 24576
v_mfma_f32_16x16x32_bf16 v[136:139], a[204:207], a[180:183], v[136:139]
s_add_u32 s67, s63, 172032
v_exp_f32 v140, v140
v_mfma_f32_16x16x32_bf16 v[124:127], a[208:211], a[160:163], v[124:127]
buffer_load_dwordx4 v234, s[44:47], s67 offen nt lds
v_mfma_f32_16x16x32_bf16 v[128:131], a[208:211], a[184:187], v[128:131]
v_exp_f32 v141, v141
v_mfma_f32_16x16x32_bf16 v[132:135], a[212:215], a[160:163], v[132:135]
ds_read_b64_tr_b16 a[232:233], v214 offset:49152
ds_read_b64_tr_b16 a[234:235], v218 offset:49152
v_mfma_f32_16x16x32_bf16 v[136:139], a[212:215], a[184:187], v[136:139]
v_exp_f32 v142, v142
v_mfma_f32_16x16x32_bf16 v[124:127], a[216:219], a[164:167], v[124:127]
v_exp_f32 v143, v143
v_mfma_f32_16x16x32_bf16 v[128:131], a[216:219], a[188:191], v[128:131]
ds_read_b64_tr_b16 a[236:237], v215 offset:49152
v_cvt_pk_bf16_f32 v160, v140, v141
v_mfma_f32_16x16x32_bf16 v[132:135], a[220:223], a[164:167], v[132:135]
ds_read_b64_tr_b16 a[238:239], v219 offset:49152
v_cvt_pk_bf16_f32 v161, v142, v143
v_mfma_f32_16x16x32_bf16 v[136:139], a[220:223], a[188:191], v[136:139]
buffer_load_dwordx4 v235, s[44:47], s67 offen offset:1024 nt lds
s_cmp_lg_u32 s83, 0
s_cbranch_scc1 K1_l21_t
K1_l21:
v_mfma_f32_16x16x32_bf16 v[0:3], v[12:15], v[144:147], v[0:3]
s_waitcnt lgkmcnt(4)
v_max3_f32 v165, v124, v125, v126
v_max3_f32 v165, v165, v127, v128
v_mfma_f32_16x16x32_bf16 v[4:7], v[12:15], v[148:151], v[4:7]
ds_read_b64_tr_b16 a[240:241], v212 offset:51200
v_max3_f32 v165, v165, v129, v130
v_mfma_f32_16x16x16_bf16 v[8:11], v[12:13], v[160:161], v[8:11]
ds_read_b64_tr_b16 a[242:243], v216 offset:51200
v_max3_f32 v165, v165, v131, v132
v_mfma_f32_16x16x32_bf16 a[0:3], a[224:227], v[144:147], a[0:3]
ds_read_b64_tr_b16 a[244:245], v213 offset:51200
v_max3_f32 v165, v165, v133, v134
v_mfma_f32_16x16x32_bf16 a[4:7], a[224:227], v[148:151], a[4:7]
ds_read_b64_tr_b16 a[246:247], v217 offset:51200
v_max3_f32 v165, v165, v135, v136
v_mfma_f32_16x16x16_bf16 a[96:99], a[224:225], v[160:161], a[96:99]
s_add_u32 m0, s59, 26624
v_max3_f32 v165, v165, v137, v138
v_max_f32 v165, v165, v139
v_mfma_f32_16x16x32_bf16 a[8:11], a[228:231], v[144:147], a[8:11]
s_waitcnt lgkmcnt(4)
v_cmp_lt_f32 vcc, s69, v165
s_cbranch_vccnz K1_l22_t
K1_l22:
v_mfma_f32_16x16x32_bf16 a[12:15], a[228:231], v[148:151], a[12:15]
s_add_u32 s67, s63, 172160
v_exp_f32 v124, v124
v_mfma_f32_16x16x16_bf16 a[100:103], a[228:229], v[160:161], a[100:103]
buffer_load_dwordx4 v234, s[44:47], s67 offen nt lds
v_mfma_f32_16x16x32_bf16 a[16:19], a[232:235], v[144:147], a[16:19]
ds_read_b64_tr_b16 a[248:249], v214 offset:51200
ds_read_b64_tr_b16 a[250:251], v218 offset:51200
v_mfma_f32_16x16x32_bf16 a[20:23], a[232:235], v[148:151], a[20:23]
ds_read_b64_tr_b16 a[252:253], v215 offset:51200
ds_read_b64_tr_b16 a[254:255], v219 offset:51200
v_mfma_f32_16x16x16_bf16 a[104:107], a[232:233], v[160:161], a[104:107]
buffer_load_dwordx4 v235, s[44:47], s67 offen offset:1024 nt lds
v_mfma_f32_16x16x32_bf16 a[24:27], a[236:239], v[144:147], a[24:27]
s_waitcnt lgkmcnt(4)
v_exp_f32 v125, v125
v_mfma_f32_16x16x32_bf16 a[28:31], a[236:239], v[148:151], a[28:31]
ds_read_b64_tr_b16 a[192:193], v212 offset:53248
ds_read_b64_tr_b16 a[194:195], v216 offset:53248
v_mfma_f32_16x16x16_bf16 a[108:111], a[236:237], v[160:161], a[108:111]
ds_read_b64_tr_b16 a[196:197], v213 offset:53248
ds_read_b64_tr_b16 a[198:199], v217 offset:53248
v_mfma_f32_16x16x32_bf16 a[32:35], a[240:243], v[144:147], a[32:35]
s_add_u32 m0, s59, 28672
v_exp_f32 v126, v126
v_mfma_f32_16x16x32_bf16 a[36:39], a[240:243], v[148:151], a[36:39]
s_add_u32 s67, s63, 172288
v_exp_f32 v127, v127
v_mfma_f32_16x16x16_bf16 a[112:115], a[240:241], v[160:161], a[112:115]
buffer_load_dwordx4 v234, s[44:47], s67 offen nt lds
v_mfma_f32_16x16x32_bf16 a[40:43], a[244:247], v[144:147], a[40:43]
s_waitcnt lgkmcnt(4)
v_cvt_pk_bf16_f32 v152, v124, v125
v_cvt_pk_bf16_f32 v153, v126, v127
v_mfma_f32_16x16x32_bf16 a[44:47], a[244:247], v[148:151], a[44:47]
ds_read_b64_tr_b16 a[200:201], v214 offset:53248
ds_read_b64_tr_b16 a[202:203], v218 offset:53248
v_mfma_f32_16x16x16_bf16 a[116:119], a[244:245], v[160:161], a[116:119]
ds_read_b64_tr_b16 a[204:205], v215 offset:53248
ds_read_b64_tr_b16 a[206:207], v219 offset:53248
v_mfma_f32_16x16x32_bf16 a[48:51], a[248:251], v[144:147], a[48:51]
buffer_load_dwordx4 v235, s[44:47], s67 offen offset:1024 nt lds
v_mfma_f32_16x16x32_bf16 a[52:55], a[248:251], v[148:151], a[52:55]
ds_read_b128 a[208:211], v204 offset:0
v_mfma_f32_16x16x16_bf16 a[120:123], a[248:249], v[160:161], a[120:123]
v_exp_f32 v132, v132
v_mfma_f32_16x16x32_bf16 a[56:59], a[252:255], v[144:147], a[56:59]
s_waitcnt lgkmcnt(5)
v_exp_f32 v133, v133
v_mfma_f32_16x16x32_bf16 a[60:63], a[252:255], v[148:151], a[60:63]
ds_read_b128 a[212:215], v205 offset:0
v_mfma_f32_16x16x16_bf16 a[124:127], a[252:253], v[160:161], a[124:127]
s_cmp_lt_u32 s19, 3
s_cselect_b32 s90, 0, 737280
s_add_u32 s8, s8, s90
s_cmp_lt_u32 s66, 3
s_cselect_b32 s6, s10, 0
s_cmp_lt_u32 s19, 3
s_cselect_b32 s6, 0, s6
v_mfma_f32_16x16x32_bf16 a[64:67], a[192:195], v[144:147], a[64:67]
s_mul_i32 s90, s9, 0
s_add_u32 s90, s90, s8
buffer_load_dword v163, v162, s[4:7], s90 offen
v_mfma_f32_16x16x32_bf16 a[68:71], a[192:195], v[148:151], a[68:71]
s_mov_b32 s60, s65
v_exp_f32 v134, v134
v_mfma_f32_16x16x16_bf16 a[128:131], a[192:193], v[160:161], a[128:131]
s_mov_b32 s50, s93
v_exp_f32 v135, v135
v_mfma_f32_16x16x32_bf16 a[72:75], a[196:199], v[144:147], a[72:75]
s_waitcnt lgkmcnt(1)
v_cvt_pk_bf16_f32 v154, v132, v133
v_cvt_pk_bf16_f32 v155, v134, v135
v_mfma_f32_16x16x32_bf16 a[76:79], a[196:199], v[148:151], a[76:79]
s_add_u32 m0, s56, 0
v_exp_f32 v128, v128
v_mfma_f32_16x16x16_bf16 a[132:135], a[196:197], v[160:161], a[132:135]
buffer_load_dwordx4 v236, s[48:51], s60 offen lds
v_mfma_f32_16x16x32_bf16 a[80:83], a[200:203], v[144:147], a[80:83]
ds_read_b128 a[216:219], v208 offset:0
ds_read_b128 a[220:223], v209 offset:0
v_mfma_f32_16x16x32_bf16 a[84:87], a[200:203], v[148:151], a[84:87]
ds_read_b128 a[224:227], v204 offset:2048
v_mfma_f32_16x16x16_bf16 a[136:139], a[200:201], v[160:161], a[136:139]
buffer_load_dwordx4 v237, s[48:51], s60 offen offset:1024 lds
v_mfma_f32_16x16x32_bf16 a[88:91], a[204:207], v[144:147], a[88:91]
ds_read_b128 a[228:231], v205 offset:2048
v_mfma_f32_16x16x32_bf16 a[92:95], a[204:207], v[148:151], a[92:95]
ds_read_b128 a[232:235], v208 offset:2048
v_mfma_f32_16x16x16_bf16 a[140:143], a[204:205], v[160:161], a[140:143]
buffer_load_dwordx4 v238, s[48:51], s60 offen offset:2048 lds
v_mfma_f32_16x16x32_bf16 v[108:111], a[208:211], a[144:147], v[168:171]
s_waitcnt lgkmcnt(5)
v_exp_f32 v129, v129
v_mfma_f32_16x16x32_bf16 v[112:115], a[208:211], a[168:171], v[172:175]
ds_read_b128 a[236:239], v209 offset:2048
v_mfma_f32_16x16x32_bf16 v[140:143], a[208:211], v[180:183], v[176:179]
s_waitcnt lgkmcnt(4)
v_exp_f32 v130, v130
v_mfma_f32_16x16x32_bf16 v[116:119], a[212:215], a[144:147], v[168:171]
ds_read_b128 a[240:243], v204 offset:4096
s_add_u32 s60, s60, 393216
v_mfma_f32_16x16x32_bf16 v[120:123], a[212:215], a[168:171], v[172:175]
s_add_u32 m0, s56, 12288
v_exp_f32 v131, v131
v_mfma_f32_16x16x32_bf16 v[108:111], a[216:219], a[148:151], v[108:111]
buffer_load_dwordx4 v236, s[48:51], s60 offen lds
v_mfma_f32_16x16x32_bf16 v[112:115], a[216:219], a[172:175], v[112:115]
ds_read_b128 a[244:247], v205 offset:4096
v_cvt_pk_bf16_f32 v156, v128, v129
v_mfma_f32_16x16x32_bf16 v[140:143], a[216:219], v[184:187], v[140:143]
s_waitcnt lgkmcnt(3)
v_cvt_pk_bf16_f32 v157, v130, v131
v_mfma_f32_16x16x32_bf16 v[116:119], a[220:223], a[148:151], v[116:119]
ds_read_b128 a[248:251], v208 offset:4096
v_mfma_f32_16x16x32_bf16 v[120:123], a[220:223], a[172:175], v[120:123]
ds_read_b128 a[252:255], v209 offset:4096
v_mfma_f32_16x16x32_bf16 v[108:111], a[224:227], a[152:155], v[108:111]
buffer_load_dwordx4 v237, s[48:51], s60 offen offset:1024 lds
v_mfma_f32_16x16x32_bf16 v[112:115], a[224:227], a[176:179], v[112:115]
v_exp_f32 v136, v136
v_mfma_f32_16x16x32_bf16 v[140:143], a[224:227], v[188:191], v[140:143]
ds_read_b64_tr_b16 a[192:193], v220 offset:49152
v_mfma_f32_16x16x32_bf16 v[116:119], a[228:231], a[152:155], v[116:119]
ds_read_b64_tr_b16 a[194:195], v224 offset:49152
v_mfma_f32_16x16x32_bf16 v[120:123], a[228:231], a[176:179], v[120:123]
v_exp_f32 v137, v137
v_mfma_f32_16x16x32_bf16 v[108:111], a[232:235], a[156:159], v[108:111]
s_waitcnt lgkmcnt(4)
v_exp_f32 v138, v138
v_mfma_f32_16x16x32_bf16 v[112:115], a[232:235], a[180:183], v[112:115]
ds_read_b64_tr_b16 a[196:197], v221 offset:49152
v_mfma_f32_16x16x32_bf16 v[140:143], a[232:235], v[192:195], v[140:143]
ds_read_b64_tr_b16 a[198:199], v225 offset:49152
v_mfma_f32_16x16x32_bf16 v[116:119], a[236:239], a[156:159], v[116:119]
buffer_load_dwordx4 v238, s[48:51], s60 offen offset:2048 lds
v_mfma_f32_16x16x32_bf16 v[120:123], a[236:239], a[180:183], v[120:123]
ds_read_b64_tr_b16 a[200:201], v222 offset:49152
v_mfma_f32_16x16x32_bf16 v[108:111], a[240:243], a[160:163], v[108:111]
ds_read_b64_tr_b16 a[202:203], v226 offset:49152
v_mfma_f32_16x16x32_bf16 v[112:115], a[240:243], a[184:187], v[112:115]
ds_read_b64_tr_b16 a[204:205], v223 offset:49152
v_mfma_f32_16x16x32_bf16 v[140:143], a[240:243], v[196:199], v[140:143]
s_waitcnt lgkmcnt(5)
v_exp_f32 v139, v139
v_mfma_f32_16x16x32_bf16 v[116:119], a[244:247], a[160:163], v[116:119]
ds_read_b64_tr_b16 a[206:207], v227 offset:49152
v_cvt_pk_bf16_f32 v158, v136, v137
v_mfma_f32_16x16x32_bf16 v[120:123], a[244:247], a[184:187], v[120:123]
ds_read_b64_tr_b16 a[208:209], v220 offset:51200
v_cvt_pk_bf16_f32 v159, v138, v139
v_mfma_f32_16x16x32_bf16 v[108:111], a[248:251], a[164:167], v[108:111]
ds_read_b64_tr_b16 a[210:211], v224 offset:51200
s_add_u32 s60, s60, 393216
v_mfma_f32_16x16x32_bf16 v[112:115], a[248:251], a[188:191], v[112:115]
ds_read_b64_tr_b16 a[212:213], v221 offset:51200
ds_read_b64_tr_b16 a[214:215], v225 offset:51200
v_mfma_f32_16x16x32_bf16 v[140:143], a[248:251], v[200:203], v[140:143]
ds_read_b64_tr_b16 a[216:217], v222 offset:51200
v_mfma_f32_16x16x32_bf16 v[116:119], a[252:255], a[164:167], v[116:119]
ds_read_b64_tr_b16 a[218:219], v226 offset:51200
v_mfma_f32_16x16x32_bf16 v[120:123], a[252:255], a[188:191], v[120:123]
ds_read_b64_tr_b16 a[220:221], v223 offset:51200
s_cmp_lg_u32 s83, 0
s_cbranch_scc1 K1_l23_t
K1_l23:
v_mfma_f32_16x16x32_bf16 v[0:3], v[12:15], v[152:155], v[0:3]
ds_read_b64_tr_b16 a[222:223], v227 offset:51200
v_max3_f32 v165, v108, v109, v110
v_mfma_f32_16x16x32_bf16 v[4:7], v[12:15], v[156:159], v[4:7]
s_waitcnt lgkmcnt(8)
v_max3_f32 v165, v165, v111, v112
v_max3_f32 v165, v165, v113, v114
v_mfma_f32_16x16x32_bf16 a[0:3], a[192:195], v[152:155], a[0:3]
ds_read_b64_tr_b16 a[224:225], v220 offset:53248
v_max3_f32 v165, v165, v115, v116
v_mfma_f32_16x16x32_bf16 a[4:7], a[192:195], v[156:159], a[4:7]
ds_read_b64_tr_b16 a[226:227], v224 offset:53248
v_max3_f32 v165, v165, v117, v118
v_mfma_f32_16x16x32_bf16 a[8:11], a[196:199], v[152:155], a[8:11]
ds_read_b64_tr_b16 a[228:229], v221 offset:53248
v_max3_f32 v165, v165, v119, v120
v_mfma_f32_16x16x32_bf16 a[12:15], a[196:199], v[156:159], a[12:15]
ds_read_b64_tr_b16 a[230:231], v225 offset:53248
v_max3_f32 v165, v165, v121, v122
v_mfma_f32_16x16x32_bf16 a[16:19], a[200:203], v[152:155], a[16:19]
ds_read_b64_tr_b16 a[232:233], v222 offset:53248
v_max3_f32 v165, v165, v123, v140
v_mfma_f32_16x16x32_bf16 a[20:23], a[200:203], v[156:159], a[20:23]
s_waitcnt lgkmcnt(6)
v_max3_f32 v165, v165, v141, v142
v_max_f32 v165, v165, v143
v_mfma_f32_16x16x32_bf16 a[24:27], a[204:207], v[152:155], a[24:27]
ds_read_b64_tr_b16 a[234:235], v226 offset:53248
v_cmp_lt_f32 vcc, s69, v165
s_cbranch_vccnz K1_l24_t
K1_l24:
v_mfma_f32_16x16x32_bf16 a[28:31], a[204:207], v[156:159], a[28:31]
ds_read_b64_tr_b16 a[236:237], v223 offset:53248
ds_read_b64_tr_b16 a[238:239], v227 offset:53248
v_mfma_f32_16x16x32_bf16 a[32:35], a[208:211], v[152:155], a[32:35]
v_exp_f32 v108, v108
v_mfma_f32_16x16x32_bf16 a[36:39], a[208:211], v[156:159], a[36:39]
ds_read_b128 a[240:243], v206 offset:0
v_mfma_f32_16x16x32_bf16 a[40:43], a[212:215], v[152:155], a[40:43]
v_exp_f32 v109, v109
v_mfma_f32_16x16x32_bf16 a[44:47], a[212:215], v[156:159], a[44:47]
s_waitcnt lgkmcnt(5)
v_exp_f32 v110, v110
v_mfma_f32_16x16x32_bf16 a[48:51], a[216:219], v[152:155], a[48:51]
ds_read_b128 a[244:247], v207 offset:0
ds_read_b128 a[248:251], v210 offset:0
v_mfma_f32_16x16x32_bf16 a[52:55], a[216:219], v[156:159], a[52:55]
v_exp_f32 v111, v111
v_mfma_f32_16x16x32_bf16 a[56:59], a[220:223], v[152:155], a[56:59]
ds_read_b128 a[252:255], v211 offset:0
v_cvt_pk_bf16_f32 v144, v108, v109
v_mfma_f32_16x16x32_bf16 a[60:63], a[220:223], v[156:159], a[60:63]
ds_read_b128 a[192:195], v206 offset:2048
v_cvt_pk_bf16_f32 v145, v110, v111
v_mfma_f32_16x16x32_bf16 a[64:67], a[224:227], v[152:155], a[64:67]
v_exp_f32 v116, v116
v_mfma_f32_16x16x32_bf16 a[68:71], a[224:227], v[156:159], a[68:71]
s_waitcnt lgkmcnt(4)
v_exp_f32 v117, v117
v_mfma_f32_16x16x32_bf16 a[72:75], a[228:231], v[152:155], a[72:75]
v_exp_f32 v118, v118
v_mfma_f32_16x16x32_bf16 a[76:79], a[228:231], v[156:159], a[76:79]
ds_read_b128 a[196:199], v207 offset:2048
v_mfma_f32_16x16x32_bf16 a[80:83], a[232:235], v[152:155], a[80:83]
v_exp_f32 v119, v119
v_mfma_f32_16x16x32_bf16 a[84:87], a[232:235], v[156:159], a[84:87]
ds_read_b128 a[200:203], v210 offset:2048
v_cvt_pk_bf16_f32 v146, v116, v117
v_mfma_f32_16x16x32_bf16 a[88:91], a[236:239], v[152:155], a[88:91]
ds_read_b128 a[204:207], v211 offset:2048
v_cvt_pk_bf16_f32 v147, v118, v119
v_mfma_f32_16x16x32_bf16 a[92:95], a[236:239], v[156:159], a[92:95]
s_waitcnt lgkmcnt(3)
v_exp_f32 v112, v112
v_mfma_f32_16x16x32_bf16 v[124:127], a[240:243], a[144:147], v[168:171]
ds_read_b128 a[208:211], v206 offset:4096
v_mfma_f32_16x16x32_bf16 v[128:131], a[240:243], a[168:171], v[172:175]
v_exp_f32 v113, v113
v_mfma_f32_16x16x32_bf16 v[132:135], a[244:247], a[144:147], v[168:171]
v_exp_f32 v114, v114
v_mfma_f32_16x16x32_bf16 v[136:139], a[244:247], a[168:171], v[172:175]
ds_read_b128 a[212:215], v207 offset:4096
v_mfma_f32_16x16x32_bf16 v[124:127], a[248:251], a[148:151], v[124:127]
v_exp_f32 v115, v115
v_mfma_f32_16x16x32_bf16 v[128:131], a[248:251], a[172:175], v[128:131]
ds_read_b128 a[216:219], v210 offset:4096
v_cvt_pk_bf16_f32 v148, v112, v113
v_mfma_f32_16x16x32_bf16 v[132:135], a[252:255], a[148:151], v[132:135]
ds_read_b128 a[220:223], v211 offset:4096
v_cvt_pk_bf16_f32 v149, v114, v115
v_mfma_f32_16x16x32_bf16 v[136:139], a[252:255], a[172:175], v[136:139]
s_waitcnt lgkmcnt(4)
v_exp_f32 v120, v120
v_mfma_f32_16x16x32_bf16 v[124:127], a[192:195], a[152:155], v[124:127]
v_exp_f32 v121, v121
v_mfma_f32_16x16x32_bf16 v[128:131], a[192:195], a[176:179], v[128:131]
v_exp_f32 v122, v122
v_mfma_f32_16x16x32_bf16 v[132:135], a[196:199], a[152:155], v[132:135]
v_exp_f32 v123, v123
v_mfma_f32_16x16x32_bf16 v[136:139], a[196:199], a[176:179], v[136:139]
s_waitcnt vmcnt(13) lgkmcnt(0)
s_barrier
v_mfma_f32_16x16x32_bf16 v[124:127], a[200:203], a[156:159], v[124:127]
ds_read_b64_tr_b16 a[224:225], v212 offset:0
v_cvt_pk_bf16_f32 v150, v120, v121
v_mfma_f32_16x16x32_bf16 v[128:131], a[200:203], a[180:183], v[128:131]
ds_read_b64_tr_b16 a[226:227], v216 offset:0
v_cvt_pk_bf16_f32 v151, v122, v123
v_mfma_f32_16x16x32_bf16 v[132:135], a[204:207], a[156:159], v[132:135]
ds_read_b64_tr_b16 a[228:229], v213 offset:0
ds_read_b64_tr_b16 a[230:231], v217 offset:0
v_mfma_f32_16x16x32_bf16 v[136:139], a[204:207], a[180:183], v[136:139]
s_add_u32 s61, s61, 736000
v_exp_f32 v140, v140
v_mfma_f32_16x16x32_bf16 v[124:127], a[208:211], a[160:163], v[124:127]
s_add_u32 s62, s62, 736000
v_exp_f32 v141, v141
v_mfma_f32_16x16x32_bf16 v[128:131], a[208:211], a[184:187], v[128:131]
s_mov_b32 s38, s32
v_exp_f32 v142, v142
v_mfma_f32_16x16x32_bf16 v[132:135], a[212:215], a[160:163], v[132:135]
s_add_u32 m0, s57, 0
v_exp_f32 v143, v143
v_mfma_f32_16x16x32_bf16 v[136:139], a[212:215], a[184:187], v[136:139]
s_add_u32 s67, s61, 0
v_cvt_pk_bf16_f32 v160, v140, v141
v_cvt_pk_bf16_f32 v161, v142, v143
v_mfma_f32_16x16x32_bf16 v[124:127], a[216:219], a[164:167], v[124:127]
buffer_load_dwordx4 v239, s[36:39], s67 offen lds
v_mfma_f32_16x16x32_bf16 v[128:131], a[216:219], a[188:191], v[128:131]
ds_read_b64_tr_b16 a[232:233], v214 offset:0
ds_read_b64_tr_b16 a[234:235], v218 offset:0
v_mfma_f32_16x16x32_bf16 v[132:135], a[220:223], a[164:167], v[132:135]
ds_read_b64_tr_b16 a[236:237], v215 offset:0
ds_read_b64_tr_b16 a[238:239], v219 offset:0
v_mfma_f32_16x16x32_bf16 v[136:139], a[220:223], a[188:191], v[136:139]
buffer_load_dwordx4 v240, s[36:39], s67 offen offset:1024 lds
s_cmp_lg_u32 s83, 0
s_cbranch_scc1 K1_l25_t
K1_l25:
v_mfma_f32_16x16x32_bf16 v[0:3], v[12:15], v[144:147], v[0:3]
s_waitcnt lgkmcnt(4)
v_max3_f32 v165, v124, v125, v126
v_max3_f32 v165, v165, v127, v128
v_mfma_f32_16x16x32_bf16 v[4:7], v[12:15], v[148:151], v[4:7]
ds_read_b64_tr_b16 a[240:241], v212 offset:2048
v_max3_f32 v165, v165, v129, v130
v_mfma_f32_16x16x16_bf16 v[8:11], v[12:13], v[160:161], v[8:11]
ds_read_b64_tr_b16 a[242:243], v216 offset:2048
v_max3_f32 v165, v165, v131, v132
v_mfma_f32_16x16x32_bf16 a[0:3], a[224:227], v[144:147], a[0:3]
ds_read_b64_tr_b16 a[244:245], v213 offset:2048
v_max3_f32 v165, v165, v133, v134
v_mfma_f32_16x16x32_bf16 a[4:7], a[224:227], v[148:151], a[4:7]
ds_read_b64_tr_b16 a[246:247], v217 offset:2048
v_max3_f32 v165, v165, v135, v136
v_mfma_f32_16x16x16_bf16 a[96:99], a[224:225], v[160:161], a[96:99]
s_add_u32 m0, s57, 2048
v_max3_f32 v165, v165, v137, v138
v_max_f32 v165, v165, v139
v_mfma_f32_16x16x32_bf16 a[8:11], a[228:231], v[144:147], a[8:11]
s_waitcnt lgkmcnt(4)
v_cmp_lt_f32 vcc, s69, v165
s_cbranch_vccnz K1_l26_t
K1_l26:
v_mfma_f32_16x16x32_bf16 a[12:15], a[228:231], v[148:151], a[12:15]
s_add_u32 s67, s61, 20480
v_exp_f32 v124, v124
v_mfma_f32_16x16x16_bf16 a[100:103], a[228:229], v[160:161], a[100:103]
buffer_load_dwordx4 v239, s[36:39], s67 offen lds
v_mfma_f32_16x16x32_bf16 a[16:19], a[232:235], v[144:147], a[16:19]
ds_read_b64_tr_b16 a[248:249], v214 offset:2048
ds_read_b64_tr_b16 a[250:251], v218 offset:2048
v_mfma_f32_16x16x32_bf16 a[20:23], a[232:235], v[148:151], a[20:23]
ds_read_b64_tr_b16 a[252:253], v215 offset:2048
ds_read_b64_tr_b16 a[254:255], v219 offset:2048
v_mfma_f32_16x16x16_bf16 a[104:107], a[232:233], v[160:161], a[104:107]
buffer_load_dwordx4 v240, s[36:39], s67 offen offset:1024 lds
v_mfma_f32_16x16x32_bf16 a[24:27], a[236:239], v[144:147], a[24:27]
s_waitcnt lgkmcnt(4)
v_exp_f32 v125, v125
v_mfma_f32_16x16x32_bf16 a[28:31], a[236:239], v[148:151], a[28:31]
ds_read_b64_tr_b16 a[192:193], v212 offset:4096
ds_read_b64_tr_b16 a[194:195], v216 offset:4096
v_mfma_f32_16x16x16_bf16 a[108:111], a[236:237], v[160:161], a[108:111]
ds_read_b64_tr_b16 a[196:197], v213 offset:4096
ds_read_b64_tr_b16 a[198:199], v217 offset:4096
v_mfma_f32_16x16x32_bf16 a[32:35], a[240:243], v[144:147], a[32:35]
s_and_b32 s67, s72, 0
v_exp_f32 v126, v126
v_mfma_f32_16x16x32_bf16 a[36:39], a[240:243], v[148:151], a[36:39]
s_add_u32 m0, s92, s67
v_exp_f32 v127, v127
v_mfma_f32_16x16x16_bf16 a[112:115], a[240:241], v[160:161], a[112:115]
s_add_u32 s61, s61, 128
v_cvt_pk_bf16_f32 v152, v124, v125
v_cvt_pk_bf16_f32 v153, v126, v127
v_mfma_f32_16x16x32_bf16 a[40:43], a[244:247], v[144:147], a[40:43]
s_waitcnt lgkmcnt(4)
v_exp_f32 v132, v132
v_mfma_f32_16x16x32_bf16 a[44:47], a[244:247], v[148:151], a[44:47]
buffer_load_dwordx4 v245, s[36:39], s62 offen lds
v_mfma_f32_16x16x16_bf16 a[116:119], a[244:245], v[160:161], a[116:119]
ds_read_b64_tr_b16 a[200:201], v214 offset:4096
ds_read_b64_tr_b16 a[202:203], v218 offset:4096
v_mfma_f32_16x16x32_bf16 a[48:51], a[248:251], v[144:147], a[48:51]
ds_read_b64_tr_b16 a[204:205], v215 offset:4096
ds_read_b64_tr_b16 a[206:207], v219 offset:4096
v_mfma_f32_16x16x32_bf16 a[52:55], a[248:251], v[148:151], a[52:55]
s_add_u32 s62, s62, 128
v_exp_f32 v133, v133
v_mfma_f32_16x16x16_bf16 a[120:123], a[248:249], v[160:161], a[120:123]
s_add_u32 m0, s57, 18432
v_exp_f32 v134, v134
v_mfma_f32_16x16x32_bf16 a[56:59], a[252:255], v[144:147], a[56:59]
s_waitcnt lgkmcnt(4)
v_exp_f32 v135, v135
v_mfma_f32_16x16x32_bf16 a[60:63], a[252:255], v[148:151], a[60:63]
s_add_u32 s67, s61, 0
v_cvt_pk_bf16_f32 v154, v132, v133
v_cvt_pk_bf16_f32 v155, v134, v135
v_mfma_f32_16x16x16_bf16 a[124:127], a[252:253], v[160:161], a[124:127]
buffer_load_dwordx4 v239, s[36:39], s67 offen lds
v_mfma_f32_16x16x32_bf16 a[64:67], a[192:195], v[144:147], a[64:67]
ds_read_b128 a[208:211], v204 offset:24576
ds_read_b128 a[212:215], v205 offset:24576
v_mfma_f32_16x16x32_bf16 a[68:71], a[192:195], v[148:151], a[68:71]
buffer_load_dwordx4 v240, s[36:39], s67 offen offset:1024 lds
v_mfma_f32_16x16x16_bf16 a[128:131], a[192:193], v[160:161], a[128:131]
ds_read_b128 a[216:219], v208 offset:24576
v_mfma_f32_16x16x32_bf16 a[72:75], a[196:199], v[144:147], a[72:75]
s_waitcnt lgkmcnt(3)
v_exp_f32 v128, v128
v_mfma_f32_16x16x32_bf16 a[76:79], a[196:199], v[148:151], a[76:79]
ds_read_b128 a[220:223], v209 offset:24576
v_mfma_f32_16x16x16_bf16 a[132:135], a[196:197], v[160:161], a[132:135]
ds_read_b128 a[224:227], v204 offset:26624
s_add_u32 m0, s57, 20480
v_mfma_f32_16x16x32_bf16 a[80:83], a[200:203], v[144:147], a[80:83]
s_add_u32 s67, s61, 20480
v_exp_f32 v129, v129
v_mfma_f32_16x16x32_bf16 a[84:87], a[200:203], v[148:151], a[84:87]
buffer_load_dwordx4 v239, s[36:39], s67 offen lds
v_mfma_f32_16x16x16_bf16 a[136:139], a[200:201], v[160:161], a[136:139]
ds_read_b128 a[228:231], v205 offset:26624
v_mfma_f32_16x16x32_bf16 a[88:91], a[204:207], v[144:147], a[88:91]
s_waitcnt lgkmcnt(4)
v_exp_f32 v130, v130
v_mfma_f32_16x16x32_bf16 a[92:95], a[204:207], v[148:151], a[92:95]
ds_read_b128 a[232:235], v208 offset:26624
v_mfma_f32_16x16x16_bf16 a[140:143], a[204:205], v[160:161], a[140:143]
buffer_load_dwordx4 v240, s[36:39], s67 offen offset:1024 lds
v_mfma_f32_16x16x32_bf16 v[108:111], a[208:211], a[144:147], v[168:171]
ds_read_b128 a[236:239], v209 offset:26624
v_mfma_f32_16x16x32_bf16 v[112:115], a[208:211], a[168:171], v[172:175]
ds_read_b128 a[240:243], v204 offset:28672
s_and_b32 s67, s72, 18432
v_mfma_f32_16x16x32_bf16 v[140:143], a[208:211], v[180:183], v[176:179]
s_waitcnt lgkmcnt(4)
v_exp_f32 v131, v131
v_mfma_f32_16x16x32_bf16 v[116:119], a[212:215], a[144:147], v[168:171]
s_add_u32 m0, s92, s67
v_cvt_pk_bf16_f32 v156, v128, v129
v_cvt_pk_bf16_f32 v157, v130, v131
v_mfma_f32_16x16x32_bf16 v[120:123], a[212:215], a[168:171], v[172:175]
s_add_u32 s61, s61, 128
v_exp_f32 v136, v136
v_mfma_f32_16x16x32_bf16 v[108:111], a[216:219], a[148:151], v[108:111]
buffer_load_dwordx4 v245, s[36:39], s62 offen lds
v_mfma_f32_16x16x32_bf16 v[112:115], a[216:219], a[172:175], v[112:115]
ds_read_b128 a[244:247], v205 offset:28672
v_mfma_f32_16x16x32_bf16 v[140:143], a[216:219], v[184:187], v[140:143]
ds_read_b128 a[248:251], v208 offset:28672
v_mfma_f32_16x16x32_bf16 v[116:119], a[220:223], a[148:151], v[116:119]
ds_read_b128 a[252:255], v209 offset:28672
s_add_u32 s62, s62, 128
v_mfma_f32_16x16x32_bf16 v[120:123], a[220:223], a[172:175], v[120:123]
s_mov_b32 s42, s91
v_exp_f32 v137, v137
v_mfma_f32_16x16x32_bf16 v[108:111], a[224:227], a[152:155], v[108:111]
s_waitcnt lgkmcnt(3)
v_exp_f32 v138, v138
v_mfma_f32_16x16x32_bf16 v[112:115], a[224:227], a[176:179], v[112:115]
s_add_u32 m0, s58, 0
v_exp_f32 v139, v139
v_mfma_f32_16x16x32_bf16 v[140:143], a[224:227], v[188:191], v[140:143]
s_add_u32 s67, s64, 0
v_cvt_pk_bf16_f32 v158, v136, v137
v_cvt_pk_bf16_f32 v159, v138, v139
v_mfma_f32_16x16x32_bf16 v[116:119], a[228:231], a[152:155], v[116:119]
buffer_load_dwordx4 v232, s[40:43], s67 offen nt lds
v_mfma_f32_16x16x32_bf16 v[120:123], a[228:231], a[176:179], v[120:123]
ds_read_b64_tr_b16 a[192:193], v220 offset:0
v_mfma_f32_16x16x32_bf16 v[108:111], a[232:235], a[156:159], v[108:111]
ds_read_b64_tr_b16 a[194:195], v224 offset:0
v_mfma_f32_16x16x32_bf16 v[112:115], a[232:235], a[180:183], v[112:115]
ds_read_b64_tr_b16 a[196:197], v221 offset:0
v_mfma_f32_16x16x32_bf16 v[140:143], a[232:235], v[192:195], v[140:143]
ds_read_b64_tr_b16 a[198:199], v225 offset:0
v_mfma_f32_16x16x32_bf16 v[116:119], a[236:239], a[156:159], v[116:119]
buffer_load_dwordx4 v233, s[40:43], s67 offen offset:1024 nt lds
v_mfma_f32_16x16x32_bf16 v[120:123], a[236:239], a[180:183], v[120:123]
ds_read_b64_tr_b16 a[200:201], v222 offset:0
v_mfma_f32_16x16x32_bf16 v[108:111], a[240:243], a[160:163], v[108:111]
s_waitcnt lgkmcnt(5)
ds_read_b64_tr_b16 a[202:203], v226 offset:0
v_mfma_f32_16x16x32_bf16 v[112:115], a[240:243], a[184:187], v[112:115]
ds_read_b64_tr_b16 a[204:205], v223 offset:0
v_mfma_f32_16x16x32_bf16 v[140:143], a[240:243], v[196:199], v[140:143]
ds_read_b64_tr_b16 a[206:207], v227 offset:0
v_mfma_f32_16x16x32_bf16 v[116:119], a[244:247], a[160:163], v[116:119]
ds_read_b64_tr_b16 a[208:209], v220 offset:2048
v_mfma_f32_16x16x32_bf16 v[120:123], a[244:247], a[184:187], v[120:123]
ds_read_b64_tr_b16 a[210:211], v224 offset:2048
s_add_u32 m0, s58, 2048
v_mfma_f32_16x16x32_bf16 v[108:111], a[248:251], a[164:167], v[108:111]
s_add_u32 s67, s64, 128
v_mfma_f32_16x16x32_bf16 v[112:115], a[248:251], a[188:191], v[112:115]
buffer_load_dwordx4 v232, s[40:43], s67 offen nt lds
v_mfma_f32_16x16x32_bf16 v[140:143], a[248:251], v[200:203], v[140:143]
ds_read_b64_tr_b16 a[212:213], v221 offset:2048
ds_read_b64_tr_b16 a[214:215], v225 offset:2048
v_mfma_f32_16x16x32_bf16 v[116:119], a[252:255], a[164:167], v[116:119]
ds_read_b64_tr_b16 a[216:217], v222 offset:2048
ds_read_b64_tr_b16 a[218:219], v226 offset:2048
v_mfma_f32_16x16x32_bf16 v[120:123], a[252:255], a[188:191], v[120:123]
s_waitcnt lgkmcnt(7)
ds_read_b64_tr_b16 a[220:221], v223 offset:2048
s_cmp_lg_u32 s83, 0
s_cbranch_scc1 K1_l27_t
K1_l27:
v_mfma_f32_16x16x32_bf16 v[0:3], v[12:15], v[152:155], v[0:3]
ds_read_b64_tr_b16 a[222:223], v227 offset:2048
v_max3_f32 v165, v108, v109, v110
v_mfma_f32_16x16x32_bf16 v[4:7], v[12:15], v[156:159], v[4:7]
buffer_load_dwordx4 v233, s[40:43], s67 offen offset:1024 nt lds
v_mfma_f32_16x16x32_bf16 a[0:3], a[192:195], v[152:155], a[0:3]
ds_read_b64_tr_b16 a[224:225], v220 offset:4096
v_max3_f32 v165, v165, v111, v112
v_mfma_f32_16x16x32_bf16 a[4:7], a[192:195], v[156:159], a[4:7]
ds_read_b64_tr_b16 a[226:227], v224 offset:4096
v_max3_f32 v165, v165, v113, v114
v_mfma_f32_16x16x32_bf16 a[8:11], a[196:199], v[152:155], a[8:11]
ds_read_b64_tr_b16 a[228:229], v221 offset:4096
v_max3_f32 v165, v165, v115, v116
v_mfma_f32_16x16x32_bf16 a[12:15], a[196:199], v[156:159], a[12:15]
s_waitcnt lgkmcnt(7)
v_max3_f32 v165, v165, v117, v118
v_max3_f32 v165, v165, v119, v120
v_mfma_f32_16x16x32_bf16 a[16:19], a[200:203], v[152:155], a[16:19]
ds_read_b64_tr_b16 a[230:231], v225 offset:4096
v_max3_f32 v165, v165, v121, v122
v_mfma_f32_16x16x32_bf16 a[20:23], a[200:203], v[156:159], a[20:23]
ds_read_b64_tr_b16 a[232:233], v222 offset:4096
v_max3_f32 v165, v165, v123, v140
v_mfma_f32_16x16x32_bf16 a[24:27], a[204:207], v[152:155], a[24:27]
ds_read_b64_tr_b16 a[234:235], v226 offset:4096
v_max3_f32 v165, v165, v141, v142
v_mfma_f32_16x16x32_bf16 a[28:31], a[204:207], v[156:159], a[28:31]
s_add_u32 m0, s58, 4096
v_max_f32 v165, v165, v143
v_cmp_lt_f32 vcc, s69, v165
s_cbranch_vccnz K1_l28_t
K1_l28:
v_mfma_f32_16x16x32_bf16 a[32:35], a[208:211], v[152:155], a[32:35]
s_add_u32 s67, s64, 256
v_exp_f32 v108, v108
v_mfma_f32_16x16x32_bf16 a[36:39], a[208:211], v[156:159], a[36:39]
s_waitcnt lgkmcnt(4)
v_exp_f32 v109, v109
v_mfma_f32_16x16x32_bf16 a[40:43], a[212:215], v[152:155], a[40:43]
buffer_load_dwordx4 v232, s[40:43], s67 offen nt lds
v_mfma_f32_16x16x32_bf16 a[44:47], a[212:215], v[156:159], a[44:47]
ds_read_b64_tr_b16 a[236:237], v223 offset:4096
ds_read_b64_tr_b16 a[238:239], v227 offset:4096
v_mfma_f32_16x16x32_bf16 a[48:51], a[216:219], v[152:155], a[48:51]
ds_read_b128 a[240:243], v206 offset:24576
ds_read_b128 a[244:247], v207 offset:24576
v_mfma_f32_16x16x32_bf16 a[52:55], a[216:219], v[156:159], a[52:55]
buffer_load_dwordx4 v233, s[40:43], s67 offen offset:1024 nt lds
v_mfma_f32_16x16x32_bf16 a[56:59], a[220:223], v[152:155], a[56:59]
v_exp_f32 v110, v110
v_mfma_f32_16x16x32_bf16 a[60:63], a[220:223], v[156:159], a[60:63]
s_waitcnt lgkmcnt(4)
v_exp_f32 v111, v111
v_mfma_f32_16x16x32_bf16 a[64:67], a[224:227], v[152:155], a[64:67]
ds_read_b128 a[248:251], v210 offset:24576
v_cvt_pk_bf16_f32 v144, v108, v109
v_mfma_f32_16x16x32_bf16 a[68:71], a[224:227], v[156:159], a[68:71]
ds_read_b128 a[252:255], v211 offset:24576
v_cvt_pk_bf16_f32 v145, v110, v111
v_mfma_f32_16x16x32_bf16 a[72:75], a[228:231], v[152:155], a[72:75]
v_exp_f32 v116, v116
v_mfma_f32_16x16x32_bf16 a[76:79], a[228:231], v[156:159], a[76:79]
s_waitcnt lgkmcnt(4)
v_exp_f32 v117, v117
v_mfma_f32_16x16x32_bf16 a[80:83], a[232:235], v[152:155], a[80:83]
ds_read_b128 a[192:195], v206 offset:26624
v_mfma_f32_16x16x32_bf16 a[84:87], a[232:235], v[156:159], a[84:87]
s_waitcnt lgkmcnt(3)
v_exp_f32 v118, v118
v_mfma_f32_16x16x32_bf16 a[88:91], a[236:239], v[152:155], a[88:91]
s_mul_i32 s90, s9, 1
s_add_u32 s90, s90, s8
buffer_load_dword v163, v162, s[4:7], s90 offen
v_mfma_f32_16x16x32_bf16 a[92:95], a[236:239], v[156:159], a[92:95]
ds_read_b128 a[196:199], v207 offset:26624
ds_read_b128 a[200:203], v210 offset:26624
v_mfma_f32_16x16x32_bf16 v[124:127], a[240:243], a[144:147], v[168:171]
v_exp_f32 v119, v119
v_mfma_f32_16x16x32_bf16 v[128:131], a[240:243], a[168:171], v[172:175]
s_waitcnt lgkmcnt(3)
v_cvt_pk_bf16_f32 v146, v116, v117
v_cvt_pk_bf16_f32 v147, v118, v119
v_mfma_f32_16x16x32_bf16 v[132:135], a[244:247], a[144:147], v[168:171]
ds_read_b128 a[204:207], v211 offset:26624
ds_read_b128 a[208:211], v206 offset:28672
v_mfma_f32_16x16x32_bf16 v[136:139], a[244:247], a[168:171], v[172:175]
v_exp_f32 v112, v112
v_mfma_f32_16x16x32_bf16 v[124:127], a[248:251], a[148:151], v[124:127]
v_exp_f32 v113, v113
v_mfma_f32_16x16x32_bf16 v[128:131], a[248:251], a[172:175], v[128:131]
s_waitcnt lgkmcnt(4)
v_exp_f32 v114, v114
v_mfma_f32_16x16x32_bf16 v[132:135], a[252:255], a[148:151], v[132:135]
ds_read_b128 a[212:215], v207 offset:28672
ds_read_b128 a[216:219], v210 offset:28672
v_mfma_f32_16x16x32_bf16 v[136:139], a[252:255], a[172:175], v[136:139]
s_waitcnt lgkmcnt(4)
v_exp_f32 v115, v115
v_mfma_f32_16x16x32_bf16 v[124:127], a[192:195], a[152:155], v[124:127]
ds_read_b128 a[220:223], v211 offset:28672
v_cvt_pk_bf16_f32 v148, v112, v113
v_mfma_f32_16x16x32_bf16 v[128:131], a[192:195], a[176:179], v[128:131]
v_cvt_pk_bf16_f32 v149, v114, v115
v_mfma_f32_16x16x32_bf16 v[132:135], a[196:199], a[152:155], v[132:135]
v_exp_f32 v120, v120
v_mfma_f32_16x16x32_bf16 v[136:139], a[196:199], a[176:179], v[136:139]
s_waitcnt lgkmcnt(3)
v_exp_f32 v121, v121
v_mfma_f32_16x16x32_bf16 v[124:127], a[200:203], a[156:159], v[124:127]
v_exp_f32 v122, v122
v_mfma_f32_16x16x32_bf16 v[128:131], a[200:203], a[180:183], v[128:131]
s_waitcnt vmcnt(24) lgkmcnt(0)
s_barrier
v_mfma_f32_16x16x32_bf16 v[132:135], a[204:207], a[156:159], v[132:135]
ds_read_b64_tr_b16 a[224:225], v212 offset:24576
ds_read_b64_tr_b16 a[226:227], v216 offset:24576
v_mfma_f32_16x16x32_bf16 v[136:139], a[204:207], a[180:183], v[136:139]
s_mov_b32 s46, s91
v_exp_f32 v123, v123
v_mfma_f32_16x16x32_bf16 v[124:127], a[208:211], a[160:163], v[124:127]
ds_read_b64_tr_b16 a[228:229], v213 offset:24576
v_cvt_pk_bf16_f32 v150, v120, v121
v_mfma_f32_16x16x32_bf16 v[128:131], a[208:211], a[184:187], v[128:131]
ds_read_b64_tr_b16 a[230:231], v217 offset:24576
v_cvt_pk_bf16_f32 v151, v122, v123
v_mfma_f32_16x16x32_bf16 v[132:135], a[212:215], a[160:163], v[132:135]
s_add_u32 m0, s59, 0
v_exp_f32 v140, v140
v_mfma_f32_16x16x32_bf16 v[136:139], a[212:215], a[184:187], v[136:139]
s_add_u32 s67, s64, 0
v_exp_f32 v141, v141
v_mfma_f32_16x16x32_bf16 v[124:127], a[216:219], a[164:167], v[124:127]
buffer_load_dwordx4 v234, s[44:47], s67 offen nt lds
v_mfma_f32_16x16x32_bf16 v[128:131], a[216:219], a[188:191], v[128:131]
ds_read_b64_tr_b16 a[232:233], v214 offset:24576
ds_read_b64_tr_b16 a[234:235], v218 offset:24576
v_mfma_f32_16x16x32_bf16 v[132:135], a[220:223], a[164:167], v[132:135]
v_exp_f32 v142, v142
v_mfma_f32_16x16x32_bf16 v[136:139], a[220:223], a[188:191], v[136:139]
v_exp_f32 v143, v143
v_cvt_pk_bf16_f32 v160, v140, v141
v_cvt_pk_bf16_f32 v161, v142, v143
s_cmp_lg_u32 s83, 0
s_cbranch_scc1 K1_l29_t
K1_l29:
v_mfma_f32_16x16x32_bf16 v[0:3], v[12:15], v[144:147], v[0:3]
s_waitcnt lgkmcnt(3)
v_max3_f32 v165, v124, v125, v126
v_max3_f32 v165, v165, v127, v128
v_mfma_f32_16x16x32_bf16 v[4:7], v[12:15], v[148:151], v[4:7]
ds_read_b64_tr_b16 a[236:237], v215 offset:24576
v_max3_f32 v165, v165, v129, v130
v_mfma_f32_16x16x16_bf16 v[8:11], v[12:13], v[160:161], v[8:11]
ds_read_b64_tr_b16 a[238:239], v219 offset:24576
v_max3_f32 v165, v165, v131, v132
v_mfma_f32_16x16x32_bf16 a[0:3], a[224:227], v[144:147], a[0:3]
s_waitcnt lgkmcnt(4)
v_max3_f32 v165, v165, v133, v134
v_max3_f32 v165, v165, v135, v136
v_mfma_f32_16x16x32_bf16 a[4:7], a[224:227], v[148:151], a[4:7]
buffer_load_dwordx4 v235, s[44:47], s67 offen offset:1024 nt lds
v_mfma_f32_16x16x16_bf16 a[96:99], a[224:225], v[160:161], a[96:99]
ds_read_b64_tr_b16 a[240:241], v212 offset:26624
v_max3_f32 v165, v165, v137, v138
v_mfma_f32_16x16x32_bf16 a[8:11], a[228:231], v[144:147], a[8:11]
s_waitcnt lgkmcnt(3)
v_max_f32 v165, v165, v139
v_cmp_lt_f32 vcc, s69, v165
s_cbranch_vccnz K1_l30_t
K1_l30:
v_mfma_f32_16x16x32_bf16 a[12:15], a[228:231], v[148:151], a[12:15]
ds_read_b64_tr_b16 a[242:243], v216 offset:26624
ds_read_b64_tr_b16 a[244:245], v213 offset:26624
v_mfma_f32_16x16x16_bf16 a[100:103], a[228:229], v[160:161], a[100:103]
ds_read_b64_tr_b16 a[246:247], v217 offset:26624
s_add_u32 m0, s59, 2048
v_mfma_f32_16x16x32_bf16 a[16:19], a[232:235], v[144:147], a[16:19]
s_waitcnt lgkmcnt(5)
v_exp_f32 v124, v124
v_mfma_f32_16x16x32_bf16 a[20:23], a[232:235], v[148:151], a[20:23]
s_waitcnt lgkmcnt(4)
v_exp_f32 v125, v125
v_mfma_f32_16x16x16_bf16 a[104:107], a[232:233], v[160:161], a[104:107]
s_add_u32 s67, s64, 128
v_exp_f32 v126, v126
v_mfma_f32_16x16x32_bf16 a[24:27], a[236:239], v[144:147], a[24:27]
buffer_load_dwordx4 v234, s[44:47], s67 offen nt lds
v_mfma_f32_16x16x32_bf16 a[28:31], a[236:239], v[148:151], a[28:31]
s_waitcnt lgkmcnt(3)
v_exp_f32 v127, v127
v_mfma_f32_16x16x16_bf16 a[108:111], a[236:237], v[160:161], a[108:111]
ds_read_b64_tr_b16 a[248:249], v214 offset:26624
v_cvt_pk_bf16_f32 v152, v124, v125
s_waitcnt lgkmcnt(3)
v_mfma_f32_16x16x32_bf16 a[32:35], a[240:243], v[144:147], a[32:35]
s_waitcnt lgkmcnt(2)
v_cvt_pk_bf16_f32 v153, v126, v127
ds_read_b64_tr_b16 a[250:251], v218 offset:26624
v_mfma_f32_16x16x32_bf16 a[36:39], a[240:243], v[148:151], a[36:39]
s_waitcnt lgkmcnt(2)
v_exp_f32 v132, v132
v_mfma_f32_16x16x16_bf16 a[112:115], a[240:241], v[160:161], a[112:115]
ds_read_b64_tr_b16 a[252:253], v215 offset:26624
ds_read_b64_tr_b16 a[254:255], v219 offset:26624
v_mfma_f32_16x16x32_bf16 a[40:43], a[244:247], v[144:147], a[40:43]
buffer_load_dwordx4 v235, s[44:47], s67 offen offset:1024 nt lds
v_mfma_f32_16x16x32_bf16 a[44:47], a[244:247], v[148:151], a[44:47]
ds_read_b64_tr_b16 a[192:193], v212 offset:28672
ds_read_b64_tr_b16 a[194:195], v216 offset:28672
v_mfma_f32_16x16x16_bf16 a[116:119], a[244:245], v[160:161], a[116:119]
ds_read_b64_tr_b16 a[196:197], v213 offset:28672
ds_read_b64_tr_b16 a[198:199], v217 offset:28672
s_waitcnt lgkmcnt(6)
v_mfma_f32_16x16x32_bf16 a[48:51], a[248:251], v[144:147], a[48:51]
s_add_u32 m0, s59, 4096
v_exp_f32 v133, v133
v_mfma_f32_16x16x32_bf16 a[52:55], a[248:251], v[148:151], a[52:55]
s_add_u32 s67, s64, 256
v_exp_f32 v134, v134
v_mfma_f32_16x16x16_bf16 a[120:123], a[248:249], v[160:161], a[120:123]
buffer_load_dwordx4 v234, s[44:47], s67 offen nt lds
s_waitcnt lgkmcnt(4)
v_mfma_f32_16x16x32_bf16 a[56:59], a[252:255], v[144:147], a[56:59]
ds_read_b64_tr_b16 a[200:201], v214 offset:28672
ds_read_b64_tr_b16 a[202:203], v218 offset:28672
v_mfma_f32_16x16x32_bf16 a[60:63], a[252:255], v[148:151], a[60:63]
ds_read_b64_tr_b16 a[204:205], v215 offset:28672
ds_read_b64_tr_b16 a[206:207], v219 offset:28672
v_mfma_f32_16x16x16_bf16 a[124:127], a[252:253], v[160:161], a[124:127]
buffer_load_dwordx4 v235, s[44:47], s67 offen offset:1024 nt lds
s_waitcnt lgkmcnt(6)
v_mfma_f32_16x16x32_bf16 a[64:67], a[192:195], v[144:147], a[64:67]
v_exp_f32 v135, v135
v_mfma_f32_16x16x32_bf16 a[68:71], a[192:195], v[148:151], a[68:71]
s_waitcnt lgkmcnt(4)
v_cvt_pk_bf16_f32 v154, v132, v133
v_cvt_pk_bf16_f32 v155, v134, v135
v_mfma_f32_16x16x16_bf16 a[128:131], a[192:193], v[160:161], a[128:131]
v_exp_f32 v128, v128
v_mfma_f32_16x16x32_bf16 a[72:75], a[196:199], v[144:147], a[72:75]
v_exp_f32 v129, v129
v_mfma_f32_16x16x32_bf16 a[76:79], a[196:199], v[148:151], a[76:79]
ds_read_b64_tr_b16 a[208:209], v220 offset:24576
ds_read_b64_tr_b16 a[210:211], v224 offset:24576
v_mfma_f32_16x16x16_bf16 a[132:135], a[196:197], v[160:161], a[132:135]
s_waitcnt lgkmcnt(4)
v_exp_f32 v130, v130
v_mfma_f32_16x16x32_bf16 a[80:83], a[200:203], v[144:147], a[80:83]
s_waitcnt lgkmcnt(2)
v_exp_f32 v131, v131
v_mfma_f32_16x16x32_bf16 a[84:87], a[200:203], v[148:151], a[84:87]
ds_read_b64_tr_b16 a[212:213], v221 offset:24576
v_cvt_pk_bf16_f32 v156, v128, v129
v_mfma_f32_16x16x16_bf16 a[136:139], a[200:201], v[160:161], a[136:139]
ds_read_b64_tr_b16 a[214:215], v225 offset:24576
v_cvt_pk_bf16_f32 v157, v130, v131
v_mfma_f32_16x16x32_bf16 a[88:91], a[204:207], v[144:147], a[88:91]
s_mul_i32 s90, s9, 2
s_add_u32 s90, s90, s8
buffer_load_dword v163, v162, s[4:7], s90 offen
v_mfma_f32_16x16x32_bf16 a[92:95], a[204:207], v[148:151], a[92:95]
ds_read_b64_tr_b16 a[216:217], v222 offset:24576
ds_read_b64_tr_b16 a[218:219], v226 offset:24576
v_mfma_f32_16x16x16_bf16 a[140:143], a[204:205], v[160:161], a[140:143]
v_exp_f32 v136, v136
v_exp_f32 v137, v137
v_exp_f32 v138, v138
v_exp_f32 v139, v139
v_cvt_pk_bf16_f32 v158, v136, v137
v_cvt_pk_bf16_f32 v159, v138, v139
s_cmp_lg_u32 s83, 0
s_cbranch_scc1 K1_l31_t
K1_l31:
v_mfma_f32_16x16x32_bf16 v[0:3], v[12:15], v[152:155], v[0:3]
s_waitcnt lgkmcnt(4)
ds_read_b64_tr_b16 a[220:221], v223 offset:24576
v_mfma_f32_16x16x32_bf16 v[4:7], v[12:15], v[156:159], v[4:7]
ds_read_b64_tr_b16 a[222:223], v227 offset:24576
ds_read_b64_tr_b16 a[224:225], v220 offset:26624
v_mfma_f32_16x16x32_bf16 a[0:3], a[208:211], v[152:155], a[0:3]
ds_read_b64_tr_b16 a[226:227], v224 offset:26624
ds_read_b64_tr_b16 a[228:229], v221 offset:26624
v_mfma_f32_16x16x32_bf16 a[4:7], a[208:211], v[156:159], a[4:7]
s_waitcnt lgkmcnt(8)
ds_read_b64_tr_b16 a[230:231], v225 offset:26624
s_waitcnt lgkmcnt(8)
v_mfma_f32_16x16x32_bf16 a[8:11], a[212:215], v[152:155], a[8:11]
ds_read_b64_tr_b16 a[232:233], v222 offset:26624
ds_read_b64_tr_b16 a[234:235], v226 offset:26624
v_mfma_f32_16x16x32_bf16 a[12:15], a[212:215], v[156:159], a[12:15]
ds_read_b64_tr_b16 a[236:237], v223 offset:26624
v_accvgpr_read_b32 v132, a96
s_waitcnt lgkmcnt(9)
v_mfma_f32_16x16x32_bf16 a[16:19], a[216:219], v[152:155], a[16:19]
ds_read_b64_tr_b16 a[238:239], v227 offset:26624
v_accvgpr_read_b32 v133, a97
v_mfma_f32_16x16x32_bf16 a[20:23], a[216:219], v[156:159], a[20:23]
ds_read_b64_tr_b16 a[240:241], v220 offset:28672
v_accvgpr_read_b32 v134, a98
s_waitcnt lgkmcnt(9)
v_mfma_f32_16x16x32_bf16 a[24:27], a[220:223], v[152:155], a[24:27]
ds_read_b64_tr_b16 a[242:243], v224 offset:28672
v_accvgpr_read_b32 v135, a99
v_mfma_f32_16x16x32_bf16 a[28:31], a[220:223], v[156:159], a[28:31]
s_waitcnt lgkmcnt(9)
v_cvt_pk_bf16_f32 v108, v132, v133
v_cvt_pk_bf16_f32 v109, v134, v135
s_waitcnt lgkmcnt(8)
v_mfma_f32_16x16x32_bf16 a[32:35], a[224:227], v[152:155], a[32:35]
s_waitcnt lgkmcnt(7)
v_accvgpr_read_b32 v136, a100
v_accvgpr_read_b32 v137, a101
v_mfma_f32_16x16x32_bf16 a[36:39], a[224:227], v[156:159], a[36:39]
s_waitcnt lgkmcnt(6)
v_accvgpr_read_b32 v138, a102
v_accvgpr_read_b32 v139, a103
v_mfma_f32_16x16x32_bf16 a[40:43], a[228:231], v[152:155], a[40:43]
s_waitcnt lgkmcnt(4)
v_cvt_pk_bf16_f32 v110, v136, v137
v_cvt_pk_bf16_f32 v111, v138, v139
v_mfma_f32_16x16x32_bf16 a[44:47], a[228:231], v[156:159], a[44:47]
s_waitcnt lgkmcnt(3)
v_accvgpr_read_b32 v132, a104
v_accvgpr_read_b32 v133, a105
v_mfma_f32_16x16x32_bf16 a[48:51], a[232:235], v[152:155], a[48:51]
s_waitcnt lgkmcnt(2)
v_accvgpr_read_b32 v134, a106
v_accvgpr_read_b32 v135, a107
v_mfma_f32_16x16x32_bf16 a[52:55], a[232:235], v[156:159], a[52:55]
s_waitcnt lgkmcnt(1)
v_cvt_pk_bf16_f32 v112, v132, v133
v_cvt_pk_bf16_f32 v113, v134, v135
v_mfma_f32_16x16x32_bf16 a[56:59], a[236:239], v[152:155], a[56:59]
s_waitcnt lgkmcnt(0)
v_accvgpr_read_b32 v136, a108
v_accvgpr_read_b32 v137, a109
v_mfma_f32_16x16x32_bf16 a[60:63], a[236:239], v[156:159], a[60:63]
ds_read_b64_tr_b16 a[244:245], v221 offset:28672
v_accvgpr_read_b32 v138, a110
v_mfma_f32_16x16x32_bf16 a[64:67], a[240:243], v[152:155], a[64:67]
ds_read_b64_tr_b16 a[246:247], v225 offset:28672
v_accvgpr_read_b32 v139, a111
v_mfma_f32_16x16x32_bf16 a[68:71], a[240:243], v[156:159], a[68:71]
ds_read_b64_tr_b16 a[248:249], v222 offset:28672
v_cvt_pk_bf16_f32 v114, v136, v137
s_waitcnt lgkmcnt(1)
v_mfma_f32_16x16x32_bf16 a[72:75], a[244:247], v[152:155], a[72:75]
ds_read_b64_tr_b16 a[250:251], v226 offset:28672
v_cvt_pk_bf16_f32 v115, v138, v139
v_mfma_f32_16x16x32_bf16 a[76:79], a[244:247], v[156:159], a[76:79]
ds_read_b64_tr_b16 a[252:253], v223 offset:28672
v_accvgpr_read_b32 v132, a112
s_waitcnt lgkmcnt(1)
v_mfma_f32_16x16x32_bf16 a[80:83], a[248:251], v[152:155], a[80:83]
ds_read_b64_tr_b16 a[254:255], v227 offset:28672
v_accvgpr_read_b32 v133, a113
v_mfma_f32_16x16x32_bf16 a[84:87], a[248:251], v[156:159], a[84:87]
v_accvgpr_read_b32 v134, a114
v_accvgpr_read_b32 v135, a115
s_waitcnt lgkmcnt(0)
v_mfma_f32_16x16x32_bf16 a[88:91], a[252:255], v[152:155], a[88:91]
v_cvt_pk_bf16_f32 v116, v132, v133
v_cvt_pk_bf16_f32 v117, v134, v135
v_mfma_f32_16x16x32_bf16 a[92:95], a[252:255], v[156:159], a[92:95]
v_accvgpr_read_b32 v136, a116
v_accvgpr_read_b32 v137, a117
s_waitcnt lgkmcnt(0)
s_barrier
v_accvgpr_read_b32 v138, a118
v_accvgpr_read_b32 v139, a119
v_cvt_pk_bf16_f32 v118, v136, v137
v_cvt_pk_bf16_f32 v119, v138, v139
v_accvgpr_read_b32 v132, a120
v_accvgpr_read_b32 v133, a121
v_accvgpr_read_b32 v134, a122
v_accvgpr_read_b32 v135, a123
v_cvt_pk_bf16_f32 v120, v132, v133
v_cvt_pk_bf16_f32 v121, v134, v135
v_accvgpr_read_b32 v136, a124
v_accvgpr_read_b32 v137, a125
v_accvgpr_read_b32 v138, a126
v_accvgpr_read_b32 v139, a127
v_cvt_pk_bf16_f32 v122, v136, v137
v_cvt_pk_bf16_f32 v123, v138, v139
v_accvgpr_read_b32 v132, a128
v_accvgpr_read_b32 v133, a129
v_accvgpr_read_b32 v134, a130
v_accvgpr_read_b32 v135, a131
v_cvt_pk_bf16_f32 v124, v132, v133
v_cvt_pk_bf16_f32 v125, v134, v135
v_accvgpr_read_b32 v136, a132
v_accvgpr_read_b32 v137, a133
v_accvgpr_read_b32 v138, a134
v_accvgpr_read_b32 v139, a135
v_cvt_pk_bf16_f32 v126, v136, v137
v_cvt_pk_bf16_f32 v127, v138, v139
v_accvgpr_read_b32 v132, a136
v_accvgpr_read_b32 v133, a137
v_accvgpr_read_b32 v134, a138
v_accvgpr_read_b32 v135, a139
v_cvt_pk_bf16_f32 v128, v132, v133
v_cvt_pk_bf16_f32 v129, v134, v135
v_accvgpr_read_b32 v136, a140
v_accvgpr_read_b32 v137, a141
v_accvgpr_read_b32 v138, a142
v_accvgpr_read_b32 v139, a143
v_cvt_pk_bf16_f32 v130, v136, v137
v_cvt_pk_bf16_f32 v131, v138, v139
; boundary
s_nop 7
s_mul_i32 s68, s19, 6144
s_add_u32 s68, s68, 98304
v_add_u32 v248, s68, v241
v_lshrrev_b32 v166, 4, v241
v_and_b32 v166, 15, v166
v_lshlrev_b32 v166, 2, v166
v_add_u32 v166, 162816, v166
s_lshl_b32 s68, s19, 7
v_add_u32 v249, s68, v166
ds_write_b128 v248, v[108:111] offset:0
ds_write_b128 v248, v[112:115] offset:1024
ds_write_b128 v248, v[116:119] offset:2048
ds_write_b128 v248, v[120:123] offset:3072
ds_write_b128 v248, v[124:127] offset:4096
ds_write_b128 v248, v[128:131] offset:5120
v_sub_f32 v165, 0, v176
ds_write_b32 v249, v165 offset:0
ds_write_b32 v249, v8 offset:64
v_rcp_f32 v242, v0
v_rcp_f32 v243, v4
s_lshl_b32 s67, s88, 4
s_add_u32 s67, s67, s87
s_mul_i32 s67, s67, 9216
s_mul_i32 s89, s86, 1769472
s_add_u32 s67, s67, s89
s_lshl_b32 s68, s19, 11
s_add_u32 s84, s67, s68
s_add_u32 s85, s67, 8192
s_mov_b32 s54, 56623104
s_mul_i32 s67, s19, 3
s_add_u32 s67, s67, 0
s_lshr_b32 s68, s67, 1
s_lshl_b32 s68, s68, 10
s_and_b32 s67, s67, 1
s_lshl_b32 s67, s67, 3
s_add_u32 s33, s67, s68
s_mul_i32 s67, s19, 3
s_add_u32 s67, s67, 1
s_lshr_b32 s68, s67, 1
s_lshl_b32 s68, s68, 10
s_and_b32 s67, s67, 1
s_lshl_b32 s67, s67, 3
s_add_u32 s34, s67, s68
s_mul_i32 s67, s19, 3
s_add_u32 s67, s67, 2
s_lshr_b32 s68, s67, 1
s_lshl_b32 s68, s68, 10
s_and_b32 s67, s67, 1
s_lshl_b32 s67, s67, 3
s_add_u32 s35, s67, s68
s_waitcnt lgkmcnt(0)
s_barrier
ds_read_b32 v108, v166 offset:0
ds_read_b32 v112, v166 offset:64
ds_read_b32 v109, v166 offset:128
ds_read_b32 v113, v166 offset:192
ds_read_b32 v110, v166 offset:256
ds_read_b32 v114, v166 offset:320
ds_read_b32 v111, v166 offset:384
ds_read_b32 v115, v166 offset:448
s_add_u32 s67, s33, 98304
v_add_u32 v250, s67, v241
ds_read_b64 v[180:181], v250 offset:0
ds_read_b64 v[186:187], v250 offset:6144
ds_read_b64 v[192:193], v250 offset:12288
ds_read_b64 v[198:199], v250 offset:18432
s_add_u32 s67, s34, 98304
v_add_u32 v251, s67, v241
ds_read_b64 v[182:183], v251 offset:0
ds_read_b64 v[188:189], v251 offset:6144
ds_read_b64 v[194:195], v251 offset:12288
ds_read_b64 v[200:201], v251 offset:18432
s_waitcnt lgkmcnt(14)
s_add_u32 s67, s35, 98304
v_add_u32 v252, s67, v241
ds_read_b64 v[184:185], v252 offset:0
s_waitcnt lgkmcnt(14)
ds_read_b64 v[190:191], v252 offset:6144
s_waitcnt lgkmcnt(14)
ds_read_b64 v[196:197], v252 offset:12288
s_waitcnt lgkmcnt(14)
ds_read_b64 v[202:203], v252 offset:18432
s_waitcnt lgkmcnt(14)
s_mov_b32 s63, s64
s_add_u32 s87, s87, 4
s_add_u32 s66, s66, 1
s_waitcnt vmcnt(19) lgkmcnt(0)
s_barrier
s_cmp_lt_u32 s66, 4
s_cbranch_scc1 K1_unit
v_max_f32 v116, v108, v109
v_max3_f32 v116, v116, v110, v111
v_sub_f32 v118, v108, v116
v_exp_f32 v118, v118
v_sub_f32 v119, v109, v116
v_exp_f32 v119, v119
v_sub_f32 v120, v110, v116
v_exp_f32 v120, v120
v_sub_f32 v121, v111, v116
v_exp_f32 v121, v121
v_mul_f32 v117, v118, v112
v_fmac_f32 v117, v119, v113
v_fmac_f32 v117, v120, v114
v_fmac_f32 v117, v121, v115
v_rcp_f32 v117, v117
s_nop 0
v_mul_f32 v118, v118, v117
v_mul_f32 v119, v119, v117
v_mul_f32 v120, v120, v117
v_mul_f32 v121, v121, v117
v_lshlrev_b32 v122, 16, v180
v_and_b32 v123, 4294901760, v180
v_lshlrev_b32 v124, 16, v181
v_and_b32 v125, 4294901760, v181
v_mul_f32 v128, v122, v118
v_mul_f32 v129, v123, v118
v_mul_f32 v130, v124, v118
v_mul_f32 v131, v125, v118
v_lshlrev_b32 v122, 16, v186
v_and_b32 v123, 4294901760, v186
v_lshlrev_b32 v124, 16, v187
v_and_b32 v125, 4294901760, v187
v_fmac_f32 v128, v122, v119
v_fmac_f32 v129, v123, v119
v_fmac_f32 v130, v124, v119
v_fmac_f32 v131, v125, v119
v_lshlrev_b32 v122, 16, v192
v_and_b32 v123, 4294901760, v192
v_lshlrev_b32 v124, 16, v193
v_and_b32 v125, 4294901760, v193
v_fmac_f32 v128, v122, v120
v_fmac_f32 v129, v123, v120
v_fmac_f32 v130, v124, v120
v_fmac_f32 v131, v125, v120
v_lshlrev_b32 v122, 16, v198
v_and_b32 v123, 4294901760, v198
v_lshlrev_b32 v124, 16, v199
v_and_b32 v125, 4294901760, v199
v_fmac_f32 v128, v122, v121
v_fmac_f32 v129, v123, v121
v_fmac_f32 v130, v124, v121
v_fmac_f32 v131, v125, v121
v_cvt_pk_bf16_f32 v156, v128, v129
v_cvt_pk_bf16_f32 v157, v130, v131
v_lshlrev_b32 v122, 16, v182
v_and_b32 v123, 4294901760, v182
v_lshlrev_b32 v124, 16, v183
v_and_b32 v125, 4294901760, v183
v_mul_f32 v132, v122, v118
v_mul_f32 v133, v123, v118
v_mul_f32 v134, v124, v118
v_mul_f32 v135, v125, v118
v_lshlrev_b32 v122, 16, v188
v_and_b32 v123, 4294901760, v188
v_lshlrev_b32 v124, 16, v189
v_and_b32 v125, 4294901760, v189
v_fmac_f32 v132, v122, v119
v_fmac_f32 v133, v123, v119
v_fmac_f32 v134, v124, v119
v_fmac_f32 v135, v125, v119
v_lshlrev_b32 v122, 16, v194
v_and_b32 v123, 4294901760, v194
v_lshlrev_b32 v124, 16, v195
v_and_b32 v125, 4294901760, v195
v_fmac_f32 v132, v122, v120
v_fmac_f32 v133, v123, v120
v_fmac_f32 v134, v124, v120
v_fmac_f32 v135, v125, v120
v_lshlrev_b32 v122, 16, v200
v_and_b32 v123, 4294901760, v200
v_lshlrev_b32 v124, 16, v201
v_and_b32 v125, 4294901760, v201
v_fmac_f32 v132, v122, v121
v_fmac_f32 v133, v123, v121
v_fmac_f32 v134, v124, v121
v_fmac_f32 v135, v125, v121
v_cvt_pk_bf16_f32 v158, v132, v133
v_cvt_pk_bf16_f32 v159, v134, v135
v_lshlrev_b32 v122, 16, v184
v_and_b32 v123, 4294901760, v184
v_lshlrev_b32 v124, 16, v185
v_and_b32 v125, 4294901760, v185
v_mul_f32 v136, v122, v118
v_mul_f32 v137, v123, v118
v_mul_f32 v138, v124, v118
v_mul_f32 v139, v125, v118
v_lshlrev_b32 v122, 16, v190
v_and_b32 v123, 4294901760, v190
v_lshlrev_b32 v124, 16, v191
v_and_b32 v125, 4294901760, v191
v_fmac_f32 v136, v122, v119
v_fmac_f32 v137, v123, v119
v_fmac_f32 v138, v124, v119
v_fmac_f32 v139, v125, v119
v_lshlrev_b32 v122, 16, v196
v_and_b32 v123, 4294901760, v196
v_lshlrev_b32 v124, 16, v197
v_and_b32 v125, 4294901760, v197
v_fmac_f32 v136, v122, v120
v_fmac_f32 v137, v123, v120
v_fmac_f32 v138, v124, v120
v_fmac_f32 v139, v125, v120
v_lshlrev_b32 v122, 16, v202
v_and_b32 v123, 4294901760, v202
v_lshlrev_b32 v124, 16, v203
v_and_b32 v125, 4294901760, v203
v_fmac_f32 v136, v122, v121
v_fmac_f32 v137, v123, v121
v_fmac_f32 v138, v124, v121
v_fmac_f32 v139, v125, v121
v_cvt_pk_bf16_f32 v160, v136, v137
v_cvt_pk_bf16_f32 v161, v138, v139
s_lshr_b32 s68, s33, 10
s_mul_i32 s68, s68, 294912
s_and_b32 s89, s33, 8
s_add_u32 s89, s89, s68
s_add_u32 s89, s89, s85
buffer_store_dwordx2 v[156:157], v241, s[52:55], s89 offen sc0 sc1
s_lshr_b32 s68, s34, 10
s_mul_i32 s68, s68, 294912
s_and_b32 s89, s34, 8
s_add_u32 s89, s89, s68
s_add_u32 s89, s89, s85
buffer_store_dwordx2 v[158:159], v241, s[52:55], s89 offen sc0 sc1
s_lshr_b32 s68, s35, 10
s_mul_i32 s68, s68, 294912
s_and_b32 s89, s35, 8
s_add_u32 s89, s89, s68
s_add_u32 s89, s89, s85
buffer_store_dwordx2 v[160:161], v241, s[52:55], s89 offen sc0 sc1
v_accvgpr_read_b32 v168, a0
v_accvgpr_read_b32 v169, a1
v_accvgpr_read_b32 v170, a2
v_accvgpr_read_b32 v171, a3
v_mul_f32 v168, v168, v242
v_mul_f32 v169, v169, v242
v_mul_f32 v170, v170, v242
v_mul_f32 v171, v171, v242
v_cvt_pk_bf16_f32 v108, v168, v169
v_cvt_pk_bf16_f32 v109, v170, v171
v_accvgpr_read_b32 v172, a8
v_accvgpr_read_b32 v173, a9
v_accvgpr_read_b32 v174, a10
v_accvgpr_read_b32 v175, a11
v_mul_f32 v172, v172, v242
v_mul_f32 v173, v173, v242
v_mul_f32 v174, v174, v242
v_mul_f32 v175, v175, v242
v_cvt_pk_bf16_f32 v110, v172, v173
v_cvt_pk_bf16_f32 v111, v174, v175
s_add_u32 s89, s84, 0
buffer_store_dwordx4 v[108:111], v241, s[52:55], s89 offen sc0 sc1
v_accvgpr_read_b32 v168, a16
v_accvgpr_read_b32 v169, a17
v_accvgpr_read_b32 v170, a18
v_accvgpr_read_b32 v171, a19
v_mul_f32 v168, v168, v242
v_mul_f32 v169, v169, v242
v_mul_f32 v170, v170, v242
v_mul_f32 v171, v171, v242
v_cvt_pk_bf16_f32 v112, v168, v169
v_cvt_pk_bf16_f32 v113, v170, v171
v_accvgpr_read_b32 v172, a24
v_accvgpr_read_b32 v173, a25
v_accvgpr_read_b32 v174, a26
v_accvgpr_read_b32 v175, a27
v_mul_f32 v172, v172, v242
v_mul_f32 v173, v173, v242
v_mul_f32 v174, v174, v242
v_mul_f32 v175, v175, v242
v_cvt_pk_bf16_f32 v114, v172, v173
v_cvt_pk_bf16_f32 v115, v174, v175
s_add_u32 s89, s84, 294912
buffer_store_dwordx4 v[112:115], v241, s[52:55], s89 offen sc0 sc1
v_accvgpr_read_b32 v168, a32
v_accvgpr_read_b32 v169, a33
v_accvgpr_read_b32 v170, a34
v_accvgpr_read_b32 v171, a35
v_mul_f32 v168, v168, v242
v_mul_f32 v169, v169, v242
v_mul_f32 v170, v170, v242
v_mul_f32 v171, v171, v242
v_cvt_pk_bf16_f32 v116, v168, v169
v_cvt_pk_bf16_f32 v117, v170, v171
v_accvgpr_read_b32 v172, a40
v_accvgpr_read_b32 v173, a41
v_accvgpr_read_b32 v174, a42
v_accvgpr_read_b32 v175, a43
v_mul_f32 v172, v172, v242
v_mul_f32 v173, v173, v242
v_mul_f32 v174, v174, v242
v_mul_f32 v175, v175, v242
v_cvt_pk_bf16_f32 v118, v172, v173
v_cvt_pk_bf16_f32 v119, v174, v175
s_add_u32 s89, s84, 589824
buffer_store_dwordx4 v[116:119], v241, s[52:55], s89 offen sc0 sc1
v_accvgpr_read_b32 v168, a48
v_accvgpr_read_b32 v169, a49
v_accvgpr_read_b32 v170, a50
v_accvgpr_read_b32 v171, a51
v_mul_f32 v168, v168, v242
v_mul_f32 v169, v169, v242
v_mul_f32 v170, v170, v242
v_mul_f32 v171, v171, v242
v_cvt_pk_bf16_f32 v120, v168, v169
v_cvt_pk_bf16_f32 v121, v170, v171
v_accvgpr_read_b32 v172, a56
v_accvgpr_read_b32 v173, a57
v_accvgpr_read_b32 v174, a58
v_accvgpr_read_b32 v175, a59
v_mul_f32 v172, v172, v242
v_mul_f32 v173, v173, v242
v_mul_f32 v174, v174, v242
v_mul_f32 v175, v175, v242
v_cvt_pk_bf16_f32 v122, v172, v173
v_cvt_pk_bf16_f32 v123, v174, v175
s_add_u32 s89, s84, 884736
buffer_store_dwordx4 v[120:123], v241, s[52:55], s89 offen sc0 sc1
v_accvgpr_read_b32 v168, a64
v_accvgpr_read_b32 v169, a65
v_accvgpr_read_b32 v170, a66
v_accvgpr_read_b32 v171, a67
v_mul_f32 v168, v168, v242
v_mul_f32 v169, v169, v242
v_mul_f32 v170, v170, v242
v_mul_f32 v171, v171, v242
v_cvt_pk_bf16_f32 v124, v168, v169
v_cvt_pk_bf16_f32 v125, v170, v171
v_accvgpr_read_b32 v172, a72
v_accvgpr_read_b32 v173, a73
v_accvgpr_read_b32 v174, a74
v_accvgpr_read_b32 v175, a75
v_mul_f32 v172, v172, v242
v_mul_f32 v173, v173, v242
v_mul_f32 v174, v174, v242
v_mul_f32 v175, v175, v242
v_cvt_pk_bf16_f32 v126, v172, v173
v_cvt_pk_bf16_f32 v127, v174, v175
s_add_u32 s89, s84, 1179648
buffer_store_dwordx4 v[124:127], v241, s[52:55], s89 offen sc0 sc1
v_accvgpr_read_b32 v168, a80
v_accvgpr_read_b32 v169, a81
v_accvgpr_read_b32 v170, a82
v_accvgpr_read_b32 v171, a83
v_mul_f32 v168, v168, v242
v_mul_f32 v169, v169, v242
v_mul_f32 v170, v170, v242
v_mul_f32 v171, v171, v242
v_cvt_pk_bf16_f32 v128, v168, v169
v_cvt_pk_bf16_f32 v129, v170, v171
v_accvgpr_read_b32 v172, a88
v_accvgpr_read_b32 v173, a89
v_accvgpr_read_b32 v174, a90
v_accvgpr_read_b32 v175, a91
v_mul_f32 v172, v172, v242
v_mul_f32 v173, v173, v242
v_mul_f32 v174, v174, v242
v_mul_f32 v175, v175, v242
v_cvt_pk_bf16_f32 v130, v172, v173
v_cvt_pk_bf16_f32 v131, v174, v175
s_add_u32 s89, s84, 1474560
buffer_store_dwordx4 v[128:131], v241, s[52:55], s89 offen sc0 sc1
v_accvgpr_read_b32 v168, a4
v_accvgpr_read_b32 v169, a5
v_accvgpr_read_b32 v170, a6
v_accvgpr_read_b32 v171, a7
v_mul_f32 v168, v168, v243
v_mul_f32 v169, v169, v243
v_mul_f32 v170, v170, v243
v_mul_f32 v171, v171, v243
v_cvt_pk_bf16_f32 v132, v168, v169
v_cvt_pk_bf16_f32 v133, v170, v171
v_accvgpr_read_b32 v172, a12
v_accvgpr_read_b32 v173, a13
v_accvgpr_read_b32 v174, a14
v_accvgpr_read_b32 v175, a15
v_mul_f32 v172, v172, v243
v_mul_f32 v173, v173, v243
v_mul_f32 v174, v174, v243
v_mul_f32 v175, v175, v243
v_cvt_pk_bf16_f32 v134, v172, v173
v_cvt_pk_bf16_f32 v135, v174, v175
s_add_u32 s89, s84, 0
buffer_store_dwordx4 v[132:135], v241, s[52:55], s89 offen offset:1024 sc0 sc1
v_accvgpr_read_b32 v168, a20
v_accvgpr_read_b32 v169, a21
v_accvgpr_read_b32 v170, a22
v_accvgpr_read_b32 v171, a23
v_mul_f32 v168, v168, v243
v_mul_f32 v169, v169, v243
v_mul_f32 v170, v170, v243
v_mul_f32 v171, v171, v243
v_cvt_pk_bf16_f32 v136, v168, v169
v_cvt_pk_bf16_f32 v137, v170, v171
v_accvgpr_read_b32 v172, a28
v_accvgpr_read_b32 v173, a29
v_accvgpr_read_b32 v174, a30
v_accvgpr_read_b32 v175, a31
v_mul_f32 v172, v172, v243
v_mul_f32 v173, v173, v243
v_mul_f32 v174, v174, v243
v_mul_f32 v175, v175, v243
v_cvt_pk_bf16_f32 v138, v172, v173
v_cvt_pk_bf16_f32 v139, v174, v175
s_add_u32 s89, s84, 294912
buffer_store_dwordx4 v[136:139], v241, s[52:55], s89 offen offset:1024 sc0 sc1
v_accvgpr_read_b32 v168, a36
v_accvgpr_read_b32 v169, a37
v_accvgpr_read_b32 v170, a38
v_accvgpr_read_b32 v171, a39
v_mul_f32 v168, v168, v243
v_mul_f32 v169, v169, v243
v_mul_f32 v170, v170, v243
v_mul_f32 v171, v171, v243
v_cvt_pk_bf16_f32 v140, v168, v169
v_cvt_pk_bf16_f32 v141, v170, v171
v_accvgpr_read_b32 v172, a44
v_accvgpr_read_b32 v173, a45
v_accvgpr_read_b32 v174, a46
v_accvgpr_read_b32 v175, a47
v_mul_f32 v172, v172, v243
v_mul_f32 v173, v173, v243
v_mul_f32 v174, v174, v243
v_mul_f32 v175, v175, v243
v_cvt_pk_bf16_f32 v142, v172, v173
v_cvt_pk_bf16_f32 v143, v174, v175
s_add_u32 s89, s84, 589824
buffer_store_dwordx4 v[140:143], v241, s[52:55], s89 offen offset:1024 sc0 sc1
v_accvgpr_read_b32 v168, a52
v_accvgpr_read_b32 v169, a53
v_accvgpr_read_b32 v170, a54
v_accvgpr_read_b32 v171, a55
v_mul_f32 v168, v168, v243
v_mul_f32 v169, v169, v243
v_mul_f32 v170, v170, v243
v_mul_f32 v171, v171, v243
v_cvt_pk_bf16_f32 v144, v168, v169
v_cvt_pk_bf16_f32 v145, v170, v171
v_accvgpr_read_b32 v172, a60
v_accvgpr_read_b32 v173, a61
v_accvgpr_read_b32 v174, a62
v_accvgpr_read_b32 v175, a63
v_mul_f32 v172, v172, v243
v_mul_f32 v173, v173, v243
v_mul_f32 v174, v174, v243
v_mul_f32 v175, v175, v243
v_cvt_pk_bf16_f32 v146, v172, v173
v_cvt_pk_bf16_f32 v147, v174, v175
s_add_u32 s89, s84, 884736
buffer_store_dwordx4 v[144:147], v241, s[52:55], s89 offen offset:1024 sc0 sc1
v_accvgpr_read_b32 v168, a68
v_accvgpr_read_b32 v169, a69
v_accvgpr_read_b32 v170, a70
v_accvgpr_read_b32 v171, a71
v_mul_f32 v168, v168, v243
v_mul_f32 v169, v169, v243
v_mul_f32 v170, v170, v243
v_mul_f32 v171, v171, v243
v_cvt_pk_bf16_f32 v148, v168, v169
v_cvt_pk_bf16_f32 v149, v170, v171
v_accvgpr_read_b32 v172, a76
v_accvgpr_read_b32 v173, a77
v_accvgpr_read_b32 v174, a78
v_accvgpr_read_b32 v175, a79
v_mul_f32 v172, v172, v243
v_mul_f32 v173, v173, v243
v_mul_f32 v174, v174, v243
v_mul_f32 v175, v175, v243
v_cvt_pk_bf16_f32 v150, v172, v173
v_cvt_pk_bf16_f32 v151, v174, v175
s_add_u32 s89, s84, 1179648
buffer_store_dwordx4 v[148:151], v241, s[52:55], s89 offen offset:1024 sc0 sc1
v_accvgpr_read_b32 v168, a84
v_accvgpr_read_b32 v169, a85
v_accvgpr_read_b32 v170, a86
v_accvgpr_read_b32 v171, a87
v_mul_f32 v168, v168, v243
v_mul_f32 v169, v169, v243
v_mul_f32 v170, v170, v243
v_mul_f32 v171, v171, v243
v_cvt_pk_bf16_f32 v152, v168, v169
v_cvt_pk_bf16_f32 v153, v170, v171
v_accvgpr_read_b32 v172, a92
v_accvgpr_read_b32 v173, a93
v_accvgpr_read_b32 v174, a94
v_accvgpr_read_b32 v175, a95
v_mul_f32 v172, v172, v243
v_mul_f32 v173, v173, v243
v_mul_f32 v174, v174, v243
v_mul_f32 v175, v175, v243
v_cvt_pk_bf16_f32 v154, v172, v173
v_cvt_pk_bf16_f32 v155, v174, v175
s_add_u32 s89, s84, 1474560
buffer_store_dwordx4 v[152:155], v241, s[52:55], s89 offen offset:1024 sc0 sc1
s_waitcnt vmcnt(0) lgkmcnt(0)
s_branch K1_end
K1_l2_t:
s_swappc_b64 s[74:75], s[78:79]
s_branch K1_l2
K1_l3_t:
s_swappc_b64 s[74:75], s[80:81]
s_branch K1_l3
K1_l4_t:
s_swappc_b64 s[74:75], s[76:77]
s_branch K1_l4
K1_l5_t:
s_swappc_b64 s[74:75], s[80:81]
s_branch K1_l5
K1_l6_t:
s_swappc_b64 s[74:75], s[78:79]
s_branch K1_l6
K1_l7_t:
s_swappc_b64 s[74:75], s[80:81]
s_branch K1_l7
K1_l8_t:
s_swappc_b64 s[74:75], s[76:77]
s_branch K1_l8
K1_l9_t:
s_swappc_b64 s[74:75], s[80:81]
s_branch K1_l9
K1_l10_t:
s_swappc_b64 s[74:75], s[78:79]
s_branch K1_l10
K1_l11_t:
s_swappc_b64 s[74:75], s[80:81]
s_branch K1_l11
K1_l12_t:
s_swappc_b64 s[74:75], s[76:77]
s_branch K1_l12
K1_l13_t:
s_swappc_b64 s[74:75], s[80:81]
s_branch K1_l13
K1_l14_t:
s_swappc_b64 s[74:75], s[78:79]
s_branch K1_l14
K1_l15_t:
s_swappc_b64 s[74:75], s[80:81]
s_branch K1_l15
K1_l16_t:
s_swappc_b64 s[74:75], s[76:77]
s_branch K1_l16
K1_l17_t:
s_swappc_b64 s[74:75], s[80:81]
s_branch K1_l17
K1_l18_t:
s_swappc_b64 s[74:75], s[78:79]
s_branch K1_l18
K1_l19_t:
s_swappc_b64 s[74:75], s[80:81]
s_branch K1_l19
K1_l20_t:
s_swappc_b64 s[74:75], s[76:77]
s_branch K1_l20
K1_l21_t:
s_swappc_b64 s[74:75], s[80:81]
s_branch K1_l21
K1_l22_t:
s_swappc_b64 s[74:75], s[78:79]
s_branch K1_l22
K1_l23_t:
s_swappc_b64 s[74:75], s[80:81]
s_branch K1_l23
K1_l24_t:
s_swappc_b64 s[74:75], s[76:77]
s_branch K1_l24
K1_l25_t:
s_swappc_b64 s[74:75], s[80:81]
s_branch K1_l25
K1_l26_t:
s_swappc_b64 s[74:75], s[78:79]
s_branch K1_l26
K1_l27_t:
s_swappc_b64 s[74:75], s[80:81]
s_branch K1_l27
K1_l28_t:
s_swappc_b64 s[74:75], s[76:77]
s_branch K1_l28
K1_l29_t:
s_swappc_b64 s[74:75], s[80:81]
s_branch K1_l29
K1_l30_t:
s_swappc_b64 s[74:75], s[78:79]
s_branch K1_l30
K1_l31_t:
s_swappc_b64 s[74:75], s[80:81]
s_branch K1_l31
K1_rare_e:
s_nop 7
v_max3_f32 v166, v108, v109, v110
v_max3_f32 v166, v166, v111, v116
v_max3_f32 v166, v166, v117, v118
v_max_f32 v166, v166, v119
v_mov_b32 v167, v166
s_nop 1
v_permlane32_swap_b32 v166, v167
v_max_f32 v166, v166, v167
v_mov_b32 v167, v166
s_nop 1
v_permlane16_swap_b32 v166, v167
v_max_f32 v166, v166, v167
v_ceil_f32 v166, v166
v_max_f32 v166, s82, v166
v_sub_f32 v108, v108, v166
v_sub_f32 v109, v109, v166
v_sub_f32 v110, v110, v166
v_sub_f32 v111, v111, v166
v_sub_f32 v116, v116, v166
v_sub_f32 v117, v117, v166
v_sub_f32 v118, v118, v166
v_sub_f32 v119, v119, v166
v_sub_f32 v168, v168, v166
v_sub_f32 v169, v169, v166
v_sub_f32 v170, v170, v166
v_sub_f32 v171, v171, v166
v_exp_f32_e64 v242, -v166
v_max3_f32 v166, v112, v113, v114
v_max3_f32 v166, v166, v115, v120
v_max3_f32 v166, v166, v121, v122
v_max_f32 v166, v166, v123
v_mov_b32 v167, v166
s_nop 1
v_permlane32_swap_b32 v166, v167
v_max_f32 v166, v166, v167
v_mov_b32 v167, v166
s_nop 1
v_permlane16_swap_b32 v166, v167
v_max_f32 v166, v166, v167
v_ceil_f32 v166, v166
v_max_f32 v166, s82, v166
v_sub_f32 v112, v112, v166
v_sub_f32 v113, v113, v166
v_sub_f32 v114, v114, v166
v_sub_f32 v115, v115, v166
v_sub_f32 v120, v120, v166
v_sub_f32 v121, v121, v166
v_sub_f32 v122, v122, v166
v_sub_f32 v123, v123, v166
v_sub_f32 v172, v172, v166
v_sub_f32 v173, v173, v166
v_sub_f32 v174, v174, v166
v_sub_f32 v175, v175, v166
v_exp_f32_e64 v243, -v166
v_max3_f32 v166, v140, v141, v142
v_max_f32 v166, v166, v143
v_mov_b32 v167, v166
s_nop 1
v_permlane32_swap_b32 v166, v167
v_max_f32 v166, v166, v167
v_mov_b32 v167, v166
s_nop 1
v_permlane16_swap_b32 v166, v167
v_max_f32 v166, v166, v167
v_ceil_f32 v166, v166
v_max_f32 v166, s82, v166
v_sub_f32 v140, v140, v166
v_sub_f32 v141, v141, v166
v_sub_f32 v142, v142, v166
v_sub_f32 v143, v143, v166
v_sub_f32 v176, v176, v166
v_sub_f32 v177, v177, v166
v_sub_f32 v178, v178, v166
v_sub_f32 v179, v179, v166
v_exp_f32_e64 v244, -v166
s_mov_b32 s83, 1
s_nop 1
s_setpc_b64 s[74:75]
K1_rare_o:
s_nop 7
v_max3_f32 v166, v124, v125, v126
v_max3_f32 v166, v166, v127, v132
v_max3_f32 v166, v166, v133, v134
v_max_f32 v166, v166, v135
v_mov_b32 v167, v166
s_nop 1
v_permlane32_swap_b32 v166, v167
v_max_f32 v166, v166, v167
v_mov_b32 v167, v166
s_nop 1
v_permlane16_swap_b32 v166, v167
v_max_f32 v166, v166, v167
v_ceil_f32 v166, v166
v_max_f32 v166, s82, v166
v_sub_f32 v124, v124, v166
v_sub_f32 v125, v125, v166
v_sub_f32 v126, v126, v166
v_sub_f32 v127, v127, v166
v_sub_f32 v132, v132, v166
v_sub_f32 v133, v133, v166
v_sub_f32 v134, v134, v166
v_sub_f32 v135, v135, v166
v_sub_f32 v168, v168, v166
v_sub_f32 v169, v169, v166
v_sub_f32 v170, v170, v166
v_sub_f32 v171, v171, v166
v_exp_f32_e64 v242, -v166
v_max3_f32 v166, v128, v129, v130
v_max3_f32 v166, v166, v131, v136
v_max3_f32 v166, v166, v137, v138
v_max_f32 v166, v166, v139
v_mov_b32 v167, v166
s_nop 1
v_permlane32_swap_b32 v166, v167
v_max_f32 v166, v166, v167
v_mov_b32 v167, v166
s_nop 1
v_permlane16_swap_b32 v166, v167
v_max_f32 v166, v166, v167
v_ceil_f32 v166, v166
v_max_f32 v166, s82, v166
v_sub_f32 v128, v128, v166
v_sub_f32 v129, v129, v166
v_sub_f32 v130, v130, v166
v_sub_f32 v131, v131, v166
v_sub_f32 v136, v136, v166
v_sub_f32 v137, v137, v166
v_sub_f32 v138, v138, v166
v_sub_f32 v139, v139, v166
v_sub_f32 v172, v172, v166
v_sub_f32 v173, v173, v166
v_sub_f32 v174, v174, v166
v_sub_f32 v175, v175, v166
v_exp_f32_e64 v243, -v166
v_mov_b32 v244, 1.0
s_mov_b32 s83, 1
s_nop 1
s_setpc_b64 s[74:75]
K1_pend:
s_nop 7
s_nop 7
v_accvgpr_read_b32 v166, a0
v_mul_f32 v166, v166, v242
v_accvgpr_write_b32 a0, v166
v_accvgpr_read_b32 v166, a1
v_mul_f32 v166, v166, v242
v_accvgpr_write_b32 a1, v166
v_accvgpr_read_b32 v166, a2
v_mul_f32 v166, v166, v242
v_accvgpr_write_b32 a2, v166
v_accvgpr_read_b32 v166, a3
v_mul_f32 v166, v166, v242
v_accvgpr_write_b32 a3, v166
v_accvgpr_read_b32 v166, a4
v_mul_f32 v166, v166, v243
v_accvgpr_write_b32 a4, v166
v_accvgpr_read_b32 v166, a5
v_mul_f32 v166, v166, v243
v_accvgpr_write_b32 a5, v166
v_accvgpr_read_b32 v166, a6
v_mul_f32 v166, v166, v243
v_accvgpr_write_b32 a6, v166
v_accvgpr_read_b32 v166, a7
v_mul_f32 v166, v166, v243
v_accvgpr_write_b32 a7, v166
v_accvgpr_read_b32 v166, a8
v_mul_f32 v166, v166, v242
v_accvgpr_write_b32 a8, v166
v_accvgpr_read_b32 v166, a9
v_mul_f32 v166, v166, v242
v_accvgpr_write_b32 a9, v166
v_accvgpr_read_b32 v166, a10
v_mul_f32 v166, v166, v242
v_accvgpr_write_b32 a10, v166
v_accvgpr_read_b32 v166, a11
v_mul_f32 v166, v166, v242
v_accvgpr_write_b32 a11, v166
v_accvgpr_read_b32 v166, a12
v_mul_f32 v166, v166, v243
v_accvgpr_write_b32 a12, v166
v_accvgpr_read_b32 v166, a13
v_mul_f32 v166, v166, v243
v_accvgpr_write_b32 a13, v166
v_accvgpr_read_b32 v166, a14
v_mul_f32 v166, v166, v243
v_accvgpr_write_b32 a14, v166
v_accvgpr_read_b32 v166, a15
v_mul_f32 v166, v166, v243
v_accvgpr_write_b32 a15, v166
v_accvgpr_read_b32 v166, a16
v_mul_f32 v166, v166, v242
v_accvgpr_write_b32 a16, v166
v_accvgpr_read_b32 v166, a17
v_mul_f32 v166, v166, v242
v_accvgpr_write_b32 a17, v166
v_accvgpr_read_b32 v166, a18
v_mul_f32 v166, v166, v242
v_accvgpr_write_b32 a18, v166
v_accvgpr_read_b32 v166, a19
v_mul_f32 v166, v166, v242
v_accvgpr_write_b32 a19, v166
v_accvgpr_read_b32 v166, a20
v_mul_f32 v166, v166, v243
v_accvgpr_write_b32 a20, v166
v_accvgpr_read_b32 v166, a21
v_mul_f32 v166, v166, v243
v_accvgpr_write_b32 a21, v166
v_accvgpr_read_b32 v166, a22
v_mul_f32 v166, v166, v243
v_accvgpr_write_b32 a22, v166
v_accvgpr_read_b32 v166, a23
v_mul_f32 v166, v166, v243
v_accvgpr_write_b32 a23, v166
v_accvgpr_read_b32 v166, a24
v_mul_f32 v166, v166, v242
v_accvgpr_write_b32 a24, v166
v_accvgpr_read_b32 v166, a25
v_mul_f32 v166, v166, v242
v_accvgpr_write_b32 a25, v166
v_accvgpr_read_b32 v166, a26
v_mul_f32 v166, v166, v242
v_accvgpr_write_b32 a26, v166
v_accvgpr_read_b32 v166, a27
v_mul_f32 v166, v166, v242
v_accvgpr_write_b32 a27, v166
v_accvgpr_read_b32 v166, a28
v_mul_f32 v166, v166, v243
v_accvgpr_write_b32 a28, v166
v_accvgpr_read_b32 v166, a29
v_mul_f32 v166, v166, v243
v_accvgpr_write_b32 a29, v166
v_accvgpr_read_b32 v166, a30
v_mul_f32 v166, v166, v243
v_accvgpr_write_b32 a30, v166
v_accvgpr_read_b32 v166, a31
v_mul_f32 v166, v166, v243
v_accvgpr_write_b32 a31, v166
v_accvgpr_read_b32 v166, a32
v_mul_f32 v166, v166, v242
v_accvgpr_write_b32 a32, v166
v_accvgpr_read_b32 v166, a33
v_mul_f32 v166, v166, v242
v_accvgpr_write_b32 a33, v166
v_accvgpr_read_b32 v166, a34
v_mul_f32 v166, v166, v242
v_accvgpr_write_b32 a34, v166
v_accvgpr_read_b32 v166, a35
v_mul_f32 v166, v166, v242
v_accvgpr_write_b32 a35, v166
v_accvgpr_read_b32 v166, a36
v_mul_f32 v166, v166, v243
v_accvgpr_write_b32 a36, v166
v_accvgpr_read_b32 v166, a37
v_mul_f32 v166, v166, v243
v_accvgpr_write_b32 a37, v166
v_accvgpr_read_b32 v166, a38
v_mul_f32 v166, v166, v243
v_accvgpr_write_b32 a38, v166
v_accvgpr_read_b32 v166, a39
v_mul_f32 v166, v166, v243
v_accvgpr_write_b32 a39, v166
v_accvgpr_read_b32 v166, a40
v_mul_f32 v166, v166, v242
v_accvgpr_write_b32 a40, v166
v_accvgpr_read_b32 v166, a41
v_mul_f32 v166, v166, v242
v_accvgpr_write_b32 a41, v166
v_accvgpr_read_b32 v166, a42
v_mul_f32 v166, v166, v242
v_accvgpr_write_b32 a42, v166
v_accvgpr_read_b32 v166, a43
v_mul_f32 v166, v166, v242
v_accvgpr_write_b32 a43, v166
v_accvgpr_read_b32 v166, a44
v_mul_f32 v166, v166, v243
v_accvgpr_write_b32 a44, v166
v_accvgpr_read_b32 v166, a45
v_mul_f32 v166, v166, v243
v_accvgpr_write_b32 a45, v166
v_accvgpr_read_b32 v166, a46
v_mul_f32 v166, v166, v243
v_accvgpr_write_b32 a46, v166
v_accvgpr_read_b32 v166, a47
v_mul_f32 v166, v166, v243
v_accvgpr_write_b32 a47, v166
v_accvgpr_read_b32 v166, a48
v_mul_f32 v166, v166, v242
v_accvgpr_write_b32 a48, v166
v_accvgpr_read_b32 v166, a49
v_mul_f32 v166, v166, v242
v_accvgpr_write_b32 a49, v166
v_accvgpr_read_b32 v166, a50
v_mul_f32 v166, v166, v242
v_accvgpr_write_b32 a50, v166
v_accvgpr_read_b32 v166, a51
v_mul_f32 v166, v166, v242
v_accvgpr_write_b32 a51, v166
v_accvgpr_read_b32 v166, a52
v_mul_f32 v166, v166, v243
v_accvgpr_write_b32 a52, v166
v_accvgpr_read_b32 v166, a53
v_mul_f32 v166, v166, v243
v_accvgpr_write_b32 a53, v166
v_accvgpr_read_b32 v166, a54
v_mul_f32 v166, v166, v243
v_accvgpr_write_b32 a54, v166
v_accvgpr_read_b32 v166, a55
v_mul_f32 v166, v166, v243
v_accvgpr_write_b32 a55, v166
v_accvgpr_read_b32 v166, a56
v_mul_f32 v166, v166, v242
v_accvgpr_write_b32 a56, v166
v_accvgpr_read_b32 v166, a57
v_mul_f32 v166, v166, v242
v_accvgpr_write_b32 a57, v166
v_accvgpr_read_b32 v166, a58
v_mul_f32 v166, v166, v242
v_accvgpr_write_b32 a58, v166
v_accvgpr_read_b32 v166, a59
v_mul_f32 v166, v166, v242
v_accvgpr_write_b32 a59, v166
v_accvgpr_read_b32 v166, a60
v_mul_f32 v166, v166, v243
v_accvgpr_write_b32 a60, v166
v_accvgpr_read_b32 v166, a61
v_mul_f32 v166, v166, v243
v_accvgpr_write_b32 a61, v166
v_accvgpr_read_b32 v166, a62
v_mul_f32 v166, v166, v243
v_accvgpr_write_b32 a62, v166
v_accvgpr_read_b32 v166, a63
v_mul_f32 v166, v166, v243
v_accvgpr_write_b32 a63, v166
v_accvgpr_read_b32 v166, a64
v_mul_f32 v166, v166, v242
v_accvgpr_write_b32 a64, v166
v_accvgpr_read_b32 v166, a65
v_mul_f32 v166, v166, v242
v_accvgpr_write_b32 a65, v166
v_accvgpr_read_b32 v166, a66
v_mul_f32 v166, v166, v242
v_accvgpr_write_b32 a66, v166
v_accvgpr_read_b32 v166, a67
v_mul_f32 v166, v166, v242
v_accvgpr_write_b32 a67, v166
v_accvgpr_read_b32 v166, a68
v_mul_f32 v166, v166, v243
v_accvgpr_write_b32 a68, v166
v_accvgpr_read_b32 v166, a69
v_mul_f32 v166, v166, v243
v_accvgpr_write_b32 a69, v166
v_accvgpr_read_b32 v166, a70
v_mul_f32 v166, v166, v243
v_accvgpr_write_b32 a70, v166
v_accvgpr_read_b32 v166, a71
v_mul_f32 v166, v166, v243
v_accvgpr_write_b32 a71, v166
v_accvgpr_read_b32 v166, a72
v_mul_f32 v166, v166, v242
v_accvgpr_write_b32 a72, v166
v_accvgpr_read_b32 v166, a73
v_mul_f32 v166, v166, v242
v_accvgpr_write_b32 a73, v166
v_accvgpr_read_b32 v166, a74
v_mul_f32 v166, v166, v242
v_accvgpr_write_b32 a74, v166
v_accvgpr_read_b32 v166, a75
v_mul_f32 v166, v166, v242
v_accvgpr_write_b32 a75, v166
v_accvgpr_read_b32 v166, a76
v_mul_f32 v166, v166, v243
v_accvgpr_write_b32 a76, v166
v_accvgpr_read_b32 v166, a77
v_mul_f32 v166, v166, v243
v_accvgpr_write_b32 a77, v166
v_accvgpr_read_b32 v166, a78
v_mul_f32 v166, v166, v243
v_accvgpr_write_b32 a78, v166
v_accvgpr_read_b32 v166, a79
v_mul_f32 v166, v166, v243
v_accvgpr_write_b32 a79, v166
v_accvgpr_read_b32 v166, a80
v_mul_f32 v166, v166, v242
v_accvgpr_write_b32 a80, v166
v_accvgpr_read_b32 v166, a81
v_mul_f32 v166, v166, v242
v_accvgpr_write_b32 a81, v166
v_accvgpr_read_b32 v166, a82
v_mul_f32 v166, v166, v242
v_accvgpr_write_b32 a82, v166
v_accvgpr_read_b32 v166, a83
v_mul_f32 v166, v166, v242
v_accvgpr_write_b32 a83, v166
v_accvgpr_read_b32 v166, a84
v_mul_f32 v166, v166, v243
v_accvgpr_write_b32 a84, v166
v_accvgpr_read_b32 v166, a85
v_mul_f32 v166, v166, v243
v_accvgpr_write_b32 a85, v166
v_accvgpr_read_b32 v166, a86
v_mul_f32 v166, v166, v243
v_accvgpr_write_b32 a86, v166
v_accvgpr_read_b32 v166, a87
v_mul_f32 v166, v166, v243
v_accvgpr_write_b32 a87, v166
v_accvgpr_read_b32 v166, a88
v_mul_f32 v166, v166, v242
v_accvgpr_write_b32 a88, v166
v_accvgpr_read_b32 v166, a89
v_mul_f32 v166, v166, v242
v_accvgpr_write_b32 a89, v166
v_accvgpr_read_b32 v166, a90
v_mul_f32 v166, v166, v242
v_accvgpr_write_b32 a90, v166
v_accvgpr_read_b32 v166, a91
v_mul_f32 v166, v166, v242
v_accvgpr_write_b32 a91, v166
v_accvgpr_read_b32 v166, a92
v_mul_f32 v166, v166, v243
v_accvgpr_write_b32 a92, v166
v_accvgpr_read_b32 v166, a93
v_mul_f32 v166, v166, v243
v_accvgpr_write_b32 a93, v166
v_accvgpr_read_b32 v166, a94
v_mul_f32 v166, v166, v243
v_accvgpr_write_b32 a94, v166
v_accvgpr_read_b32 v166, a95
v_mul_f32 v166, v166, v243
v_accvgpr_write_b32 a95, v166
v_accvgpr_read_b32 v166, a96
v_mul_f32 v166, v166, v244
v_accvgpr_write_b32 a96, v166
v_accvgpr_read_b32 v166, a97
v_mul_f32 v166, v166, v244
v_accvgpr_write_b32 a97, v166
v_accvgpr_read_b32 v166, a98
v_mul_f32 v166, v166, v244
v_accvgpr_write_b32 a98, v166
v_accvgpr_read_b32 v166, a99
v_mul_f32 v166, v166, v244
v_accvgpr_write_b32 a99, v166
v_accvgpr_read_b32 v166, a100
v_mul_f32 v166, v166, v244
v_accvgpr_write_b32 a100, v166
v_accvgpr_read_b32 v166, a101
v_mul_f32 v166, v166, v244
v_accvgpr_write_b32 a101, v166
v_accvgpr_read_b32 v166, a102
v_mul_f32 v166, v166, v244
v_accvgpr_write_b32 a102, v166
v_accvgpr_read_b32 v166, a103
v_mul_f32 v166, v166, v244
v_accvgpr_write_b32 a103, v166
v_accvgpr_read_b32 v166, a104
v_mul_f32 v166, v166, v244
v_accvgpr_write_b32 a104, v166
v_accvgpr_read_b32 v166, a105
v_mul_f32 v166, v166, v244
v_accvgpr_write_b32 a105, v166
v_accvgpr_read_b32 v166, a106
v_mul_f32 v166, v166, v244
v_accvgpr_write_b32 a106, v166
v_accvgpr_read_b32 v166, a107
v_mul_f32 v166, v166, v244
v_accvgpr_write_b32 a107, v166
v_accvgpr_read_b32 v166, a108
v_mul_f32 v166, v166, v244
v_accvgpr_write_b32 a108, v166
v_accvgpr_read_b32 v166, a109
v_mul_f32 v166, v166, v244
v_accvgpr_write_b32 a109, v166
v_accvgpr_read_b32 v166, a110
v_mul_f32 v166, v166, v244
v_accvgpr_write_b32 a110, v166
v_accvgpr_read_b32 v166, a111
v_mul_f32 v166, v166, v244
v_accvgpr_write_b32 a111, v166
v_accvgpr_read_b32 v166, a112
v_mul_f32 v166, v166, v244
v_accvgpr_write_b32 a112, v166
v_accvgpr_read_b32 v166, a113
v_mul_f32 v166, v166, v244
v_accvgpr_write_b32 a113, v166
v_accvgpr_read_b32 v166, a114
v_mul_f32 v166, v166, v244
v_accvgpr_write_b32 a114, v166
v_accvgpr_read_b32 v166, a115
v_mul_f32 v166, v166, v244
v_accvgpr_write_b32 a115, v166
v_accvgpr_read_b32 v166, a116
v_mul_f32 v166, v166, v244
v_accvgpr_write_b32 a116, v166
v_accvgpr_read_b32 v166, a117
v_mul_f32 v166, v166, v244
v_accvgpr_write_b32 a117, v166
v_accvgpr_read_b32 v166, a118
v_mul_f32 v166, v166, v244
v_accvgpr_write_b32 a118, v166
v_accvgpr_read_b32 v166, a119
v_mul_f32 v166, v166, v244
v_accvgpr_write_b32 a119, v166
v_accvgpr_read_b32 v166, a120
v_mul_f32 v166, v166, v244
v_accvgpr_write_b32 a120, v166
v_accvgpr_read_b32 v166, a121
v_mul_f32 v166, v166, v244
v_accvgpr_write_b32 a121, v166
v_accvgpr_read_b32 v166, a122
v_mul_f32 v166, v166, v244
v_accvgpr_write_b32 a122, v166
v_accvgpr_read_b32 v166, a123
v_mul_f32 v166, v166, v244
v_accvgpr_write_b32 a123, v166
v_accvgpr_read_b32 v166, a124
v_mul_f32 v166, v166, v244
v_accvgpr_write_b32 a124, v166
v_accvgpr_read_b32 v166, a125
v_mul_f32 v166, v166, v244
v_accvgpr_write_b32 a125, v166
v_accvgpr_read_b32 v166, a126
v_mul_f32 v166, v166, v244
v_accvgpr_write_b32 a126, v166
v_accvgpr_read_b32 v166, a127
v_mul_f32 v166, v166, v244
v_accvgpr_write_b32 a127, v166
v_accvgpr_read_b32 v166, a128
v_mul_f32 v166, v166, v244
v_accvgpr_write_b32 a128, v166
v_accvgpr_read_b32 v166, a129
v_mul_f32 v166, v166, v244
v_accvgpr_write_b32 a129, v166
v_accvgpr_read_b32 v166, a130
v_mul_f32 v166, v166, v244
v_accvgpr_write_b32 a130, v166
v_accvgpr_read_b32 v166, a131
v_mul_f32 v166, v166, v244
v_accvgpr_write_b32 a131, v166
v_accvgpr_read_b32 v166, a132
v_mul_f32 v166, v166, v244
v_accvgpr_write_b32 a132, v166
v_accvgpr_read_b32 v166, a133
v_mul_f32 v166, v166, v244
v_accvgpr_write_b32 a133, v166
v_accvgpr_read_b32 v166, a134
v_mul_f32 v166, v166, v244
v_accvgpr_write_b32 a134, v166
v_accvgpr_read_b32 v166, a135
v_mul_f32 v166, v166, v244
v_accvgpr_write_b32 a135, v166
v_accvgpr_read_b32 v166, a136
v_mul_f32 v166, v166, v244
v_accvgpr_write_b32 a136, v166
v_accvgpr_read_b32 v166, a137
v_mul_f32 v166, v166, v244
v_accvgpr_write_b32 a137, v166
v_accvgpr_read_b32 v166, a138
v_mul_f32 v166, v166, v244
v_accvgpr_write_b32 a138, v166
v_accvgpr_read_b32 v166, a139
v_mul_f32 v166, v166, v244
v_accvgpr_write_b32 a139, v166
v_accvgpr_read_b32 v166, a140
v_mul_f32 v166, v166, v244
v_accvgpr_write_b32 a140, v166
v_accvgpr_read_b32 v166, a141
v_mul_f32 v166, v166, v244
v_accvgpr_write_b32 a141, v166
v_accvgpr_read_b32 v166, a142
v_mul_f32 v166, v166, v244
v_accvgpr_write_b32 a142, v166
v_accvgpr_read_b32 v166, a143
v_mul_f32 v166, v166, v244
v_accvgpr_write_b32 a143, v166
v_mul_f32 v0, v0, v242
v_mul_f32 v1, v1, v242
v_mul_f32 v2, v2, v242
v_mul_f32 v3, v3, v242
v_mul_f32 v4, v4, v243
v_mul_f32 v5, v5, v243
v_mul_f32 v6, v6, v243
v_mul_f32 v7, v7, v243
v_mul_f32 v8, v8, v244
v_mul_f32 v9, v9, v244
v_mul_f32 v10, v10, v244
v_mul_f32 v11, v11, v244
s_mov_b32 s83, 0
s_nop 1
s_setpc_b64 s[74:75]
K1_end:
)ASM"
    : "+{v0}"(tid)
    : "{s[16:17]}"(__builtin_amdgcn_kernarg_segment_ptr()), "{s18}"(blockIdx.x)
    : "v1", "v2", "v3", "v4", "v5", "v6", "v7", "v8", "v9", "v10", "v11", "v12", "v13", "v14", "v15", "v16", "v17", "v18", "v19", "v20", "v21", "v22", "v23", "v24", "v25", "v26", "v27", "v28", "v29", "v30", "v31", "v32", "v33", "v34", "v35", "v36", "v37", "v38", "v39", "v40", "v41", "v42", "v43", "v44", "v45", "v46", "v47", "v48", "v49", "v50", "v51", "v52", "v53", "v54", "v55", "v56", "v57", "v58", "v59", "v60", "v61", "v62", "v63", "v64", "v65", "v66", "v67", "v68", "v69", "v70", "v71", "v72", "v73", "v74", "v75", "v76", "v77", "v78", "v79", "v80", "v81", "v82", "v83", "v84", "v85", "v86", "v87", "v88", "v89", "v90", "v91", "v92", "v93", "v94", "v95", "v96", "v97", "v98", "v99", "v100", "v101", "v102", "v103", "v104", "v105", "v106", "v107", "v108", "v109", "v110", "v111", "v112", "v113", "v114", "v115", "v116", "v117", "v118", "v119", "v120", "v121", "v122", "v123", "v124", "v125", "v126", "v127", "v128", "v129", "v130", "v131", "v132", "v133", "v134", "v135", "v136", "v137", "v138", "v139", "v140", "v141", "v142", "v143", "v144", "v145", "v146", "v147", "v148", "v149", "v150", "v151", "v152", "v153", "v154", "v155", "v156", "v157", "v158", "v159", "v160", "v161", "v162", "v163", "v164", "v165", "v166", "v167", "v168", "v169", "v170", "v171", "v172", "v173", "v174", "v175", "v176", "v177", "v178", "v179", "v180", "v181", "v182", "v183", "v184", "v185", "v186", "v187", "v188", "v189", "v190", "v191", "v192", "v193", "v194", "v195", "v196", "v197", "v198", "v199", "v200", "v201", "v202", "v203", "v204", "v205", "v206", "v207", "v208", "v209", "v210", "v211", "v212", "v213", "v214", "v215", "v216", "v217", "v218", "v219", "v220", "v221", "v222", "v223", "v224", "v225", "v226", "v227", "v228", "v229", "v230", "v231", "v232", "v233", "v234", "v235", "v236", "v237", "v238", "v239", "v240", "v241", "v242", "v243", "v244", "v245", "v246", "v247", "v248", "v249", "v250", "v251", "v252", "v253", "v254", "v255", "a0", "a1", "a2", "a3", "a4", "a5", "a6", "a7", "a8", "a9", "a10", "a11", "a12", "a13", "a14", "a15", "a16", "a17", "a18", "a19", "a20", "a21", "a22", "a23", "a24", "a25", "a26", "a27", "a28", "a29", "a30", "a31", "a32", "a33", "a34", "a35", "a36", "a37", "a38", "a39", "a40", "a41", "a42", "a43", "a44", "a45", "a46", "a47", "a48", "a49", "a50", "a51", "a52", "a53", "a54", "a55", "a56", "a57", "a58", "a59", "a60", "a61", "a62", "a63", "a64", "a65", "a66", "a67", "a68", "a69", "a70", "a71", "a72", "a73", "a74", "a75", "a76", "a77", "a78", "a79", "a80", "a81", "a82", "a83", "a84", "a85", "a86", "a87", "a88", "a89", "a90", "a91", "a92", "a93", "a94", "a95", "a96", "a97", "a98", "a99", "a100", "a101", "a102", "a103", "a104", "a105", "a106", "a107", "a108", "a109", "a110", "a111", "a112", "a113", "a114", "a115", "a116", "a117", "a118", "a119", "a120", "a121", "a122", "a123", "a124", "a125", "a126", "a127", "a128", "a129", "a130", "a131", "a132", "a133", "a134", "a135", "a136", "a137", "a138", "a139", "a140", "a141", "a142", "a143", "a144", "a145", "a146", "a147", "a148", "a149", "a150", "a151", "a152", "a153", "a154", "a155", "a156", "a157", "a158", "a159", "a160", "a161", "a162", "a163", "a164", "a165", "a166", "a167", "a168", "a169", "a170", "a171", "a172", "a173", "a174", "a175", "a176", "a177", "a178", "a179", "a180", "a181", "a182", "a183", "a184", "a185", "a186", "a187", "a188", "a189", "a190", "a191", "a192", "a193", "a194", "a195", "a196", "a197", "a198", "a199", "a200", "a201", "a202", "a203", "a204", "a205", "a206", "a207", "a208", "a209", "a210", "a211", "a212", "a213", "a214", "a215", "a216", "a217", "a218", "a219", "a220", "a221", "a222", "a223", "a224", "a225", "a226", "a227", "a228", "a229", "a230", "a231", "a232", "a233", "a234", "a235", "a236", "a237", "a238", "a239", "a240", "a241", "a242", "a243", "a244", "a245", "a246", "a247", "a248", "a249", "a250", "a251", "a252", "a253", "a254", "a255", "s4", "s5", "s6", "s7", "s8", "s9", "s10", "s11", "s12", "s13", "s14", "s15", "s19", "s20", "s21", "s22", "s23", "s24", "s25", "s26", "s27", "s28", "s29", "s30", "s31", "s32", "s33", "s34", "s35", "s36", "s37", "s38", "s39", "s40", "s41", "s42", "s43", "s44", "s45", "s46", "s47", "s48", "s49", "s50", "s51", "s52", "s53", "s54", "s55", "s56", "s57", "s58", "s59", "s60", "s61", "s62", "s63", "s64", "s65", "s66", "s67", "s68", "s69", "s70", "s71", "s72", "s73", "s74", "s75", "s76", "s77", "s78", "s79", "s80", "s81", "s82", "s83", "s84", "s85", "s86", "s87", "s88", "s89", "s90", "s91", "s92", "s93", "s94", "s95", "s96", "s97", "s98", "s99", "vcc", "scc", "memory");
}
__global__ __launch_bounds__(256, 1) void k2_oproj(void* ws, const void* woA, const void* woB, void* out) {
  unsigned tid = threadIdx.x;
  asm volatile(R"ASM(
s_getpc_b64 s[12:13]
K2_pcw:
v_and_b32 v1, 63, v0
v_lshrrev_b32 v2, 6, v0
v_lshlrev_b32 v1, 7, v1
v_lshl_add_u32 v1, v2, 14, v1
v_add_u32 v2, 8192, v1
v_min_u32 v1, K2_end-K2_pcw-4, v1
v_min_u32 v2, K2_end-K2_pcw-4, v2
v_and_b32 v1, -4, v1
v_and_b32 v2, -4, v2
global_load_dword v3, v1, s[12:13]
global_load_dword v4, v2, s[12:13]
v_and_b32 v208, 63, v0
v_lshrrev_b32 v214, 6, v0
s_nop 0
v_readfirstlane_b32 s19, v214
s_load_dwordx8 s[20:27], s[16:17], 0x0
s_and_b32 s58, s18, 7
s_lshr_b32 s62, s58, 2
s_and_b32 s63, s58, 3
s_lshr_b32 s59, s18, 3
s_lshr_b32 s64, s59, 2
s_and_b32 s65, s59, 3
s_lshl_b32 s70, s62, 5
s_add_u32 s70, s70, s59
s_lshl_b32 s73, s65, 1
s_add_u32 s73, s73, s64
s_and_b32 s73, s73, 7
s_lshr_b32 s66, s19, 1
s_and_b32 s67, s19, 1
v_lshrrev_b32 v209, 4, v208
v_lshrrev_b32 v210, 2, v208
v_and_b32 v210, 3, v210
v_and_b32 v211, 3, v208
v_and_b32 v214, 1, v209
v_lshlrev_b32 v212, 1, v214
v_lshrrev_b32 v215, 1, v210
v_add_u32 v212, v212, v215
v_lshlrev_b32 v213, 3, v211
v_lshlrev_b32 v205, 4, v208
v_lshrrev_b32 v215, 1, v209
v_mul_u32_u24 v215, 3072, v215
v_lshl_add_u32 v216, v214, 2, v210
v_lshl_add_u32 v215, v216, 7, v215
v_add_u32 v215, v215, v213
s_and_b32 s58, s65, 1
s_lshl_b32 s58, s58, 1
s_mul_i32 s59, s67, 5
s_add_u32 s58, s58, s59
s_add_u32 s59, s58, 0
s_and_b32 s60, s59, 3
v_xor_b32 v216, s60, v212
v_lshl_add_u32 v216, v216, 5, v215
s_lshr_b32 s59, s59, 2
s_lshl_b32 s59, s59, 10
s_add_u32 s60, s59, 18432
v_add_u32 v180, s60, v216
s_add_u32 s60, s59, 79872
v_add_u32 v186, s60, v216
s_add_u32 s60, s59, 141312
v_add_u32 v192, s60, v216
s_add_u32 s59, s58, 1
s_and_b32 s60, s59, 3
v_xor_b32 v216, s60, v212
v_lshl_add_u32 v216, v216, 5, v215
s_lshr_b32 s59, s59, 2
s_lshl_b32 s59, s59, 10
s_add_u32 s60, s59, 18432
v_add_u32 v181, s60, v216
s_add_u32 s60, s59, 79872
v_add_u32 v187, s60, v216
s_add_u32 s60, s59, 141312
v_add_u32 v193, s60, v216
s_add_u32 s59, s58, 2
s_and_b32 s60, s59, 3
v_xor_b32 v216, s60, v212
v_lshl_add_u32 v216, v216, 5, v215
s_lshr_b32 s59, s59, 2
s_lshl_b32 s59, s59, 10
s_add_u32 s60, s59, 18432
v_add_u32 v182, s60, v216
s_add_u32 s60, s59, 79872
v_add_u32 v188, s60, v216
s_add_u32 s60, s59, 141312
v_add_u32 v194, s60, v216
s_add_u32 s59, s58, 3
s_and_b32 s60, s59, 3
v_xor_b32 v216, s60, v212
v_lshl_add_u32 v216, v216, 5, v215
s_lshr_b32 s59, s59, 2
s_lshl_b32 s59, s59, 10
s_add_u32 s60, s59, 18432
v_add_u32 v183, s60, v216
s_add_u32 s60, s59, 79872
v_add_u32 v189, s60, v216
s_add_u32 s60, s59, 141312
v_add_u32 v195, s60, v216
s_add_u32 s59, s58, 4
s_and_b32 s60, s59, 3
v_xor_b32 v216, s60, v212
v_lshl_add_u32 v216, v216, 5, v215
s_lshr_b32 s59, s59, 2
s_lshl_b32 s59, s59, 10
s_add_u32 s60, s59, 18432
v_add_u32 v184, s60, v216
s_add_u32 s60, s59, 79872
v_add_u32 v190, s60, v216
s_add_u32 s60, s59, 141312
v_add_u32 v196, s60, v216
s_mul_i32 s59, s66, 9216
s_add_u32 s60, s59, 0
v_add_u32 v185, s60, v205
s_add_u32 s60, s59, 61440
v_add_u32 v191, s60, v205
s_add_u32 s60, s59, 122880
v_add_u32 v197, s60, v205
v_add_u32 v198, 4096, v205
v_lshrrev_b32 v217, 3, v208
v_and_b32 v218, 7, v208
v_lshrrev_b32 v219, 4, v208
v_mul_u32_u24 v214, 1280, v217
v_lshlrev_b32 v215, 1, v219
v_xor_b32 v215, v218, v215
v_lshl_add_u32 v215, v215, 4, v214
v_add_u32 v202, 4096, v215
v_add_u32 v203, 3200, v215
v_add_u32 v204, 2304, v215
v_and_b32 v214, 15, v208
v_mul_u32_u24 v214, 2560, v214
v_lshl_add_u32 v206, v209, 3, v214
s_lshl_b32 s58, s19, 12
s_mov_b32 s52, s58
s_lshl_b32 s58, s19, 10
s_add_u32 s58, s58, 16384
s_cmp_lt_u32 s19, 2
s_cselect_b32 s53, s58, 153600
s_cselect_b32 s68, -1, 0
s_mul_i32 s58, s19, 3072
s_add_u32 s54, s58, 18432
s_waitcnt lgkmcnt(0)
s_cmp_eq_u32 s62, 0
s_cselect_b32 s22, s22, s24
s_cselect_b32 s23, s23, s25
s_sub_u32 s36, s20, 4096
s_subb_u32 s37, s21, 0
s_and_b32 s37, s37, 65535
s_mov_b32 s38, 56627200
s_mov_b32 s39, 131072
s_sub_u32 s40, s22, 4096
s_subb_u32 s41, s23, 0
s_and_b32 s41, s41, 65535
s_mov_b32 s42, 7868416
s_mov_b32 s43, 131072
s_sub_u32 s48, s26, 0
s_subb_u32 s49, s27, 0
s_and_b32 s49, s49, 65535
s_mov_b32 s50, 5898240
s_mov_b32 s51, 131072
s_add_u32 s44, s20, 67108864
s_addc_u32 s45, s21, 0
s_and_b32 s45, s45, 65535
s_mov_b32 s46, 25165824
s_mov_b32 s47, 131072
s_lshl_b32 s58, s70, 8
s_add_u32 s58, s58, 58720256
s_add_u32 s74, s20, s58
s_addc_u32 s75, s21, 0
s_lshl_b32 s58, s62, 4
s_lshl_b32 s59, s64, 1
s_add_u32 s58, s58, s59
s_mul_i32 s58, s58, 9216
s_mul_i32 s60, s63, 14155776
s_add_u32 s58, s58, s60
s_lshl_b32 s59, s19, 12
s_add_u32 s77, s58, s59
s_add_u32 s59, s19, 16
s_lshl_b32 s59, s59, 10
s_add_u32 s78, s58, s59
s_cmp_lt_u32 s19, 2
s_cselect_b32 s78, s78, s77
s_cselect_b32 s59, 0, 2146435072
v_add_u32 v199, s59, v198
s_mul_i32 s58, s63, 1536
s_lshl_b32 s59, s19, 3
s_add_u32 s58, s58, s59
s_mul_i32 s58, s58, 1280
s_lshr_b32 s59, s65, 1
s_mul_i32 s59, s59, 640
s_add_u32 s58, s58, s59
s_and_b32 s59, s65, 1
s_lshl_b32 s59, s59, 8
s_add_u32 s76, s58, s59
s_lshl_b32 s58, s70, 4
s_add_u32 s71, s58, s19
s_mul_i32 s58, s64, 288
s_mul_i32 s59, s66, 144
s_add_u32 s58, s58, s59
s_mul_i32 s58, s58, 2560
s_mul_i32 s59, s62, 640
s_mul_i32 s60, s65, 160
s_add_u32 s59, s59, s60
s_mul_i32 s60, s67, 80
s_add_u32 s59, s59, s60
s_lshl_b32 s59, s59, 1
s_add_u32 s72, s58, s59
s_lshr_b32 s11, s18, 3
v_lshrrev_b32 v208, 4, v205
s_lshl_b32 s60, s19, 6
v_add_u32 v209, s60, v208
v_min_u32 v209, 287, v209
v_mul_u32_u24 v210, 1821, v209
v_lshrrev_b32 v210, 16, v210
v_mul_u32_u24 v211, 36, v210
v_sub_u32 v211, v209, v211
s_mul_i32 s58, s11, 36
v_add_u32 v211, s58, v211
v_lshlrev_b32 v211, 7, v211
s_mov_b32 s59, 294912
v_mul_lo_u32 v210, s59, v210
v_add_u32 v200, v210, v211
v_add_u32 v200, 4096, v200
v_add_u32 v209, 256, v208
v_min_u32 v209, 287, v209
v_mul_u32_u24 v210, 1821, v209
v_lshrrev_b32 v210, 16, v210
v_mul_u32_u24 v211, 36, v210
v_sub_u32 v211, v209, v211
s_mul_i32 s58, s11, 36
v_add_u32 v211, s58, v211
v_lshlrev_b32 v211, 7, v211
s_mov_b32 s59, 294912
v_mul_lo_u32 v210, s59, v210
v_add_u32 v201, v210, v211
v_add_u32 v201, 4096, v201
s_sub_u32 s60, s19, 1
s_lshl_b32 s60, s60, 6
v_add_u32 v209, s60, v208
v_min_u32 v209, 79, v209
s_mul_i32 s58, s11, 80
v_add_u32 v209, s58, v209
v_lshlrev_b32 v209, 7, v209
v_add_u32 v209, 4096, v209
s_mul_i32 s8, s63, 14155776
s_mul_i32 s58, s62, 147456
s_add_u32 s8, s8, s58
s_mul_i32 s58, s63, 1966080
s_mov_b32 s59, 327680
s_cmp_eq_u32 s19, 0
s_cselect_b64 s[12:13], -1, 0
v_cndmask_b32 v201, v209, v201, s[12:13]
s_cselect_b32 s4, s36, s40
s_cselect_b32 s5, s37, s41
s_cselect_b32 s6, s38, s42
s_cselect_b32 s7, s39, s43
s_cselect_b32 s9, s8, s58
s_cselect_b32 s10, 2359296, s59
s_cmp_eq_u32 s19, 3
s_cselect_b32 s6, 0, s6
s_add_u32 s58, s73, 0
s_and_b32 s58, s58, 7
s_add_u32 s58, s58, 0
s_mul_i32 s59, s58, 40960
s_add_u32 s57, s76, s59
s_mul_i32 s59, s58, 294912
s_add_u32 s55, s77, s59
s_add_u32 s56, s78, s59
s_add_u32 m0, s54, 0
s_nop 0
buffer_load_dwordx4 v202, s[40:43], s57 offen lds
buffer_load_dwordx4 v203, s[40:43], s57 offen offset:1024 lds
buffer_load_dwordx4 v204, s[40:43], s57 offen offset:2048 lds
s_add_u32 m0, s52, 0
s_nop 0
buffer_load_dwordx4 v198, s[36:39], s55 offen lds
buffer_load_dwordx4 v198, s[36:39], s55 offen offset:1024 lds
buffer_load_dwordx4 v198, s[36:39], s55 offen offset:2048 lds
buffer_load_dwordx4 v198, s[36:39], s55 offen offset:3072 lds
s_and_b32 s60, s68, 0
s_add_u32 m0, s53, s60
s_nop 0
buffer_load_dwordx4 v199, s[36:39], s56 offen lds
s_add_u32 s58, s73, 1
s_and_b32 s58, s58, 7
s_add_u32 s58, s58, 0
s_mul_i32 s59, s58, 40960
s_add_u32 s57, s76, s59
s_mul_i32 s59, s58, 294912
s_add_u32 s55, s77, s59
s_add_u32 s56, s78, s59
s_add_u32 m0, s54, 30720
s_nop 0
buffer_load_dwordx4 v202, s[40:43], s57 offen lds
buffer_load_dwordx4 v203, s[40:43], s57 offen offset:1024 lds
buffer_load_dwordx4 v204, s[40:43], s57 offen offset:2048 lds
s_add_u32 m0, s52, 30720
s_nop 0
buffer_load_dwordx4 v198, s[36:39], s55 offen lds
buffer_load_dwordx4 v198, s[36:39], s55 offen offset:1024 lds
buffer_load_dwordx4 v198, s[36:39], s55 offen offset:2048 lds
buffer_load_dwordx4 v198, s[36:39], s55 offen offset:3072 lds
s_and_b32 s60, s68, 30720
s_add_u32 m0, s53, s60
s_nop 0
buffer_load_dwordx4 v199, s[36:39], s56 offen lds
s_add_u32 s58, s73, 2
s_and_b32 s58, s58, 7
s_add_u32 s58, s58, 0
s_mul_i32 s59, s58, 40960
s_add_u32 s57, s76, s59
s_mul_i32 s59, s58, 294912
s_add_u32 s55, s77, s59
s_add_u32 s56, s78, s59
s_add_u32 m0, s54, 61440
s_nop 0
buffer_load_dwordx4 v202, s[40:43], s57 offen lds
buffer_load_dwordx4 v203, s[40:43], s57 offen offset:1024 lds
buffer_load_dwordx4 v204, s[40:43], s57 offen offset:2048 lds
s_add_u32 m0, s52, 61440
s_nop 0
buffer_load_dwordx4 v198, s[36:39], s55 offen lds
buffer_load_dwordx4 v198, s[36:39], s55 offen offset:1024 lds
buffer_load_dwordx4 v198, s[36:39], s55 offen offset:2048 lds
buffer_load_dwordx4 v198, s[36:39], s55 offen offset:3072 lds
s_and_b32 s60, s68, 61440
s_add_u32 m0, s53, s60
s_nop 0
buffer_load_dwordx4 v199, s[36:39], s56 offen lds
s_add_u32 s58, s73, 3
s_and_b32 s58, s58, 7
s_add_u32 s58, s58, 0
s_mul_i32 s59, s58, 40960
s_add_u32 s57, s76, s59
s_mul_i32 s59, s58, 294912
s_add_u32 s55, s77, s59
s_add_u32 s56, s78, s59
s_add_u32 m0, s54, 92160
s_nop 0
buffer_load_dwordx4 v202, s[40:43], s57 offen lds
buffer_load_dwordx4 v203, s[40:43], s57 offen offset:1024 lds
buffer_load_dwordx4 v204, s[40:43], s57 offen offset:2048 lds
s_add_u32 m0, s52, 92160
s_nop 0
buffer_load_dwordx4 v198, s[36:39], s55 offen lds
buffer_load_dwordx4 v198, s[36:39], s55 offen offset:1024 lds
buffer_load_dwordx4 v198, s[36:39], s55 offen offset:2048 lds
buffer_load_dwordx4 v198, s[36:39], s55 offen offset:3072 lds
s_and_b32 s60, s68, 92160
s_add_u32 m0, s53, s60
s_nop 0
buffer_load_dwordx4 v199, s[36:39], s56 offen lds
s_add_u32 s58, s73, 4
s_and_b32 s58, s58, 7
s_add_u32 s58, s58, 0
s_mul_i32 s59, s58, 40960
s_add_u32 s57, s76, s59
s_mul_i32 s59, s58, 294912
s_add_u32 s55, s77, s59
s_add_u32 s56, s78, s59
s_add_u32 m0, s54, 122880
s_nop 0
buffer_load_dwordx4 v202, s[40:43], s57 offen lds
buffer_load_dwordx4 v203, s[40:43], s57 offen offset:1024 lds
buffer_load_dwordx4 v204, s[40:43], s57 offen offset:2048 lds
s_add_u32 m0, s52, 122880
s_nop 0
buffer_load_dwordx4 v198, s[36:39], s55 offen lds
buffer_load_dwordx4 v198, s[36:39], s55 offen offset:1024 lds
buffer_load_dwordx4 v198, s[36:39], s55 offen offset:2048 lds
buffer_load_dwordx4 v198, s[36:39], s55 offen offset:3072 lds
s_and_b32 s60, s68, 122880
s_add_u32 m0, s53, s60
s_nop 0
buffer_load_dwordx4 v199, s[36:39], s56 offen lds
s_waitcnt vmcnt(24) lgkmcnt(0)
s_barrier
ds_read_b64_tr_b16 a[0:1], v180 offset:0
ds_read_b64_tr_b16 a[2:3], v180 offset:6144
ds_read_b128 a[4:7], v185 offset:0
s_waitcnt lgkmcnt(0)
v_mfma_f32_16x16x32_bf16 v[0:3], a[0:3], a[4:7], 0
ds_read_b64_tr_b16 a[8:9], v181 offset:0
ds_read_b64_tr_b16 a[10:11], v181 offset:6144
s_waitcnt lgkmcnt(0)
v_mfma_f32_16x16x32_bf16 v[4:7], a[8:11], a[4:7], 0
ds_read_b64_tr_b16 a[12:13], v182 offset:0
ds_read_b64_tr_b16 a[14:15], v182 offset:6144
s_waitcnt lgkmcnt(0)
v_mfma_f32_16x16x32_bf16 v[8:11], a[12:15], a[4:7], 0
ds_read_b64_tr_b16 a[16:17], v183 offset:0
ds_read_b64_tr_b16 a[18:19], v183 offset:6144
s_waitcnt lgkmcnt(0)
v_mfma_f32_16x16x32_bf16 v[12:15], a[16:19], a[4:7], 0
ds_read_b64_tr_b16 a[20:21], v184 offset:0
ds_read_b64_tr_b16 a[22:23], v184 offset:6144
s_waitcnt lgkmcnt(0)
v_mfma_f32_16x16x32_bf16 v[16:19], a[20:23], a[4:7], 0
ds_read_b128 a[24:27], v185 offset:1024
ds_read_b128 a[28:31], v185 offset:2048
s_waitcnt lgkmcnt(1)
v_mfma_f32_16x16x32_bf16 v[20:23], a[0:3], a[24:27], 0
ds_read_b128 a[32:35], v185 offset:3072
v_mfma_f32_16x16x32_bf16 v[24:27], a[8:11], a[24:27], 0
ds_read_b128 a[36:39], v185 offset:4096
v_mfma_f32_16x16x32_bf16 v[28:31], a[12:15], a[24:27], 0
ds_read_b128 a[40:43], v185 offset:5120
v_mfma_f32_16x16x32_bf16 v[32:35], a[16:19], a[24:27], 0
ds_read_b128 a[44:47], v185 offset:6144
v_mfma_f32_16x16x32_bf16 v[36:39], a[20:23], a[24:27], 0
s_waitcnt lgkmcnt(4)
v_mfma_f32_16x16x32_bf16 v[40:43], a[0:3], a[28:31], 0
v_mfma_f32_16x16x32_bf16 v[44:47], a[8:11], a[28:31], 0
ds_read_b128 a[48:51], v185 offset:7168
v_mfma_f32_16x16x32_bf16 v[48:51], a[12:15], a[28:31], 0
v_mfma_f32_16x16x32_bf16 v[52:55], a[16:19], a[28:31], 0
s_waitcnt lgkmcnt(4)
v_mfma_f32_16x16x32_bf16 v[56:59], a[20:23], a[28:31], 0
v_mfma_f32_16x16x32_bf16 v[60:63], a[0:3], a[32:35], 0
v_mfma_f32_16x16x32_bf16 v[64:67], a[8:11], a[32:35], 0
ds_read_b128 a[52:55], v185 offset:8192
v_mfma_f32_16x16x32_bf16 v[68:71], a[12:15], a[32:35], 0
s_waitcnt lgkmcnt(2)
v_mfma_f32_16x16x32_bf16 v[72:75], a[16:19], a[32:35], 0
v_mfma_f32_16x16x32_bf16 v[76:79], a[20:23], a[32:35], 0
v_mfma_f32_16x16x32_bf16 v[80:83], a[0:3], a[36:39], 0
ds_read_b64_tr_b16 a[56:57], v180 offset:30720
v_mfma_f32_16x16x32_bf16 v[84:87], a[8:11], a[36:39], 0
ds_read_b64_tr_b16 a[58:59], v180 offset:36864
v_mfma_f32_16x16x32_bf16 v[88:91], a[12:15], a[36:39], 0
ds_read_b128 a[60:63], v185 offset:30720
v_mfma_f32_16x16x32_bf16 v[92:95], a[16:19], a[36:39], 0
ds_read_b64_tr_b16 a[64:65], v181 offset:30720
v_mfma_f32_16x16x32_bf16 v[96:99], a[20:23], a[36:39], 0
ds_read_b64_tr_b16 a[66:67], v181 offset:36864
v_mfma_f32_16x16x32_bf16 v[100:103], a[0:3], a[40:43], 0
ds_read_b64_tr_b16 a[68:69], v182 offset:30720
v_mfma_f32_16x16x32_bf16 v[104:107], a[8:11], a[40:43], 0
ds_read_b64_tr_b16 a[70:71], v182 offset:36864
v_mfma_f32_16x16x32_bf16 v[108:111], a[12:15], a[40:43], 0
ds_read_b64_tr_b16 a[72:73], v183 offset:30720
v_mfma_f32_16x16x32_bf16 v[112:115], a[16:19], a[40:43], 0
ds_read_b64_tr_b16 a[74:75], v183 offset:36864
v_mfma_f32_16x16x32_bf16 v[116:119], a[20:23], a[40:43], 0
ds_read_b64_tr_b16 a[76:77], v184 offset:30720
v_mfma_f32_16x16x32_bf16 v[120:123], a[0:3], a[44:47], 0
ds_read_b64_tr_b16 a[78:79], v184 offset:36864
v_mfma_f32_16x16x32_bf16 v[124:127], a[8:11], a[44:47], 0
ds_read_b128 a[80:83], v185 offset:31744
v_mfma_f32_16x16x32_bf16 v[128:131], a[12:15], a[44:47], 0
s_waitcnt lgkmcnt(7)
ds_read_b128 a[84:87], v185 offset:32768
v_mfma_f32_16x16x32_bf16 v[132:135], a[16:19], a[44:47], 0
v_mfma_f32_16x16x32_bf16 v[136:139], a[20:23], a[44:47], 0
v_mfma_f32_16x16x32_bf16 v[140:143], a[0:3], a[48:51], 0
ds_read_b128 a[88:91], v185 offset:33792
v_mfma_f32_16x16x32_bf16 v[144:147], a[8:11], a[48:51], 0
v_mfma_f32_16x16x32_bf16 v[148:151], a[12:15], a[48:51], 0
v_mfma_f32_16x16x32_bf16 v[152:155], a[16:19], a[48:51], 0
v_mfma_f32_16x16x32_bf16 v[156:159], a[20:23], a[48:51], 0
v_mfma_f32_16x16x32_bf16 v[160:163], a[0:3], a[52:55], 0
ds_read_b128 a[92:95], v185 offset:34816
v_mfma_f32_16x16x32_bf16 v[164:167], a[8:11], a[52:55], 0
v_mfma_f32_16x16x32_bf16 v[168:171], a[12:15], a[52:55], 0
v_mfma_f32_16x16x32_bf16 v[172:175], a[16:19], a[52:55], 0
v_mfma_f32_16x16x32_bf16 v[176:179], a[20:23], a[52:55], 0
s_waitcnt lgkmcnt(1)
v_mfma_f32_16x16x32_bf16 v[0:3], a[56:59], a[60:63], v[0:3]
ds_read_b128 a[4:7], v185 offset:35840
v_mfma_f32_16x16x32_bf16 v[4:7], a[64:67], a[60:63], v[4:7]
v_mfma_f32_16x16x32_bf16 v[8:11], a[68:71], a[60:63], v[8:11]
v_mfma_f32_16x16x32_bf16 v[12:15], a[72:75], a[60:63], v[12:15]
v_mfma_f32_16x16x32_bf16 v[16:19], a[76:79], a[60:63], v[16:19]
v_mfma_f32_16x16x32_bf16 v[20:23], a[56:59], a[80:83], v[20:23]
ds_read_b128 a[24:27], v185 offset:36864
v_mfma_f32_16x16x32_bf16 v[24:27], a[64:67], a[80:83], v[24:27]
v_mfma_f32_16x16x32_bf16 v[28:31], a[68:71], a[80:83], v[28:31]
v_mfma_f32_16x16x32_bf16 v[32:35], a[72:75], a[80:83], v[32:35]
v_mfma_f32_16x16x32_bf16 v[36:39], a[76:79], a[80:83], v[36:39]
v_mfma_f32_16x16x32_bf16 v[40:43], a[56:59], a[84:87], v[40:43]
ds_read_b128 a[28:31], v185 offset:37888
v_mfma_f32_16x16x32_bf16 v[44:47], a[64:67], a[84:87], v[44:47]
v_mfma_f32_16x16x32_bf16 v[48:51], a[68:71], a[84:87], v[48:51]
v_mfma_f32_16x16x32_bf16 v[52:55], a[72:75], a[84:87], v[52:55]
v_mfma_f32_16x16x32_bf16 v[56:59], a[76:79], a[84:87], v[56:59]
v_mfma_f32_16x16x32_bf16 v[60:63], a[56:59], a[88:91], v[60:63]
ds_read_b128 a[32:35], v185 offset:38912
v_mfma_f32_16x16x32_bf16 v[64:67], a[64:67], a[88:91], v[64:67]
v_mfma_f32_16x16x32_bf16 v[68:71], a[68:71], a[88:91], v[68:71]
s_waitcnt lgkmcnt(2)
v_mfma_f32_16x16x32_bf16 v[72:75], a[72:75], a[88:91], v[72:75]
v_mfma_f32_16x16x32_bf16 v[76:79], a[76:79], a[88:91], v[76:79]
v_mfma_f32_16x16x32_bf16 v[80:83], a[56:59], a[92:95], v[80:83]
v_mfma_f32_16x16x32_bf16 v[84:87], a[64:67], a[92:95], v[84:87]
v_mfma_f32_16x16x32_bf16 v[88:91], a[68:71], a[92:95], v[88:91]
v_mfma_f32_16x16x32_bf16 v[92:95], a[72:75], a[92:95], v[92:95]
v_mfma_f32_16x16x32_bf16 v[96:99], a[76:79], a[92:95], v[96:99]
v_mfma_f32_16x16x32_bf16 v[100:103], a[56:59], a[4:7], v[100:103]
v_mfma_f32_16x16x32_bf16 v[104:107], a[64:67], a[4:7], v[104:107]
v_mfma_f32_16x16x32_bf16 v[108:111], a[68:71], a[4:7], v[108:111]
v_mfma_f32_16x16x32_bf16 v[112:115], a[72:75], a[4:7], v[112:115]
v_mfma_f32_16x16x32_bf16 v[116:119], a[76:79], a[4:7], v[116:119]
v_mfma_f32_16x16x32_bf16 v[120:123], a[56:59], a[24:27], v[120:123]
s_waitcnt vmcnt(8) lgkmcnt(0)
s_barrier
v_mfma_f32_16x16x32_bf16 v[124:127], a[64:67], a[24:27], v[124:127]
ds_read_b64_tr_b16 a[36:37], v186 offset:0
v_mfma_f32_16x16x32_bf16 v[128:131], a[68:71], a[24:27], v[128:131]
ds_read_b64_tr_b16 a[38:39], v186 offset:6144
v_mfma_f32_16x16x32_bf16 v[132:135], a[72:75], a[24:27], v[132:135]
ds_read_b128 a[40:43], v191 offset:0
v_mfma_f32_16x16x32_bf16 v[136:139], a[76:79], a[24:27], v[136:139]
ds_read_b64_tr_b16 a[44:45], v187 offset:0
ds_read_b64_tr_b16 a[46:47], v187 offset:6144
v_mfma_f32_16x16x32_bf16 v[140:143], a[56:59], a[28:31], v[140:143]
ds_read_b64_tr_b16 a[48:49], v188 offset:0
ds_read_b64_tr_b16 a[50:51], v188 offset:6144
v_mfma_f32_16x16x32_bf16 v[144:147], a[64:67], a[28:31], v[144:147]
ds_read_b64_tr_b16 a[0:1], v189 offset:0
ds_read_b64_tr_b16 a[2:3], v189 offset:6144
v_mfma_f32_16x16x32_bf16 v[148:151], a[68:71], a[28:31], v[148:151]
ds_read_b64_tr_b16 a[8:9], v190 offset:0
ds_read_b64_tr_b16 a[10:11], v190 offset:6144
v_mfma_f32_16x16x32_bf16 v[152:155], a[72:75], a[28:31], v[152:155]
ds_read_b128 a[12:15], v191 offset:1024
s_add_u32 s58, s73, 5
v_mfma_f32_16x16x32_bf16 v[156:159], a[76:79], a[28:31], v[156:159]
s_and_b32 s58, s58, 7
s_add_u32 s58, s58, 0
s_mul_i32 s59, s58, 40960
v_mfma_f32_16x16x32_bf16 v[160:163], a[56:59], a[32:35], v[160:163]
s_add_u32 s57, s76, s59
s_mul_i32 s59, s58, 294912
s_add_u32 s55, s77, s59
v_mfma_f32_16x16x32_bf16 v[164:167], a[64:67], a[32:35], v[164:167]
s_add_u32 s56, s78, s59
s_add_u32 m0, s54, 0
v_mfma_f32_16x16x32_bf16 v[168:171], a[68:71], a[32:35], v[168:171]
s_waitcnt lgkmcnt(7)
v_mfma_f32_16x16x32_bf16 v[172:175], a[72:75], a[32:35], v[172:175]
buffer_load_dwordx4 v202, s[40:43], s57 offen lds
v_mfma_f32_16x16x32_bf16 v[176:179], a[76:79], a[32:35], v[176:179]
s_waitcnt lgkmcnt(3)
ds_read_b128 a[16:19], v191 offset:2048
v_mfma_f32_16x16x32_bf16 v[0:3], a[36:39], a[40:43], v[0:3]
buffer_load_dwordx4 v203, s[40:43], s57 offen offset:1024 lds
v_mfma_f32_16x16x32_bf16 v[4:7], a[44:47], a[40:43], v[4:7]
s_waitcnt lgkmcnt(1)
ds_read_b128 a[20:23], v191 offset:3072
v_mfma_f32_16x16x32_bf16 v[8:11], a[48:51], a[40:43], v[8:11]
buffer_load_dwordx4 v204, s[40:43], s57 offen offset:2048 lds
v_mfma_f32_16x16x32_bf16 v[12:15], a[0:3], a[40:43], v[12:15]
ds_read_b128 a[52:55], v191 offset:4096
s_add_u32 m0, s52, 0
v_mfma_f32_16x16x32_bf16 v[16:19], a[8:11], a[40:43], v[16:19]
buffer_load_dwordx4 v198, s[36:39], s55 offen lds
v_mfma_f32_16x16x32_bf16 v[20:23], a[36:39], a[12:15], v[20:23]
ds_read_b128 a[60:63], v191 offset:5120
v_mfma_f32_16x16x32_bf16 v[24:27], a[44:47], a[12:15], v[24:27]
buffer_load_dwordx4 v198, s[36:39], s55 offen offset:1024 lds
v_mfma_f32_16x16x32_bf16 v[28:31], a[48:51], a[12:15], v[28:31]
s_waitcnt lgkmcnt(3)
ds_read_b128 a[80:83], v191 offset:6144
v_mfma_f32_16x16x32_bf16 v[32:35], a[0:3], a[12:15], v[32:35]
v_mfma_f32_16x16x32_bf16 v[36:39], a[8:11], a[12:15], v[36:39]
v_mfma_f32_16x16x32_bf16 v[40:43], a[36:39], a[16:19], v[40:43]
ds_read_b128 a[84:87], v191 offset:7168
v_mfma_f32_16x16x32_bf16 v[44:47], a[44:47], a[16:19], v[44:47]
buffer_load_dwordx4 v198, s[36:39], s55 offen offset:2048 lds
v_mfma_f32_16x16x32_bf16 v[48:51], a[48:51], a[16:19], v[48:51]
s_waitcnt lgkmcnt(3)
v_mfma_f32_16x16x32_bf16 v[52:55], a[0:3], a[16:19], v[52:55]
v_mfma_f32_16x16x32_bf16 v[56:59], a[8:11], a[16:19], v[56:59]
v_mfma_f32_16x16x32_bf16 v[60:63], a[36:39], a[20:23], v[60:63]
ds_read_b128 a[88:91], v191 offset:8192
v_mfma_f32_16x16x32_bf16 v[64:67], a[44:47], a[20:23], v[64:67]
buffer_load_dwordx4 v198, s[36:39], s55 offen offset:3072 lds
v_mfma_f32_16x16x32_bf16 v[68:71], a[48:51], a[20:23], v[68:71]
v_mfma_f32_16x16x32_bf16 v[72:75], a[0:3], a[20:23], v[72:75]
v_mfma_f32_16x16x32_bf16 v[76:79], a[8:11], a[20:23], v[76:79]
v_mfma_f32_16x16x32_bf16 v[80:83], a[36:39], a[52:55], v[80:83]
ds_read_b64_tr_b16 a[92:93], v186 offset:30720
v_mfma_f32_16x16x32_bf16 v[84:87], a[44:47], a[52:55], v[84:87]
s_and_b32 s60, s68, 0
s_add_u32 m0, s53, s60
v_mfma_f32_16x16x32_bf16 v[88:91], a[48:51], a[52:55], v[88:91]
s_waitcnt lgkmcnt(2)
v_mfma_f32_16x16x32_bf16 v[92:95], a[0:3], a[52:55], v[92:95]
buffer_load_dwordx4 v199, s[36:39], s56 offen lds
v_mfma_f32_16x16x32_bf16 v[96:99], a[8:11], a[52:55], v[96:99]
ds_read_b64_tr_b16 a[94:95], v186 offset:36864
v_mfma_f32_16x16x32_bf16 v[100:103], a[36:39], a[60:63], v[100:103]
ds_read_b128 a[4:7], v191 offset:30720
v_mfma_f32_16x16x32_bf16 v[104:107], a[44:47], a[60:63], v[104:107]
ds_read_b64_tr_b16 a[24:25], v187 offset:30720
v_mfma_f32_16x16x32_bf16 v[108:111], a[48:51], a[60:63], v[108:111]
ds_read_b64_tr_b16 a[26:27], v187 offset:36864
v_mfma_f32_16x16x32_bf16 v[112:115], a[0:3], a[60:63], v[112:115]
ds_read_b64_tr_b16 a[28:29], v188 offset:30720
v_mfma_f32_16x16x32_bf16 v[116:119], a[8:11], a[60:63], v[116:119]
ds_read_b64_tr_b16 a[30:31], v188 offset:36864
v_mfma_f32_16x16x32_bf16 v[120:123], a[36:39], a[80:83], v[120:123]
ds_read_b64_tr_b16 a[56:57], v189 offset:30720
v_mfma_f32_16x16x32_bf16 v[124:127], a[44:47], a[80:83], v[124:127]
ds_read_b64_tr_b16 a[58:59], v189 offset:36864
v_mfma_f32_16x16x32_bf16 v[128:131], a[48:51], a[80:83], v[128:131]
ds_read_b64_tr_b16 a[64:65], v190 offset:30720
v_mfma_f32_16x16x32_bf16 v[132:135], a[0:3], a[80:83], v[132:135]
ds_read_b64_tr_b16 a[66:67], v190 offset:36864
s_add_u32 s58, s73, 6
v_mfma_f32_16x16x32_bf16 v[136:139], a[8:11], a[80:83], v[136:139]
s_and_b32 s58, s58, 7
s_add_u32 s58, s58, 0
s_mul_i32 s59, s58, 40960
v_mfma_f32_16x16x32_bf16 v[140:143], a[36:39], a[84:87], v[140:143]
s_add_u32 s57, s76, s59
s_mul_i32 s59, s58, 294912
s_add_u32 s55, s77, s59
v_mfma_f32_16x16x32_bf16 v[144:147], a[44:47], a[84:87], v[144:147]
s_add_u32 s56, s78, s59
s_add_u32 m0, s54, 30720
v_mfma_f32_16x16x32_bf16 v[148:151], a[48:51], a[84:87], v[148:151]
s_waitcnt lgkmcnt(4)
v_mfma_f32_16x16x32_bf16 v[152:155], a[0:3], a[84:87], v[152:155]
buffer_load_dwordx4 v202, s[40:43], s57 offen lds
v_mfma_f32_16x16x32_bf16 v[156:159], a[8:11], a[84:87], v[156:159]
ds_read_b128 a[68:71], v191 offset:31744
v_mfma_f32_16x16x32_bf16 v[160:163], a[36:39], a[88:91], v[160:163]
buffer_load_dwordx4 v203, s[40:43], s57 offen offset:1024 lds
v_mfma_f32_16x16x32_bf16 v[164:167], a[44:47], a[88:91], v[164:167]
ds_read_b128 a[72:75], v191 offset:32768
v_mfma_f32_16x16x32_bf16 v[168:171], a[48:51], a[88:91], v[168:171]
buffer_load_dwordx4 v204, s[40:43], s57 offen offset:2048 lds
v_mfma_f32_16x16x32_bf16 v[172:175], a[0:3], a[88:91], v[172:175]
ds_read_b128 a[76:79], v191 offset:33792
s_add_u32 m0, s52, 30720
v_mfma_f32_16x16x32_bf16 v[176:179], a[8:11], a[88:91], v[176:179]
buffer_load_dwordx4 v198, s[36:39], s55 offen lds
v_mfma_f32_16x16x32_bf16 v[0:3], a[92:95], a[4:7], v[0:3]
s_waitcnt lgkmcnt(3)
ds_read_b128 a[32:35], v191 offset:34816
v_mfma_f32_16x16x32_bf16 v[4:7], a[24:27], a[4:7], v[4:7]
buffer_load_dwordx4 v198, s[36:39], s55 offen offset:1024 lds
v_mfma_f32_16x16x32_bf16 v[8:11], a[28:31], a[4:7], v[8:11]
s_waitcnt lgkmcnt(3)
ds_read_b128 a[40:43], v191 offset:35840
v_mfma_f32_16x16x32_bf16 v[12:15], a[56:59], a[4:7], v[12:15]
v_mfma_f32_16x16x32_bf16 v[16:19], a[64:67], a[4:7], v[16:19]
v_mfma_f32_16x16x32_bf16 v[20:23], a[92:95], a[68:71], v[20:23]
ds_read_b128 a[12:15], v191 offset:36864
v_mfma_f32_16x16x32_bf16 v[24:27], a[24:27], a[68:71], v[24:27]
buffer_load_dwordx4 v198, s[36:39], s55 offen offset:2048 lds
v_mfma_f32_16x16x32_bf16 v[28:31], a[28:31], a[68:71], v[28:31]
s_waitcnt lgkmcnt(3)
v_mfma_f32_16x16x32_bf16 v[32:35], a[56:59], a[68:71], v[32:35]
v_mfma_f32_16x16x32_bf16 v[36:39], a[64:67], a[68:71], v[36:39]
v_mfma_f32_16x16x32_bf16 v[40:43], a[92:95], a[72:75], v[40:43]
ds_read_b128 a[16:19], v191 offset:37888
v_mfma_f32_16x16x32_bf16 v[44:47], a[24:27], a[72:75], v[44:47]
buffer_load_dwordx4 v198, s[36:39], s55 offen offset:3072 lds
v_mfma_f32_16x16x32_bf16 v[48:51], a[28:31], a[72:75], v[48:51]
v_mfma_f32_16x16x32_bf16 v[52:55], a[56:59], a[72:75], v[52:55]
v_mfma_f32_16x16x32_bf16 v[56:59], a[64:67], a[72:75], v[56:59]
v_mfma_f32_16x16x32_bf16 v[60:63], a[92:95], a[76:79], v[60:63]
ds_read_b128 a[20:23], v191 offset:38912
v_mfma_f32_16x16x32_bf16 v[64:67], a[24:27], a[76:79], v[64:67]
s_and_b32 s60, s68, 30720
s_add_u32 m0, s53, s60
v_mfma_f32_16x16x32_bf16 v[68:71], a[28:31], a[76:79], v[68:71]
s_waitcnt lgkmcnt(2)
v_mfma_f32_16x16x32_bf16 v[72:75], a[56:59], a[76:79], v[72:75]
buffer_load_dwordx4 v199, s[36:39], s56 offen lds
v_mfma_f32_16x16x32_bf16 v[76:79], a[64:67], a[76:79], v[76:79]
v_mfma_f32_16x16x32_bf16 v[80:83], a[92:95], a[32:35], v[80:83]
v_mfma_f32_16x16x32_bf16 v[84:87], a[24:27], a[32:35], v[84:87]
v_mfma_f32_16x16x32_bf16 v[88:91], a[28:31], a[32:35], v[88:91]
v_mfma_f32_16x16x32_bf16 v[92:95], a[56:59], a[32:35], v[92:95]
v_mfma_f32_16x16x32_bf16 v[96:99], a[64:67], a[32:35], v[96:99]
v_mfma_f32_16x16x32_bf16 v[100:103], a[92:95], a[40:43], v[100:103]
v_mfma_f32_16x16x32_bf16 v[104:107], a[24:27], a[40:43], v[104:107]
v_mfma_f32_16x16x32_bf16 v[108:111], a[28:31], a[40:43], v[108:111]
v_mfma_f32_16x16x32_bf16 v[112:115], a[56:59], a[40:43], v[112:115]
v_mfma_f32_16x16x32_bf16 v[116:119], a[64:67], a[40:43], v[116:119]
v_mfma_f32_16x16x32_bf16 v[120:123], a[92:95], a[12:15], v[120:123]
s_waitcnt vmcnt(8) lgkmcnt(0)
s_barrier
v_mfma_f32_16x16x32_bf16 v[124:127], a[24:27], a[12:15], v[124:127]
ds_read_b64_tr_b16 a[52:53], v192 offset:0
v_mfma_f32_16x16x32_bf16 v[128:131], a[28:31], a[12:15], v[128:131]
ds_read_b64_tr_b16 a[54:55], v192 offset:6144
v_mfma_f32_16x16x32_bf16 v[132:135], a[56:59], a[12:15], v[132:135]
ds_read_b128 a[60:63], v197 offset:0
v_mfma_f32_16x16x32_bf16 v[136:139], a[64:67], a[12:15], v[136:139]
ds_read_b64_tr_b16 a[80:81], v193 offset:0
ds_read_b64_tr_b16 a[82:83], v193 offset:6144
v_mfma_f32_16x16x32_bf16 v[140:143], a[92:95], a[16:19], v[140:143]
ds_read_b64_tr_b16 a[84:85], v194 offset:0
ds_read_b64_tr_b16 a[86:87], v194 offset:6144
v_mfma_f32_16x16x32_bf16 v[144:147], a[24:27], a[16:19], v[144:147]
ds_read_b64_tr_b16 a[36:37], v195 offset:0
ds_read_b64_tr_b16 a[38:39], v195 offset:6144
v_mfma_f32_16x16x32_bf16 v[148:151], a[28:31], a[16:19], v[148:151]
ds_read_b64_tr_b16 a[44:45], v196 offset:0
ds_read_b64_tr_b16 a[46:47], v196 offset:6144
v_mfma_f32_16x16x32_bf16 v[152:155], a[56:59], a[16:19], v[152:155]
ds_read_b128 a[48:51], v197 offset:1024
s_add_u32 s58, s73, 7
v_mfma_f32_16x16x32_bf16 v[156:159], a[64:67], a[16:19], v[156:159]
s_and_b32 s58, s58, 7
s_add_u32 s58, s58, 0
s_mul_i32 s59, s58, 40960
v_mfma_f32_16x16x32_bf16 v[160:163], a[92:95], a[20:23], v[160:163]
s_add_u32 s57, s76, s59
s_mul_i32 s59, s58, 294912
s_add_u32 s55, s77, s59
v_mfma_f32_16x16x32_bf16 v[164:167], a[24:27], a[20:23], v[164:167]
s_add_u32 s56, s78, s59
s_add_u32 m0, s54, 61440
v_mfma_f32_16x16x32_bf16 v[168:171], a[28:31], a[20:23], v[168:171]
s_waitcnt lgkmcnt(7)
v_mfma_f32_16x16x32_bf16 v[172:175], a[56:59], a[20:23], v[172:175]
buffer_load_dwordx4 v202, s[40:43], s57 offen lds
v_mfma_f32_16x16x32_bf16 v[176:179], a[64:67], a[20:23], v[176:179]
s_waitcnt lgkmcnt(3)
ds_read_b128 a[0:3], v197 offset:2048
v_mfma_f32_16x16x32_bf16 v[0:3], a[52:55], a[60:63], v[0:3]
buffer_load_dwordx4 v203, s[40:43], s57 offen offset:1024 lds
v_mfma_f32_16x16x32_bf16 v[4:7], a[80:83], a[60:63], v[4:7]
s_waitcnt lgkmcnt(1)
ds_read_b128 a[8:11], v197 offset:3072
v_mfma_f32_16x16x32_bf16 v[8:11], a[84:87], a[60:63], v[8:11]
buffer_load_dwordx4 v204, s[40:43], s57 offen offset:2048 lds
v_mfma_f32_16x16x32_bf16 v[12:15], a[36:39], a[60:63], v[12:15]
ds_read_b128 a[88:91], v197 offset:4096
s_add_u32 m0, s52, 61440
v_mfma_f32_16x16x32_bf16 v[16:19], a[44:47], a[60:63], v[16:19]
buffer_load_dwordx4 v198, s[36:39], s55 offen lds
v_mfma_f32_16x16x32_bf16 v[20:23], a[52:55], a[48:51], v[20:23]
ds_read_b128 a[4:7], v197 offset:5120
v_mfma_f32_16x16x32_bf16 v[24:27], a[80:83], a[48:51], v[24:27]
buffer_load_dwordx4 v198, s[36:39], s55 offen offset:1024 lds
v_mfma_f32_16x16x32_bf16 v[28:31], a[84:87], a[48:51], v[28:31]
s_waitcnt lgkmcnt(3)
ds_read_b128 a[68:71], v197 offset:6144
v_mfma_f32_16x16x32_bf16 v[32:35], a[36:39], a[48:51], v[32:35]
v_mfma_f32_16x16x32_bf16 v[36:39], a[44:47], a[48:51], v[36:39]
v_mfma_f32_16x16x32_bf16 v[40:43], a[52:55], a[0:3], v[40:43]
ds_read_b128 a[72:75], v197 offset:7168
v_mfma_f32_16x16x32_bf16 v[44:47], a[80:83], a[0:3], v[44:47]
buffer_load_dwordx4 v198, s[36:39], s55 offen offset:2048 lds
v_mfma_f32_16x16x32_bf16 v[48:51], a[84:87], a[0:3], v[48:51]
s_waitcnt lgkmcnt(3)
v_mfma_f32_16x16x32_bf16 v[52:55], a[36:39], a[0:3], v[52:55]
v_mfma_f32_16x16x32_bf16 v[56:59], a[44:47], a[0:3], v[56:59]
v_mfma_f32_16x16x32_bf16 v[60:63], a[52:55], a[8:11], v[60:63]
ds_read_b128 a[76:79], v197 offset:8192
v_mfma_f32_16x16x32_bf16 v[64:67], a[80:83], a[8:11], v[64:67]
buffer_load_dwordx4 v198, s[36:39], s55 offen offset:3072 lds
v_mfma_f32_16x16x32_bf16 v[68:71], a[84:87], a[8:11], v[68:71]
v_mfma_f32_16x16x32_bf16 v[72:75], a[36:39], a[8:11], v[72:75]
v_mfma_f32_16x16x32_bf16 v[76:79], a[44:47], a[8:11], v[76:79]
v_mfma_f32_16x16x32_bf16 v[80:83], a[52:55], a[88:91], v[80:83]
ds_read_b64_tr_b16 a[32:33], v180 offset:0
v_mfma_f32_16x16x32_bf16 v[84:87], a[80:83], a[88:91], v[84:87]
s_and_b32 s60, s68, 61440
s_add_u32 m0, s53, s60
v_mfma_f32_16x16x32_bf16 v[88:91], a[84:87], a[88:91], v[88:91]
s_waitcnt lgkmcnt(2)
v_mfma_f32_16x16x32_bf16 v[92:95], a[36:39], a[88:91], v[92:95]
buffer_load_dwordx4 v199, s[36:39], s56 offen lds
v_mfma_f32_16x16x32_bf16 v[96:99], a[44:47], a[88:91], v[96:99]
ds_read_b64_tr_b16 a[34:35], v180 offset:6144
v_mfma_f32_16x16x32_bf16 v[100:103], a[52:55], a[4:7], v[100:103]
ds_read_b128 a[40:43], v185 offset:0
v_mfma_f32_16x16x32_bf16 v[104:107], a[80:83], a[4:7], v[104:107]
ds_read_b64_tr_b16 a[12:13], v181 offset:0
v_mfma_f32_16x16x32_bf16 v[108:111], a[84:87], a[4:7], v[108:111]
ds_read_b64_tr_b16 a[14:15], v181 offset:6144
v_mfma_f32_16x16x32_bf16 v[112:115], a[36:39], a[4:7], v[112:115]
ds_read_b64_tr_b16 a[16:17], v182 offset:0
v_mfma_f32_16x16x32_bf16 v[116:119], a[44:47], a[4:7], v[116:119]
ds_read_b64_tr_b16 a[18:19], v182 offset:6144
v_mfma_f32_16x16x32_bf16 v[120:123], a[52:55], a[68:71], v[120:123]
ds_read_b64_tr_b16 a[92:93], v183 offset:0
v_mfma_f32_16x16x32_bf16 v[124:127], a[80:83], a[68:71], v[124:127]
ds_read_b64_tr_b16 a[94:95], v183 offset:6144
v_mfma_f32_16x16x32_bf16 v[128:131], a[84:87], a[68:71], v[128:131]
ds_read_b64_tr_b16 a[24:25], v184 offset:0
v_mfma_f32_16x16x32_bf16 v[132:135], a[36:39], a[68:71], v[132:135]
ds_read_b64_tr_b16 a[26:27], v184 offset:6144
s_add_u32 s58, s73, 0
v_mfma_f32_16x16x32_bf16 v[136:139], a[44:47], a[68:71], v[136:139]
s_and_b32 s58, s58, 7
s_add_u32 s58, s58, 8
s_mul_i32 s59, s58, 40960
v_mfma_f32_16x16x32_bf16 v[140:143], a[52:55], a[72:75], v[140:143]
s_add_u32 s57, s76, s59
s_mul_i32 s59, s58, 294912
s_add_u32 s55, s77, s59
v_mfma_f32_16x16x32_bf16 v[144:147], a[80:83], a[72:75], v[144:147]
s_add_u32 s56, s78, s59
s_add_u32 m0, s54, 92160
v_mfma_f32_16x16x32_bf16 v[148:151], a[84:87], a[72:75], v[148:151]
s_waitcnt lgkmcnt(4)
v_mfma_f32_16x16x32_bf16 v[152:155], a[36:39], a[72:75], v[152:155]
buffer_load_dwordx4 v202, s[40:43], s57 offen lds
v_mfma_f32_16x16x32_bf16 v[156:159], a[44:47], a[72:75], v[156:159]
ds_read_b128 a[28:31], v185 offset:1024
v_mfma_f32_16x16x32_bf16 v[160:163], a[52:55], a[76:79], v[160:163]
buffer_load_dwordx4 v203, s[40:43], s57 offen offset:1024 lds
v_mfma_f32_16x16x32_bf16 v[164:167], a[80:83], a[76:79], v[164:167]
ds_read_b128 a[56:59], v185 offset:2048
v_mfma_f32_16x16x32_bf16 v[168:171], a[84:87], a[76:79], v[168:171]
buffer_load_dwordx4 v204, s[40:43], s57 offen offset:2048 lds
v_mfma_f32_16x16x32_bf16 v[172:175], a[36:39], a[76:79], v[172:175]
ds_read_b128 a[64:67], v185 offset:3072
s_add_u32 m0, s52, 92160
v_mfma_f32_16x16x32_bf16 v[176:179], a[44:47], a[76:79], v[176:179]
buffer_load_dwordx4 v198, s[36:39], s55 offen lds
v_mfma_f32_16x16x32_bf16 v[0:3], a[32:35], a[40:43], v[0:3]
s_waitcnt lgkmcnt(3)
ds_read_b128 a[20:23], v185 offset:4096
v_mfma_f32_16x16x32_bf16 v[4:7], a[12:15], a[40:43], v[4:7]
buffer_load_dwordx4 v198, s[36:39], s55 offen offset:1024 lds
v_mfma_f32_16x16x32_bf16 v[8:11], a[16:19], a[40:43], v[8:11]
s_waitcnt lgkmcnt(3)
ds_read_b128 a[60:63], v185 offset:5120
v_mfma_f32_16x16x32_bf16 v[12:15], a[92:95], a[40:43], v[12:15]
v_mfma_f32_16x16x32_bf16 v[16:19], a[24:27], a[40:43], v[16:19]
v_mfma_f32_16x16x32_bf16 v[20:23], a[32:35], a[28:31], v[20:23]
ds_read_b128 a[48:51], v185 offset:6144
v_mfma_f32_16x16x32_bf16 v[24:27], a[12:15], a[28:31], v[24:27]
buffer_load_dwordx4 v198, s[36:39], s55 offen offset:2048 lds
v_mfma_f32_16x16x32_bf16 v[28:31], a[16:19], a[28:31], v[28:31]
s_waitcnt lgkmcnt(3)
v_mfma_f32_16x16x32_bf16 v[32:35], a[92:95], a[28:31], v[32:35]
v_mfma_f32_16x16x32_bf16 v[36:39], a[24:27], a[28:31], v[36:39]
v_mfma_f32_16x16x32_bf16 v[40:43], a[32:35], a[56:59], v[40:43]
ds_read_b128 a[0:3], v185 offset:7168
v_mfma_f32_16x16x32_bf16 v[44:47], a[12:15], a[56:59], v[44:47]
buffer_load_dwordx4 v198, s[36:39], s55 offen offset:3072 lds
v_mfma_f32_16x16x32_bf16 v[48:51], a[16:19], a[56:59], v[48:51]
v_mfma_f32_16x16x32_bf16 v[52:55], a[92:95], a[56:59], v[52:55]
v_mfma_f32_16x16x32_bf16 v[56:59], a[24:27], a[56:59], v[56:59]
v_mfma_f32_16x16x32_bf16 v[60:63], a[32:35], a[64:67], v[60:63]
ds_read_b128 a[8:11], v185 offset:8192
v_mfma_f32_16x16x32_bf16 v[64:67], a[12:15], a[64:67], v[64:67]
s_and_b32 s60, s68, 92160
s_add_u32 m0, s53, s60
v_mfma_f32_16x16x32_bf16 v[68:71], a[16:19], a[64:67], v[68:71]
s_waitcnt lgkmcnt(2)
v_mfma_f32_16x16x32_bf16 v[72:75], a[92:95], a[64:67], v[72:75]
buffer_load_dwordx4 v199, s[36:39], s56 offen lds
v_mfma_f32_16x16x32_bf16 v[76:79], a[24:27], a[64:67], v[76:79]
v_mfma_f32_16x16x32_bf16 v[80:83], a[32:35], a[20:23], v[80:83]
v_mfma_f32_16x16x32_bf16 v[84:87], a[12:15], a[20:23], v[84:87]
v_mfma_f32_16x16x32_bf16 v[88:91], a[16:19], a[20:23], v[88:91]
v_mfma_f32_16x16x32_bf16 v[92:95], a[92:95], a[20:23], v[92:95]
v_mfma_f32_16x16x32_bf16 v[96:99], a[24:27], a[20:23], v[96:99]
v_mfma_f32_16x16x32_bf16 v[100:103], a[32:35], a[60:63], v[100:103]
v_mfma_f32_16x16x32_bf16 v[104:107], a[12:15], a[60:63], v[104:107]
v_mfma_f32_16x16x32_bf16 v[108:111], a[16:19], a[60:63], v[108:111]
v_mfma_f32_16x16x32_bf16 v[112:115], a[92:95], a[60:63], v[112:115]
v_mfma_f32_16x16x32_bf16 v[116:119], a[24:27], a[60:63], v[116:119]
v_mfma_f32_16x16x32_bf16 v[120:123], a[32:35], a[48:51], v[120:123]
s_waitcnt vmcnt(8) lgkmcnt(0)
s_barrier
v_mfma_f32_16x16x32_bf16 v[124:127], a[12:15], a[48:51], v[124:127]
ds_read_b64_tr_b16 a[88:89], v180 offset:30720
v_mfma_f32_16x16x32_bf16 v[128:131], a[16:19], a[48:51], v[128:131]
ds_read_b64_tr_b16 a[90:91], v180 offset:36864
v_mfma_f32_16x16x32_bf16 v[132:135], a[92:95], a[48:51], v[132:135]
ds_read_b128 a[4:7], v185 offset:30720
v_mfma_f32_16x16x32_bf16 v[136:139], a[24:27], a[48:51], v[136:139]
ds_read_b64_tr_b16 a[68:69], v181 offset:30720
ds_read_b64_tr_b16 a[70:71], v181 offset:36864
v_mfma_f32_16x16x32_bf16 v[140:143], a[32:35], a[0:3], v[140:143]
ds_read_b64_tr_b16 a[72:73], v182 offset:30720
ds_read_b64_tr_b16 a[74:75], v182 offset:36864
v_mfma_f32_16x16x32_bf16 v[144:147], a[12:15], a[0:3], v[144:147]
ds_read_b64_tr_b16 a[52:53], v183 offset:30720
ds_read_b64_tr_b16 a[54:55], v183 offset:36864
v_mfma_f32_16x16x32_bf16 v[148:151], a[16:19], a[0:3], v[148:151]
ds_read_b64_tr_b16 a[80:81], v184 offset:30720
ds_read_b64_tr_b16 a[82:83], v184 offset:36864
v_mfma_f32_16x16x32_bf16 v[152:155], a[92:95], a[0:3], v[152:155]
ds_read_b128 a[84:87], v185 offset:31744
s_add_u32 s58, s73, 1
v_mfma_f32_16x16x32_bf16 v[156:159], a[24:27], a[0:3], v[156:159]
s_and_b32 s58, s58, 7
s_add_u32 s58, s58, 8
s_mul_i32 s59, s58, 40960
v_mfma_f32_16x16x32_bf16 v[160:163], a[32:35], a[8:11], v[160:163]
s_add_u32 s57, s76, s59
s_mul_i32 s59, s58, 294912
s_add_u32 s55, s77, s59
v_mfma_f32_16x16x32_bf16 v[164:167], a[12:15], a[8:11], v[164:167]
s_add_u32 s56, s78, s59
s_add_u32 m0, s54, 122880
v_mfma_f32_16x16x32_bf16 v[168:171], a[16:19], a[8:11], v[168:171]
s_waitcnt lgkmcnt(7)
v_mfma_f32_16x16x32_bf16 v[172:175], a[92:95], a[8:11], v[172:175]
buffer_load_dwordx4 v202, s[40:43], s57 offen lds
v_mfma_f32_16x16x32_bf16 v[176:179], a[24:27], a[8:11], v[176:179]
s_waitcnt lgkmcnt(3)
ds_read_b128 a[36:39], v185 offset:32768
v_mfma_f32_16x16x32_bf16 v[0:3], a[88:91], a[4:7], v[0:3]
buffer_load_dwordx4 v203, s[40:43], s57 offen offset:1024 lds
v_mfma_f32_16x16x32_bf16 v[4:7], a[68:71], a[4:7], v[4:7]
s_waitcnt lgkmcnt(1)
ds_read_b128 a[44:47], v185 offset:33792
v_mfma_f32_16x16x32_bf16 v[8:11], a[72:75], a[4:7], v[8:11]
buffer_load_dwordx4 v204, s[40:43], s57 offen offset:2048 lds
v_mfma_f32_16x16x32_bf16 v[12:15], a[52:55], a[4:7], v[12:15]
ds_read_b128 a[76:79], v185 offset:34816
s_add_u32 m0, s52, 122880
v_mfma_f32_16x16x32_bf16 v[16:19], a[80:83], a[4:7], v[16:19]
buffer_load_dwordx4 v198, s[36:39], s55 offen lds
v_mfma_f32_16x16x32_bf16 v[20:23], a[88:91], a[84:87], v[20:23]
ds_read_b128 a[40:43], v185 offset:35840
v_mfma_f32_16x16x32_bf16 v[24:27], a[68:71], a[84:87], v[24:27]
buffer_load_dwordx4 v198, s[36:39], s55 offen offset:1024 lds
v_mfma_f32_16x16x32_bf16 v[28:31], a[72:75], a[84:87], v[28:31]
s_waitcnt lgkmcnt(3)
ds_read_b128 a[28:31], v185 offset:36864
v_mfma_f32_16x16x32_bf16 v[32:35], a[52:55], a[84:87], v[32:35]
v_mfma_f32_16x16x32_bf16 v[36:39], a[80:83], a[84:87], v[36:39]
v_mfma_f32_16x16x32_bf16 v[40:43], a[88:91], a[36:39], v[40:43]
ds_read_b128 a[56:59], v185 offset:37888
v_mfma_f32_16x16x32_bf16 v[44:47], a[68:71], a[36:39], v[44:47]
buffer_load_dwordx4 v198, s[36:39], s55 offen offset:2048 lds
v_mfma_f32_16x16x32_bf16 v[48:51], a[72:75], a[36:39], v[48:51]
s_waitcnt lgkmcnt(3)
v_mfma_f32_16x16x32_bf16 v[52:55], a[52:55], a[36:39], v[52:55]
v_mfma_f32_16x16x32_bf16 v[56:59], a[80:83], a[36:39], v[56:59]
v_mfma_f32_16x16x32_bf16 v[60:63], a[88:91], a[44:47], v[60:63]
ds_read_b128 a[64:67], v185 offset:38912
v_mfma_f32_16x16x32_bf16 v[64:67], a[68:71], a[44:47], v[64:67]
buffer_load_dwordx4 v198, s[36:39], s55 offen offset:3072 lds
v_mfma_f32_16x16x32_bf16 v[68:71], a[72:75], a[44:47], v[68:71]
v_mfma_f32_16x16x32_bf16 v[72:75], a[52:55], a[44:47], v[72:75]
v_mfma_f32_16x16x32_bf16 v[76:79], a[80:83], a[44:47], v[76:79]
v_mfma_f32_16x16x32_bf16 v[80:83], a[88:91], a[76:79], v[80:83]
ds_read_b64_tr_b16 a[20:21], v186 offset:0
v_mfma_f32_16x16x32_bf16 v[84:87], a[68:71], a[76:79], v[84:87]
s_and_b32 s60, s68, 122880
s_add_u32 m0, s53, s60
v_mfma_f32_16x16x32_bf16 v[88:91], a[72:75], a[76:79], v[88:91]
s_waitcnt lgkmcnt(2)
v_mfma_f32_16x16x32_bf16 v[92:95], a[52:55], a[76:79], v[92:95]
buffer_load_dwordx4 v199, s[36:39], s56 offen lds
v_mfma_f32_16x16x32_bf16 v[96:99], a[80:83], a[76:79], v[96:99]
ds_read_b64_tr_b16 a[22:23], v186 offset:6144
v_mfma_f32_16x16x32_bf16 v[100:103], a[88:91], a[40:43], v[100:103]
ds_read_b128 a[60:63], v191 offset:0
v_mfma_f32_16x16x32_bf16 v[104:107], a[68:71], a[40:43], v[104:107]
ds_read_b64_tr_b16 a[48:49], v187 offset:0
v_mfma_f32_16x16x32_bf16 v[108:111], a[72:75], a[40:43], v[108:111]
ds_read_b64_tr_b16 a[50:51], v187 offset:6144
v_mfma_f32_16x16x32_bf16 v[112:115], a[52:55], a[40:43], v[112:115]
ds_read_b64_tr_b16 a[0:1], v188 offset:0
v_mfma_f32_16x16x32_bf16 v[116:119], a[80:83], a[40:43], v[116:119]
ds_read_b64_tr_b16 a[2:3], v188 offset:6144
v_mfma_f32_16x16x32_bf16 v[120:123], a[88:91], a[28:31], v[120:123]
ds_read_b64_tr_b16 a[32:33], v189 offset:0
v_mfma_f32_16x16x32_bf16 v[124:127], a[68:71], a[28:31], v[124:127]
ds_read_b64_tr_b16 a[34:35], v189 offset:6144
v_mfma_f32_16x16x32_bf16 v[128:131], a[72:75], a[28:31], v[128:131]
ds_read_b64_tr_b16 a[12:13], v190 offset:0
v_mfma_f32_16x16x32_bf16 v[132:135], a[52:55], a[28:31], v[132:135]
ds_read_b64_tr_b16 a[14:15], v190 offset:6144
s_add_u32 s58, s73, 2
v_mfma_f32_16x16x32_bf16 v[136:139], a[80:83], a[28:31], v[136:139]
s_and_b32 s58, s58, 7
s_add_u32 s58, s58, 8
s_mul_i32 s59, s58, 40960
v_mfma_f32_16x16x32_bf16 v[140:143], a[88:91], a[56:59], v[140:143]
s_add_u32 s57, s76, s59
s_mul_i32 s59, s58, 294912
s_add_u32 s55, s77, s59
v_mfma_f32_16x16x32_bf16 v[144:147], a[68:71], a[56:59], v[144:147]
s_add_u32 s56, s78, s59
s_add_u32 m0, s54, 0
v_mfma_f32_16x16x32_bf16 v[148:151], a[72:75], a[56:59], v[148:151]
s_waitcnt lgkmcnt(4)
v_mfma_f32_16x16x32_bf16 v[152:155], a[52:55], a[56:59], v[152:155]
buffer_load_dwordx4 v202, s[40:43], s57 offen lds
v_mfma_f32_16x16x32_bf16 v[156:159], a[80:83], a[56:59], v[156:159]
ds_read_b128 a[16:19], v191 offset:1024
v_mfma_f32_16x16x32_bf16 v[160:163], a[88:91], a[64:67], v[160:163]
buffer_load_dwordx4 v203, s[40:43], s57 offen offset:1024 lds
v_mfma_f32_16x16x32_bf16 v[164:167], a[68:71], a[64:67], v[164:167]
ds_read_b128 a[92:95], v191 offset:2048
v_mfma_f32_16x16x32_bf16 v[168:171], a[72:75], a[64:67], v[168:171]
buffer_load_dwordx4 v204, s[40:43], s57 offen offset:2048 lds
v_mfma_f32_16x16x32_bf16 v[172:175], a[52:55], a[64:67], v[172:175]
ds_read_b128 a[8:11], v191 offset:3072
s_add_u32 m0, s52, 0
v_mfma_f32_16x16x32_bf16 v[176:179], a[80:83], a[64:67], v[176:179]
buffer_load_dwordx4 v198, s[36:39], s55 offen lds
v_mfma_f32_16x16x32_bf16 v[0:3], a[20:23], a[60:63], v[0:3]
s_waitcnt lgkmcnt(3)
ds_read_b128 a[24:27], v191 offset:4096
v_mfma_f32_16x16x32_bf16 v[4:7], a[48:51], a[60:63], v[4:7]
buffer_load_dwordx4 v198, s[36:39], s55 offen offset:1024 lds
v_mfma_f32_16x16x32_bf16 v[8:11], a[0:3], a[60:63], v[8:11]
s_waitcnt lgkmcnt(3)
ds_read_b128 a[4:7], v191 offset:5120
v_mfma_f32_16x16x32_bf16 v[12:15], a[32:35], a[60:63], v[12:15]
v_mfma_f32_16x16x32_bf16 v[16:19], a[12:15], a[60:63], v[16:19]
v_mfma_f32_16x16x32_bf16 v[20:23], a[20:23], a[16:19], v[20:23]
ds_read_b128 a[84:87], v191 offset:6144
v_mfma_f32_16x16x32_bf16 v[24:27], a[48:51], a[16:19], v[24:27]
buffer_load_dwordx4 v198, s[36:39], s55 offen offset:2048 lds
v_mfma_f32_16x16x32_bf16 v[28:31], a[0:3], a[16:19], v[28:31]
s_waitcnt lgkmcnt(3)
v_mfma_f32_16x16x32_bf16 v[32:35], a[32:35], a[16:19], v[32:35]
v_mfma_f32_16x16x32_bf16 v[36:39], a[12:15], a[16:19], v[36:39]
v_mfma_f32_16x16x32_bf16 v[40:43], a[20:23], a[92:95], v[40:43]
ds_read_b128 a[36:39], v191 offset:7168
v_mfma_f32_16x16x32_bf16 v[44:47], a[48:51], a[92:95], v[44:47]
buffer_load_dwordx4 v198, s[36:39], s55 offen offset:3072 lds
v_mfma_f32_16x16x32_bf16 v[48:51], a[0:3], a[92:95], v[48:51]
v_mfma_f32_16x16x32_bf16 v[52:55], a[32:35], a[92:95], v[52:55]
v_mfma_f32_16x16x32_bf16 v[56:59], a[12:15], a[92:95], v[56:59]
v_mfma_f32_16x16x32_bf16 v[60:63], a[20:23], a[8:11], v[60:63]
ds_read_b128 a[44:47], v191 offset:8192
v_mfma_f32_16x16x32_bf16 v[64:67], a[48:51], a[8:11], v[64:67]
s_and_b32 s60, s68, 0
s_add_u32 m0, s53, s60
v_mfma_f32_16x16x32_bf16 v[68:71], a[0:3], a[8:11], v[68:71]
s_waitcnt lgkmcnt(2)
v_mfma_f32_16x16x32_bf16 v[72:75], a[32:35], a[8:11], v[72:75]
buffer_load_dwordx4 v199, s[36:39], s56 offen lds
v_mfma_f32_16x16x32_bf16 v[76:79], a[12:15], a[8:11], v[76:79]
v_mfma_f32_16x16x32_bf16 v[80:83], a[20:23], a[24:27], v[80:83]
v_mfma_f32_16x16x32_bf16 v[84:87], a[48:51], a[24:27], v[84:87]
v_mfma_f32_16x16x32_bf16 v[88:91], a[0:3], a[24:27], v[88:91]
v_mfma_f32_16x16x32_bf16 v[92:95], a[32:35], a[24:27], v[92:95]
v_mfma_f32_16x16x32_bf16 v[96:99], a[12:15], a[24:27], v[96:99]
v_mfma_f32_16x16x32_bf16 v[100:103], a[20:23], a[4:7], v[100:103]
v_mfma_f32_16x16x32_bf16 v[104:107], a[48:51], a[4:7], v[104:107]
v_mfma_f32_16x16x32_bf16 v[108:111], a[0:3], a[4:7], v[108:111]
v_mfma_f32_16x16x32_bf16 v[112:115], a[32:35], a[4:7], v[112:115]
v_mfma_f32_16x16x32_bf16 v[116:119], a[12:15], a[4:7], v[116:119]
v_mfma_f32_16x16x32_bf16 v[120:123], a[20:23], a[84:87], v[120:123]
s_waitcnt vmcnt(8) lgkmcnt(0)
s_barrier
v_mfma_f32_16x16x32_bf16 v[124:127], a[48:51], a[84:87], v[124:127]
ds_read_b64_tr_b16 a[76:77], v186 offset:30720
v_mfma_f32_16x16x32_bf16 v[128:131], a[0:3], a[84:87], v[128:131]
ds_read_b64_tr_b16 a[78:79], v186 offset:36864
v_mfma_f32_16x16x32_bf16 v[132:135], a[32:35], a[84:87], v[132:135]
ds_read_b128 a[40:43], v191 offset:30720
v_mfma_f32_16x16x32_bf16 v[136:139], a[12:15], a[84:87], v[136:139]
ds_read_b64_tr_b16 a[28:29], v187 offset:30720
ds_read_b64_tr_b16 a[30:31], v187 offset:36864
v_mfma_f32_16x16x32_bf16 v[140:143], a[20:23], a[36:39], v[140:143]
ds_read_b64_tr_b16 a[56:57], v188 offset:30720
ds_read_b64_tr_b16 a[58:59], v188 offset:36864
v_mfma_f32_16x16x32_bf16 v[144:147], a[48:51], a[36:39], v[144:147]
ds_read_b64_tr_b16 a[88:89], v189 offset:30720
ds_read_b64_tr_b16 a[90:91], v189 offset:36864
v_mfma_f32_16x16x32_bf16 v[148:151], a[0:3], a[36:39], v[148:151]
ds_read_b64_tr_b16 a[68:69], v190 offset:30720
ds_read_b64_tr_b16 a[70:71], v190 offset:36864
v_mfma_f32_16x16x32_bf16 v[152:155], a[32:35], a[36:39], v[152:155]
ds_read_b128 a[72:75], v191 offset:31744
s_add_u32 s58, s73, 3
v_mfma_f32_16x16x32_bf16 v[156:159], a[12:15], a[36:39], v[156:159]
s_and_b32 s58, s58, 7
s_add_u32 s58, s58, 8
s_mul_i32 s59, s58, 40960
v_mfma_f32_16x16x32_bf16 v[160:163], a[20:23], a[44:47], v[160:163]
s_add_u32 s57, s76, s59
s_mul_i32 s59, s58, 294912
s_add_u32 s55, s77, s59
v_mfma_f32_16x16x32_bf16 v[164:167], a[48:51], a[44:47], v[164:167]
s_add_u32 s56, s78, s59
s_add_u32 m0, s54, 30720
v_mfma_f32_16x16x32_bf16 v[168:171], a[0:3], a[44:47], v[168:171]
s_waitcnt lgkmcnt(7)
v_mfma_f32_16x16x32_bf16 v[172:175], a[32:35], a[44:47], v[172:175]
buffer_load_dwordx4 v202, s[40:43], s57 offen lds
v_mfma_f32_16x16x32_bf16 v[176:179], a[12:15], a[44:47], v[176:179]
s_waitcnt lgkmcnt(3)
ds_read_b128 a[52:55], v191 offset:32768
v_mfma_f32_16x16x32_bf16 v[0:3], a[76:79], a[40:43], v[0:3]
buffer_load_dwordx4 v203, s[40:43], s57 offen offset:1024 lds
v_mfma_f32_16x16x32_bf16 v[4:7], a[28:31], a[40:43], v[4:7]
s_waitcnt lgkmcnt(1)
ds_read_b128 a[64:67], v191 offset:33792
v_mfma_f32_16x16x32_bf16 v[8:11], a[56:59], a[40:43], v[8:11]
buffer_load_dwordx4 v204, s[40:43], s57 offen offset:2048 lds
v_mfma_f32_16x16x32_bf16 v[12:15], a[88:91], a[40:43], v[12:15]
ds_read_b128 a[80:83], v191 offset:34816
s_add_u32 m0, s52, 30720
v_mfma_f32_16x16x32_bf16 v[16:19], a[68:71], a[40:43], v[16:19]
buffer_load_dwordx4 v198, s[36:39], s55 offen lds
v_mfma_f32_16x16x32_bf16 v[20:23], a[76:79], a[72:75], v[20:23]
ds_read_b128 a[60:63], v191 offset:35840
v_mfma_f32_16x16x32_bf16 v[24:27], a[28:31], a[72:75], v[24:27]
buffer_load_dwordx4 v198, s[36:39], s55 offen offset:1024 lds
v_mfma_f32_16x16x32_bf16 v[28:31], a[56:59], a[72:75], v[28:31]
s_waitcnt lgkmcnt(3)
ds_read_b128 a[16:19], v191 offset:36864
v_mfma_f32_16x16x32_bf16 v[32:35], a[88:91], a[72:75], v[32:35]
v_mfma_f32_16x16x32_bf16 v[36:39], a[68:71], a[72:75], v[36:39]
v_mfma_f32_16x16x32_bf16 v[40:43], a[76:79], a[52:55], v[40:43]
ds_read_b128 a[92:95], v191 offset:37888
v_mfma_f32_16x16x32_bf16 v[44:47], a[28:31], a[52:55], v[44:47]
buffer_load_dwordx4 v198, s[36:39], s55 offen offset:2048 lds
v_mfma_f32_16x16x32_bf16 v[48:51], a[56:59], a[52:55], v[48:51]
s_waitcnt lgkmcnt(3)
v_mfma_f32_16x16x32_bf16 v[52:55], a[88:91], a[52:55], v[52:55]
v_mfma_f32_16x16x32_bf16 v[56:59], a[68:71], a[52:55], v[56:59]
v_mfma_f32_16x16x32_bf16 v[60:63], a[76:79], a[64:67], v[60:63]
ds_read_b128 a[8:11], v191 offset:38912
v_mfma_f32_16x16x32_bf16 v[64:67], a[28:31], a[64:67], v[64:67]
buffer_load_dwordx4 v198, s[36:39], s55 offen offset:3072 lds
v_mfma_f32_16x16x32_bf16 v[68:71], a[56:59], a[64:67], v[68:71]
v_mfma_f32_16x16x32_bf16 v[72:75], a[88:91], a[64:67], v[72:75]
v_mfma_f32_16x16x32_bf16 v[76:79], a[68:71], a[64:67], v[76:79]
v_mfma_f32_16x16x32_bf16 v[80:83], a[76:79], a[80:83], v[80:83]
ds_read_b64_tr_b16 a[24:25], v192 offset:0
v_mfma_f32_16x16x32_bf16 v[84:87], a[28:31], a[80:83], v[84:87]
s_and_b32 s60, s68, 30720
s_add_u32 m0, s53, s60
v_mfma_f32_16x16x32_bf16 v[88:91], a[56:59], a[80:83], v[88:91]
s_waitcnt lgkmcnt(2)
v_mfma_f32_16x16x32_bf16 v[92:95], a[88:91], a[80:83], v[92:95]
buffer_load_dwordx4 v199, s[36:39], s56 offen lds
v_mfma_f32_16x16x32_bf16 v[96:99], a[68:71], a[80:83], v[96:99]
ds_read_b64_tr_b16 a[26:27], v192 offset:6144
v_mfma_f32_16x16x32_bf16 v[100:103], a[76:79], a[60:63], v[100:103]
ds_read_b128 a[4:7], v197 offset:0
v_mfma_f32_16x16x32_bf16 v[104:107], a[28:31], a[60:63], v[104:107]
ds_read_b64_tr_b16 a[84:85], v193 offset:0
v_mfma_f32_16x16x32_bf16 v[108:111], a[56:59], a[60:63], v[108:111]
ds_read_b64_tr_b16 a[86:87], v193 offset:6144
v_mfma_f32_16x16x32_bf16 v[112:115], a[88:91], a[60:63], v[112:115]
ds_read_b64_tr_b16 a[36:37], v194 offset:0
v_mfma_f32_16x16x32_bf16 v[116:119], a[68:71], a[60:63], v[116:119]
ds_read_b64_tr_b16 a[38:39], v194 offset:6144
v_mfma_f32_16x16x32_bf16 v[120:123], a[76:79], a[16:19], v[120:123]
ds_read_b64_tr_b16 a[20:21], v195 offset:0
v_mfma_f32_16x16x32_bf16 v[124:127], a[28:31], a[16:19], v[124:127]
ds_read_b64_tr_b16 a[22:23], v195 offset:6144
v_mfma_f32_16x16x32_bf16 v[128:131], a[56:59], a[16:19], v[128:131]
ds_read_b64_tr_b16 a[48:49], v196 offset:0
v_mfma_f32_16x16x32_bf16 v[132:135], a[88:91], a[16:19], v[132:135]
ds_read_b64_tr_b16 a[50:51], v196 offset:6144
s_add_u32 s58, s73, 4
v_mfma_f32_16x16x32_bf16 v[136:139], a[68:71], a[16:19], v[136:139]
s_and_b32 s58, s58, 7
s_add_u32 s58, s58, 8
s_mul_i32 s59, s58, 40960
v_mfma_f32_16x16x32_bf16 v[140:143], a[76:79], a[92:95], v[140:143]
s_add_u32 s57, s76, s59
s_mul_i32 s59, s58, 294912
s_add_u32 s55, s77, s59
v_mfma_f32_16x16x32_bf16 v[144:147], a[28:31], a[92:95], v[144:147]
s_add_u32 s56, s78, s59
s_add_u32 m0, s54, 61440
v_mfma_f32_16x16x32_bf16 v[148:151], a[56:59], a[92:95], v[148:151]
s_waitcnt lgkmcnt(4)
v_mfma_f32_16x16x32_bf16 v[152:155], a[88:91], a[92:95], v[152:155]
buffer_load_dwordx4 v202, s[40:43], s57 offen lds
v_mfma_f32_16x16x32_bf16 v[156:159], a[68:71], a[92:95], v[156:159]
ds_read_b128 a[0:3], v197 offset:1024
v_mfma_f32_16x16x32_bf16 v[160:163], a[76:79], a[8:11], v[160:163]
buffer_load_dwordx4 v203, s[40:43], s57 offen offset:1024 lds
v_mfma_f32_16x16x32_bf16 v[164:167], a[28:31], a[8:11], v[164:167]
ds_read_b128 a[32:35], v197 offset:2048
v_mfma_f32_16x16x32_bf16 v[168:171], a[56:59], a[8:11], v[168:171]
buffer_load_dwordx4 v204, s[40:43], s57 offen offset:2048 lds
v_mfma_f32_16x16x32_bf16 v[172:175], a[88:91], a[8:11], v[172:175]
ds_read_b128 a[44:47], v197 offset:3072
s_add_u32 m0, s52, 61440
v_mfma_f32_16x16x32_bf16 v[176:179], a[68:71], a[8:11], v[176:179]
buffer_load_dwordx4 v198, s[36:39], s55 offen lds
v_mfma_f32_16x16x32_bf16 v[0:3], a[24:27], a[4:7], v[0:3]
s_waitcnt lgkmcnt(3)
ds_read_b128 a[12:15], v197 offset:4096
v_mfma_f32_16x16x32_bf16 v[4:7], a[84:87], a[4:7], v[4:7]
buffer_load_dwordx4 v198, s[36:39], s55 offen offset:1024 lds
v_mfma_f32_16x16x32_bf16 v[8:11], a[36:39], a[4:7], v[8:11]
s_waitcnt lgkmcnt(3)
ds_read_b128 a[40:43], v197 offset:5120
v_mfma_f32_16x16x32_bf16 v[12:15], a[20:23], a[4:7], v[12:15]
v_mfma_f32_16x16x32_bf16 v[16:19], a[48:51], a[4:7], v[16:19]
v_mfma_f32_16x16x32_bf16 v[20:23], a[24:27], a[0:3], v[20:23]
ds_read_b128 a[72:75], v197 offset:6144
v_mfma_f32_16x16x32_bf16 v[24:27], a[84:87], a[0:3], v[24:27]
buffer_load_dwordx4 v198, s[36:39], s55 offen offset:2048 lds
v_mfma_f32_16x16x32_bf16 v[28:31], a[36:39], a[0:3], v[28:31]
s_waitcnt lgkmcnt(3)
v_mfma_f32_16x16x32_bf16 v[32:35], a[20:23], a[0:3], v[32:35]
v_mfma_f32_16x16x32_bf16 v[36:39], a[48:51], a[0:3], v[36:39]
v_mfma_f32_16x16x32_bf16 v[40:43], a[24:27], a[32:35], v[40:43]
ds_read_b128 a[52:55], v197 offset:7168
v_mfma_f32_16x16x32_bf16 v[44:47], a[84:87], a[32:35], v[44:47]
buffer_load_dwordx4 v198, s[36:39], s55 offen offset:3072 lds
v_mfma_f32_16x16x32_bf16 v[48:51], a[36:39], a[32:35], v[48:51]
v_mfma_f32_16x16x32_bf16 v[52:55], a[20:23], a[32:35], v[52:55]
v_mfma_f32_16x16x32_bf16 v[56:59], a[48:51], a[32:35], v[56:59]
v_mfma_f32_16x16x32_bf16 v[60:63], a[24:27], a[44:47], v[60:63]
ds_read_b128 a[64:67], v197 offset:8192
v_mfma_f32_16x16x32_bf16 v[64:67], a[84:87], a[44:47], v[64:67]
s_and_b32 s60, s68, 61440
s_add_u32 m0, s53, s60
v_mfma_f32_16x16x32_bf16 v[68:71], a[36:39], a[44:47], v[68:71]
s_waitcnt lgkmcnt(2)
v_mfma_f32_16x16x32_bf16 v[72:75], a[20:23], a[44:47], v[72:75]
buffer_load_dwordx4 v199, s[36:39], s56 offen lds
v_mfma_f32_16x16x32_bf16 v[76:79], a[48:51], a[44:47], v[76:79]
v_mfma_f32_16x16x32_bf16 v[80:83], a[24:27], a[12:15], v[80:83]
v_mfma_f32_16x16x32_bf16 v[84:87], a[84:87], a[12:15], v[84:87]
v_mfma_f32_16x16x32_bf16 v[88:91], a[36:39], a[12:15], v[88:91]
v_mfma_f32_16x16x32_bf16 v[92:95], a[20:23], a[12:15], v[92:95]
v_mfma_f32_16x16x32_bf16 v[96:99], a[48:51], a[12:15], v[96:99]
v_mfma_f32_16x16x32_bf16 v[100:103], a[24:27], a[40:43], v[100:103]
v_mfma_f32_16x16x32_bf16 v[104:107], a[84:87], a[40:43], v[104:107]
v_mfma_f32_16x16x32_bf16 v[108:111], a[36:39], a[40:43], v[108:111]
v_mfma_f32_16x16x32_bf16 v[112:115], a[20:23], a[40:43], v[112:115]
v_mfma_f32_16x16x32_bf16 v[116:119], a[48:51], a[40:43], v[116:119]
v_mfma_f32_16x16x32_bf16 v[120:123], a[24:27], a[72:75], v[120:123]
s_waitcnt vmcnt(8) lgkmcnt(0)
s_barrier
v_mfma_f32_16x16x32_bf16 v[124:127], a[84:87], a[72:75], v[124:127]
ds_read_b64_tr_b16 a[80:81], v180 offset:0
v_mfma_f32_16x16x32_bf16 v[128:131], a[36:39], a[72:75], v[128:131]
ds_read_b64_tr_b16 a[82:83], v180 offset:6144
v_mfma_f32_16x16x32_bf16 v[132:135], a[20:23], a[72:75], v[132:135]
ds_read_b128 a[60:63], v185 offset:0
v_mfma_f32_16x16x32_bf16 v[136:139], a[48:51], a[72:75], v[136:139]
ds_read_b64_tr_b16 a[16:17], v181 offset:0
ds_read_b64_tr_b16 a[18:19], v181 offset:6144
v_mfma_f32_16x16x32_bf16 v[140:143], a[24:27], a[52:55], v[140:143]
ds_read_b64_tr_b16 a[92:93], v182 offset:0
ds_read_b64_tr_b16 a[94:95], v182 offset:6144
v_mfma_f32_16x16x32_bf16 v[144:147], a[84:87], a[52:55], v[144:147]
ds_read_b64_tr_b16 a[76:77], v183 offset:0
ds_read_b64_tr_b16 a[78:79], v183 offset:6144
v_mfma_f32_16x16x32_bf16 v[148:151], a[36:39], a[52:55], v[148:151]
ds_read_b64_tr_b16 a[28:29], v184 offset:0
ds_read_b64_tr_b16 a[30:31], v184 offset:6144
v_mfma_f32_16x16x32_bf16 v[152:155], a[20:23], a[52:55], v[152:155]
ds_read_b128 a[56:59], v185 offset:1024
s_add_u32 s58, s73, 5
v_mfma_f32_16x16x32_bf16 v[156:159], a[48:51], a[52:55], v[156:159]
s_and_b32 s58, s58, 7
s_add_u32 s58, s58, 8
s_mul_i32 s59, s58, 40960
v_mfma_f32_16x16x32_bf16 v[160:163], a[24:27], a[64:67], v[160:163]
s_add_u32 s57, s76, s59
s_mul_i32 s59, s58, 294912
s_add_u32 s55, s77, s59
v_mfma_f32_16x16x32_bf16 v[164:167], a[84:87], a[64:67], v[164:167]
s_add_u32 s56, s78, s59
s_add_u32 m0, s54, 92160
v_mfma_f32_16x16x32_bf16 v[168:171], a[36:39], a[64:67], v[168:171]
s_waitcnt lgkmcnt(7)
v_mfma_f32_16x16x32_bf16 v[172:175], a[20:23], a[64:67], v[172:175]
buffer_load_dwordx4 v202, s[40:43], s57 offen lds
v_mfma_f32_16x16x32_bf16 v[176:179], a[48:51], a[64:67], v[176:179]
s_waitcnt lgkmcnt(3)
ds_read_b128 a[88:91], v185 offset:2048
v_mfma_f32_16x16x32_bf16 v[0:3], a[80:83], a[60:63], v[0:3]
buffer_load_dwordx4 v203, s[40:43], s57 offen offset:1024 lds
v_mfma_f32_16x16x32_bf16 v[4:7], a[16:19], a[60:63], v[4:7]
s_waitcnt lgkmcnt(1)
ds_read_b128 a[8:11], v185 offset:3072
v_mfma_f32_16x16x32_bf16 v[8:11], a[92:95], a[60:63], v[8:11]
buffer_load_dwordx4 v204, s[40:43], s57 offen offset:2048 lds
v_mfma_f32_16x16x32_bf16 v[12:15], a[76:79], a[60:63], v[12:15]
ds_read_b128 a[68:71], v185 offset:4096
s_add_u32 m0, s52, 92160
v_mfma_f32_16x16x32_bf16 v[16:19], a[28:31], a[60:63], v[16:19]
buffer_load_dwordx4 v198, s[36:39], s55 offen lds
v_mfma_f32_16x16x32_bf16 v[20:23], a[80:83], a[56:59], v[20:23]
ds_read_b128 a[4:7], v185 offset:5120
v_mfma_f32_16x16x32_bf16 v[24:27], a[16:19], a[56:59], v[24:27]
buffer_load_dwordx4 v198, s[36:39], s55 offen offset:1024 lds
v_mfma_f32_16x16x32_bf16 v[28:31], a[92:95], a[56:59], v[28:31]
s_waitcnt lgkmcnt(3)
ds_read_b128 a[0:3], v185 offset:6144
v_mfma_f32_16x16x32_bf16 v[32:35], a[76:79], a[56:59], v[32:35]
v_mfma_f32_16x16x32_bf16 v[36:39], a[28:31], a[56:59], v[36:39]
v_mfma_f32_16x16x32_bf16 v[40:43], a[80:83], a[88:91], v[40:43]
ds_read_b128 a[32:35], v185 offset:7168
v_mfma_f32_16x16x32_bf16 v[44:47], a[16:19], a[88:91], v[44:47]
buffer_load_dwordx4 v198, s[36:39], s55 offen offset:2048 lds
v_mfma_f32_16x16x32_bf16 v[48:51], a[92:95], a[88:91], v[48:51]
s_waitcnt lgkmcnt(3)
v_mfma_f32_16x16x32_bf16 v[52:55], a[76:79], a[88:91], v[52:55]
v_mfma_f32_16x16x32_bf16 v[56:59], a[28:31], a[88:91], v[56:59]
v_mfma_f32_16x16x32_bf16 v[60:63], a[80:83], a[8:11], v[60:63]
ds_read_b128 a[44:47], v185 offset:8192
v_mfma_f32_16x16x32_bf16 v[64:67], a[16:19], a[8:11], v[64:67]
buffer_load_dwordx4 v198, s[36:39], s55 offen offset:3072 lds
v_mfma_f32_16x16x32_bf16 v[68:71], a[92:95], a[8:11], v[68:71]
v_mfma_f32_16x16x32_bf16 v[72:75], a[76:79], a[8:11], v[72:75]
v_mfma_f32_16x16x32_bf16 v[76:79], a[28:31], a[8:11], v[76:79]
v_mfma_f32_16x16x32_bf16 v[80:83], a[80:83], a[68:71], v[80:83]
ds_read_b64_tr_b16 a[12:13], v180 offset:30720
v_mfma_f32_16x16x32_bf16 v[84:87], a[16:19], a[68:71], v[84:87]
s_and_b32 s60, s68, 92160
s_add_u32 m0, s53, s60
v_mfma_f32_16x16x32_bf16 v[88:91], a[92:95], a[68:71], v[88:91]
s_waitcnt lgkmcnt(2)
v_mfma_f32_16x16x32_bf16 v[92:95], a[76:79], a[68:71], v[92:95]
buffer_load_dwordx4 v199, s[36:39], s56 offen lds
v_mfma_f32_16x16x32_bf16 v[96:99], a[28:31], a[68:71], v[96:99]
ds_read_b64_tr_b16 a[14:15], v180 offset:36864
v_mfma_f32_16x16x32_bf16 v[100:103], a[80:83], a[4:7], v[100:103]
ds_read_b128 a[40:43], v185 offset:30720
v_mfma_f32_16x16x32_bf16 v[104:107], a[16:19], a[4:7], v[104:107]
ds_read_b64_tr_b16 a[72:73], v181 offset:30720
v_mfma_f32_16x16x32_bf16 v[108:111], a[92:95], a[4:7], v[108:111]
ds_read_b64_tr_b16 a[74:75], v181 offset:36864
v_mfma_f32_16x16x32_bf16 v[112:115], a[76:79], a[4:7], v[112:115]
ds_read_b64_tr_b16 a[52:53], v182 offset:30720
v_mfma_f32_16x16x32_bf16 v[116:119], a[28:31], a[4:7], v[116:119]
ds_read_b64_tr_b16 a[54:55], v182 offset:36864
v_mfma_f32_16x16x32_bf16 v[120:123], a[80:83], a[0:3], v[120:123]
ds_read_b64_tr_b16 a[24:25], v183 offset:30720
v_mfma_f32_16x16x32_bf16 v[124:127], a[16:19], a[0:3], v[124:127]
ds_read_b64_tr_b16 a[26:27], v183 offset:36864
v_mfma_f32_16x16x32_bf16 v[128:131], a[92:95], a[0:3], v[128:131]
ds_read_b64_tr_b16 a[84:85], v184 offset:30720
v_mfma_f32_16x16x32_bf16 v[132:135], a[76:79], a[0:3], v[132:135]
ds_read_b64_tr_b16 a[86:87], v184 offset:36864
s_add_u32 s58, s73, 6
v_mfma_f32_16x16x32_bf16 v[136:139], a[28:31], a[0:3], v[136:139]
s_and_b32 s58, s58, 7
s_add_u32 s58, s58, 8
s_mul_i32 s59, s58, 40960
v_mfma_f32_16x16x32_bf16 v[140:143], a[80:83], a[32:35], v[140:143]
s_add_u32 s57, s76, s59
s_mul_i32 s59, s58, 294912
s_add_u32 s55, s77, s59
v_mfma_f32_16x16x32_bf16 v[144:147], a[16:19], a[32:35], v[144:147]
s_add_u32 s56, s78, s59
s_add_u32 m0, s54, 122880
v_mfma_f32_16x16x32_bf16 v[148:151], a[92:95], a[32:35], v[148:151]
s_waitcnt lgkmcnt(4)
v_mfma_f32_16x16x32_bf16 v[152:155], a[76:79], a[32:35], v[152:155]
buffer_load_dwordx4 v202, s[40:43], s57 offen lds
v_mfma_f32_16x16x32_bf16 v[156:159], a[28:31], a[32:35], v[156:159]
ds_read_b128 a[36:39], v185 offset:31744
v_mfma_f32_16x16x32_bf16 v[160:163], a[80:83], a[44:47], v[160:163]
buffer_load_dwordx4 v203, s[40:43], s57 offen offset:1024 lds
v_mfma_f32_16x16x32_bf16 v[164:167], a[16:19], a[44:47], v[164:167]
ds_read_b128 a[20:23], v185 offset:32768
v_mfma_f32_16x16x32_bf16 v[168:171], a[92:95], a[44:47], v[168:171]
buffer_load_dwordx4 v204, s[40:43], s57 offen offset:2048 lds
v_mfma_f32_16x16x32_bf16 v[172:175], a[76:79], a[44:47], v[172:175]
ds_read_b128 a[48:51], v185 offset:33792
s_add_u32 m0, s52, 122880
v_mfma_f32_16x16x32_bf16 v[176:179], a[28:31], a[44:47], v[176:179]
buffer_load_dwordx4 v198, s[36:39], s55 offen lds
v_mfma_f32_16x16x32_bf16 v[0:3], a[12:15], a[40:43], v[0:3]
s_waitcnt lgkmcnt(3)
ds_read_b128 a[64:67], v185 offset:34816
v_mfma_f32_16x16x32_bf16 v[4:7], a[72:75], a[40:43], v[4:7]
buffer_load_dwordx4 v198, s[36:39], s55 offen offset:1024 lds
v_mfma_f32_16x16x32_bf16 v[8:11], a[52:55], a[40:43], v[8:11]
s_waitcnt lgkmcnt(3)
ds_read_b128 a[60:63], v185 offset:35840
v_mfma_f32_16x16x32_bf16 v[12:15], a[24:27], a[40:43], v[12:15]
v_mfma_f32_16x16x32_bf16 v[16:19], a[84:87], a[40:43], v[16:19]
v_mfma_f32_16x16x32_bf16 v[20:23], a[12:15], a[36:39], v[20:23]
ds_read_b128 a[56:59], v185 offset:36864
v_mfma_f32_16x16x32_bf16 v[24:27], a[72:75], a[36:39], v[24:27]
buffer_load_dwordx4 v198, s[36:39], s55 offen offset:2048 lds
v_mfma_f32_16x16x32_bf16 v[28:31], a[52:55], a[36:39], v[28:31]
s_waitcnt lgkmcnt(3)
v_mfma_f32_16x16x32_bf16 v[32:35], a[24:27], a[36:39], v[32:35]
v_mfma_f32_16x16x32_bf16 v[36:39], a[84:87], a[36:39], v[36:39]
v_mfma_f32_16x16x32_bf16 v[40:43], a[12:15], a[20:23], v[40:43]
ds_read_b128 a[88:91], v185 offset:37888
v_mfma_f32_16x16x32_bf16 v[44:47], a[72:75], a[20:23], v[44:47]
buffer_load_dwordx4 v198, s[36:39], s55 offen offset:3072 lds
v_mfma_f32_16x16x32_bf16 v[48:51], a[52:55], a[20:23], v[48:51]
v_mfma_f32_16x16x32_bf16 v[52:55], a[24:27], a[20:23], v[52:55]
v_mfma_f32_16x16x32_bf16 v[56:59], a[84:87], a[20:23], v[56:59]
v_mfma_f32_16x16x32_bf16 v[60:63], a[12:15], a[48:51], v[60:63]
ds_read_b128 a[8:11], v185 offset:38912
v_mfma_f32_16x16x32_bf16 v[64:67], a[72:75], a[48:51], v[64:67]
s_and_b32 s60, s68, 122880
s_add_u32 m0, s53, s60
v_mfma_f32_16x16x32_bf16 v[68:71], a[52:55], a[48:51], v[68:71]
s_waitcnt lgkmcnt(2)
v_mfma_f32_16x16x32_bf16 v[72:75], a[24:27], a[48:51], v[72:75]
buffer_load_dwordx4 v199, s[36:39], s56 offen lds
v_mfma_f32_16x16x32_bf16 v[76:79], a[84:87], a[48:51], v[76:79]
v_mfma_f32_16x16x32_bf16 v[80:83], a[12:15], a[64:67], v[80:83]
v_mfma_f32_16x16x32_bf16 v[84:87], a[72:75], a[64:67], v[84:87]
v_mfma_f32_16x16x32_bf16 v[88:91], a[52:55], a[64:67], v[88:91]
v_mfma_f32_16x16x32_bf16 v[92:95], a[24:27], a[64:67], v[92:95]
v_mfma_f32_16x16x32_bf16 v[96:99], a[84:87], a[64:67], v[96:99]
v_mfma_f32_16x16x32_bf16 v[100:103], a[12:15], a[60:63], v[100:103]
v_mfma_f32_16x16x32_bf16 v[104:107], a[72:75], a[60:63], v[104:107]
v_mfma_f32_16x16x32_bf16 v[108:111], a[52:55], a[60:63], v[108:111]
v_mfma_f32_16x16x32_bf16 v[112:115], a[24:27], a[60:63], v[112:115]
v_mfma_f32_16x16x32_bf16 v[116:119], a[84:87], a[60:63], v[116:119]
v_mfma_f32_16x16x32_bf16 v[120:123], a[12:15], a[56:59], v[120:123]
s_waitcnt vmcnt(8) lgkmcnt(0)
s_barrier
v_mfma_f32_16x16x32_bf16 v[124:127], a[72:75], a[56:59], v[124:127]
ds_read_b64_tr_b16 a[68:69], v186 offset:0
v_mfma_f32_16x16x32_bf16 v[128:131], a[52:55], a[56:59], v[128:131]
ds_read_b64_tr_b16 a[70:71], v186 offset:6144
v_mfma_f32_16x16x32_bf16 v[132:135], a[24:27], a[56:59], v[132:135]
ds_read_b128 a[4:7], v191 offset:0
v_mfma_f32_16x16x32_bf16 v[136:139], a[84:87], a[56:59], v[136:139]
ds_read_b64_tr_b16 a[0:1], v187 offset:0
ds_read_b64_tr_b16 a[2:3], v187 offset:6144
v_mfma_f32_16x16x32_bf16 v[140:143], a[12:15], a[88:91], v[140:143]
ds_read_b64_tr_b16 a[32:33], v188 offset:0
ds_read_b64_tr_b16 a[34:35], v188 offset:6144
v_mfma_f32_16x16x32_bf16 v[144:147], a[72:75], a[88:91], v[144:147]
ds_read_b64_tr_b16 a[80:81], v189 offset:0
ds_read_b64_tr_b16 a[82:83], v189 offset:6144
v_mfma_f32_16x16x32_bf16 v[148:151], a[52:55], a[88:91], v[148:151]
ds_read_b64_tr_b16 a[16:17], v190 offset:0
ds_read_b64_tr_b16 a[18:19], v190 offset:6144
v_mfma_f32_16x16x32_bf16 v[152:155], a[24:27], a[88:91], v[152:155]
ds_read_b128 a[92:95], v191 offset:1024
s_add_u32 s58, s73, 7
v_mfma_f32_16x16x32_bf16 v[156:159], a[84:87], a[88:91], v[156:159]
s_and_b32 s58, s58, 7
s_add_u32 s58, s58, 8
s_mul_i32 s59, s58, 40960
v_mfma_f32_16x16x32_bf16 v[160:163], a[12:15], a[8:11], v[160:163]
s_add_u32 s57, s76, s59
s_mul_i32 s59, s58, 294912
s_add_u32 s55, s77, s59
v_mfma_f32_16x16x32_bf16 v[164:167], a[72:75], a[8:11], v[164:167]
s_add_u32 s56, s78, s59
s_add_u32 m0, s54, 0
v_mfma_f32_16x16x32_bf16 v[168:171], a[52:55], a[8:11], v[168:171]
s_waitcnt lgkmcnt(7)
v_mfma_f32_16x16x32_bf16 v[172:175], a[24:27], a[8:11], v[172:175]
buffer_load_dwordx4 v202, s[40:43], s57 offen lds
v_mfma_f32_16x16x32_bf16 v[176:179], a[84:87], a[8:11], v[176:179]
s_waitcnt lgkmcnt(3)
ds_read_b128 a[76:79], v191 offset:2048
v_mfma_f32_16x16x32_bf16 v[0:3], a[68:71], a[4:7], v[0:3]
buffer_load_dwordx4 v203, s[40:43], s57 offen offset:1024 lds
v_mfma_f32_16x16x32_bf16 v[4:7], a[0:3], a[4:7], v[4:7]
s_waitcnt lgkmcnt(1)
ds_read_b128 a[28:31], v191 offset:3072
v_mfma_f32_16x16x32_bf16 v[8:11], a[32:35], a[4:7], v[8:11]
buffer_load_dwordx4 v204, s[40:43], s57 offen offset:2048 lds
v_mfma_f32_16x16x32_bf16 v[12:15], a[80:83], a[4:7], v[12:15]
ds_read_b128 a[44:47], v191 offset:4096
s_add_u32 m0, s52, 0
v_mfma_f32_16x16x32_bf16 v[16:19], a[16:19], a[4:7], v[16:19]
buffer_load_dwordx4 v198, s[36:39], s55 offen lds
v_mfma_f32_16x16x32_bf16 v[20:23], a[68:71], a[92:95], v[20:23]
ds_read_b128 a[40:43], v191 offset:5120
v_mfma_f32_16x16x32_bf16 v[24:27], a[0:3], a[92:95], v[24:27]
buffer_load_dwordx4 v198, s[36:39], s55 offen offset:1024 lds
v_mfma_f32_16x16x32_bf16 v[28:31], a[32:35], a[92:95], v[28:31]
s_waitcnt lgkmcnt(3)
ds_read_b128 a[36:39], v191 offset:6144
v_mfma_f32_16x16x32_bf16 v[32:35], a[80:83], a[92:95], v[32:35]
v_mfma_f32_16x16x32_bf16 v[36:39], a[16:19], a[92:95], v[36:39]
v_mfma_f32_16x16x32_bf16 v[40:43], a[68:71], a[76:79], v[40:43]
ds_read_b128 a[20:23], v191 offset:7168
v_mfma_f32_16x16x32_bf16 v[44:47], a[0:3], a[76:79], v[44:47]
buffer_load_dwordx4 v198, s[36:39], s55 offen offset:2048 lds
v_mfma_f32_16x16x32_bf16 v[48:51], a[32:35], a[76:79], v[48:51]
s_waitcnt lgkmcnt(3)
v_mfma_f32_16x16x32_bf16 v[52:55], a[80:83], a[76:79], v[52:55]
v_mfma_f32_16x16x32_bf16 v[56:59], a[16:19], a[76:79], v[56:59]
v_mfma_f32_16x16x32_bf16 v[60:63], a[68:71], a[28:31], v[60:63]
ds_read_b128 a[48:51], v191 offset:8192
v_mfma_f32_16x16x32_bf16 v[64:67], a[0:3], a[28:31], v[64:67]
buffer_load_dwordx4 v198, s[36:39], s55 offen offset:3072 lds
v_mfma_f32_16x16x32_bf16 v[68:71], a[32:35], a[28:31], v[68:71]
v_mfma_f32_16x16x32_bf16 v[72:75], a[80:83], a[28:31], v[72:75]
v_mfma_f32_16x16x32_bf16 v[76:79], a[16:19], a[28:31], v[76:79]
v_mfma_f32_16x16x32_bf16 v[80:83], a[68:71], a[44:47], v[80:83]
ds_read_b64_tr_b16 a[64:65], v186 offset:30720
v_mfma_f32_16x16x32_bf16 v[84:87], a[0:3], a[44:47], v[84:87]
s_and_b32 s60, s68, 0
s_add_u32 m0, s53, s60
v_mfma_f32_16x16x32_bf16 v[88:91], a[32:35], a[44:47], v[88:91]
s_waitcnt lgkmcnt(2)
v_mfma_f32_16x16x32_bf16 v[92:95], a[80:83], a[44:47], v[92:95]
buffer_load_dwordx4 v199, s[36:39], s56 offen lds
v_mfma_f32_16x16x32_bf16 v[96:99], a[16:19], a[44:47], v[96:99]
ds_read_b64_tr_b16 a[66:67], v186 offset:36864
v_mfma_f32_16x16x32_bf16 v[100:103], a[68:71], a[40:43], v[100:103]
ds_read_b128 a[60:63], v191 offset:30720
v_mfma_f32_16x16x32_bf16 v[104:107], a[0:3], a[40:43], v[104:107]
ds_read_b64_tr_b16 a[56:57], v187 offset:30720
v_mfma_f32_16x16x32_bf16 v[108:111], a[32:35], a[40:43], v[108:111]
ds_read_b64_tr_b16 a[58:59], v187 offset:36864
v_mfma_f32_16x16x32_bf16 v[112:115], a[80:83], a[40:43], v[112:115]
ds_read_b64_tr_b16 a[88:89], v188 offset:30720
v_mfma_f32_16x16x32_bf16 v[116:119], a[16:19], a[40:43], v[116:119]
ds_read_b64_tr_b16 a[90:91], v188 offset:36864
v_mfma_f32_16x16x32_bf16 v[120:123], a[68:71], a[36:39], v[120:123]
ds_read_b64_tr_b16 a[12:13], v189 offset:30720
v_mfma_f32_16x16x32_bf16 v[124:127], a[0:3], a[36:39], v[124:127]
ds_read_b64_tr_b16 a[14:15], v189 offset:36864
v_mfma_f32_16x16x32_bf16 v[128:131], a[32:35], a[36:39], v[128:131]
ds_read_b64_tr_b16 a[72:73], v190 offset:30720
v_mfma_f32_16x16x32_bf16 v[132:135], a[80:83], a[36:39], v[132:135]
ds_read_b64_tr_b16 a[74:75], v190 offset:36864
s_add_u32 s58, s73, 0
v_mfma_f32_16x16x32_bf16 v[136:139], a[16:19], a[36:39], v[136:139]
s_and_b32 s58, s58, 7
s_add_u32 s58, s58, 16
s_mul_i32 s59, s58, 40960
v_mfma_f32_16x16x32_bf16 v[140:143], a[68:71], a[20:23], v[140:143]
s_add_u32 s57, s76, s59
s_mul_i32 s59, s58, 294912
s_add_u32 s55, s77, s59
v_mfma_f32_16x16x32_bf16 v[144:147], a[0:3], a[20:23], v[144:147]
s_add_u32 s56, s78, s59
s_add_u32 m0, s54, 30720
v_mfma_f32_16x16x32_bf16 v[148:151], a[32:35], a[20:23], v[148:151]
s_waitcnt lgkmcnt(4)
v_mfma_f32_16x16x32_bf16 v[152:155], a[80:83], a[20:23], v[152:155]
buffer_load_dwordx4 v202, s[40:43], s57 offen lds
v_mfma_f32_16x16x32_bf16 v[156:159], a[16:19], a[20:23], v[156:159]
ds_read_b128 a[52:55], v191 offset:31744
v_mfma_f32_16x16x32_bf16 v[160:163], a[68:71], a[48:51], v[160:163]
buffer_load_dwordx4 v203, s[40:43], s57 offen offset:1024 lds
v_mfma_f32_16x16x32_bf16 v[164:167], a[0:3], a[48:51], v[164:167]
ds_read_b128 a[24:27], v191 offset:32768
v_mfma_f32_16x16x32_bf16 v[168:171], a[32:35], a[48:51], v[168:171]
buffer_load_dwordx4 v204, s[40:43], s57 offen offset:2048 lds
v_mfma_f32_16x16x32_bf16 v[172:175], a[80:83], a[48:51], v[172:175]
ds_read_b128 a[84:87], v191 offset:33792
s_add_u32 m0, s52, 30720
v_mfma_f32_16x16x32_bf16 v[176:179], a[16:19], a[48:51], v[176:179]
buffer_load_dwordx4 v198, s[36:39], s55 offen lds
v_mfma_f32_16x16x32_bf16 v[0:3], a[64:67], a[60:63], v[0:3]
s_waitcnt lgkmcnt(3)
ds_read_b128 a[8:11], v191 offset:34816
v_mfma_f32_16x16x32_bf16 v[4:7], a[56:59], a[60:63], v[4:7]
buffer_load_dwordx4 v198, s[36:39], s55 offen offset:1024 lds
v_mfma_f32_16x16x32_bf16 v[8:11], a[88:91], a[60:63], v[8:11]
s_waitcnt lgkmcnt(3)
ds_read_b128 a[4:7], v191 offset:35840
v_mfma_f32_16x16x32_bf16 v[12:15], a[12:15], a[60:63], v[12:15]
v_mfma_f32_16x16x32_bf16 v[16:19], a[72:75], a[60:63], v[16:19]
v_mfma_f32_16x16x32_bf16 v[20:23], a[64:67], a[52:55], v[20:23]
ds_read_b128 a[92:95], v191 offset:36864
v_mfma_f32_16x16x32_bf16 v[24:27], a[56:59], a[52:55], v[24:27]
buffer_load_dwordx4 v198, s[36:39], s55 offen offset:2048 lds
v_mfma_f32_16x16x32_bf16 v[28:31], a[88:91], a[52:55], v[28:31]
s_waitcnt lgkmcnt(3)
v_mfma_f32_16x16x32_bf16 v[32:35], a[12:15], a[52:55], v[32:35]
v_mfma_f32_16x16x32_bf16 v[36:39], a[72:75], a[52:55], v[36:39]
v_mfma_f32_16x16x32_bf16 v[40:43], a[64:67], a[24:27], v[40:43]
ds_read_b128 a[76:79], v191 offset:37888
v_mfma_f32_16x16x32_bf16 v[44:47], a[56:59], a[24:27], v[44:47]
buffer_load_dwordx4 v198, s[36:39], s55 offen offset:3072 lds
v_mfma_f32_16x16x32_bf16 v[48:51], a[88:91], a[24:27], v[48:51]
v_mfma_f32_16x16x32_bf16 v[52:55], a[12:15], a[24:27], v[52:55]
v_mfma_f32_16x16x32_bf16 v[56:59], a[72:75], a[24:27], v[56:59]
v_mfma_f32_16x16x32_bf16 v[60:63], a[64:67], a[84:87], v[60:63]
ds_read_b128 a[28:31], v191 offset:38912
v_mfma_f32_16x16x32_bf16 v[64:67], a[56:59], a[84:87], v[64:67]
s_and_b32 s60, s68, 30720
s_add_u32 m0, s53, s60
v_mfma_f32_16x16x32_bf16 v[68:71], a[88:91], a[84:87], v[68:71]
s_waitcnt lgkmcnt(2)
v_mfma_f32_16x16x32_bf16 v[72:75], a[12:15], a[84:87], v[72:75]
buffer_load_dwordx4 v199, s[36:39], s56 offen lds
v_mfma_f32_16x16x32_bf16 v[76:79], a[72:75], a[84:87], v[76:79]
v_mfma_f32_16x16x32_bf16 v[80:83], a[64:67], a[8:11], v[80:83]
v_mfma_f32_16x16x32_bf16 v[84:87], a[56:59], a[8:11], v[84:87]
v_mfma_f32_16x16x32_bf16 v[88:91], a[88:91], a[8:11], v[88:91]
v_mfma_f32_16x16x32_bf16 v[92:95], a[12:15], a[8:11], v[92:95]
v_mfma_f32_16x16x32_bf16 v[96:99], a[72:75], a[8:11], v[96:99]
v_mfma_f32_16x16x32_bf16 v[100:103], a[64:67], a[4:7], v[100:103]
v_mfma_f32_16x16x32_bf16 v[104:107], a[56:59], a[4:7], v[104:107]
v_mfma_f32_16x16x32_bf16 v[108:111], a[88:91], a[4:7], v[108:111]
v_mfma_f32_16x16x32_bf16 v[112:115], a[12:15], a[4:7], v[112:115]
v_mfma_f32_16x16x32_bf16 v[116:119], a[72:75], a[4:7], v[116:119]
v_mfma_f32_16x16x32_bf16 v[120:123], a[64:67], a[92:95], v[120:123]
s_waitcnt vmcnt(8) lgkmcnt(0)
s_barrier
v_mfma_f32_16x16x32_bf16 v[124:127], a[56:59], a[92:95], v[124:127]
ds_read_b64_tr_b16 a[44:45], v192 offset:0
v_mfma_f32_16x16x32_bf16 v[128:131], a[88:91], a[92:95], v[128:131]
ds_read_b64_tr_b16 a[46:47], v192 offset:6144
v_mfma_f32_16x16x32_bf16 v[132:135], a[12:15], a[92:95], v[132:135]
ds_read_b128 a[40:43], v197 offset:0
v_mfma_f32_16x16x32_bf16 v[136:139], a[72:75], a[92:95], v[136:139]
ds_read_b64_tr_b16 a[36:37], v193 offset:0
ds_read_b64_tr_b16 a[38:39], v193 offset:6144
v_mfma_f32_16x16x32_bf16 v[140:143], a[64:67], a[76:79], v[140:143]
ds_read_b64_tr_b16 a[20:21], v194 offset:0
ds_read_b64_tr_b16 a[22:23], v194 offset:6144
v_mfma_f32_16x16x32_bf16 v[144:147], a[56:59], a[76:79], v[144:147]
ds_read_b64_tr_b16 a[68:69], v195 offset:0
ds_read_b64_tr_b16 a[70:71], v195 offset:6144
v_mfma_f32_16x16x32_bf16 v[148:151], a[88:91], a[76:79], v[148:151]
ds_read_b64_tr_b16 a[0:1], v196 offset:0
ds_read_b64_tr_b16 a[2:3], v196 offset:6144
v_mfma_f32_16x16x32_bf16 v[152:155], a[12:15], a[76:79], v[152:155]
ds_read_b128 a[32:35], v197 offset:1024
s_add_u32 s58, s73, 1
v_mfma_f32_16x16x32_bf16 v[156:159], a[72:75], a[76:79], v[156:159]
s_and_b32 s58, s58, 7
s_add_u32 s58, s58, 16
s_mul_i32 s59, s58, 40960
v_mfma_f32_16x16x32_bf16 v[160:163], a[64:67], a[28:31], v[160:163]
s_add_u32 s57, s76, s59
s_mul_i32 s59, s58, 294912
s_add_u32 s55, s77, s59
v_mfma_f32_16x16x32_bf16 v[164:167], a[56:59], a[28:31], v[164:167]
s_add_u32 s56, s78, s59
s_add_u32 m0, s54, 61440
v_mfma_f32_16x16x32_bf16 v[168:171], a[88:91], a[28:31], v[168:171]
s_waitcnt lgkmcnt(7)
v_mfma_f32_16x16x32_bf16 v[172:175], a[12:15], a[28:31], v[172:175]
buffer_load_dwordx4 v202, s[40:43], s57 offen lds
v_mfma_f32_16x16x32_bf16 v[176:179], a[72:75], a[28:31], v[176:179]
s_waitcnt lgkmcnt(3)
ds_read_b128 a[80:83], v197 offset:2048
v_mfma_f32_16x16x32_bf16 v[0:3], a[44:47], a[40:43], v[0:3]
buffer_load_dwordx4 v203, s[40:43], s57 offen offset:1024 lds
v_mfma_f32_16x16x32_bf16 v[4:7], a[36:39], a[40:43], v[4:7]
s_waitcnt lgkmcnt(1)
ds_read_b128 a[16:19], v197 offset:3072
v_mfma_f32_16x16x32_bf16 v[8:11], a[20:23], a[40:43], v[8:11]
buffer_load_dwordx4 v204, s[40:43], s57 offen offset:2048 lds
v_mfma_f32_16x16x32_bf16 v[12:15], a[68:71], a[40:43], v[12:15]
ds_read_b128 a[48:51], v197 offset:4096
s_add_u32 m0, s52, 61440
v_mfma_f32_16x16x32_bf16 v[16:19], a[0:3], a[40:43], v[16:19]
buffer_load_dwordx4 v198, s[36:39], s55 offen lds
v_mfma_f32_16x16x32_bf16 v[20:23], a[44:47], a[32:35], v[20:23]
ds_read_b128 a[60:63], v197 offset:5120
v_mfma_f32_16x16x32_bf16 v[24:27], a[36:39], a[32:35], v[24:27]
buffer_load_dwordx4 v198, s[36:39], s55 offen offset:1024 lds
v_mfma_f32_16x16x32_bf16 v[28:31], a[20:23], a[32:35], v[28:31]
s_waitcnt lgkmcnt(3)
ds_read_b128 a[52:55], v197 offset:6144
v_mfma_f32_16x16x32_bf16 v[32:35], a[68:71], a[32:35], v[32:35]
v_mfma_f32_16x16x32_bf16 v[36:39], a[0:3], a[32:35], v[36:39]
v_mfma_f32_16x16x32_bf16 v[40:43], a[44:47], a[80:83], v[40:43]
ds_read_b128 a[24:27], v197 offset:7168
v_mfma_f32_16x16x32_bf16 v[44:47], a[36:39], a[80:83], v[44:47]
buffer_load_dwordx4 v198, s[36:39], s55 offen offset:2048 lds
v_mfma_f32_16x16x32_bf16 v[48:51], a[20:23], a[80:83], v[48:51]
s_waitcnt lgkmcnt(3)
v_mfma_f32_16x16x32_bf16 v[52:55], a[68:71], a[80:83], v[52:55]
v_mfma_f32_16x16x32_bf16 v[56:59], a[0:3], a[80:83], v[56:59]
v_mfma_f32_16x16x32_bf16 v[60:63], a[44:47], a[16:19], v[60:63]
ds_read_b128 a[84:87], v197 offset:8192
v_mfma_f32_16x16x32_bf16 v[64:67], a[36:39], a[16:19], v[64:67]
buffer_load_dwordx4 v198, s[36:39], s55 offen offset:3072 lds
v_mfma_f32_16x16x32_bf16 v[68:71], a[20:23], a[16:19], v[68:71]
v_mfma_f32_16x16x32_bf16 v[72:75], a[68:71], a[16:19], v[72:75]
v_mfma_f32_16x16x32_bf16 v[76:79], a[0:3], a[16:19], v[76:79]
v_mfma_f32_16x16x32_bf16 v[80:83], a[44:47], a[48:51], v[80:83]
ds_read_b64_tr_b16 a[8:9], v180 offset:0
v_mfma_f32_16x16x32_bf16 v[84:87], a[36:39], a[48:51], v[84:87]
s_and_b32 s60, s68, 61440
s_add_u32 m0, s53, s60
v_mfma_f32_16x16x32_bf16 v[88:91], a[20:23], a[48:51], v[88:91]
s_waitcnt lgkmcnt(2)
v_mfma_f32_16x16x32_bf16 v[92:95], a[68:71], a[48:51], v[92:95]
buffer_load_dwordx4 v199, s[36:39], s56 offen lds
v_mfma_f32_16x16x32_bf16 v[96:99], a[0:3], a[48:51], v[96:99]
ds_read_b64_tr_b16 a[10:11], v180 offset:6144
v_mfma_f32_16x16x32_bf16 v[100:103], a[44:47], a[60:63], v[100:103]
ds_read_b128 a[4:7], v185 offset:0
v_mfma_f32_16x16x32_bf16 v[104:107], a[36:39], a[60:63], v[104:107]
ds_read_b64_tr_b16 a[92:93], v181 offset:0
v_mfma_f32_16x16x32_bf16 v[108:111], a[20:23], a[60:63], v[108:111]
ds_read_b64_tr_b16 a[94:95], v181 offset:6144
v_mfma_f32_16x16x32_bf16 v[112:115], a[68:71], a[60:63], v[112:115]
ds_read_b64_tr_b16 a[76:77], v182 offset:0
v_mfma_f32_16x16x32_bf16 v[116:119], a[0:3], a[60:63], v[116:119]
ds_read_b64_tr_b16 a[78:79], v182 offset:6144
v_mfma_f32_16x16x32_bf16 v[120:123], a[44:47], a[52:55], v[120:123]
ds_read_b64_tr_b16 a[64:65], v183 offset:0
v_mfma_f32_16x16x32_bf16 v[124:127], a[36:39], a[52:55], v[124:127]
ds_read_b64_tr_b16 a[66:67], v183 offset:6144
v_mfma_f32_16x16x32_bf16 v[128:131], a[20:23], a[52:55], v[128:131]
ds_read_b64_tr_b16 a[56:57], v184 offset:0
v_mfma_f32_16x16x32_bf16 v[132:135], a[68:71], a[52:55], v[132:135]
ds_read_b64_tr_b16 a[58:59], v184 offset:6144
s_add_u32 s58, s73, 2
v_mfma_f32_16x16x32_bf16 v[136:139], a[0:3], a[52:55], v[136:139]
s_and_b32 s58, s58, 7
s_add_u32 s58, s58, 16
s_mul_i32 s59, s58, 40960
v_mfma_f32_16x16x32_bf16 v[140:143], a[44:47], a[24:27], v[140:143]
s_add_u32 s57, s76, s59
s_mul_i32 s59, s58, 294912
s_add_u32 s55, s77, s59
v_mfma_f32_16x16x32_bf16 v[144:147], a[36:39], a[24:27], v[144:147]
s_add_u32 s56, s78, s59
s_add_u32 m0, s54, 92160
v_mfma_f32_16x16x32_bf16 v[148:151], a[20:23], a[24:27], v[148:151]
s_waitcnt lgkmcnt(4)
v_mfma_f32_16x16x32_bf16 v[152:155], a[68:71], a[24:27], v[152:155]
buffer_load_dwordx4 v202, s[40:43], s57 offen lds
v_mfma_f32_16x16x32_bf16 v[156:159], a[0:3], a[24:27], v[156:159]
ds_read_b128 a[88:91], v185 offset:1024
v_mfma_f32_16x16x32_bf16 v[160:163], a[44:47], a[84:87], v[160:163]
buffer_load_dwordx4 v203, s[40:43], s57 offen offset:1024 lds
v_mfma_f32_16x16x32_bf16 v[164:167], a[36:39], a[84:87], v[164:167]
ds_read_b128 a[12:15], v185 offset:2048
v_mfma_f32_16x16x32_bf16 v[168:171], a[20:23], a[84:87], v[168:171]
buffer_load_dwordx4 v204, s[40:43], s57 offen offset:2048 lds
v_mfma_f32_16x16x32_bf16 v[172:175], a[68:71], a[84:87], v[172:175]
ds_read_b128 a[28:31], v185 offset:3072
s_add_u32 m0, s52, 92160
v_mfma_f32_16x16x32_bf16 v[176:179], a[0:3], a[84:87], v[176:179]
buffer_load_dwordx4 v198, s[36:39], s55 offen lds
v_mfma_f32_16x16x32_bf16 v[0:3], a[8:11], a[4:7], v[0:3]
s_waitcnt lgkmcnt(3)
ds_read_b128 a[72:75], v185 offset:4096
v_mfma_f32_16x16x32_bf16 v[4:7], a[92:95], a[4:7], v[4:7]
buffer_load_dwordx4 v198, s[36:39], s55 offen offset:1024 lds
v_mfma_f32_16x16x32_bf16 v[8:11], a[76:79], a[4:7], v[8:11]
s_waitcnt lgkmcnt(3)
ds_read_b128 a[40:43], v185 offset:5120
v_mfma_f32_16x16x32_bf16 v[12:15], a[64:67], a[4:7], v[12:15]
v_mfma_f32_16x16x32_bf16 v[16:19], a[56:59], a[4:7], v[16:19]
v_mfma_f32_16x16x32_bf16 v[20:23], a[8:11], a[88:91], v[20:23]
ds_read_b128 a[32:35], v185 offset:6144
v_mfma_f32_16x16x32_bf16 v[24:27], a[92:95], a[88:91], v[24:27]
buffer_load_dwordx4 v198, s[36:39], s55 offen offset:2048 lds
v_mfma_f32_16x16x32_bf16 v[28:31], a[76:79], a[88:91], v[28:31]
s_waitcnt lgkmcnt(3)
v_mfma_f32_16x16x32_bf16 v[32:35], a[64:67], a[88:91], v[32:35]
v_mfma_f32_16x16x32_bf16 v[36:39], a[56:59], a[88:91], v[36:39]
v_mfma_f32_16x16x32_bf16 v[40:43], a[8:11], a[12:15], v[40:43]
ds_read_b128 a[80:83], v185 offset:7168
v_mfma_f32_16x16x32_bf16 v[44:47], a[92:95], a[12:15], v[44:47]
buffer_load_dwordx4 v198, s[36:39], s55 offen offset:3072 lds
v_mfma_f32_16x16x32_bf16 v[48:51], a[76:79], a[12:15], v[48:51]
v_mfma_f32_16x16x32_bf16 v[52:55], a[64:67], a[12:15], v[52:55]
v_mfma_f32_16x16x32_bf16 v[56:59], a[56:59], a[12:15], v[56:59]
v_mfma_f32_16x16x32_bf16 v[60:63], a[8:11], a[28:31], v[60:63]
ds_read_b128 a[16:19], v185 offset:8192
v_mfma_f32_16x16x32_bf16 v[64:67], a[92:95], a[28:31], v[64:67]
s_and_b32 s60, s68, 92160
s_add_u32 m0, s53, s60
v_mfma_f32_16x16x32_bf16 v[68:71], a[76:79], a[28:31], v[68:71]
s_waitcnt lgkmcnt(2)
v_mfma_f32_16x16x32_bf16 v[72:75], a[64:67], a[28:31], v[72:75]
buffer_load_dwordx4 v199, s[36:39], s56 offen lds
v_mfma_f32_16x16x32_bf16 v[76:79], a[56:59], a[28:31], v[76:79]
v_mfma_f32_16x16x32_bf16 v[80:83], a[8:11], a[72:75], v[80:83]
v_mfma_f32_16x16x32_bf16 v[84:87], a[92:95], a[72:75], v[84:87]
v_mfma_f32_16x16x32_bf16 v[88:91], a[76:79], a[72:75], v[88:91]
v_mfma_f32_16x16x32_bf16 v[92:95], a[64:67], a[72:75], v[92:95]
v_mfma_f32_16x16x32_bf16 v[96:99], a[56:59], a[72:75], v[96:99]
v_mfma_f32_16x16x32_bf16 v[100:103], a[8:11], a[40:43], v[100:103]
v_mfma_f32_16x16x32_bf16 v[104:107], a[92:95], a[40:43], v[104:107]
v_mfma_f32_16x16x32_bf16 v[108:111], a[76:79], a[40:43], v[108:111]
v_mfma_f32_16x16x32_bf16 v[112:115], a[64:67], a[40:43], v[112:115]
v_mfma_f32_16x16x32_bf16 v[116:119], a[56:59], a[40:43], v[116:119]
v_mfma_f32_16x16x32_bf16 v[120:123], a[8:11], a[32:35], v[120:123]
s_waitcnt vmcnt(8) lgkmcnt(0)
s_barrier
v_mfma_f32_16x16x32_bf16 v[124:127], a[92:95], a[32:35], v[124:127]
ds_read_b64_tr_b16 a[48:49], v180 offset:30720
v_mfma_f32_16x16x32_bf16 v[128:131], a[76:79], a[32:35], v[128:131]
ds_read_b64_tr_b16 a[50:51], v180 offset:36864
v_mfma_f32_16x16x32_bf16 v[132:135], a[64:67], a[32:35], v[132:135]
ds_read_b128 a[60:63], v185 offset:30720
v_mfma_f32_16x16x32_bf16 v[136:139], a[56:59], a[32:35], v[136:139]
ds_read_b64_tr_b16 a[52:53], v181 offset:30720
ds_read_b64_tr_b16 a[54:55], v181 offset:36864
v_mfma_f32_16x16x32_bf16 v[140:143], a[8:11], a[80:83], v[140:143]
ds_read_b64_tr_b16 a[24:25], v182 offset:30720
ds_read_b64_tr_b16 a[26:27], v182 offset:36864
v_mfma_f32_16x16x32_bf16 v[144:147], a[92:95], a[80:83], v[144:147]
ds_read_b64_tr_b16 a[44:45], v183 offset:30720
ds_read_b64_tr_b16 a[46:47], v183 offset:36864
v_mfma_f32_16x16x32_bf16 v[148:151], a[76:79], a[80:83], v[148:151]
ds_read_b64_tr_b16 a[36:37], v184 offset:30720
ds_read_b64_tr_b16 a[38:39], v184 offset:36864
v_mfma_f32_16x16x32_bf16 v[152:155], a[64:67], a[80:83], v[152:155]
ds_read_b128 a[20:23], v185 offset:31744
s_add_u32 s58, s73, 3
v_mfma_f32_16x16x32_bf16 v[156:159], a[56:59], a[80:83], v[156:159]
s_and_b32 s58, s58, 7
s_add_u32 s58, s58, 16
s_mul_i32 s59, s58, 40960
v_mfma_f32_16x16x32_bf16 v[160:163], a[8:11], a[16:19], v[160:163]
s_add_u32 s57, s76, s59
s_mul_i32 s59, s58, 294912
s_add_u32 s55, s77, s59
v_mfma_f32_16x16x32_bf16 v[164:167], a[92:95], a[16:19], v[164:167]
s_add_u32 s56, s78, s59
s_add_u32 m0, s54, 122880
v_mfma_f32_16x16x32_bf16 v[168:171], a[76:79], a[16:19], v[168:171]
s_waitcnt lgkmcnt(7)
v_mfma_f32_16x16x32_bf16 v[172:175], a[64:67], a[16:19], v[172:175]
buffer_load_dwordx4 v202, s[40:43], s57 offen lds
v_mfma_f32_16x16x32_bf16 v[176:179], a[56:59], a[16:19], v[176:179]
s_waitcnt lgkmcnt(3)
ds_read_b128 a[68:71], v185 offset:32768
v_mfma_f32_16x16x32_bf16 v[0:3], a[48:51], a[60:63], v[0:3]
buffer_load_dwordx4 v203, s[40:43], s57 offen offset:1024 lds
v_mfma_f32_16x16x32_bf16 v[4:7], a[52:55], a[60:63], v[4:7]
s_waitcnt lgkmcnt(1)
ds_read_b128 a[84:87], v185 offset:33792
v_mfma_f32_16x16x32_bf16 v[8:11], a[24:27], a[60:63], v[8:11]
buffer_load_dwordx4 v204, s[40:43], s57 offen offset:2048 lds
v_mfma_f32_16x16x32_bf16 v[12:15], a[44:47], a[60:63], v[12:15]
ds_read_b128 a[0:3], v185 offset:34816
s_add_u32 m0, s52, 122880
v_mfma_f32_16x16x32_bf16 v[16:19], a[36:39], a[60:63], v[16:19]
buffer_load_dwordx4 v198, s[36:39], s55 offen lds
v_mfma_f32_16x16x32_bf16 v[20:23], a[48:51], a[20:23], v[20:23]
ds_read_b128 a[4:7], v185 offset:35840
v_mfma_f32_16x16x32_bf16 v[24:27], a[52:55], a[20:23], v[24:27]
buffer_load_dwordx4 v198, s[36:39], s55 offen offset:1024 lds
v_mfma_f32_16x16x32_bf16 v[28:31], a[24:27], a[20:23], v[28:31]
s_waitcnt lgkmcnt(3)
ds_read_b128 a[88:91], v185 offset:36864
v_mfma_f32_16x16x32_bf16 v[32:35], a[44:47], a[20:23], v[32:35]
v_mfma_f32_16x16x32_bf16 v[36:39], a[36:39], a[20:23], v[36:39]
v_mfma_f32_16x16x32_bf16 v[40:43], a[48:51], a[68:71], v[40:43]
ds_read_b128 a[12:15], v185 offset:37888
v_mfma_f32_16x16x32_bf16 v[44:47], a[52:55], a[68:71], v[44:47]
buffer_load_dwordx4 v198, s[36:39], s55 offen offset:2048 lds
v_mfma_f32_16x16x32_bf16 v[48:51], a[24:27], a[68:71], v[48:51]
s_waitcnt lgkmcnt(3)
v_mfma_f32_16x16x32_bf16 v[52:55], a[44:47], a[68:71], v[52:55]
v_mfma_f32_16x16x32_bf16 v[56:59], a[36:39], a[68:71], v[56:59]
v_mfma_f32_16x16x32_bf16 v[60:63], a[48:51], a[84:87], v[60:63]
ds_read_b128 a[28:31], v185 offset:38912
v_mfma_f32_16x16x32_bf16 v[64:67], a[52:55], a[84:87], v[64:67]
buffer_load_dwordx4 v198, s[36:39], s55 offen offset:3072 lds
v_mfma_f32_16x16x32_bf16 v[68:71], a[24:27], a[84:87], v[68:71]
v_mfma_f32_16x16x32_bf16 v[72:75], a[44:47], a[84:87], v[72:75]
v_mfma_f32_16x16x32_bf16 v[76:79], a[36:39], a[84:87], v[76:79]
v_mfma_f32_16x16x32_bf16 v[80:83], a[48:51], a[0:3], v[80:83]
ds_read_b64_tr_b16 a[72:73], v186 offset:0
v_mfma_f32_16x16x32_bf16 v[84:87], a[52:55], a[0:3], v[84:87]
s_and_b32 s60, s68, 122880
s_add_u32 m0, s53, s60
v_mfma_f32_16x16x32_bf16 v[88:91], a[24:27], a[0:3], v[88:91]
s_waitcnt lgkmcnt(2)
v_mfma_f32_16x16x32_bf16 v[92:95], a[44:47], a[0:3], v[92:95]
buffer_load_dwordx4 v199, s[36:39], s56 offen lds
v_mfma_f32_16x16x32_bf16 v[96:99], a[36:39], a[0:3], v[96:99]
ds_read_b64_tr_b16 a[74:75], v186 offset:6144
v_mfma_f32_16x16x32_bf16 v[100:103], a[48:51], a[4:7], v[100:103]
ds_read_b128 a[40:43], v191 offset:0
v_mfma_f32_16x16x32_bf16 v[104:107], a[52:55], a[4:7], v[104:107]
ds_read_b64_tr_b16 a[32:33], v187 offset:0
v_mfma_f32_16x16x32_bf16 v[108:111], a[24:27], a[4:7], v[108:111]
ds_read_b64_tr_b16 a[34:35], v187 offset:6144
v_mfma_f32_16x16x32_bf16 v[112:115], a[44:47], a[4:7], v[112:115]
ds_read_b64_tr_b16 a[80:81], v188 offset:0
v_mfma_f32_16x16x32_bf16 v[116:119], a[36:39], a[4:7], v[116:119]
ds_read_b64_tr_b16 a[82:83], v188 offset:6144
v_mfma_f32_16x16x32_bf16 v[120:123], a[48:51], a[88:91], v[120:123]
ds_read_b64_tr_b16 a[8:9], v189 offset:0
v_mfma_f32_16x16x32_bf16 v[124:127], a[52:55], a[88:91], v[124:127]
ds_read_b64_tr_b16 a[10:11], v189 offset:6144
v_mfma_f32_16x16x32_bf16 v[128:131], a[24:27], a[88:91], v[128:131]
ds_read_b64_tr_b16 a[92:93], v190 offset:0
v_mfma_f32_16x16x32_bf16 v[132:135], a[44:47], a[88:91], v[132:135]
ds_read_b64_tr_b16 a[94:95], v190 offset:6144
s_add_u32 s58, s73, 4
v_mfma_f32_16x16x32_bf16 v[136:139], a[36:39], a[88:91], v[136:139]
s_and_b32 s58, s58, 7
s_add_u32 s58, s58, 16
s_mul_i32 s59, s58, 40960
v_mfma_f32_16x16x32_bf16 v[140:143], a[48:51], a[12:15], v[140:143]
s_add_u32 s57, s76, s59
s_mul_i32 s59, s58, 294912
s_add_u32 s55, s77, s59
v_mfma_f32_16x16x32_bf16 v[144:147], a[52:55], a[12:15], v[144:147]
s_add_u32 s56, s78, s59
s_add_u32 m0, s54, 0
v_mfma_f32_16x16x32_bf16 v[148:151], a[24:27], a[12:15], v[148:151]
s_waitcnt lgkmcnt(4)
v_mfma_f32_16x16x32_bf16 v[152:155], a[44:47], a[12:15], v[152:155]
buffer_load_dwordx4 v202, s[40:43], s57 offen lds
v_mfma_f32_16x16x32_bf16 v[156:159], a[36:39], a[12:15], v[156:159]
ds_read_b128 a[76:79], v191 offset:1024
v_mfma_f32_16x16x32_bf16 v[160:163], a[48:51], a[28:31], v[160:163]
buffer_load_dwordx4 v203, s[40:43], s57 offen offset:1024 lds
v_mfma_f32_16x16x32_bf16 v[164:167], a[52:55], a[28:31], v[164:167]
ds_read_b128 a[64:67], v191 offset:2048
v_mfma_f32_16x16x32_bf16 v[168:171], a[24:27], a[28:31], v[168:171]
buffer_load_dwordx4 v204, s[40:43], s57 offen offset:2048 lds
v_mfma_f32_16x16x32_bf16 v[172:175], a[44:47], a[28:31], v[172:175]
ds_read_b128 a[16:19], v191 offset:3072
s_add_u32 m0, s52, 0
v_mfma_f32_16x16x32_bf16 v[176:179], a[36:39], a[28:31], v[176:179]
buffer_load_dwordx4 v198, s[36:39], s55 offen lds
v_mfma_f32_16x16x32_bf16 v[0:3], a[72:75], a[40:43], v[0:3]
s_waitcnt lgkmcnt(3)
ds_read_b128 a[56:59], v191 offset:4096
v_mfma_f32_16x16x32_bf16 v[4:7], a[32:35], a[40:43], v[4:7]
buffer_load_dwordx4 v198, s[36:39], s55 offen offset:1024 lds
v_mfma_f32_16x16x32_bf16 v[8:11], a[80:83], a[40:43], v[8:11]
s_waitcnt lgkmcnt(3)
ds_read_b128 a[60:63], v191 offset:5120
v_mfma_f32_16x16x32_bf16 v[12:15], a[8:11], a[40:43], v[12:15]
v_mfma_f32_16x16x32_bf16 v[16:19], a[92:95], a[40:43], v[16:19]
v_mfma_f32_16x16x32_bf16 v[20:23], a[72:75], a[76:79], v[20:23]
ds_read_b128 a[20:23], v191 offset:6144
v_mfma_f32_16x16x32_bf16 v[24:27], a[32:35], a[76:79], v[24:27]
buffer_load_dwordx4 v198, s[36:39], s55 offen offset:2048 lds
v_mfma_f32_16x16x32_bf16 v[28:31], a[80:83], a[76:79], v[28:31]
s_waitcnt lgkmcnt(3)
v_mfma_f32_16x16x32_bf16 v[32:35], a[8:11], a[76:79], v[32:35]
v_mfma_f32_16x16x32_bf16 v[36:39], a[92:95], a[76:79], v[36:39]
v_mfma_f32_16x16x32_bf16 v[40:43], a[72:75], a[64:67], v[40:43]
ds_read_b128 a[68:71], v191 offset:7168
v_mfma_f32_16x16x32_bf16 v[44:47], a[32:35], a[64:67], v[44:47]
buffer_load_dwordx4 v198, s[36:39], s55 offen offset:3072 lds
v_mfma_f32_16x16x32_bf16 v[48:51], a[80:83], a[64:67], v[48:51]
v_mfma_f32_16x16x32_bf16 v[52:55], a[8:11], a[64:67], v[52:55]
v_mfma_f32_16x16x32_bf16 v[56:59], a[92:95], a[64:67], v[56:59]
v_mfma_f32_16x16x32_bf16 v[60:63], a[72:75], a[16:19], v[60:63]
ds_read_b128 a[84:87], v191 offset:8192
v_mfma_f32_16x16x32_bf16 v[64:67], a[32:35], a[16:19], v[64:67]
s_and_b32 s60, s68, 0
s_add_u32 m0, s53, s60
v_mfma_f32_16x16x32_bf16 v[68:71], a[80:83], a[16:19], v[68:71]
s_waitcnt lgkmcnt(2)
v_mfma_f32_16x16x32_bf16 v[72:75], a[8:11], a[16:19], v[72:75]
buffer_load_dwordx4 v199, s[36:39], s56 offen lds
v_mfma_f32_16x16x32_bf16 v[76:79], a[92:95], a[16:19], v[76:79]
v_mfma_f32_16x16x32_bf16 v[80:83], a[72:75], a[56:59], v[80:83]
v_mfma_f32_16x16x32_bf16 v[84:87], a[32:35], a[56:59], v[84:87]
v_mfma_f32_16x16x32_bf16 v[88:91], a[80:83], a[56:59], v[88:91]
v_mfma_f32_16x16x32_bf16 v[92:95], a[8:11], a[56:59], v[92:95]
v_mfma_f32_16x16x32_bf16 v[96:99], a[92:95], a[56:59], v[96:99]
v_mfma_f32_16x16x32_bf16 v[100:103], a[72:75], a[60:63], v[100:103]
v_mfma_f32_16x16x32_bf16 v[104:107], a[32:35], a[60:63], v[104:107]
v_mfma_f32_16x16x32_bf16 v[108:111], a[80:83], a[60:63], v[108:111]
v_mfma_f32_16x16x32_bf16 v[112:115], a[8:11], a[60:63], v[112:115]
v_mfma_f32_16x16x32_bf16 v[116:119], a[92:95], a[60:63], v[116:119]
v_mfma_f32_16x16x32_bf16 v[120:123], a[72:75], a[20:23], v[120:123]
s_waitcnt vmcnt(8) lgkmcnt(0)
s_barrier
v_mfma_f32_16x16x32_bf16 v[124:127], a[32:35], a[20:23], v[124:127]
ds_read_b64_tr_b16 a[0:1], v186 offset:30720
v_mfma_f32_16x16x32_bf16 v[128:131], a[80:83], a[20:23], v[128:131]
ds_read_b64_tr_b16 a[2:3], v186 offset:36864
v_mfma_f32_16x16x32_bf16 v[132:135], a[8:11], a[20:23], v[132:135]
ds_read_b128 a[4:7], v191 offset:30720
v_mfma_f32_16x16x32_bf16 v[136:139], a[92:95], a[20:23], v[136:139]
ds_read_b64_tr_b16 a[88:89], v187 offset:30720
ds_read_b64_tr_b16 a[90:91], v187 offset:36864
v_mfma_f32_16x16x32_bf16 v[140:143], a[72:75], a[68:71], v[140:143]
ds_read_b64_tr_b16 a[12:13], v188 offset:30720
ds_read_b64_tr_b16 a[14:15], v188 offset:36864
v_mfma_f32_16x16x32_bf16 v[144:147], a[32:35], a[68:71], v[144:147]
ds_read_b64_tr_b16 a[48:49], v189 offset:30720
ds_read_b64_tr_b16 a[50:51], v189 offset:36864
v_mfma_f32_16x16x32_bf16 v[148:151], a[80:83], a[68:71], v[148:151]
ds_read_b64_tr_b16 a[52:53], v190 offset:30720
ds_read_b64_tr_b16 a[54:55], v190 offset:36864
v_mfma_f32_16x16x32_bf16 v[152:155], a[8:11], a[68:71], v[152:155]
ds_read_b128 a[24:27], v191 offset:31744
s_add_u32 s58, s73, 5
v_mfma_f32_16x16x32_bf16 v[156:159], a[92:95], a[68:71], v[156:159]
s_and_b32 s58, s58, 7
s_add_u32 s58, s58, 16
s_mul_i32 s59, s58, 40960
v_mfma_f32_16x16x32_bf16 v[160:163], a[72:75], a[84:87], v[160:163]
s_add_u32 s57, s76, s59
s_mul_i32 s59, s58, 294912
s_add_u32 s55, s77, s59
v_mfma_f32_16x16x32_bf16 v[164:167], a[32:35], a[84:87], v[164:167]
s_add_u32 s56, s78, s59
s_add_u32 m0, s54, 30720
v_mfma_f32_16x16x32_bf16 v[168:171], a[80:83], a[84:87], v[168:171]
s_waitcnt lgkmcnt(7)
v_mfma_f32_16x16x32_bf16 v[172:175], a[8:11], a[84:87], v[172:175]
buffer_load_dwordx4 v202, s[40:43], s57 offen lds
v_mfma_f32_16x16x32_bf16 v[176:179], a[92:95], a[84:87], v[176:179]
s_waitcnt lgkmcnt(3)
ds_read_b128 a[44:47], v191 offset:32768
v_mfma_f32_16x16x32_bf16 v[0:3], a[0:3], a[4:7], v[0:3]
buffer_load_dwordx4 v203, s[40:43], s57 offen offset:1024 lds
v_mfma_f32_16x16x32_bf16 v[4:7], a[88:91], a[4:7], v[4:7]
s_waitcnt lgkmcnt(1)
ds_read_b128 a[28:31], v191 offset:33792
v_mfma_f32_16x16x32_bf16 v[8:11], a[12:15], a[4:7], v[8:11]
buffer_load_dwordx4 v204, s[40:43], s57 offen offset:2048 lds
v_mfma_f32_16x16x32_bf16 v[12:15], a[48:51], a[4:7], v[12:15]
ds_read_b128 a[36:39], v191 offset:34816
s_add_u32 m0, s52, 30720
v_mfma_f32_16x16x32_bf16 v[16:19], a[52:55], a[4:7], v[16:19]
buffer_load_dwordx4 v198, s[36:39], s55 offen lds
v_mfma_f32_16x16x32_bf16 v[20:23], a[0:3], a[24:27], v[20:23]
ds_read_b128 a[40:43], v191 offset:35840
v_mfma_f32_16x16x32_bf16 v[24:27], a[88:91], a[24:27], v[24:27]
buffer_load_dwordx4 v198, s[36:39], s55 offen offset:1024 lds
v_mfma_f32_16x16x32_bf16 v[28:31], a[12:15], a[24:27], v[28:31]
s_waitcnt lgkmcnt(3)
ds_read_b128 a[76:79], v191 offset:36864
v_mfma_f32_16x16x32_bf16 v[32:35], a[48:51], a[24:27], v[32:35]
v_mfma_f32_16x16x32_bf16 v[36:39], a[52:55], a[24:27], v[36:39]
v_mfma_f32_16x16x32_bf16 v[40:43], a[0:3], a[44:47], v[40:43]
ds_read_b128 a[64:67], v191 offset:37888
v_mfma_f32_16x16x32_bf16 v[44:47], a[88:91], a[44:47], v[44:47]
buffer_load_dwordx4 v198, s[36:39], s55 offen offset:2048 lds
v_mfma_f32_16x16x32_bf16 v[48:51], a[12:15], a[44:47], v[48:51]
s_waitcnt lgkmcnt(3)
v_mfma_f32_16x16x32_bf16 v[52:55], a[48:51], a[44:47], v[52:55]
v_mfma_f32_16x16x32_bf16 v[56:59], a[52:55], a[44:47], v[56:59]
v_mfma_f32_16x16x32_bf16 v[60:63], a[0:3], a[28:31], v[60:63]
ds_read_b128 a[16:19], v191 offset:38912
v_mfma_f32_16x16x32_bf16 v[64:67], a[88:91], a[28:31], v[64:67]
buffer_load_dwordx4 v198, s[36:39], s55 offen offset:3072 lds
v_mfma_f32_16x16x32_bf16 v[68:71], a[12:15], a[28:31], v[68:71]
v_mfma_f32_16x16x32_bf16 v[72:75], a[48:51], a[28:31], v[72:75]
v_mfma_f32_16x16x32_bf16 v[76:79], a[52:55], a[28:31], v[76:79]
v_mfma_f32_16x16x32_bf16 v[80:83], a[0:3], a[36:39], v[80:83]
ds_read_b64_tr_b16 a[56:57], v192 offset:0
v_mfma_f32_16x16x32_bf16 v[84:87], a[88:91], a[36:39], v[84:87]
s_and_b32 s60, s68, 30720
s_add_u32 m0, s53, s60
v_mfma_f32_16x16x32_bf16 v[88:91], a[12:15], a[36:39], v[88:91]
s_waitcnt lgkmcnt(2)
v_mfma_f32_16x16x32_bf16 v[92:95], a[48:51], a[36:39], v[92:95]
buffer_load_dwordx4 v199, s[36:39], s56 offen lds
v_mfma_f32_16x16x32_bf16 v[96:99], a[52:55], a[36:39], v[96:99]
ds_read_b64_tr_b16 a[58:59], v192 offset:6144
v_mfma_f32_16x16x32_bf16 v[100:103], a[0:3], a[40:43], v[100:103]
ds_read_b128 a[60:63], v197 offset:0
v_mfma_f32_16x16x32_bf16 v[104:107], a[88:91], a[40:43], v[104:107]
ds_read_b64_tr_b16 a[20:21], v193 offset:0
v_mfma_f32_16x16x32_bf16 v[108:111], a[12:15], a[40:43], v[108:111]
ds_read_b64_tr_b16 a[22:23], v193 offset:6144
v_mfma_f32_16x16x32_bf16 v[112:115], a[48:51], a[40:43], v[112:115]
ds_read_b64_tr_b16 a[68:69], v194 offset:0
v_mfma_f32_16x16x32_bf16 v[116:119], a[52:55], a[40:43], v[116:119]
ds_read_b64_tr_b16 a[70:71], v194 offset:6144
v_mfma_f32_16x16x32_bf16 v[120:123], a[0:3], a[76:79], v[120:123]
ds_read_b64_tr_b16 a[72:73], v195 offset:0
v_mfma_f32_16x16x32_bf16 v[124:127], a[88:91], a[76:79], v[124:127]
ds_read_b64_tr_b16 a[74:75], v195 offset:6144
v_mfma_f32_16x16x32_bf16 v[128:131], a[12:15], a[76:79], v[128:131]
ds_read_b64_tr_b16 a[32:33], v196 offset:0
v_mfma_f32_16x16x32_bf16 v[132:135], a[48:51], a[76:79], v[132:135]
ds_read_b64_tr_b16 a[34:35], v196 offset:6144
s_add_u32 s58, s73, 6
v_mfma_f32_16x16x32_bf16 v[136:139], a[52:55], a[76:79], v[136:139]
s_and_b32 s58, s58, 7
s_add_u32 s58, s58, 16
s_mul_i32 s59, s58, 40960
v_mfma_f32_16x16x32_bf16 v[140:143], a[0:3], a[64:67], v[140:143]
s_add_u32 s57, s76, s59
s_mul_i32 s59, s58, 294912
s_add_u32 s55, s77, s59
v_mfma_f32_16x16x32_bf16 v[144:147], a[88:91], a[64:67], v[144:147]
s_add_u32 s56, s78, s59
s_add_u32 m0, s54, 61440
v_mfma_f32_16x16x32_bf16 v[148:151], a[12:15], a[64:67], v[148:151]
s_waitcnt lgkmcnt(4)
v_mfma_f32_16x16x32_bf16 v[152:155], a[48:51], a[64:67], v[152:155]
buffer_load_dwordx4 v202, s[40:43], s57 offen lds
v_mfma_f32_16x16x32_bf16 v[156:159], a[52:55], a[64:67], v[156:159]
ds_read_b128 a[80:83], v197 offset:1024
v_mfma_f32_16x16x32_bf16 v[160:163], a[0:3], a[16:19], v[160:163]
buffer_load_dwordx4 v203, s[40:43], s57 offen offset:1024 lds
v_mfma_f32_16x16x32_bf16 v[164:167], a[88:91], a[16:19], v[164:167]
ds_read_b128 a[8:11], v197 offset:2048
v_mfma_f32_16x16x32_bf16 v[168:171], a[12:15], a[16:19], v[168:171]
buffer_load_dwordx4 v204, s[40:43], s57 offen offset:2048 lds
v_mfma_f32_16x16x32_bf16 v[172:175], a[48:51], a[16:19], v[172:175]
ds_read_b128 a[84:87], v197 offset:3072
s_add_u32 m0, s52, 61440
v_mfma_f32_16x16x32_bf16 v[176:179], a[52:55], a[16:19], v[176:179]
buffer_load_dwordx4 v198, s[36:39], s55 offen lds
v_mfma_f32_16x16x32_bf16 v[0:3], a[56:59], a[60:63], v[0:3]
s_waitcnt lgkmcnt(3)
ds_read_b128 a[92:95], v197 offset:4096
v_mfma_f32_16x16x32_bf16 v[4:7], a[20:23], a[60:63], v[4:7]
buffer_load_dwordx4 v198, s[36:39], s55 offen offset:1024 lds
v_mfma_f32_16x16x32_bf16 v[8:11], a[68:71], a[60:63], v[8:11]
s_waitcnt lgkmcnt(3)
ds_read_b128 a[4:7], v197 offset:5120
v_mfma_f32_16x16x32_bf16 v[12:15], a[72:75], a[60:63], v[12:15]
v_mfma_f32_16x16x32_bf16 v[16:19], a[32:35], a[60:63], v[16:19]
v_mfma_f32_16x16x32_bf16 v[20:23], a[56:59], a[80:83], v[20:23]
ds_read_b128 a[24:27], v197 offset:6144
v_mfma_f32_16x16x32_bf16 v[24:27], a[20:23], a[80:83], v[24:27]
buffer_load_dwordx4 v198, s[36:39], s55 offen offset:2048 lds
v_mfma_f32_16x16x32_bf16 v[28:31], a[68:71], a[80:83], v[28:31]
s_waitcnt lgkmcnt(3)
v_mfma_f32_16x16x32_bf16 v[32:35], a[72:75], a[80:83], v[32:35]
v_mfma_f32_16x16x32_bf16 v[36:39], a[32:35], a[80:83], v[36:39]
v_mfma_f32_16x16x32_bf16 v[40:43], a[56:59], a[8:11], v[40:43]
ds_read_b128 a[44:47], v197 offset:7168
v_mfma_f32_16x16x32_bf16 v[44:47], a[20:23], a[8:11], v[44:47]
buffer_load_dwordx4 v198, s[36:39], s55 offen offset:3072 lds
v_mfma_f32_16x16x32_bf16 v[48:51], a[68:71], a[8:11], v[48:51]
v_mfma_f32_16x16x32_bf16 v[52:55], a[72:75], a[8:11], v[52:55]
v_mfma_f32_16x16x32_bf16 v[56:59], a[32:35], a[8:11], v[56:59]
v_mfma_f32_16x16x32_bf16 v[60:63], a[56:59], a[84:87], v[60:63]
ds_read_b128 a[28:31], v197 offset:8192
v_mfma_f32_16x16x32_bf16 v[64:67], a[20:23], a[84:87], v[64:67]
s_and_b32 s60, s68, 61440
s_add_u32 m0, s53, s60
v_mfma_f32_16x16x32_bf16 v[68:71], a[68:71], a[84:87], v[68:71]
s_waitcnt lgkmcnt(2)
v_mfma_f32_16x16x32_bf16 v[72:75], a[72:75], a[84:87], v[72:75]
buffer_load_dwordx4 v199, s[36:39], s56 offen lds
v_mfma_f32_16x16x32_bf16 v[76:79], a[32:35], a[84:87], v[76:79]
v_mfma_f32_16x16x32_bf16 v[80:83], a[56:59], a[92:95], v[80:83]
v_mfma_f32_16x16x32_bf16 v[84:87], a[20:23], a[92:95], v[84:87]
v_mfma_f32_16x16x32_bf16 v[88:91], a[68:71], a[92:95], v[88:91]
v_mfma_f32_16x16x32_bf16 v[92:95], a[72:75], a[92:95], v[92:95]
v_mfma_f32_16x16x32_bf16 v[96:99], a[32:35], a[92:95], v[96:99]
v_mfma_f32_16x16x32_bf16 v[100:103], a[56:59], a[4:7], v[100:103]
v_mfma_f32_16x16x32_bf16 v[104:107], a[20:23], a[4:7], v[104:107]
v_mfma_f32_16x16x32_bf16 v[108:111], a[68:71], a[4:7], v[108:111]
v_mfma_f32_16x16x32_bf16 v[112:115], a[72:75], a[4:7], v[112:115]
v_mfma_f32_16x16x32_bf16 v[116:119], a[32:35], a[4:7], v[116:119]
v_mfma_f32_16x16x32_bf16 v[120:123], a[56:59], a[24:27], v[120:123]
s_waitcnt vmcnt(8) lgkmcnt(0)
s_barrier
v_mfma_f32_16x16x32_bf16 v[124:127], a[20:23], a[24:27], v[124:127]
ds_read_b64_tr_b16 a[36:37], v180 offset:0
v_mfma_f32_16x16x32_bf16 v[128:131], a[68:71], a[24:27], v[128:131]
ds_read_b64_tr_b16 a[38:39], v180 offset:6144
v_mfma_f32_16x16x32_bf16 v[132:135], a[72:75], a[24:27], v[132:135]
ds_read_b128 a[40:43], v185 offset:0
v_mfma_f32_16x16x32_bf16 v[136:139], a[32:35], a[24:27], v[136:139]
ds_read_b64_tr_b16 a[76:77], v181 offset:0
ds_read_b64_tr_b16 a[78:79], v181 offset:6144
v_mfma_f32_16x16x32_bf16 v[140:143], a[56:59], a[44:47], v[140:143]
ds_read_b64_tr_b16 a[64:65], v182 offset:0
ds_read_b64_tr_b16 a[66:67], v182 offset:6144
v_mfma_f32_16x16x32_bf16 v[144:147], a[20:23], a[44:47], v[144:147]
ds_read_b64_tr_b16 a[0:1], v183 offset:0
ds_read_b64_tr_b16 a[2:3], v183 offset:6144
v_mfma_f32_16x16x32_bf16 v[148:151], a[68:71], a[44:47], v[148:151]
ds_read_b64_tr_b16 a[88:89], v184 offset:0
ds_read_b64_tr_b16 a[90:91], v184 offset:6144
v_mfma_f32_16x16x32_bf16 v[152:155], a[72:75], a[44:47], v[152:155]
ds_read_b128 a[12:15], v185 offset:1024
s_add_u32 s58, s73, 7
v_mfma_f32_16x16x32_bf16 v[156:159], a[32:35], a[44:47], v[156:159]
s_and_b32 s58, s58, 7
s_add_u32 s58, s58, 16
s_mul_i32 s59, s58, 40960
v_mfma_f32_16x16x32_bf16 v[160:163], a[56:59], a[28:31], v[160:163]
s_add_u32 s57, s76, s59
s_mul_i32 s59, s58, 294912
s_add_u32 s55, s77, s59
v_mfma_f32_16x16x32_bf16 v[164:167], a[20:23], a[28:31], v[164:167]
s_add_u32 s56, s78, s59
s_add_u32 m0, s54, 92160
v_mfma_f32_16x16x32_bf16 v[168:171], a[68:71], a[28:31], v[168:171]
s_waitcnt lgkmcnt(7)
v_mfma_f32_16x16x32_bf16 v[172:175], a[72:75], a[28:31], v[172:175]
buffer_load_dwordx4 v202, s[40:43], s57 offen lds
v_mfma_f32_16x16x32_bf16 v[176:179], a[32:35], a[28:31], v[176:179]
s_waitcnt lgkmcnt(3)
ds_read_b128 a[48:51], v185 offset:2048
v_mfma_f32_16x16x32_bf16 v[0:3], a[36:39], a[40:43], v[0:3]
buffer_load_dwordx4 v203, s[40:43], s57 offen offset:1024 lds
v_mfma_f32_16x16x32_bf16 v[4:7], a[76:79], a[40:43], v[4:7]
s_waitcnt lgkmcnt(1)
ds_read_b128 a[52:55], v185 offset:3072
v_mfma_f32_16x16x32_bf16 v[8:11], a[64:67], a[40:43], v[8:11]
buffer_load_dwordx4 v204, s[40:43], s57 offen offset:2048 lds
v_mfma_f32_16x16x32_bf16 v[12:15], a[0:3], a[40:43], v[12:15]
ds_read_b128 a[16:19], v185 offset:4096
s_add_u32 m0, s52, 92160
v_mfma_f32_16x16x32_bf16 v[16:19], a[88:91], a[40:43], v[16:19]
buffer_load_dwordx4 v198, s[36:39], s55 offen lds
v_mfma_f32_16x16x32_bf16 v[20:23], a[36:39], a[12:15], v[20:23]
ds_read_b128 a[60:63], v185 offset:5120
v_mfma_f32_16x16x32_bf16 v[24:27], a[76:79], a[12:15], v[24:27]
buffer_load_dwordx4 v198, s[36:39], s55 offen offset:1024 lds
v_mfma_f32_16x16x32_bf16 v[28:31], a[64:67], a[12:15], v[28:31]
s_waitcnt lgkmcnt(3)
ds_read_b128 a[80:83], v185 offset:6144
v_mfma_f32_16x16x32_bf16 v[32:35], a[0:3], a[12:15], v[32:35]
v_mfma_f32_16x16x32_bf16 v[36:39], a[88:91], a[12:15], v[36:39]
v_mfma_f32_16x16x32_bf16 v[40:43], a[36:39], a[48:51], v[40:43]
ds_read_b128 a[8:11], v185 offset:7168
v_mfma_f32_16x16x32_bf16 v[44:47], a[76:79], a[48:51], v[44:47]
buffer_load_dwordx4 v198, s[36:39], s55 offen offset:2048 lds
v_mfma_f32_16x16x32_bf16 v[48:51], a[64:67], a[48:51], v[48:51]
s_waitcnt lgkmcnt(3)
v_mfma_f32_16x16x32_bf16 v[52:55], a[0:3], a[48:51], v[52:55]
v_mfma_f32_16x16x32_bf16 v[56:59], a[88:91], a[48:51], v[56:59]
v_mfma_f32_16x16x32_bf16 v[60:63], a[36:39], a[52:55], v[60:63]
ds_read_b128 a[84:87], v185 offset:8192
v_mfma_f32_16x16x32_bf16 v[64:67], a[76:79], a[52:55], v[64:67]
buffer_load_dwordx4 v198, s[36:39], s55 offen offset:3072 lds
v_mfma_f32_16x16x32_bf16 v[68:71], a[64:67], a[52:55], v[68:71]
v_mfma_f32_16x16x32_bf16 v[72:75], a[0:3], a[52:55], v[72:75]
v_mfma_f32_16x16x32_bf16 v[76:79], a[88:91], a[52:55], v[76:79]
v_mfma_f32_16x16x32_bf16 v[80:83], a[36:39], a[16:19], v[80:83]
ds_read_b64_tr_b16 a[92:93], v180 offset:30720
v_mfma_f32_16x16x32_bf16 v[84:87], a[76:79], a[16:19], v[84:87]
s_and_b32 s60, s68, 92160
s_add_u32 m0, s53, s60
v_mfma_f32_16x16x32_bf16 v[88:91], a[64:67], a[16:19], v[88:91]
s_waitcnt lgkmcnt(2)
v_mfma_f32_16x16x32_bf16 v[92:95], a[0:3], a[16:19], v[92:95]
buffer_load_dwordx4 v199, s[36:39], s56 offen lds
v_mfma_f32_16x16x32_bf16 v[96:99], a[88:91], a[16:19], v[96:99]
ds_read_b64_tr_b16 a[94:95], v180 offset:36864
v_mfma_f32_16x16x32_bf16 v[100:103], a[36:39], a[60:63], v[100:103]
ds_read_b128 a[4:7], v185 offset:30720
v_mfma_f32_16x16x32_bf16 v[104:107], a[76:79], a[60:63], v[104:107]
ds_read_b64_tr_b16 a[24:25], v181 offset:30720
v_mfma_f32_16x16x32_bf16 v[108:111], a[64:67], a[60:63], v[108:111]
ds_read_b64_tr_b16 a[26:27], v181 offset:36864
v_mfma_f32_16x16x32_bf16 v[112:115], a[0:3], a[60:63], v[112:115]
ds_read_b64_tr_b16 a[44:45], v182 offset:30720
v_mfma_f32_16x16x32_bf16 v[116:119], a[88:91], a[60:63], v[116:119]
ds_read_b64_tr_b16 a[46:47], v182 offset:36864
v_mfma_f32_16x16x32_bf16 v[120:123], a[36:39], a[80:83], v[120:123]
ds_read_b64_tr_b16 a[56:57], v183 offset:30720
v_mfma_f32_16x16x32_bf16 v[124:127], a[76:79], a[80:83], v[124:127]
ds_read_b64_tr_b16 a[58:59], v183 offset:36864
v_mfma_f32_16x16x32_bf16 v[128:131], a[64:67], a[80:83], v[128:131]
ds_read_b64_tr_b16 a[20:21], v184 offset:30720
v_mfma_f32_16x16x32_bf16 v[132:135], a[0:3], a[80:83], v[132:135]
ds_read_b64_tr_b16 a[22:23], v184 offset:36864
s_add_u32 s58, s73, 0
v_mfma_f32_16x16x32_bf16 v[136:139], a[88:91], a[80:83], v[136:139]
s_and_b32 s58, s58, 7
s_add_u32 s58, s58, 24
s_mul_i32 s59, s58, 40960
v_mfma_f32_16x16x32_bf16 v[140:143], a[36:39], a[8:11], v[140:143]
s_add_u32 s57, s76, s59
s_mul_i32 s59, s58, 294912
s_add_u32 s55, s77, s59
v_mfma_f32_16x16x32_bf16 v[144:147], a[76:79], a[8:11], v[144:147]
s_add_u32 s56, s78, s59
s_add_u32 m0, s54, 122880
v_mfma_f32_16x16x32_bf16 v[148:151], a[64:67], a[8:11], v[148:151]
s_waitcnt lgkmcnt(4)
v_mfma_f32_16x16x32_bf16 v[152:155], a[0:3], a[8:11], v[152:155]
buffer_load_dwordx4 v202, s[40:43], s57 offen lds
v_mfma_f32_16x16x32_bf16 v[156:159], a[88:91], a[8:11], v[156:159]
ds_read_b128 a[68:71], v185 offset:31744
v_mfma_f32_16x16x32_bf16 v[160:163], a[36:39], a[84:87], v[160:163]
buffer_load_dwordx4 v203, s[40:43], s57 offen offset:1024 lds
v_mfma_f32_16x16x32_bf16 v[164:167], a[76:79], a[84:87], v[164:167]
ds_read_b128 a[72:75], v185 offset:32768
v_mfma_f32_16x16x32_bf16 v[168:171], a[64:67], a[84:87], v[168:171]
buffer_load_dwordx4 v204, s[40:43], s57 offen offset:2048 lds
v_mfma_f32_16x16x32_bf16 v[172:175], a[0:3], a[84:87], v[172:175]
ds_read_b128 a[28:31], v185 offset:33792
s_add_u32 m0, s52, 122880
v_mfma_f32_16x16x32_bf16 v[176:179], a[88:91], a[84:87], v[176:179]
buffer_load_dwordx4 v198, s[36:39], s55 offen lds
v_mfma_f32_16x16x32_bf16 v[0:3], a[92:95], a[4:7], v[0:3]
s_waitcnt lgkmcnt(3)
ds_read_b128 a[32:35], v185 offset:34816
v_mfma_f32_16x16x32_bf16 v[4:7], a[24:27], a[4:7], v[4:7]
buffer_load_dwordx4 v198, s[36:39], s55 offen offset:1024 lds
v_mfma_f32_16x16x32_bf16 v[8:11], a[44:47], a[4:7], v[8:11]
s_waitcnt lgkmcnt(3)
ds_read_b128 a[40:43], v185 offset:35840
v_mfma_f32_16x16x32_bf16 v[12:15], a[56:59], a[4:7], v[12:15]
v_mfma_f32_16x16x32_bf16 v[16:19], a[20:23], a[4:7], v[16:19]
v_mfma_f32_16x16x32_bf16 v[20:23], a[92:95], a[68:71], v[20:23]
ds_read_b128 a[12:15], v185 offset:36864
v_mfma_f32_16x16x32_bf16 v[24:27], a[24:27], a[68:71], v[24:27]
buffer_load_dwordx4 v198, s[36:39], s55 offen offset:2048 lds
v_mfma_f32_16x16x32_bf16 v[28:31], a[44:47], a[68:71], v[28:31]
s_waitcnt lgkmcnt(3)
v_mfma_f32_16x16x32_bf16 v[32:35], a[56:59], a[68:71], v[32:35]
v_mfma_f32_16x16x32_bf16 v[36:39], a[20:23], a[68:71], v[36:39]
v_mfma_f32_16x16x32_bf16 v[40:43], a[92:95], a[72:75], v[40:43]
ds_read_b128 a[48:51], v185 offset:37888
v_mfma_f32_16x16x32_bf16 v[44:47], a[24:27], a[72:75], v[44:47]
buffer_load_dwordx4 v198, s[36:39], s55 offen offset:3072 lds
v_mfma_f32_16x16x32_bf16 v[48:51], a[44:47], a[72:75], v[48:51]
v_mfma_f32_16x16x32_bf16 v[52:55], a[56:59], a[72:75], v[52:55]
v_mfma_f32_16x16x32_bf16 v[56:59], a[20:23], a[72:75], v[56:59]
v_mfma_f32_16x16x32_bf16 v[60:63], a[92:95], a[28:31], v[60:63]
ds_read_b128 a[52:55], v185 offset:38912
v_mfma_f32_16x16x32_bf16 v[64:67], a[24:27], a[28:31], v[64:67]
s_and_b32 s60, s68, 122880
s_add_u32 m0, s53, s60
v_mfma_f32_16x16x32_bf16 v[68:71], a[44:47], a[28:31], v[68:71]
s_waitcnt lgkmcnt(2)
v_mfma_f32_16x16x32_bf16 v[72:75], a[56:59], a[28:31], v[72:75]
buffer_load_dwordx4 v199, s[36:39], s56 offen lds
v_mfma_f32_16x16x32_bf16 v[76:79], a[20:23], a[28:31], v[76:79]
v_mfma_f32_16x16x32_bf16 v[80:83], a[92:95], a[32:35], v[80:83]
v_mfma_f32_16x16x32_bf16 v[84:87], a[24:27], a[32:35], v[84:87]
v_mfma_f32_16x16x32_bf16 v[88:91], a[44:47], a[32:35], v[88:91]
v_mfma_f32_16x16x32_bf16 v[92:95], a[56:59], a[32:35], v[92:95]
v_mfma_f32_16x16x32_bf16 v[96:99], a[20:23], a[32:35], v[96:99]
v_mfma_f32_16x16x32_bf16 v[100:103], a[92:95], a[40:43], v[100:103]
v_mfma_f32_16x16x32_bf16 v[104:107], a[24:27], a[40:43], v[104:107]
v_mfma_f32_16x16x32_bf16 v[108:111], a[44:47], a[40:43], v[108:111]
v_mfma_f32_16x16x32_bf16 v[112:115], a[56:59], a[40:43], v[112:115]
v_mfma_f32_16x16x32_bf16 v[116:119], a[20:23], a[40:43], v[116:119]
v_mfma_f32_16x16x32_bf16 v[120:123], a[92:95], a[12:15], v[120:123]
s_waitcnt vmcnt(8) lgkmcnt(0)
s_barrier
v_mfma_f32_16x16x32_bf16 v[124:127], a[24:27], a[12:15], v[124:127]
ds_read_b64_tr_b16 a[16:17], v186 offset:0
v_mfma_f32_16x16x32_bf16 v[128:131], a[44:47], a[12:15], v[128:131]
ds_read_b64_tr_b16 a[18:19], v186 offset:6144
v_mfma_f32_16x16x32_bf16 v[132:135], a[56:59], a[12:15], v[132:135]
ds_read_b128 a[60:63], v191 offset:0
v_mfma_f32_16x16x32_bf16 v[136:139], a[20:23], a[12:15], v[136:139]
ds_read_b64_tr_b16 a[80:81], v187 offset:0
ds_read_b64_tr_b16 a[82:83], v187 offset:6144
v_mfma_f32_16x16x32_bf16 v[140:143], a[92:95], a[48:51], v[140:143]
ds_read_b64_tr_b16 a[8:9], v188 offset:0
ds_read_b64_tr_b16 a[10:11], v188 offset:6144
v_mfma_f32_16x16x32_bf16 v[144:147], a[24:27], a[48:51], v[144:147]
ds_read_b64_tr_b16 a[36:37], v189 offset:0
ds_read_b64_tr_b16 a[38:39], v189 offset:6144
v_mfma_f32_16x16x32_bf16 v[148:151], a[44:47], a[48:51], v[148:151]
ds_read_b64_tr_b16 a[76:77], v190 offset:0
ds_read_b64_tr_b16 a[78:79], v190 offset:6144
v_mfma_f32_16x16x32_bf16 v[152:155], a[56:59], a[48:51], v[152:155]
ds_read_b128 a[64:67], v191 offset:1024
s_add_u32 s58, s73, 1
v_mfma_f32_16x16x32_bf16 v[156:159], a[20:23], a[48:51], v[156:159]
s_and_b32 s58, s58, 7
s_add_u32 s58, s58, 24
s_mul_i32 s59, s58, 40960
v_mfma_f32_16x16x32_bf16 v[160:163], a[92:95], a[52:55], v[160:163]
s_add_u32 s57, s76, s59
s_mul_i32 s59, s58, 294912
s_add_u32 s55, s77, s59
v_mfma_f32_16x16x32_bf16 v[164:167], a[24:27], a[52:55], v[164:167]
s_add_u32 s56, s78, s59
s_add_u32 m0, s54, 0
v_mfma_f32_16x16x32_bf16 v[168:171], a[44:47], a[52:55], v[168:171]
s_waitcnt lgkmcnt(7)
v_mfma_f32_16x16x32_bf16 v[172:175], a[56:59], a[52:55], v[172:175]
buffer_load_dwordx4 v202, s[40:43], s57 offen lds
v_mfma_f32_16x16x32_bf16 v[176:179], a[20:23], a[52:55], v[176:179]
s_waitcnt lgkmcnt(3)
ds_read_b128 a[0:3], v191 offset:2048
v_mfma_f32_16x16x32_bf16 v[0:3], a[16:19], a[60:63], v[0:3]
buffer_load_dwordx4 v203, s[40:43], s57 offen offset:1024 lds
v_mfma_f32_16x16x32_bf16 v[4:7], a[80:83], a[60:63], v[4:7]
s_waitcnt lgkmcnt(1)
ds_read_b128 a[84:87], v191 offset:3072
v_mfma_f32_16x16x32_bf16 v[8:11], a[8:11], a[60:63], v[8:11]
buffer_load_dwordx4 v204, s[40:43], s57 offen offset:2048 lds
v_mfma_f32_16x16x32_bf16 v[12:15], a[36:39], a[60:63], v[12:15]
ds_read_b128 a[88:91], v191 offset:4096
s_add_u32 m0, s52, 0
v_mfma_f32_16x16x32_bf16 v[16:19], a[76:79], a[60:63], v[16:19]
buffer_load_dwordx4 v198, s[36:39], s55 offen lds
v_mfma_f32_16x16x32_bf16 v[20:23], a[16:19], a[64:67], v[20:23]
ds_read_b128 a[4:7], v191 offset:5120
v_mfma_f32_16x16x32_bf16 v[24:27], a[80:83], a[64:67], v[24:27]
buffer_load_dwordx4 v198, s[36:39], s55 offen offset:1024 lds
v_mfma_f32_16x16x32_bf16 v[28:31], a[8:11], a[64:67], v[28:31]
s_waitcnt lgkmcnt(3)
ds_read_b128 a[68:71], v191 offset:6144
v_mfma_f32_16x16x32_bf16 v[32:35], a[36:39], a[64:67], v[32:35]
v_mfma_f32_16x16x32_bf16 v[36:39], a[76:79], a[64:67], v[36:39]
v_mfma_f32_16x16x32_bf16 v[40:43], a[16:19], a[0:3], v[40:43]
ds_read_b128 a[72:75], v191 offset:7168
v_mfma_f32_16x16x32_bf16 v[44:47], a[80:83], a[0:3], v[44:47]
buffer_load_dwordx4 v198, s[36:39], s55 offen offset:2048 lds
v_mfma_f32_16x16x32_bf16 v[48:51], a[8:11], a[0:3], v[48:51]
s_waitcnt lgkmcnt(3)
v_mfma_f32_16x16x32_bf16 v[52:55], a[36:39], a[0:3], v[52:55]
v_mfma_f32_16x16x32_bf16 v[56:59], a[76:79], a[0:3], v[56:59]
v_mfma_f32_16x16x32_bf16 v[60:63], a[16:19], a[84:87], v[60:63]
ds_read_b128 a[28:31], v191 offset:8192
v_mfma_f32_16x16x32_bf16 v[64:67], a[80:83], a[84:87], v[64:67]
buffer_load_dwordx4 v198, s[36:39], s55 offen offset:3072 lds
v_mfma_f32_16x16x32_bf16 v[68:71], a[8:11], a[84:87], v[68:71]
v_mfma_f32_16x16x32_bf16 v[72:75], a[36:39], a[84:87], v[72:75]
v_mfma_f32_16x16x32_bf16 v[76:79], a[76:79], a[84:87], v[76:79]
v_mfma_f32_16x16x32_bf16 v[80:83], a[16:19], a[88:91], v[80:83]
ds_read_b64_tr_b16 a[32:33], v186 offset:30720
v_mfma_f32_16x16x32_bf16 v[84:87], a[80:83], a[88:91], v[84:87]
s_and_b32 s60, s68, 0
s_add_u32 m0, s53, s60
v_mfma_f32_16x16x32_bf16 v[88:91], a[8:11], a[88:91], v[88:91]
s_waitcnt lgkmcnt(2)
v_mfma_f32_16x16x32_bf16 v[92:95], a[36:39], a[88:91], v[92:95]
buffer_load_dwordx4 v199, s[36:39], s56 offen lds
v_mfma_f32_16x16x32_bf16 v[96:99], a[76:79], a[88:91], v[96:99]
ds_read_b64_tr_b16 a[34:35], v186 offset:36864
v_mfma_f32_16x16x32_bf16 v[100:103], a[16:19], a[4:7], v[100:103]
ds_read_b128 a[40:43], v191 offset:30720
v_mfma_f32_16x16x32_bf16 v[104:107], a[80:83], a[4:7], v[104:107]
ds_read_b64_tr_b16 a[12:13], v187 offset:30720
v_mfma_f32_16x16x32_bf16 v[108:111], a[8:11], a[4:7], v[108:111]
ds_read_b64_tr_b16 a[14:15], v187 offset:36864
v_mfma_f32_16x16x32_bf16 v[112:115], a[36:39], a[4:7], v[112:115]
ds_read_b64_tr_b16 a[48:49], v188 offset:30720
v_mfma_f32_16x16x32_bf16 v[116:119], a[76:79], a[4:7], v[116:119]
ds_read_b64_tr_b16 a[50:51], v188 offset:36864
v_mfma_f32_16x16x32_bf16 v[120:123], a[16:19], a[68:71], v[120:123]
ds_read_b64_tr_b16 a[92:93], v189 offset:30720
v_mfma_f32_16x16x32_bf16 v[124:127], a[80:83], a[68:71], v[124:127]
ds_read_b64_tr_b16 a[94:95], v189 offset:36864
v_mfma_f32_16x16x32_bf16 v[128:131], a[8:11], a[68:71], v[128:131]
ds_read_b64_tr_b16 a[24:25], v190 offset:30720
v_mfma_f32_16x16x32_bf16 v[132:135], a[36:39], a[68:71], v[132:135]
ds_read_b64_tr_b16 a[26:27], v190 offset:36864
s_add_u32 s58, s73, 2
v_mfma_f32_16x16x32_bf16 v[136:139], a[76:79], a[68:71], v[136:139]
s_and_b32 s58, s58, 7
s_add_u32 s58, s58, 24
s_mul_i32 s59, s58, 40960
v_mfma_f32_16x16x32_bf16 v[140:143], a[16:19], a[72:75], v[140:143]
s_add_u32 s57, s76, s59
s_mul_i32 s59, s58, 294912
s_add_u32 s55, s77, s59
v_mfma_f32_16x16x32_bf16 v[144:147], a[80:83], a[72:75], v[144:147]
s_add_u32 s56, s78, s59
s_add_u32 m0, s54, 30720
v_mfma_f32_16x16x32_bf16 v[148:151], a[8:11], a[72:75], v[148:151]
s_waitcnt lgkmcnt(4)
v_mfma_f32_16x16x32_bf16 v[152:155], a[36:39], a[72:75], v[152:155]
buffer_load_dwordx4 v202, s[40:43], s57 offen lds
v_mfma_f32_16x16x32_bf16 v[156:159], a[76:79], a[72:75], v[156:159]
ds_read_b128 a[44:47], v191 offset:31744
v_mfma_f32_16x16x32_bf16 v[160:163], a[16:19], a[28:31], v[160:163]
buffer_load_dwordx4 v203, s[40:43], s57 offen offset:1024 lds
v_mfma_f32_16x16x32_bf16 v[164:167], a[80:83], a[28:31], v[164:167]
ds_read_b128 a[56:59], v191 offset:32768
v_mfma_f32_16x16x32_bf16 v[168:171], a[8:11], a[28:31], v[168:171]
buffer_load_dwordx4 v204, s[40:43], s57 offen offset:2048 lds
v_mfma_f32_16x16x32_bf16 v[172:175], a[36:39], a[28:31], v[172:175]
ds_read_b128 a[20:23], v191 offset:33792
s_add_u32 m0, s52, 30720
v_mfma_f32_16x16x32_bf16 v[176:179], a[76:79], a[28:31], v[176:179]
buffer_load_dwordx4 v198, s[36:39], s55 offen lds
v_mfma_f32_16x16x32_bf16 v[0:3], a[32:35], a[40:43], v[0:3]
s_waitcnt lgkmcnt(3)
ds_read_b128 a[52:55], v191 offset:34816
v_mfma_f32_16x16x32_bf16 v[4:7], a[12:15], a[40:43], v[4:7]
buffer_load_dwordx4 v198, s[36:39], s55 offen offset:1024 lds
v_mfma_f32_16x16x32_bf16 v[8:11], a[48:51], a[40:43], v[8:11]
s_waitcnt lgkmcnt(3)
ds_read_b128 a[60:63], v191 offset:35840
v_mfma_f32_16x16x32_bf16 v[12:15], a[92:95], a[40:43], v[12:15]
v_mfma_f32_16x16x32_bf16 v[16:19], a[24:27], a[40:43], v[16:19]
v_mfma_f32_16x16x32_bf16 v[20:23], a[32:35], a[44:47], v[20:23]
ds_read_b128 a[64:67], v191 offset:36864
v_mfma_f32_16x16x32_bf16 v[24:27], a[12:15], a[44:47], v[24:27]
buffer_load_dwordx4 v198, s[36:39], s55 offen offset:2048 lds
v_mfma_f32_16x16x32_bf16 v[28:31], a[48:51], a[44:47], v[28:31]
s_waitcnt lgkmcnt(3)
v_mfma_f32_16x16x32_bf16 v[32:35], a[92:95], a[44:47], v[32:35]
v_mfma_f32_16x16x32_bf16 v[36:39], a[24:27], a[44:47], v[36:39]
v_mfma_f32_16x16x32_bf16 v[40:43], a[32:35], a[56:59], v[40:43]
ds_read_b128 a[0:3], v191 offset:37888
v_mfma_f32_16x16x32_bf16 v[44:47], a[12:15], a[56:59], v[44:47]
buffer_load_dwordx4 v198, s[36:39], s55 offen offset:3072 lds
v_mfma_f32_16x16x32_bf16 v[48:51], a[48:51], a[56:59], v[48:51]
v_mfma_f32_16x16x32_bf16 v[52:55], a[92:95], a[56:59], v[52:55]
v_mfma_f32_16x16x32_bf16 v[56:59], a[24:27], a[56:59], v[56:59]
v_mfma_f32_16x16x32_bf16 v[60:63], a[32:35], a[20:23], v[60:63]
ds_read_b128 a[84:87], v191 offset:38912
v_mfma_f32_16x16x32_bf16 v[64:67], a[12:15], a[20:23], v[64:67]
s_and_b32 s60, s68, 30720
s_add_u32 m0, s53, s60
v_mfma_f32_16x16x32_bf16 v[68:71], a[48:51], a[20:23], v[68:71]
s_waitcnt lgkmcnt(2)
v_mfma_f32_16x16x32_bf16 v[72:75], a[92:95], a[20:23], v[72:75]
buffer_load_dwordx4 v199, s[36:39], s56 offen lds
v_mfma_f32_16x16x32_bf16 v[76:79], a[24:27], a[20:23], v[76:79]
v_mfma_f32_16x16x32_bf16 v[80:83], a[32:35], a[52:55], v[80:83]
v_mfma_f32_16x16x32_bf16 v[84:87], a[12:15], a[52:55], v[84:87]
v_mfma_f32_16x16x32_bf16 v[88:91], a[48:51], a[52:55], v[88:91]
v_mfma_f32_16x16x32_bf16 v[92:95], a[92:95], a[52:55], v[92:95]
v_mfma_f32_16x16x32_bf16 v[96:99], a[24:27], a[52:55], v[96:99]
v_mfma_f32_16x16x32_bf16 v[100:103], a[32:35], a[60:63], v[100:103]
v_mfma_f32_16x16x32_bf16 v[104:107], a[12:15], a[60:63], v[104:107]
v_mfma_f32_16x16x32_bf16 v[108:111], a[48:51], a[60:63], v[108:111]
v_mfma_f32_16x16x32_bf16 v[112:115], a[92:95], a[60:63], v[112:115]
v_mfma_f32_16x16x32_bf16 v[116:119], a[24:27], a[60:63], v[116:119]
v_mfma_f32_16x16x32_bf16 v[120:123], a[32:35], a[64:67], v[120:123]
s_waitcnt vmcnt(8) lgkmcnt(0)
s_barrier
v_mfma_f32_16x16x32_bf16 v[124:127], a[12:15], a[64:67], v[124:127]
ds_read_b64_tr_b16 a[88:89], v192 offset:0
v_mfma_f32_16x16x32_bf16 v[128:131], a[48:51], a[64:67], v[128:131]
ds_read_b64_tr_b16 a[90:91], v192 offset:6144
v_mfma_f32_16x16x32_bf16 v[132:135], a[92:95], a[64:67], v[132:135]
ds_read_b128 a[4:7], v197 offset:0
v_mfma_f32_16x16x32_bf16 v[136:139], a[24:27], a[64:67], v[136:139]
ds_read_b64_tr_b16 a[68:69], v193 offset:0
ds_read_b64_tr_b16 a[70:71], v193 offset:6144
v_mfma_f32_16x16x32_bf16 v[140:143], a[32:35], a[0:3], v[140:143]
ds_read_b64_tr_b16 a[72:73], v194 offset:0
ds_read_b64_tr_b16 a[74:75], v194 offset:6144
v_mfma_f32_16x16x32_bf16 v[144:147], a[12:15], a[0:3], v[144:147]
ds_read_b64_tr_b16 a[16:17], v195 offset:0
ds_read_b64_tr_b16 a[18:19], v195 offset:6144
v_mfma_f32_16x16x32_bf16 v[148:151], a[48:51], a[0:3], v[148:151]
ds_read_b64_tr_b16 a[80:81], v196 offset:0
ds_read_b64_tr_b16 a[82:83], v196 offset:6144
v_mfma_f32_16x16x32_bf16 v[152:155], a[92:95], a[0:3], v[152:155]
ds_read_b128 a[8:11], v197 offset:1024
s_add_u32 s58, s73, 3
v_mfma_f32_16x16x32_bf16 v[156:159], a[24:27], a[0:3], v[156:159]
s_and_b32 s58, s58, 7
s_add_u32 s58, s58, 24
s_mul_i32 s59, s58, 40960
v_mfma_f32_16x16x32_bf16 v[160:163], a[32:35], a[84:87], v[160:163]
s_add_u32 s57, s76, s59
s_mul_i32 s59, s58, 294912
s_add_u32 s55, s77, s59
v_mfma_f32_16x16x32_bf16 v[164:167], a[12:15], a[84:87], v[164:167]
s_add_u32 s56, s78, s59
s_add_u32 m0, s54, 61440
v_mfma_f32_16x16x32_bf16 v[168:171], a[48:51], a[84:87], v[168:171]
s_waitcnt lgkmcnt(7)
v_mfma_f32_16x16x32_bf16 v[172:175], a[92:95], a[84:87], v[172:175]
buffer_load_dwordx4 v202, s[40:43], s57 offen lds
v_mfma_f32_16x16x32_bf16 v[176:179], a[24:27], a[84:87], v[176:179]
s_waitcnt lgkmcnt(3)
ds_read_b128 a[36:39], v197 offset:2048
v_mfma_f32_16x16x32_bf16 v[0:3], a[88:91], a[4:7], v[0:3]
buffer_load_dwordx4 v203, s[40:43], s57 offen offset:1024 lds
v_mfma_f32_16x16x32_bf16 v[4:7], a[68:71], a[4:7], v[4:7]
s_waitcnt lgkmcnt(1)
ds_read_b128 a[76:79], v197 offset:3072
v_mfma_f32_16x16x32_bf16 v[8:11], a[72:75], a[4:7], v[8:11]
buffer_load_dwordx4 v204, s[40:43], s57 offen offset:2048 lds
v_mfma_f32_16x16x32_bf16 v[12:15], a[16:19], a[4:7], v[12:15]
ds_read_b128 a[28:31], v197 offset:4096
s_add_u32 m0, s52, 61440
v_mfma_f32_16x16x32_bf16 v[16:19], a[80:83], a[4:7], v[16:19]
buffer_load_dwordx4 v198, s[36:39], s55 offen lds
v_mfma_f32_16x16x32_bf16 v[20:23], a[88:91], a[8:11], v[20:23]
ds_read_b128 a[40:43], v197 offset:5120
v_mfma_f32_16x16x32_bf16 v[24:27], a[68:71], a[8:11], v[24:27]
buffer_load_dwordx4 v198, s[36:39], s55 offen offset:1024 lds
v_mfma_f32_16x16x32_bf16 v[28:31], a[72:75], a[8:11], v[28:31]
s_waitcnt lgkmcnt(3)
ds_read_b128 a[44:47], v197 offset:6144
v_mfma_f32_16x16x32_bf16 v[32:35], a[16:19], a[8:11], v[32:35]
v_mfma_f32_16x16x32_bf16 v[36:39], a[80:83], a[8:11], v[36:39]
v_mfma_f32_16x16x32_bf16 v[40:43], a[88:91], a[36:39], v[40:43]
ds_read_b128 a[56:59], v197 offset:7168
v_mfma_f32_16x16x32_bf16 v[44:47], a[68:71], a[36:39], v[44:47]
buffer_load_dwordx4 v198, s[36:39], s55 offen offset:2048 lds
v_mfma_f32_16x16x32_bf16 v[48:51], a[72:75], a[36:39], v[48:51]
s_waitcnt lgkmcnt(3)
v_mfma_f32_16x16x32_bf16 v[52:55], a[16:19], a[36:39], v[52:55]
v_mfma_f32_16x16x32_bf16 v[56:59], a[80:83], a[36:39], v[56:59]
v_mfma_f32_16x16x32_bf16 v[60:63], a[88:91], a[76:79], v[60:63]
ds_read_b128 a[20:23], v197 offset:8192
v_mfma_f32_16x16x32_bf16 v[64:67], a[68:71], a[76:79], v[64:67]
buffer_load_dwordx4 v198, s[36:39], s55 offen offset:3072 lds
v_mfma_f32_16x16x32_bf16 v[68:71], a[72:75], a[76:79], v[68:71]
v_mfma_f32_16x16x32_bf16 v[72:75], a[16:19], a[76:79], v[72:75]
v_mfma_f32_16x16x32_bf16 v[76:79], a[80:83], a[76:79], v[76:79]
v_mfma_f32_16x16x32_bf16 v[80:83], a[88:91], a[28:31], v[80:83]
ds_read_b64_tr_b16 a[52:53], v180 offset:0
v_mfma_f32_16x16x32_bf16 v[84:87], a[68:71], a[28:31], v[84:87]
s_and_b32 s60, s68, 61440
s_add_u32 m0, s53, s60
v_mfma_f32_16x16x32_bf16 v[88:91], a[72:75], a[28:31], v[88:91]
s_waitcnt lgkmcnt(2)
v_mfma_f32_16x16x32_bf16 v[92:95], a[16:19], a[28:31], v[92:95]
buffer_load_dwordx4 v199, s[36:39], s56 offen lds
v_mfma_f32_16x16x32_bf16 v[96:99], a[80:83], a[28:31], v[96:99]
ds_read_b64_tr_b16 a[54:55], v180 offset:6144
v_mfma_f32_16x16x32_bf16 v[100:103], a[88:91], a[40:43], v[100:103]
ds_read_b128 a[60:63], v185 offset:0
v_mfma_f32_16x16x32_bf16 v[104:107], a[68:71], a[40:43], v[104:107]
ds_read_b64_tr_b16 a[64:65], v181 offset:0
v_mfma_f32_16x16x32_bf16 v[108:111], a[72:75], a[40:43], v[108:111]
ds_read_b64_tr_b16 a[66:67], v181 offset:6144
v_mfma_f32_16x16x32_bf16 v[112:115], a[16:19], a[40:43], v[112:115]
ds_read_b64_tr_b16 a[0:1], v182 offset:0
v_mfma_f32_16x16x32_bf16 v[116:119], a[80:83], a[40:43], v[116:119]
ds_read_b64_tr_b16 a[2:3], v182 offset:6144
v_mfma_f32_16x16x32_bf16 v[120:123], a[88:91], a[44:47], v[120:123]
ds_read_b64_tr_b16 a[32:33], v183 offset:0
v_mfma_f32_16x16x32_bf16 v[124:127], a[68:71], a[44:47], v[124:127]
ds_read_b64_tr_b16 a[34:35], v183 offset:6144
v_mfma_f32_16x16x32_bf16 v[128:131], a[72:75], a[44:47], v[128:131]
ds_read_b64_tr_b16 a[12:13], v184 offset:0
v_mfma_f32_16x16x32_bf16 v[132:135], a[16:19], a[44:47], v[132:135]
ds_read_b64_tr_b16 a[14:15], v184 offset:6144
s_add_u32 s58, s73, 4
v_mfma_f32_16x16x32_bf16 v[136:139], a[80:83], a[44:47], v[136:139]
s_and_b32 s58, s58, 7
s_add_u32 s58, s58, 24
s_mul_i32 s59, s58, 40960
v_mfma_f32_16x16x32_bf16 v[140:143], a[88:91], a[56:59], v[140:143]
s_add_u32 s57, s76, s59
s_mul_i32 s59, s58, 294912
s_add_u32 s55, s77, s59
v_mfma_f32_16x16x32_bf16 v[144:147], a[68:71], a[56:59], v[144:147]
s_add_u32 s56, s78, s59
s_add_u32 m0, s54, 92160
v_mfma_f32_16x16x32_bf16 v[148:151], a[72:75], a[56:59], v[148:151]
s_waitcnt lgkmcnt(4)
v_mfma_f32_16x16x32_bf16 v[152:155], a[16:19], a[56:59], v[152:155]
buffer_load_dwordx4 v202, s[40:43], s57 offen lds
v_mfma_f32_16x16x32_bf16 v[156:159], a[80:83], a[56:59], v[156:159]
ds_read_b128 a[48:51], v185 offset:1024
v_mfma_f32_16x16x32_bf16 v[160:163], a[88:91], a[20:23], v[160:163]
buffer_load_dwordx4 v203, s[40:43], s57 offen offset:1024 lds
v_mfma_f32_16x16x32_bf16 v[164:167], a[68:71], a[20:23], v[164:167]
ds_read_b128 a[92:95], v185 offset:2048
v_mfma_f32_16x16x32_bf16 v[168:171], a[72:75], a[20:23], v[168:171]
buffer_load_dwordx4 v204, s[40:43], s57 offen offset:2048 lds
v_mfma_f32_16x16x32_bf16 v[172:175], a[16:19], a[20:23], v[172:175]
ds_read_b128 a[24:27], v185 offset:3072
s_add_u32 m0, s52, 92160
v_mfma_f32_16x16x32_bf16 v[176:179], a[80:83], a[20:23], v[176:179]
buffer_load_dwordx4 v198, s[36:39], s55 offen lds
v_mfma_f32_16x16x32_bf16 v[0:3], a[52:55], a[60:63], v[0:3]
s_waitcnt lgkmcnt(3)
ds_read_b128 a[84:87], v185 offset:4096
v_mfma_f32_16x16x32_bf16 v[4:7], a[64:67], a[60:63], v[4:7]
buffer_load_dwordx4 v198, s[36:39], s55 offen offset:1024 lds
v_mfma_f32_16x16x32_bf16 v[8:11], a[0:3], a[60:63], v[8:11]
s_waitcnt lgkmcnt(3)
ds_read_b128 a[4:7], v185 offset:5120
v_mfma_f32_16x16x32_bf16 v[12:15], a[32:35], a[60:63], v[12:15]
v_mfma_f32_16x16x32_bf16 v[16:19], a[12:15], a[60:63], v[16:19]
v_mfma_f32_16x16x32_bf16 v[20:23], a[52:55], a[48:51], v[20:23]
ds_read_b128 a[8:11], v185 offset:6144
v_mfma_f32_16x16x32_bf16 v[24:27], a[64:67], a[48:51], v[24:27]
buffer_load_dwordx4 v198, s[36:39], s55 offen offset:2048 lds
v_mfma_f32_16x16x32_bf16 v[28:31], a[0:3], a[48:51], v[28:31]
s_waitcnt lgkmcnt(3)
v_mfma_f32_16x16x32_bf16 v[32:35], a[32:35], a[48:51], v[32:35]
v_mfma_f32_16x16x32_bf16 v[36:39], a[12:15], a[48:51], v[36:39]
v_mfma_f32_16x16x32_bf16 v[40:43], a[52:55], a[92:95], v[40:43]
ds_read_b128 a[36:39], v185 offset:7168
v_mfma_f32_16x16x32_bf16 v[44:47], a[64:67], a[92:95], v[44:47]
buffer_load_dwordx4 v198, s[36:39], s55 offen offset:3072 lds
v_mfma_f32_16x16x32_bf16 v[48:51], a[0:3], a[92:95], v[48:51]
v_mfma_f32_16x16x32_bf16 v[52:55], a[32:35], a[92:95], v[52:55]
v_mfma_f32_16x16x32_bf16 v[56:59], a[12:15], a[92:95], v[56:59]
v_mfma_f32_16x16x32_bf16 v[60:63], a[52:55], a[24:27], v[60:63]
ds_read_b128 a[76:79], v185 offset:8192
v_mfma_f32_16x16x32_bf16 v[64:67], a[64:67], a[24:27], v[64:67]
s_and_b32 s60, s68, 92160
s_add_u32 m0, s53, s60
v_mfma_f32_16x16x32_bf16 v[68:71], a[0:3], a[24:27], v[68:71]
s_waitcnt lgkmcnt(2)
v_mfma_f32_16x16x32_bf16 v[72:75], a[32:35], a[24:27], v[72:75]
buffer_load_dwordx4 v199, s[36:39], s56 offen lds
v_mfma_f32_16x16x32_bf16 v[76:79], a[12:15], a[24:27], v[76:79]
v_mfma_f32_16x16x32_bf16 v[80:83], a[52:55], a[84:87], v[80:83]
v_mfma_f32_16x16x32_bf16 v[84:87], a[64:67], a[84:87], v[84:87]
v_mfma_f32_16x16x32_bf16 v[88:91], a[0:3], a[84:87], v[88:91]
v_mfma_f32_16x16x32_bf16 v[92:95], a[32:35], a[84:87], v[92:95]
v_mfma_f32_16x16x32_bf16 v[96:99], a[12:15], a[84:87], v[96:99]
v_mfma_f32_16x16x32_bf16 v[100:103], a[52:55], a[4:7], v[100:103]
v_mfma_f32_16x16x32_bf16 v[104:107], a[64:67], a[4:7], v[104:107]
v_mfma_f32_16x16x32_bf16 v[108:111], a[0:3], a[4:7], v[108:111]
v_mfma_f32_16x16x32_bf16 v[112:115], a[32:35], a[4:7], v[112:115]
v_mfma_f32_16x16x32_bf16 v[116:119], a[12:15], a[4:7], v[116:119]
v_mfma_f32_16x16x32_bf16 v[120:123], a[52:55], a[8:11], v[120:123]
s_waitcnt vmcnt(8) lgkmcnt(0)
s_barrier
v_mfma_f32_16x16x32_bf16 v[124:127], a[64:67], a[8:11], v[124:127]
ds_read_b64_tr_b16 a[28:29], v180 offset:30720
v_mfma_f32_16x16x32_bf16 v[128:131], a[0:3], a[8:11], v[128:131]
ds_read_b64_tr_b16 a[30:31], v180 offset:36864
v_mfma_f32_16x16x32_bf16 v[132:135], a[32:35], a[8:11], v[132:135]
ds_read_b128 a[40:43], v185 offset:30720
v_mfma_f32_16x16x32_bf16 v[136:139], a[12:15], a[8:11], v[136:139]
ds_read_b64_tr_b16 a[44:45], v181 offset:30720
ds_read_b64_tr_b16 a[46:47], v181 offset:36864
v_mfma_f32_16x16x32_bf16 v[140:143], a[52:55], a[36:39], v[140:143]
ds_read_b64_tr_b16 a[56:57], v182 offset:30720
ds_read_b64_tr_b16 a[58:59], v182 offset:36864
v_mfma_f32_16x16x32_bf16 v[144:147], a[64:67], a[36:39], v[144:147]
ds_read_b64_tr_b16 a[88:89], v183 offset:30720
ds_read_b64_tr_b16 a[90:91], v183 offset:36864
v_mfma_f32_16x16x32_bf16 v[148:151], a[0:3], a[36:39], v[148:151]
ds_read_b64_tr_b16 a[68:69], v184 offset:30720
ds_read_b64_tr_b16 a[70:71], v184 offset:36864
v_mfma_f32_16x16x32_bf16 v[152:155], a[32:35], a[36:39], v[152:155]
ds_read_b128 a[72:75], v185 offset:31744
s_add_u32 s58, s73, 5
v_mfma_f32_16x16x32_bf16 v[156:159], a[12:15], a[36:39], v[156:159]
s_and_b32 s58, s58, 7
s_add_u32 s58, s58, 24
s_mul_i32 s59, s58, 40960
v_mfma_f32_16x16x32_bf16 v[160:163], a[52:55], a[76:79], v[160:163]
s_add_u32 s57, s76, s59
s_mul_i32 s59, s58, 294912
s_add_u32 s55, s77, s59
v_mfma_f32_16x16x32_bf16 v[164:167], a[64:67], a[76:79], v[164:167]
s_add_u32 s56, s78, s59
s_add_u32 m0, s54, 122880
v_mfma_f32_16x16x32_bf16 v[168:171], a[0:3], a[76:79], v[168:171]
s_waitcnt lgkmcnt(7)
v_mfma_f32_16x16x32_bf16 v[172:175], a[32:35], a[76:79], v[172:175]
buffer_load_dwordx4 v202, s[40:43], s57 offen lds
v_mfma_f32_16x16x32_bf16 v[176:179], a[12:15], a[76:79], v[176:179]
s_waitcnt lgkmcnt(3)
ds_read_b128 a[16:19], v185 offset:32768
v_mfma_f32_16x16x32_bf16 v[0:3], a[28:31], a[40:43], v[0:3]
buffer_load_dwordx4 v203, s[40:43], s57 offen offset:1024 lds
v_mfma_f32_16x16x32_bf16 v[4:7], a[44:47], a[40:43], v[4:7]
s_waitcnt lgkmcnt(1)
ds_read_b128 a[20:23], v185 offset:33792
v_mfma_f32_16x16x32_bf16 v[8:11], a[56:59], a[40:43], v[8:11]
buffer_load_dwordx4 v204, s[40:43], s57 offen offset:2048 lds
v_mfma_f32_16x16x32_bf16 v[12:15], a[88:91], a[40:43], v[12:15]
ds_read_b128 a[80:83], v185 offset:34816
s_add_u32 m0, s52, 122880
v_mfma_f32_16x16x32_bf16 v[16:19], a[68:71], a[40:43], v[16:19]
buffer_load_dwordx4 v198, s[36:39], s55 offen lds
v_mfma_f32_16x16x32_bf16 v[20:23], a[28:31], a[72:75], v[20:23]
ds_read_b128 a[60:63], v185 offset:35840
v_mfma_f32_16x16x32_bf16 v[24:27], a[44:47], a[72:75], v[24:27]
buffer_load_dwordx4 v198, s[36:39], s55 offen offset:1024 lds
v_mfma_f32_16x16x32_bf16 v[28:31], a[56:59], a[72:75], v[28:31]
s_waitcnt lgkmcnt(3)
ds_read_b128 a[48:51], v185 offset:36864
v_mfma_f32_16x16x32_bf16 v[32:35], a[88:91], a[72:75], v[32:35]
v_mfma_f32_16x16x32_bf16 v[36:39], a[68:71], a[72:75], v[36:39]
v_mfma_f32_16x16x32_bf16 v[40:43], a[28:31], a[16:19], v[40:43]
ds_read_b128 a[92:95], v185 offset:37888
v_mfma_f32_16x16x32_bf16 v[44:47], a[44:47], a[16:19], v[44:47]
buffer_load_dwordx4 v198, s[36:39], s55 offen offset:2048 lds
v_mfma_f32_16x16x32_bf16 v[48:51], a[56:59], a[16:19], v[48:51]
s_waitcnt lgkmcnt(3)
v_mfma_f32_16x16x32_bf16 v[52:55], a[88:91], a[16:19], v[52:55]
v_mfma_f32_16x16x32_bf16 v[56:59], a[68:71], a[16:19], v[56:59]
v_mfma_f32_16x16x32_bf16 v[60:63], a[28:31], a[20:23], v[60:63]
ds_read_b128 a[24:27], v185 offset:38912
v_mfma_f32_16x16x32_bf16 v[64:67], a[44:47], a[20:23], v[64:67]
buffer_load_dwordx4 v198, s[36:39], s55 offen offset:3072 lds
v_mfma_f32_16x16x32_bf16 v[68:71], a[56:59], a[20:23], v[68:71]
v_mfma_f32_16x16x32_bf16 v[72:75], a[88:91], a[20:23], v[72:75]
v_mfma_f32_16x16x32_bf16 v[76:79], a[68:71], a[20:23], v[76:79]
v_mfma_f32_16x16x32_bf16 v[80:83], a[28:31], a[80:83], v[80:83]
ds_read_b64_tr_b16 a[84:85], v186 offset:0
v_mfma_f32_16x16x32_bf16 v[84:87], a[44:47], a[80:83], v[84:87]
s_and_b32 s60, s68, 122880
s_add_u32 m0, s53, s60
v_mfma_f32_16x16x32_bf16 v[88:91], a[56:59], a[80:83], v[88:91]
s_waitcnt lgkmcnt(2)
v_mfma_f32_16x16x32_bf16 v[92:95], a[88:91], a[80:83], v[92:95]
buffer_load_dwordx4 v199, s[36:39], s56 offen lds
v_mfma_f32_16x16x32_bf16 v[96:99], a[68:71], a[80:83], v[96:99]
ds_read_b64_tr_b16 a[86:87], v186 offset:6144
v_mfma_f32_16x16x32_bf16 v[100:103], a[28:31], a[60:63], v[100:103]
ds_read_b128 a[4:7], v191 offset:0
v_mfma_f32_16x16x32_bf16 v[104:107], a[44:47], a[60:63], v[104:107]
ds_read_b64_tr_b16 a[8:9], v187 offset:0
v_mfma_f32_16x16x32_bf16 v[108:111], a[56:59], a[60:63], v[108:111]
ds_read_b64_tr_b16 a[10:11], v187 offset:6144
v_mfma_f32_16x16x32_bf16 v[112:115], a[88:91], a[60:63], v[112:115]
ds_read_b64_tr_b16 a[36:37], v188 offset:0
v_mfma_f32_16x16x32_bf16 v[116:119], a[68:71], a[60:63], v[116:119]
ds_read_b64_tr_b16 a[38:39], v188 offset:6144
v_mfma_f32_16x16x32_bf16 v[120:123], a[28:31], a[48:51], v[120:123]
ds_read_b64_tr_b16 a[52:53], v189 offset:0
v_mfma_f32_16x16x32_bf16 v[124:127], a[44:47], a[48:51], v[124:127]
ds_read_b64_tr_b16 a[54:55], v189 offset:6144
v_mfma_f32_16x16x32_bf16 v[128:131], a[56:59], a[48:51], v[128:131]
ds_read_b64_tr_b16 a[64:65], v190 offset:0
v_mfma_f32_16x16x32_bf16 v[132:135], a[88:91], a[48:51], v[132:135]
ds_read_b64_tr_b16 a[66:67], v190 offset:6144
s_add_u32 s58, s73, 6
v_mfma_f32_16x16x32_bf16 v[136:139], a[68:71], a[48:51], v[136:139]
s_and_b32 s58, s58, 7
s_add_u32 s58, s58, 24
s_mul_i32 s59, s58, 40960
v_mfma_f32_16x16x32_bf16 v[140:143], a[28:31], a[92:95], v[140:143]
s_add_u32 s57, s76, s59
s_mul_i32 s59, s58, 294912
s_add_u32 s55, s77, s59
v_mfma_f32_16x16x32_bf16 v[144:147], a[44:47], a[92:95], v[144:147]
s_add_u32 s56, s78, s59
s_add_u32 m0, s54, 0
v_mfma_f32_16x16x32_bf16 v[148:151], a[56:59], a[92:95], v[148:151]
s_waitcnt lgkmcnt(4)
v_mfma_f32_16x16x32_bf16 v[152:155], a[88:91], a[92:95], v[152:155]
buffer_load_dwordx4 v202, s[40:43], s57 offen lds
v_mfma_f32_16x16x32_bf16 v[156:159], a[68:71], a[92:95], v[156:159]
ds_read_b128 a[0:3], v191 offset:1024
v_mfma_f32_16x16x32_bf16 v[160:163], a[28:31], a[24:27], v[160:163]
buffer_load_dwordx4 v203, s[40:43], s57 offen offset:1024 lds
v_mfma_f32_16x16x32_bf16 v[164:167], a[44:47], a[24:27], v[164:167]
ds_read_b128 a[32:35], v191 offset:2048
v_mfma_f32_16x16x32_bf16 v[168:171], a[56:59], a[24:27], v[168:171]
buffer_load_dwordx4 v204, s[40:43], s57 offen offset:2048 lds
v_mfma_f32_16x16x32_bf16 v[172:175], a[88:91], a[24:27], v[172:175]
ds_read_b128 a[76:79], v191 offset:3072
s_add_u32 m0, s52, 0
v_mfma_f32_16x16x32_bf16 v[176:179], a[68:71], a[24:27], v[176:179]
buffer_load_dwordx4 v198, s[36:39], s55 offen lds
v_mfma_f32_16x16x32_bf16 v[0:3], a[84:87], a[4:7], v[0:3]
s_waitcnt lgkmcnt(3)
ds_read_b128 a[12:15], v191 offset:4096
v_mfma_f32_16x16x32_bf16 v[4:7], a[8:11], a[4:7], v[4:7]
buffer_load_dwordx4 v198, s[36:39], s55 offen offset:1024 lds
v_mfma_f32_16x16x32_bf16 v[8:11], a[36:39], a[4:7], v[8:11]
s_waitcnt lgkmcnt(3)
ds_read_b128 a[40:43], v191 offset:5120
v_mfma_f32_16x16x32_bf16 v[12:15], a[52:55], a[4:7], v[12:15]
v_mfma_f32_16x16x32_bf16 v[16:19], a[64:67], a[4:7], v[16:19]
v_mfma_f32_16x16x32_bf16 v[20:23], a[84:87], a[0:3], v[20:23]
ds_read_b128 a[72:75], v191 offset:6144
v_mfma_f32_16x16x32_bf16 v[24:27], a[8:11], a[0:3], v[24:27]
buffer_load_dwordx4 v198, s[36:39], s55 offen offset:2048 lds
v_mfma_f32_16x16x32_bf16 v[28:31], a[36:39], a[0:3], v[28:31]
s_waitcnt lgkmcnt(3)
v_mfma_f32_16x16x32_bf16 v[32:35], a[52:55], a[0:3], v[32:35]
v_mfma_f32_16x16x32_bf16 v[36:39], a[64:67], a[0:3], v[36:39]
v_mfma_f32_16x16x32_bf16 v[40:43], a[84:87], a[32:35], v[40:43]
ds_read_b128 a[16:19], v191 offset:7168
v_mfma_f32_16x16x32_bf16 v[44:47], a[8:11], a[32:35], v[44:47]
buffer_load_dwordx4 v198, s[36:39], s55 offen offset:3072 lds
v_mfma_f32_16x16x32_bf16 v[48:51], a[36:39], a[32:35], v[48:51]
v_mfma_f32_16x16x32_bf16 v[52:55], a[52:55], a[32:35], v[52:55]
v_mfma_f32_16x16x32_bf16 v[56:59], a[64:67], a[32:35], v[56:59]
v_mfma_f32_16x16x32_bf16 v[60:63], a[84:87], a[76:79], v[60:63]
ds_read_b128 a[20:23], v191 offset:8192
v_mfma_f32_16x16x32_bf16 v[64:67], a[8:11], a[76:79], v[64:67]
s_and_b32 s60, s68, 0
s_add_u32 m0, s53, s60
v_mfma_f32_16x16x32_bf16 v[68:71], a[36:39], a[76:79], v[68:71]
s_waitcnt lgkmcnt(2)
v_mfma_f32_16x16x32_bf16 v[72:75], a[52:55], a[76:79], v[72:75]
buffer_load_dwordx4 v199, s[36:39], s56 offen lds
v_mfma_f32_16x16x32_bf16 v[76:79], a[64:67], a[76:79], v[76:79]
v_mfma_f32_16x16x32_bf16 v[80:83], a[84:87], a[12:15], v[80:83]
v_mfma_f32_16x16x32_bf16 v[84:87], a[8:11], a[12:15], v[84:87]
v_mfma_f32_16x16x32_bf16 v[88:91], a[36:39], a[12:15], v[88:91]
v_mfma_f32_16x16x32_bf16 v[92:95], a[52:55], a[12:15], v[92:95]
v_mfma_f32_16x16x32_bf16 v[96:99], a[64:67], a[12:15], v[96:99]
v_mfma_f32_16x16x32_bf16 v[100:103], a[84:87], a[40:43], v[100:103]
v_mfma_f32_16x16x32_bf16 v[104:107], a[8:11], a[40:43], v[104:107]
v_mfma_f32_16x16x32_bf16 v[108:111], a[36:39], a[40:43], v[108:111]
v_mfma_f32_16x16x32_bf16 v[112:115], a[52:55], a[40:43], v[112:115]
v_mfma_f32_16x16x32_bf16 v[116:119], a[64:67], a[40:43], v[116:119]
v_mfma_f32_16x16x32_bf16 v[120:123], a[84:87], a[72:75], v[120:123]
s_waitcnt vmcnt(8) lgkmcnt(0)
s_barrier
v_mfma_f32_16x16x32_bf16 v[124:127], a[8:11], a[72:75], v[124:127]
ds_read_b64_tr_b16 a[80:81], v186 offset:30720
v_mfma_f32_16x16x32_bf16 v[128:131], a[36:39], a[72:75], v[128:131]
ds_read_b64_tr_b16 a[82:83], v186 offset:36864
v_mfma_f32_16x16x32_bf16 v[132:135], a[52:55], a[72:75], v[132:135]
ds_read_b128 a[60:63], v191 offset:30720
v_mfma_f32_16x16x32_bf16 v[136:139], a[64:67], a[72:75], v[136:139]
ds_read_b64_tr_b16 a[48:49], v187 offset:30720
ds_read_b64_tr_b16 a[50:51], v187 offset:36864
v_mfma_f32_16x16x32_bf16 v[140:143], a[84:87], a[16:19], v[140:143]
ds_read_b64_tr_b16 a[92:93], v188 offset:30720
ds_read_b64_tr_b16 a[94:95], v188 offset:36864
v_mfma_f32_16x16x32_bf16 v[144:147], a[8:11], a[16:19], v[144:147]
ds_read_b64_tr_b16 a[28:29], v189 offset:30720
ds_read_b64_tr_b16 a[30:31], v189 offset:36864
v_mfma_f32_16x16x32_bf16 v[148:151], a[36:39], a[16:19], v[148:151]
ds_read_b64_tr_b16 a[44:45], v190 offset:30720
ds_read_b64_tr_b16 a[46:47], v190 offset:36864
v_mfma_f32_16x16x32_bf16 v[152:155], a[52:55], a[16:19], v[152:155]
ds_read_b128 a[56:59], v191 offset:31744
s_add_u32 s58, s73, 7
v_mfma_f32_16x16x32_bf16 v[156:159], a[64:67], a[16:19], v[156:159]
s_and_b32 s58, s58, 7
s_add_u32 s58, s58, 24
s_mul_i32 s59, s58, 40960
v_mfma_f32_16x16x32_bf16 v[160:163], a[84:87], a[20:23], v[160:163]
s_add_u32 s57, s76, s59
s_mul_i32 s59, s58, 294912
s_add_u32 s55, s77, s59
v_mfma_f32_16x16x32_bf16 v[164:167], a[8:11], a[20:23], v[164:167]
s_add_u32 s56, s78, s59
s_add_u32 m0, s54, 30720
v_mfma_f32_16x16x32_bf16 v[168:171], a[36:39], a[20:23], v[168:171]
s_waitcnt lgkmcnt(7)
v_mfma_f32_16x16x32_bf16 v[172:175], a[52:55], a[20:23], v[172:175]
buffer_load_dwordx4 v202, s[40:43], s57 offen lds
v_mfma_f32_16x16x32_bf16 v[176:179], a[64:67], a[20:23], v[176:179]
s_waitcnt lgkmcnt(3)
ds_read_b128 a[88:91], v191 offset:32768
v_mfma_f32_16x16x32_bf16 v[0:3], a[80:83], a[60:63], v[0:3]
buffer_load_dwordx4 v203, s[40:43], s57 offen offset:1024 lds
v_mfma_f32_16x16x32_bf16 v[4:7], a[48:51], a[60:63], v[4:7]
s_waitcnt lgkmcnt(1)
ds_read_b128 a[24:27], v191 offset:33792
v_mfma_f32_16x16x32_bf16 v[8:11], a[92:95], a[60:63], v[8:11]
buffer_load_dwordx4 v204, s[40:43], s57 offen offset:2048 lds
v_mfma_f32_16x16x32_bf16 v[12:15], a[28:31], a[60:63], v[12:15]
ds_read_b128 a[68:71], v191 offset:34816
s_add_u32 m0, s52, 30720
v_mfma_f32_16x16x32_bf16 v[16:19], a[44:47], a[60:63], v[16:19]
buffer_load_dwordx4 v198, s[36:39], s55 offen lds
v_mfma_f32_16x16x32_bf16 v[20:23], a[80:83], a[56:59], v[20:23]
ds_read_b128 a[4:7], v191 offset:35840
v_mfma_f32_16x16x32_bf16 v[24:27], a[48:51], a[56:59], v[24:27]
buffer_load_dwordx4 v198, s[36:39], s55 offen offset:1024 lds
v_mfma_f32_16x16x32_bf16 v[28:31], a[92:95], a[56:59], v[28:31]
s_waitcnt lgkmcnt(3)
ds_read_b128 a[0:3], v191 offset:36864
v_mfma_f32_16x16x32_bf16 v[32:35], a[28:31], a[56:59], v[32:35]
v_mfma_f32_16x16x32_bf16 v[36:39], a[44:47], a[56:59], v[36:39]
v_mfma_f32_16x16x32_bf16 v[40:43], a[80:83], a[88:91], v[40:43]
ds_read_b128 a[32:35], v191 offset:37888
v_mfma_f32_16x16x32_bf16 v[44:47], a[48:51], a[88:91], v[44:47]
buffer_load_dwordx4 v198, s[36:39], s55 offen offset:2048 lds
v_mfma_f32_16x16x32_bf16 v[48:51], a[92:95], a[88:91], v[48:51]
s_waitcnt lgkmcnt(3)
v_mfma_f32_16x16x32_bf16 v[52:55], a[28:31], a[88:91], v[52:55]
v_mfma_f32_16x16x32_bf16 v[56:59], a[44:47], a[88:91], v[56:59]
v_mfma_f32_16x16x32_bf16 v[60:63], a[80:83], a[24:27], v[60:63]
ds_read_b128 a[76:79], v191 offset:38912
v_mfma_f32_16x16x32_bf16 v[64:67], a[48:51], a[24:27], v[64:67]
buffer_load_dwordx4 v198, s[36:39], s55 offen offset:3072 lds
v_mfma_f32_16x16x32_bf16 v[68:71], a[92:95], a[24:27], v[68:71]
v_mfma_f32_16x16x32_bf16 v[72:75], a[28:31], a[24:27], v[72:75]
v_mfma_f32_16x16x32_bf16 v[76:79], a[44:47], a[24:27], v[76:79]
v_mfma_f32_16x16x32_bf16 v[80:83], a[80:83], a[68:71], v[80:83]
ds_read_b64_tr_b16 a[12:13], v192 offset:0
v_mfma_f32_16x16x32_bf16 v[84:87], a[48:51], a[68:71], v[84:87]
s_and_b32 s60, s68, 30720
s_add_u32 m0, s53, s60
v_mfma_f32_16x16x32_bf16 v[88:91], a[92:95], a[68:71], v[88:91]
s_waitcnt lgkmcnt(2)
v_mfma_f32_16x16x32_bf16 v[92:95], a[28:31], a[68:71], v[92:95]
buffer_load_dwordx4 v199, s[36:39], s56 offen lds
v_mfma_f32_16x16x32_bf16 v[96:99], a[44:47], a[68:71], v[96:99]
ds_read_b64_tr_b16 a[14:15], v192 offset:6144
v_mfma_f32_16x16x32_bf16 v[100:103], a[80:83], a[4:7], v[100:103]
ds_read_b128 a[40:43], v197 offset:0
v_mfma_f32_16x16x32_bf16 v[104:107], a[48:51], a[4:7], v[104:107]
ds_read_b64_tr_b16 a[72:73], v193 offset:0
v_mfma_f32_16x16x32_bf16 v[108:111], a[92:95], a[4:7], v[108:111]
ds_read_b64_tr_b16 a[74:75], v193 offset:6144
v_mfma_f32_16x16x32_bf16 v[112:115], a[28:31], a[4:7], v[112:115]
ds_read_b64_tr_b16 a[16:17], v194 offset:0
v_mfma_f32_16x16x32_bf16 v[116:119], a[44:47], a[4:7], v[116:119]
ds_read_b64_tr_b16 a[18:19], v194 offset:6144
v_mfma_f32_16x16x32_bf16 v[120:123], a[80:83], a[0:3], v[120:123]
ds_read_b64_tr_b16 a[84:85], v195 offset:0
v_mfma_f32_16x16x32_bf16 v[124:127], a[48:51], a[0:3], v[124:127]
ds_read_b64_tr_b16 a[86:87], v195 offset:6144
v_mfma_f32_16x16x32_bf16 v[128:131], a[92:95], a[0:3], v[128:131]
ds_read_b64_tr_b16 a[8:9], v196 offset:0
v_mfma_f32_16x16x32_bf16 v[132:135], a[28:31], a[0:3], v[132:135]
ds_read_b64_tr_b16 a[10:11], v196 offset:6144
s_add_u32 s58, s73, 0
v_mfma_f32_16x16x32_bf16 v[136:139], a[44:47], a[0:3], v[136:139]
s_and_b32 s58, s58, 7
s_add_u32 s58, s58, 32
s_mul_i32 s59, s58, 40960
v_mfma_f32_16x16x32_bf16 v[140:143], a[80:83], a[32:35], v[140:143]
s_add_u32 s57, s76, s59
s_mul_i32 s59, s58, 294912
s_add_u32 s55, s77, s59
v_mfma_f32_16x16x32_bf16 v[144:147], a[48:51], a[32:35], v[144:147]
s_add_u32 s56, s78, s59
s_add_u32 m0, s54, 61440
v_mfma_f32_16x16x32_bf16 v[148:151], a[92:95], a[32:35], v[148:151]
s_waitcnt lgkmcnt(4)
v_mfma_f32_16x16x32_bf16 v[152:155], a[28:31], a[32:35], v[152:155]
buffer_load_dwordx4 v202, s[40:43], s57 offen lds
v_mfma_f32_16x16x32_bf16 v[156:159], a[44:47], a[32:35], v[156:159]
ds_read_b128 a[36:39], v197 offset:1024
v_mfma_f32_16x16x32_bf16 v[160:163], a[80:83], a[76:79], v[160:163]
buffer_load_dwordx4 v203, s[40:43], s57 offen offset:1024 lds
v_mfma_f32_16x16x32_bf16 v[164:167], a[48:51], a[76:79], v[164:167]
ds_read_b128 a[52:55], v197 offset:2048
v_mfma_f32_16x16x32_bf16 v[168:171], a[92:95], a[76:79], v[168:171]
buffer_load_dwordx4 v204, s[40:43], s57 offen offset:2048 lds
v_mfma_f32_16x16x32_bf16 v[172:175], a[28:31], a[76:79], v[172:175]
ds_read_b128 a[64:67], v197 offset:3072
s_add_u32 m0, s52, 61440
v_mfma_f32_16x16x32_bf16 v[176:179], a[44:47], a[76:79], v[176:179]
buffer_load_dwordx4 v198, s[36:39], s55 offen lds
v_mfma_f32_16x16x32_bf16 v[0:3], a[12:15], a[40:43], v[0:3]
s_waitcnt lgkmcnt(3)
ds_read_b128 a[20:23], v197 offset:4096
v_mfma_f32_16x16x32_bf16 v[4:7], a[72:75], a[40:43], v[4:7]
buffer_load_dwordx4 v198, s[36:39], s55 offen offset:1024 lds
v_mfma_f32_16x16x32_bf16 v[8:11], a[16:19], a[40:43], v[8:11]
s_waitcnt lgkmcnt(3)
ds_read_b128 a[60:63], v197 offset:5120
v_mfma_f32_16x16x32_bf16 v[12:15], a[84:87], a[40:43], v[12:15]
v_mfma_f32_16x16x32_bf16 v[16:19], a[8:11], a[40:43], v[16:19]
v_mfma_f32_16x16x32_bf16 v[20:23], a[12:15], a[36:39], v[20:23]
ds_read_b128 a[56:59], v197 offset:6144
v_mfma_f32_16x16x32_bf16 v[24:27], a[72:75], a[36:39], v[24:27]
buffer_load_dwordx4 v198, s[36:39], s55 offen offset:2048 lds
v_mfma_f32_16x16x32_bf16 v[28:31], a[16:19], a[36:39], v[28:31]
s_waitcnt lgkmcnt(3)
v_mfma_f32_16x16x32_bf16 v[32:35], a[84:87], a[36:39], v[32:35]
v_mfma_f32_16x16x32_bf16 v[36:39], a[8:11], a[36:39], v[36:39]
v_mfma_f32_16x16x32_bf16 v[40:43], a[12:15], a[52:55], v[40:43]
ds_read_b128 a[88:91], v197 offset:7168
v_mfma_f32_16x16x32_bf16 v[44:47], a[72:75], a[52:55], v[44:47]
buffer_load_dwordx4 v198, s[36:39], s55 offen offset:3072 lds
v_mfma_f32_16x16x32_bf16 v[48:51], a[16:19], a[52:55], v[48:51]
v_mfma_f32_16x16x32_bf16 v[52:55], a[84:87], a[52:55], v[52:55]
v_mfma_f32_16x16x32_bf16 v[56:59], a[8:11], a[52:55], v[56:59]
v_mfma_f32_16x16x32_bf16 v[60:63], a[12:15], a[64:67], v[60:63]
ds_read_b128 a[24:27], v197 offset:8192
v_mfma_f32_16x16x32_bf16 v[64:67], a[72:75], a[64:67], v[64:67]
s_and_b32 s60, s68, 61440
s_add_u32 m0, s53, s60
v_mfma_f32_16x16x32_bf16 v[68:71], a[16:19], a[64:67], v[68:71]
s_waitcnt lgkmcnt(2)
v_mfma_f32_16x16x32_bf16 v[72:75], a[84:87], a[64:67], v[72:75]
buffer_load_dwordx4 v199, s[36:39], s56 offen lds
v_mfma_f32_16x16x32_bf16 v[76:79], a[8:11], a[64:67], v[76:79]
v_mfma_f32_16x16x32_bf16 v[80:83], a[12:15], a[20:23], v[80:83]
v_mfma_f32_16x16x32_bf16 v[84:87], a[72:75], a[20:23], v[84:87]
v_mfma_f32_16x16x32_bf16 v[88:91], a[16:19], a[20:23], v[88:91]
v_mfma_f32_16x16x32_bf16 v[92:95], a[84:87], a[20:23], v[92:95]
v_mfma_f32_16x16x32_bf16 v[96:99], a[8:11], a[20:23], v[96:99]
v_mfma_f32_16x16x32_bf16 v[100:103], a[12:15], a[60:63], v[100:103]
v_mfma_f32_16x16x32_bf16 v[104:107], a[72:75], a[60:63], v[104:107]
v_mfma_f32_16x16x32_bf16 v[108:111], a[16:19], a[60:63], v[108:111]
v_mfma_f32_16x16x32_bf16 v[112:115], a[84:87], a[60:63], v[112:115]
v_mfma_f32_16x16x32_bf16 v[116:119], a[8:11], a[60:63], v[116:119]
v_mfma_f32_16x16x32_bf16 v[120:123], a[12:15], a[56:59], v[120:123]
s_waitcnt vmcnt(8) lgkmcnt(0)
s_barrier
v_mfma_f32_16x16x32_bf16 v[124:127], a[72:75], a[56:59], v[124:127]
ds_read_b64_tr_b16 a[68:69], v180 offset:0
v_mfma_f32_16x16x32_bf16 v[128:131], a[16:19], a[56:59], v[128:131]
ds_read_b64_tr_b16 a[70:71], v180 offset:6144
v_mfma_f32_16x16x32_bf16 v[132:135], a[84:87], a[56:59], v[132:135]
ds_read_b128 a[4:7], v185 offset:0
v_mfma_f32_16x16x32_bf16 v[136:139], a[8:11], a[56:59], v[136:139]
ds_read_b64_tr_b16 a[0:1], v181 offset:0
ds_read_b64_tr_b16 a[2:3], v181 offset:6144
v_mfma_f32_16x16x32_bf16 v[140:143], a[12:15], a[88:91], v[140:143]
ds_read_b64_tr_b16 a[32:33], v182 offset:0
ds_read_b64_tr_b16 a[34:35], v182 offset:6144
v_mfma_f32_16x16x32_bf16 v[144:147], a[72:75], a[88:91], v[144:147]
ds_read_b64_tr_b16 a[80:81], v183 offset:0
ds_read_b64_tr_b16 a[82:83], v183 offset:6144
v_mfma_f32_16x16x32_bf16 v[148:151], a[16:19], a[88:91], v[148:151]
ds_read_b64_tr_b16 a[48:49], v184 offset:0
ds_read_b64_tr_b16 a[50:51], v184 offset:6144
v_mfma_f32_16x16x32_bf16 v[152:155], a[84:87], a[88:91], v[152:155]
ds_read_b128 a[92:95], v185 offset:1024
s_add_u32 s58, s73, 1
v_mfma_f32_16x16x32_bf16 v[156:159], a[8:11], a[88:91], v[156:159]
s_and_b32 s58, s58, 7
s_add_u32 s58, s58, 32
s_mul_i32 s59, s58, 40960
v_mfma_f32_16x16x32_bf16 v[160:163], a[12:15], a[24:27], v[160:163]
s_add_u32 s57, s76, s59
s_mul_i32 s59, s58, 294912
s_add_u32 s55, s77, s59
v_mfma_f32_16x16x32_bf16 v[164:167], a[72:75], a[24:27], v[164:167]
s_add_u32 s56, s78, s59
s_add_u32 m0, s54, 92160
v_mfma_f32_16x16x32_bf16 v[168:171], a[16:19], a[24:27], v[168:171]
s_waitcnt lgkmcnt(7)
v_mfma_f32_16x16x32_bf16 v[172:175], a[84:87], a[24:27], v[172:175]
buffer_load_dwordx4 v202, s[40:43], s57 offen lds
v_mfma_f32_16x16x32_bf16 v[176:179], a[8:11], a[24:27], v[176:179]
s_waitcnt lgkmcnt(3)
ds_read_b128 a[28:31], v185 offset:2048
v_mfma_f32_16x16x32_bf16 v[0:3], a[68:71], a[4:7], v[0:3]
buffer_load_dwordx4 v203, s[40:43], s57 offen offset:1024 lds
v_mfma_f32_16x16x32_bf16 v[4:7], a[0:3], a[4:7], v[4:7]
s_waitcnt lgkmcnt(1)
ds_read_b128 a[44:47], v185 offset:3072
v_mfma_f32_16x16x32_bf16 v[8:11], a[32:35], a[4:7], v[8:11]
buffer_load_dwordx4 v204, s[40:43], s57 offen offset:2048 lds
v_mfma_f32_16x16x32_bf16 v[12:15], a[80:83], a[4:7], v[12:15]
ds_read_b128 a[76:79], v185 offset:4096
s_add_u32 m0, s52, 92160
v_mfma_f32_16x16x32_bf16 v[16:19], a[48:51], a[4:7], v[16:19]
buffer_load_dwordx4 v198, s[36:39], s55 offen lds
v_mfma_f32_16x16x32_bf16 v[20:23], a[68:71], a[92:95], v[20:23]
ds_read_b128 a[40:43], v185 offset:5120
v_mfma_f32_16x16x32_bf16 v[24:27], a[0:3], a[92:95], v[24:27]
buffer_load_dwordx4 v198, s[36:39], s55 offen offset:1024 lds
v_mfma_f32_16x16x32_bf16 v[28:31], a[32:35], a[92:95], v[28:31]
s_waitcnt lgkmcnt(3)
ds_read_b128 a[36:39], v185 offset:6144
v_mfma_f32_16x16x32_bf16 v[32:35], a[80:83], a[92:95], v[32:35]
v_mfma_f32_16x16x32_bf16 v[36:39], a[48:51], a[92:95], v[36:39]
v_mfma_f32_16x16x32_bf16 v[40:43], a[68:71], a[28:31], v[40:43]
ds_read_b128 a[52:55], v185 offset:7168
v_mfma_f32_16x16x32_bf16 v[44:47], a[0:3], a[28:31], v[44:47]
buffer_load_dwordx4 v198, s[36:39], s55 offen offset:2048 lds
v_mfma_f32_16x16x32_bf16 v[48:51], a[32:35], a[28:31], v[48:51]
s_waitcnt lgkmcnt(3)
v_mfma_f32_16x16x32_bf16 v[52:55], a[80:83], a[28:31], v[52:55]
v_mfma_f32_16x16x32_bf16 v[56:59], a[48:51], a[28:31], v[56:59]
v_mfma_f32_16x16x32_bf16 v[60:63], a[68:71], a[44:47], v[60:63]
ds_read_b128 a[64:67], v185 offset:8192
v_mfma_f32_16x16x32_bf16 v[64:67], a[0:3], a[44:47], v[64:67]
buffer_load_dwordx4 v198, s[36:39], s55 offen offset:3072 lds
v_mfma_f32_16x16x32_bf16 v[68:71], a[32:35], a[44:47], v[68:71]
v_mfma_f32_16x16x32_bf16 v[72:75], a[80:83], a[44:47], v[72:75]
v_mfma_f32_16x16x32_bf16 v[76:79], a[48:51], a[44:47], v[76:79]
v_mfma_f32_16x16x32_bf16 v[80:83], a[68:71], a[76:79], v[80:83]
ds_read_b64_tr_b16 a[20:21], v180 offset:30720
v_mfma_f32_16x16x32_bf16 v[84:87], a[0:3], a[76:79], v[84:87]
s_and_b32 s60, s68, 92160
s_add_u32 m0, s53, s60
v_mfma_f32_16x16x32_bf16 v[88:91], a[32:35], a[76:79], v[88:91]
s_waitcnt lgkmcnt(2)
v_mfma_f32_16x16x32_bf16 v[92:95], a[80:83], a[76:79], v[92:95]
buffer_load_dwordx4 v199, s[36:39], s56 offen lds
v_mfma_f32_16x16x32_bf16 v[96:99], a[48:51], a[76:79], v[96:99]
ds_read_b64_tr_b16 a[22:23], v180 offset:36864
v_mfma_f32_16x16x32_bf16 v[100:103], a[68:71], a[40:43], v[100:103]
ds_read_b128 a[60:63], v185 offset:30720
v_mfma_f32_16x16x32_bf16 v[104:107], a[0:3], a[40:43], v[104:107]
ds_read_b64_tr_b16 a[56:57], v181 offset:30720
v_mfma_f32_16x16x32_bf16 v[108:111], a[32:35], a[40:43], v[108:111]
ds_read_b64_tr_b16 a[58:59], v181 offset:36864
v_mfma_f32_16x16x32_bf16 v[112:115], a[80:83], a[40:43], v[112:115]
ds_read_b64_tr_b16 a[88:89], v182 offset:30720
v_mfma_f32_16x16x32_bf16 v[116:119], a[48:51], a[40:43], v[116:119]
ds_read_b64_tr_b16 a[90:91], v182 offset:36864
v_mfma_f32_16x16x32_bf16 v[120:123], a[68:71], a[36:39], v[120:123]
ds_read_b64_tr_b16 a[12:13], v183 offset:30720
v_mfma_f32_16x16x32_bf16 v[124:127], a[0:3], a[36:39], v[124:127]
ds_read_b64_tr_b16 a[14:15], v183 offset:36864
v_mfma_f32_16x16x32_bf16 v[128:131], a[32:35], a[36:39], v[128:131]
ds_read_b64_tr_b16 a[72:73], v184 offset:30720
v_mfma_f32_16x16x32_bf16 v[132:135], a[80:83], a[36:39], v[132:135]
ds_read_b64_tr_b16 a[74:75], v184 offset:36864
s_add_u32 s58, s73, 2
v_mfma_f32_16x16x32_bf16 v[136:139], a[48:51], a[36:39], v[136:139]
s_and_b32 s58, s58, 7
s_add_u32 s58, s58, 32
s_mul_i32 s59, s58, 40960
v_mfma_f32_16x16x32_bf16 v[140:143], a[68:71], a[52:55], v[140:143]
s_add_u32 s57, s76, s59
s_mul_i32 s59, s58, 294912
s_add_u32 s55, s77, s59
v_mfma_f32_16x16x32_bf16 v[144:147], a[0:3], a[52:55], v[144:147]
s_add_u32 s56, s78, s59
s_add_u32 m0, s54, 122880
v_mfma_f32_16x16x32_bf16 v[148:151], a[32:35], a[52:55], v[148:151]
s_waitcnt lgkmcnt(4)
v_mfma_f32_16x16x32_bf16 v[152:155], a[80:83], a[52:55], v[152:155]
buffer_load_dwordx4 v202, s[40:43], s57 offen lds
v_mfma_f32_16x16x32_bf16 v[156:159], a[48:51], a[52:55], v[156:159]
ds_read_b128 a[16:19], v185 offset:31744
v_mfma_f32_16x16x32_bf16 v[160:163], a[68:71], a[64:67], v[160:163]
buffer_load_dwordx4 v203, s[40:43], s57 offen offset:1024 lds
v_mfma_f32_16x16x32_bf16 v[164:167], a[0:3], a[64:67], v[164:167]
ds_read_b128 a[84:87], v185 offset:32768
v_mfma_f32_16x16x32_bf16 v[168:171], a[32:35], a[64:67], v[168:171]
buffer_load_dwordx4 v204, s[40:43], s57 offen offset:2048 lds
v_mfma_f32_16x16x32_bf16 v[172:175], a[80:83], a[64:67], v[172:175]
ds_read_b128 a[8:11], v185 offset:33792
s_add_u32 m0, s52, 122880
v_mfma_f32_16x16x32_bf16 v[176:179], a[48:51], a[64:67], v[176:179]
buffer_load_dwordx4 v198, s[36:39], s55 offen lds
v_mfma_f32_16x16x32_bf16 v[0:3], a[20:23], a[60:63], v[0:3]
s_waitcnt lgkmcnt(3)
ds_read_b128 a[24:27], v185 offset:34816
v_mfma_f32_16x16x32_bf16 v[4:7], a[56:59], a[60:63], v[4:7]
buffer_load_dwordx4 v198, s[36:39], s55 offen offset:1024 lds
v_mfma_f32_16x16x32_bf16 v[8:11], a[88:91], a[60:63], v[8:11]
s_waitcnt lgkmcnt(3)
ds_read_b128 a[4:7], v185 offset:35840
v_mfma_f32_16x16x32_bf16 v[12:15], a[12:15], a[60:63], v[12:15]
v_mfma_f32_16x16x32_bf16 v[16:19], a[72:75], a[60:63], v[16:19]
v_mfma_f32_16x16x32_bf16 v[20:23], a[20:23], a[16:19], v[20:23]
ds_read_b128 a[92:95], v185 offset:36864
v_mfma_f32_16x16x32_bf16 v[24:27], a[56:59], a[16:19], v[24:27]
buffer_load_dwordx4 v198, s[36:39], s55 offen offset:2048 lds
v_mfma_f32_16x16x32_bf16 v[28:31], a[88:91], a[16:19], v[28:31]
s_waitcnt lgkmcnt(3)
v_mfma_f32_16x16x32_bf16 v[32:35], a[12:15], a[16:19], v[32:35]
v_mfma_f32_16x16x32_bf16 v[36:39], a[72:75], a[16:19], v[36:39]
v_mfma_f32_16x16x32_bf16 v[40:43], a[20:23], a[84:87], v[40:43]
ds_read_b128 a[28:31], v185 offset:37888
v_mfma_f32_16x16x32_bf16 v[44:47], a[56:59], a[84:87], v[44:47]
buffer_load_dwordx4 v198, s[36:39], s55 offen offset:3072 lds
v_mfma_f32_16x16x32_bf16 v[48:51], a[88:91], a[84:87], v[48:51]
v_mfma_f32_16x16x32_bf16 v[52:55], a[12:15], a[84:87], v[52:55]
v_mfma_f32_16x16x32_bf16 v[56:59], a[72:75], a[84:87], v[56:59]
v_mfma_f32_16x16x32_bf16 v[60:63], a[20:23], a[8:11], v[60:63]
ds_read_b128 a[44:47], v185 offset:38912
v_mfma_f32_16x16x32_bf16 v[64:67], a[56:59], a[8:11], v[64:67]
s_and_b32 s60, s68, 122880
s_add_u32 m0, s53, s60
v_mfma_f32_16x16x32_bf16 v[68:71], a[88:91], a[8:11], v[68:71]
s_waitcnt lgkmcnt(2)
v_mfma_f32_16x16x32_bf16 v[72:75], a[12:15], a[8:11], v[72:75]
buffer_load_dwordx4 v199, s[36:39], s56 offen lds
v_mfma_f32_16x16x32_bf16 v[76:79], a[72:75], a[8:11], v[76:79]
v_mfma_f32_16x16x32_bf16 v[80:83], a[20:23], a[24:27], v[80:83]
v_mfma_f32_16x16x32_bf16 v[84:87], a[56:59], a[24:27], v[84:87]
v_mfma_f32_16x16x32_bf16 v[88:91], a[88:91], a[24:27], v[88:91]
v_mfma_f32_16x16x32_bf16 v[92:95], a[12:15], a[24:27], v[92:95]
v_mfma_f32_16x16x32_bf16 v[96:99], a[72:75], a[24:27], v[96:99]
v_mfma_f32_16x16x32_bf16 v[100:103], a[20:23], a[4:7], v[100:103]
v_mfma_f32_16x16x32_bf16 v[104:107], a[56:59], a[4:7], v[104:107]
v_mfma_f32_16x16x32_bf16 v[108:111], a[88:91], a[4:7], v[108:111]
v_mfma_f32_16x16x32_bf16 v[112:115], a[12:15], a[4:7], v[112:115]
v_mfma_f32_16x16x32_bf16 v[116:119], a[72:75], a[4:7], v[116:119]
v_mfma_f32_16x16x32_bf16 v[120:123], a[20:23], a[92:95], v[120:123]
s_waitcnt vmcnt(8) lgkmcnt(0)
s_barrier
v_mfma_f32_16x16x32_bf16 v[124:127], a[56:59], a[92:95], v[124:127]
ds_read_b64_tr_b16 a[76:77], v186 offset:0
v_mfma_f32_16x16x32_bf16 v[128:131], a[88:91], a[92:95], v[128:131]
ds_read_b64_tr_b16 a[78:79], v186 offset:6144
v_mfma_f32_16x16x32_bf16 v[132:135], a[12:15], a[92:95], v[132:135]
ds_read_b128 a[40:43], v191 offset:0
v_mfma_f32_16x16x32_bf16 v[136:139], a[72:75], a[92:95], v[136:139]
ds_read_b64_tr_b16 a[36:37], v187 offset:0
ds_read_b64_tr_b16 a[38:39], v187 offset:6144
v_mfma_f32_16x16x32_bf16 v[140:143], a[20:23], a[28:31], v[140:143]
ds_read_b64_tr_b16 a[52:53], v188 offset:0
ds_read_b64_tr_b16 a[54:55], v188 offset:6144
v_mfma_f32_16x16x32_bf16 v[144:147], a[56:59], a[28:31], v[144:147]
ds_read_b64_tr_b16 a[68:69], v189 offset:0
ds_read_b64_tr_b16 a[70:71], v189 offset:6144
v_mfma_f32_16x16x32_bf16 v[148:151], a[88:91], a[28:31], v[148:151]
ds_read_b64_tr_b16 a[0:1], v190 offset:0
ds_read_b64_tr_b16 a[2:3], v190 offset:6144
v_mfma_f32_16x16x32_bf16 v[152:155], a[12:15], a[28:31], v[152:155]
ds_read_b128 a[32:35], v191 offset:1024
s_add_u32 s58, s73, 3
v_mfma_f32_16x16x32_bf16 v[156:159], a[72:75], a[28:31], v[156:159]
s_and_b32 s58, s58, 7
s_add_u32 s58, s58, 32
s_mul_i32 s59, s58, 40960
v_mfma_f32_16x16x32_bf16 v[160:163], a[20:23], a[44:47], v[160:163]
s_add_u32 s57, s76, s59
s_mul_i32 s59, s58, 294912
s_add_u32 s55, s77, s59
v_mfma_f32_16x16x32_bf16 v[164:167], a[56:59], a[44:47], v[164:167]
s_add_u32 s56, s78, s59
s_add_u32 m0, s54, 0
v_mfma_f32_16x16x32_bf16 v[168:171], a[88:91], a[44:47], v[168:171]
s_waitcnt lgkmcnt(7)
v_mfma_f32_16x16x32_bf16 v[172:175], a[12:15], a[44:47], v[172:175]
buffer_load_dwordx4 v202, s[40:43], s57 offen lds
v_mfma_f32_16x16x32_bf16 v[176:179], a[72:75], a[44:47], v[176:179]
s_waitcnt lgkmcnt(3)
ds_read_b128 a[80:83], v191 offset:2048
v_mfma_f32_16x16x32_bf16 v[0:3], a[76:79], a[40:43], v[0:3]
buffer_load_dwordx4 v203, s[40:43], s57 offen offset:1024 lds
v_mfma_f32_16x16x32_bf16 v[4:7], a[36:39], a[40:43], v[4:7]
s_waitcnt lgkmcnt(1)
ds_read_b128 a[48:51], v191 offset:3072
v_mfma_f32_16x16x32_bf16 v[8:11], a[52:55], a[40:43], v[8:11]
buffer_load_dwordx4 v204, s[40:43], s57 offen offset:2048 lds
v_mfma_f32_16x16x32_bf16 v[12:15], a[68:71], a[40:43], v[12:15]
ds_read_b128 a[64:67], v191 offset:4096
s_add_u32 m0, s52, 0
v_mfma_f32_16x16x32_bf16 v[16:19], a[0:3], a[40:43], v[16:19]
buffer_load_dwordx4 v198, s[36:39], s55 offen lds
v_mfma_f32_16x16x32_bf16 v[20:23], a[76:79], a[32:35], v[20:23]
ds_read_b128 a[60:63], v191 offset:5120
v_mfma_f32_16x16x32_bf16 v[24:27], a[36:39], a[32:35], v[24:27]
buffer_load_dwordx4 v198, s[36:39], s55 offen offset:1024 lds
v_mfma_f32_16x16x32_bf16 v[28:31], a[52:55], a[32:35], v[28:31]
s_waitcnt lgkmcnt(3)
ds_read_b128 a[16:19], v191 offset:6144
v_mfma_f32_16x16x32_bf16 v[32:35], a[68:71], a[32:35], v[32:35]
v_mfma_f32_16x16x32_bf16 v[36:39], a[0:3], a[32:35], v[36:39]
v_mfma_f32_16x16x32_bf16 v[40:43], a[76:79], a[80:83], v[40:43]
ds_read_b128 a[84:87], v191 offset:7168
v_mfma_f32_16x16x32_bf16 v[44:47], a[36:39], a[80:83], v[44:47]
buffer_load_dwordx4 v198, s[36:39], s55 offen offset:2048 lds
v_mfma_f32_16x16x32_bf16 v[48:51], a[52:55], a[80:83], v[48:51]
s_waitcnt lgkmcnt(3)
v_mfma_f32_16x16x32_bf16 v[52:55], a[68:71], a[80:83], v[52:55]
v_mfma_f32_16x16x32_bf16 v[56:59], a[0:3], a[80:83], v[56:59]
v_mfma_f32_16x16x32_bf16 v[60:63], a[76:79], a[48:51], v[60:63]
ds_read_b128 a[8:11], v191 offset:8192
v_mfma_f32_16x16x32_bf16 v[64:67], a[36:39], a[48:51], v[64:67]
buffer_load_dwordx4 v198, s[36:39], s55 offen offset:3072 lds
v_mfma_f32_16x16x32_bf16 v[68:71], a[52:55], a[48:51], v[68:71]
v_mfma_f32_16x16x32_bf16 v[72:75], a[68:71], a[48:51], v[72:75]
v_mfma_f32_16x16x32_bf16 v[76:79], a[0:3], a[48:51], v[76:79]
v_mfma_f32_16x16x32_bf16 v[80:83], a[76:79], a[64:67], v[80:83]
ds_read_b64_tr_b16 a[24:25], v186 offset:30720
v_mfma_f32_16x16x32_bf16 v[84:87], a[36:39], a[64:67], v[84:87]
s_and_b32 s60, s68, 0
s_add_u32 m0, s53, s60
v_mfma_f32_16x16x32_bf16 v[88:91], a[52:55], a[64:67], v[88:91]
s_waitcnt lgkmcnt(2)
v_mfma_f32_16x16x32_bf16 v[92:95], a[68:71], a[64:67], v[92:95]
buffer_load_dwordx4 v199, s[36:39], s56 offen lds
v_mfma_f32_16x16x32_bf16 v[96:99], a[0:3], a[64:67], v[96:99]
ds_read_b64_tr_b16 a[26:27], v186 offset:36864
v_mfma_f32_16x16x32_bf16 v[100:103], a[76:79], a[60:63], v[100:103]
ds_read_b128 a[4:7], v191 offset:30720
v_mfma_f32_16x16x32_bf16 v[104:107], a[36:39], a[60:63], v[104:107]
ds_read_b64_tr_b16 a[92:93], v187 offset:30720
v_mfma_f32_16x16x32_bf16 v[108:111], a[52:55], a[60:63], v[108:111]
ds_read_b64_tr_b16 a[94:95], v187 offset:36864
v_mfma_f32_16x16x32_bf16 v[112:115], a[68:71], a[60:63], v[112:115]
ds_read_b64_tr_b16 a[28:29], v188 offset:30720
v_mfma_f32_16x16x32_bf16 v[116:119], a[0:3], a[60:63], v[116:119]
ds_read_b64_tr_b16 a[30:31], v188 offset:36864
v_mfma_f32_16x16x32_bf16 v[120:123], a[76:79], a[16:19], v[120:123]
ds_read_b64_tr_b16 a[20:21], v189 offset:30720
v_mfma_f32_16x16x32_bf16 v[124:127], a[36:39], a[16:19], v[124:127]
ds_read_b64_tr_b16 a[22:23], v189 offset:36864
v_mfma_f32_16x16x32_bf16 v[128:131], a[52:55], a[16:19], v[128:131]
ds_read_b64_tr_b16 a[56:57], v190 offset:30720
v_mfma_f32_16x16x32_bf16 v[132:135], a[68:71], a[16:19], v[132:135]
ds_read_b64_tr_b16 a[58:59], v190 offset:36864
s_add_u32 s58, s73, 4
v_mfma_f32_16x16x32_bf16 v[136:139], a[0:3], a[16:19], v[136:139]
s_and_b32 s58, s58, 7
s_add_u32 s58, s58, 32
s_mul_i32 s59, s58, 40960
v_mfma_f32_16x16x32_bf16 v[140:143], a[76:79], a[84:87], v[140:143]
s_add_u32 s57, s76, s59
s_mul_i32 s59, s58, 294912
s_add_u32 s55, s77, s59
v_mfma_f32_16x16x32_bf16 v[144:147], a[36:39], a[84:87], v[144:147]
s_add_u32 s56, s78, s59
s_add_u32 m0, s54, 30720
v_mfma_f32_16x16x32_bf16 v[148:151], a[52:55], a[84:87], v[148:151]
s_waitcnt lgkmcnt(4)
v_mfma_f32_16x16x32_bf16 v[152:155], a[68:71], a[84:87], v[152:155]
buffer_load_dwordx4 v202, s[40:43], s57 offen lds
v_mfma_f32_16x16x32_bf16 v[156:159], a[0:3], a[84:87], v[156:159]
ds_read_b128 a[88:91], v191 offset:31744
v_mfma_f32_16x16x32_bf16 v[160:163], a[76:79], a[8:11], v[160:163]
buffer_load_dwordx4 v203, s[40:43], s57 offen offset:1024 lds
v_mfma_f32_16x16x32_bf16 v[164:167], a[36:39], a[8:11], v[164:167]
ds_read_b128 a[12:15], v191 offset:32768
v_mfma_f32_16x16x32_bf16 v[168:171], a[52:55], a[8:11], v[168:171]
buffer_load_dwordx4 v204, s[40:43], s57 offen offset:2048 lds
v_mfma_f32_16x16x32_bf16 v[172:175], a[68:71], a[8:11], v[172:175]
ds_read_b128 a[44:47], v191 offset:33792
s_add_u32 m0, s52, 30720
v_mfma_f32_16x16x32_bf16 v[176:179], a[0:3], a[8:11], v[176:179]
buffer_load_dwordx4 v198, s[36:39], s55 offen lds
v_mfma_f32_16x16x32_bf16 v[0:3], a[24:27], a[4:7], v[0:3]
s_waitcnt lgkmcnt(3)
ds_read_b128 a[72:75], v191 offset:34816
v_mfma_f32_16x16x32_bf16 v[4:7], a[92:95], a[4:7], v[4:7]
buffer_load_dwordx4 v198, s[36:39], s55 offen offset:1024 lds
v_mfma_f32_16x16x32_bf16 v[8:11], a[28:31], a[4:7], v[8:11]
s_waitcnt lgkmcnt(3)
ds_read_b128 a[40:43], v191 offset:35840
v_mfma_f32_16x16x32_bf16 v[12:15], a[20:23], a[4:7], v[12:15]
v_mfma_f32_16x16x32_bf16 v[16:19], a[56:59], a[4:7], v[16:19]
v_mfma_f32_16x16x32_bf16 v[20:23], a[24:27], a[88:91], v[20:23]
ds_read_b128 a[32:35], v191 offset:36864
v_mfma_f32_16x16x32_bf16 v[24:27], a[92:95], a[88:91], v[24:27]
buffer_load_dwordx4 v198, s[36:39], s55 offen offset:2048 lds
v_mfma_f32_16x16x32_bf16 v[28:31], a[28:31], a[88:91], v[28:31]
s_waitcnt lgkmcnt(3)
v_mfma_f32_16x16x32_bf16 v[32:35], a[20:23], a[88:91], v[32:35]
v_mfma_f32_16x16x32_bf16 v[36:39], a[56:59], a[88:91], v[36:39]
v_mfma_f32_16x16x32_bf16 v[40:43], a[24:27], a[12:15], v[40:43]
ds_read_b128 a[80:83], v191 offset:37888
v_mfma_f32_16x16x32_bf16 v[44:47], a[92:95], a[12:15], v[44:47]
buffer_load_dwordx4 v198, s[36:39], s55 offen offset:3072 lds
v_mfma_f32_16x16x32_bf16 v[48:51], a[28:31], a[12:15], v[48:51]
v_mfma_f32_16x16x32_bf16 v[52:55], a[20:23], a[12:15], v[52:55]
v_mfma_f32_16x16x32_bf16 v[56:59], a[56:59], a[12:15], v[56:59]
v_mfma_f32_16x16x32_bf16 v[60:63], a[24:27], a[44:47], v[60:63]
ds_read_b128 a[48:51], v191 offset:38912
v_mfma_f32_16x16x32_bf16 v[64:67], a[92:95], a[44:47], v[64:67]
s_and_b32 s60, s68, 30720
s_add_u32 m0, s53, s60
v_mfma_f32_16x16x32_bf16 v[68:71], a[28:31], a[44:47], v[68:71]
s_waitcnt lgkmcnt(2)
v_mfma_f32_16x16x32_bf16 v[72:75], a[20:23], a[44:47], v[72:75]
buffer_load_dwordx4 v199, s[36:39], s56 offen lds
v_mfma_f32_16x16x32_bf16 v[76:79], a[56:59], a[44:47], v[76:79]
v_mfma_f32_16x16x32_bf16 v[80:83], a[24:27], a[72:75], v[80:83]
v_mfma_f32_16x16x32_bf16 v[84:87], a[92:95], a[72:75], v[84:87]
v_mfma_f32_16x16x32_bf16 v[88:91], a[28:31], a[72:75], v[88:91]
v_mfma_f32_16x16x32_bf16 v[92:95], a[20:23], a[72:75], v[92:95]
v_mfma_f32_16x16x32_bf16 v[96:99], a[56:59], a[72:75], v[96:99]
v_mfma_f32_16x16x32_bf16 v[100:103], a[24:27], a[40:43], v[100:103]
v_mfma_f32_16x16x32_bf16 v[104:107], a[92:95], a[40:43], v[104:107]
v_mfma_f32_16x16x32_bf16 v[108:111], a[28:31], a[40:43], v[108:111]
v_mfma_f32_16x16x32_bf16 v[112:115], a[20:23], a[40:43], v[112:115]
v_mfma_f32_16x16x32_bf16 v[116:119], a[56:59], a[40:43], v[116:119]
v_mfma_f32_16x16x32_bf16 v[120:123], a[24:27], a[32:35], v[120:123]
s_waitcnt vmcnt(8) lgkmcnt(0)
s_barrier
v_mfma_f32_16x16x32_bf16 v[124:127], a[92:95], a[32:35], v[124:127]
ds_read_b64_tr_b16 a[64:65], v192 offset:0
v_mfma_f32_16x16x32_bf16 v[128:131], a[28:31], a[32:35], v[128:131]
ds_read_b64_tr_b16 a[66:67], v192 offset:6144
v_mfma_f32_16x16x32_bf16 v[132:135], a[20:23], a[32:35], v[132:135]
ds_read_b128 a[60:63], v197 offset:0
v_mfma_f32_16x16x32_bf16 v[136:139], a[56:59], a[32:35], v[136:139]
ds_read_b64_tr_b16 a[16:17], v193 offset:0
ds_read_b64_tr_b16 a[18:19], v193 offset:6144
v_mfma_f32_16x16x32_bf16 v[140:143], a[24:27], a[80:83], v[140:143]
ds_read_b64_tr_b16 a[84:85], v194 offset:0
ds_read_b64_tr_b16 a[86:87], v194 offset:6144
v_mfma_f32_16x16x32_bf16 v[144:147], a[92:95], a[80:83], v[144:147]
ds_read_b64_tr_b16 a[76:77], v195 offset:0
ds_read_b64_tr_b16 a[78:79], v195 offset:6144
v_mfma_f32_16x16x32_bf16 v[148:151], a[28:31], a[80:83], v[148:151]
ds_read_b64_tr_b16 a[36:37], v196 offset:0
ds_read_b64_tr_b16 a[38:39], v196 offset:6144
v_mfma_f32_16x16x32_bf16 v[152:155], a[20:23], a[80:83], v[152:155]
ds_read_b128 a[52:55], v197 offset:1024
s_add_u32 s58, s73, 5
v_mfma_f32_16x16x32_bf16 v[156:159], a[56:59], a[80:83], v[156:159]
s_and_b32 s58, s58, 7
s_add_u32 s58, s58, 32
s_mul_i32 s59, s58, 40960
v_mfma_f32_16x16x32_bf16 v[160:163], a[24:27], a[48:51], v[160:163]
s_add_u32 s57, s76, s59
s_mul_i32 s59, s58, 294912
s_add_u32 s55, s77, s59
v_mfma_f32_16x16x32_bf16 v[164:167], a[92:95], a[48:51], v[164:167]
s_add_u32 s56, s78, s59
s_add_u32 m0, s54, 61440
v_mfma_f32_16x16x32_bf16 v[168:171], a[28:31], a[48:51], v[168:171]
s_waitcnt lgkmcnt(7)
v_mfma_f32_16x16x32_bf16 v[172:175], a[20:23], a[48:51], v[172:175]
buffer_load_dwordx4 v202, s[40:43], s57 offen lds
v_mfma_f32_16x16x32_bf16 v[176:179], a[56:59], a[48:51], v[176:179]
s_waitcnt lgkmcnt(3)
ds_read_b128 a[68:71], v197 offset:2048
v_mfma_f32_16x16x32_bf16 v[0:3], a[64:67], a[60:63], v[0:3]
buffer_load_dwordx4 v203, s[40:43], s57 offen offset:1024 lds
v_mfma_f32_16x16x32_bf16 v[4:7], a[16:19], a[60:63], v[4:7]
s_waitcnt lgkmcnt(1)
ds_read_b128 a[0:3], v197 offset:3072
v_mfma_f32_16x16x32_bf16 v[8:11], a[84:87], a[60:63], v[8:11]
buffer_load_dwordx4 v204, s[40:43], s57 offen offset:2048 lds
v_mfma_f32_16x16x32_bf16 v[12:15], a[76:79], a[60:63], v[12:15]
ds_read_b128 a[8:11], v197 offset:4096
s_add_u32 m0, s52, 61440
v_mfma_f32_16x16x32_bf16 v[16:19], a[36:39], a[60:63], v[16:19]
buffer_load_dwordx4 v198, s[36:39], s55 offen lds
v_mfma_f32_16x16x32_bf16 v[20:23], a[64:67], a[52:55], v[20:23]
ds_read_b128 a[4:7], v197 offset:5120
v_mfma_f32_16x16x32_bf16 v[24:27], a[16:19], a[52:55], v[24:27]
buffer_load_dwordx4 v198, s[36:39], s55 offen offset:1024 lds
v_mfma_f32_16x16x32_bf16 v[28:31], a[84:87], a[52:55], v[28:31]
s_waitcnt lgkmcnt(3)
ds_read_b128 a[88:91], v197 offset:6144
v_mfma_f32_16x16x32_bf16 v[32:35], a[76:79], a[52:55], v[32:35]
v_mfma_f32_16x16x32_bf16 v[36:39], a[36:39], a[52:55], v[36:39]
v_mfma_f32_16x16x32_bf16 v[40:43], a[64:67], a[68:71], v[40:43]
ds_read_b128 a[12:15], v197 offset:7168
v_mfma_f32_16x16x32_bf16 v[44:47], a[16:19], a[68:71], v[44:47]
buffer_load_dwordx4 v198, s[36:39], s55 offen offset:2048 lds
v_mfma_f32_16x16x32_bf16 v[48:51], a[84:87], a[68:71], v[48:51]
s_waitcnt lgkmcnt(3)
v_mfma_f32_16x16x32_bf16 v[52:55], a[76:79], a[68:71], v[52:55]
v_mfma_f32_16x16x32_bf16 v[56:59], a[36:39], a[68:71], v[56:59]
v_mfma_f32_16x16x32_bf16 v[60:63], a[64:67], a[0:3], v[60:63]
ds_read_b128 a[44:47], v197 offset:8192
v_mfma_f32_16x16x32_bf16 v[64:67], a[16:19], a[0:3], v[64:67]
buffer_load_dwordx4 v198, s[36:39], s55 offen offset:3072 lds
v_mfma_f32_16x16x32_bf16 v[68:71], a[84:87], a[0:3], v[68:71]
v_mfma_f32_16x16x32_bf16 v[72:75], a[76:79], a[0:3], v[72:75]
v_mfma_f32_16x16x32_bf16 v[76:79], a[36:39], a[0:3], v[76:79]
v_mfma_f32_16x16x32_bf16 v[80:83], a[64:67], a[8:11], v[80:83]
ds_read_b64_tr_b16 a[72:73], v180 offset:0
v_mfma_f32_16x16x32_bf16 v[84:87], a[16:19], a[8:11], v[84:87]
s_and_b32 s60, s68, 61440
s_add_u32 m0, s53, s60
v_mfma_f32_16x16x32_bf16 v[88:91], a[84:87], a[8:11], v[88:91]
s_waitcnt lgkmcnt(2)
v_mfma_f32_16x16x32_bf16 v[92:95], a[76:79], a[8:11], v[92:95]
buffer_load_dwordx4 v199, s[36:39], s56 offen lds
v_mfma_f32_16x16x32_bf16 v[96:99], a[36:39], a[8:11], v[96:99]
ds_read_b64_tr_b16 a[74:75], v180 offset:6144
v_mfma_f32_16x16x32_bf16 v[100:103], a[64:67], a[4:7], v[100:103]
ds_read_b128 a[40:43], v185 offset:0
v_mfma_f32_16x16x32_bf16 v[104:107], a[16:19], a[4:7], v[104:107]
ds_read_b64_tr_b16 a[32:33], v181 offset:0
v_mfma_f32_16x16x32_bf16 v[108:111], a[84:87], a[4:7], v[108:111]
ds_read_b64_tr_b16 a[34:35], v181 offset:6144
v_mfma_f32_16x16x32_bf16 v[112:115], a[76:79], a[4:7], v[112:115]
ds_read_b64_tr_b16 a[80:81], v182 offset:0
v_mfma_f32_16x16x32_bf16 v[116:119], a[36:39], a[4:7], v[116:119]
ds_read_b64_tr_b16 a[82:83], v182 offset:6144
v_mfma_f32_16x16x32_bf16 v[120:123], a[64:67], a[88:91], v[120:123]
ds_read_b64_tr_b16 a[24:25], v183 offset:0
v_mfma_f32_16x16x32_bf16 v[124:127], a[16:19], a[88:91], v[124:127]
ds_read_b64_tr_b16 a[26:27], v183 offset:6144
v_mfma_f32_16x16x32_bf16 v[128:131], a[84:87], a[88:91], v[128:131]
ds_read_b64_tr_b16 a[92:93], v184 offset:0
v_mfma_f32_16x16x32_bf16 v[132:135], a[76:79], a[88:91], v[132:135]
ds_read_b64_tr_b16 a[94:95], v184 offset:6144
s_add_u32 s58, s73, 6
v_mfma_f32_16x16x32_bf16 v[136:139], a[36:39], a[88:91], v[136:139]
s_and_b32 s58, s58, 7
s_add_u32 s58, s58, 32
s_mul_i32 s59, s58, 40960
v_mfma_f32_16x16x32_bf16 v[140:143], a[64:67], a[12:15], v[140:143]
s_add_u32 s57, s76, s59
s_mul_i32 s59, s58, 294912
s_add_u32 s55, s77, s59
v_mfma_f32_16x16x32_bf16 v[144:147], a[16:19], a[12:15], v[144:147]
s_add_u32 s56, s78, s59
s_add_u32 m0, s54, 92160
v_mfma_f32_16x16x32_bf16 v[148:151], a[84:87], a[12:15], v[148:151]
s_waitcnt lgkmcnt(4)
v_mfma_f32_16x16x32_bf16 v[152:155], a[76:79], a[12:15], v[152:155]
buffer_load_dwordx4 v202, s[40:43], s57 offen lds
v_mfma_f32_16x16x32_bf16 v[156:159], a[36:39], a[12:15], v[156:159]
ds_read_b128 a[28:31], v185 offset:1024
v_mfma_f32_16x16x32_bf16 v[160:163], a[64:67], a[44:47], v[160:163]
buffer_load_dwordx4 v203, s[40:43], s57 offen offset:1024 lds
v_mfma_f32_16x16x32_bf16 v[164:167], a[16:19], a[44:47], v[164:167]
ds_read_b128 a[20:23], v185 offset:2048
v_mfma_f32_16x16x32_bf16 v[168:171], a[84:87], a[44:47], v[168:171]
buffer_load_dwordx4 v204, s[40:43], s57 offen offset:2048 lds
v_mfma_f32_16x16x32_bf16 v[172:175], a[76:79], a[44:47], v[172:175]
ds_read_b128 a[48:51], v185 offset:3072
s_add_u32 m0, s52, 92160
v_mfma_f32_16x16x32_bf16 v[176:179], a[36:39], a[44:47], v[176:179]
buffer_load_dwordx4 v198, s[36:39], s55 offen lds
v_mfma_f32_16x16x32_bf16 v[0:3], a[72:75], a[40:43], v[0:3]
s_waitcnt lgkmcnt(3)
ds_read_b128 a[56:59], v185 offset:4096
v_mfma_f32_16x16x32_bf16 v[4:7], a[32:35], a[40:43], v[4:7]
buffer_load_dwordx4 v198, s[36:39], s55 offen offset:1024 lds
v_mfma_f32_16x16x32_bf16 v[8:11], a[80:83], a[40:43], v[8:11]
s_waitcnt lgkmcnt(3)
ds_read_b128 a[60:63], v185 offset:5120
v_mfma_f32_16x16x32_bf16 v[12:15], a[24:27], a[40:43], v[12:15]
v_mfma_f32_16x16x32_bf16 v[16:19], a[92:95], a[40:43], v[16:19]
v_mfma_f32_16x16x32_bf16 v[20:23], a[72:75], a[28:31], v[20:23]
ds_read_b128 a[52:55], v185 offset:6144
v_mfma_f32_16x16x32_bf16 v[24:27], a[32:35], a[28:31], v[24:27]
buffer_load_dwordx4 v198, s[36:39], s55 offen offset:2048 lds
v_mfma_f32_16x16x32_bf16 v[28:31], a[80:83], a[28:31], v[28:31]
s_waitcnt lgkmcnt(3)
v_mfma_f32_16x16x32_bf16 v[32:35], a[24:27], a[28:31], v[32:35]
v_mfma_f32_16x16x32_bf16 v[36:39], a[92:95], a[28:31], v[36:39]
v_mfma_f32_16x16x32_bf16 v[40:43], a[72:75], a[20:23], v[40:43]
ds_read_b128 a[68:71], v185 offset:7168
v_mfma_f32_16x16x32_bf16 v[44:47], a[32:35], a[20:23], v[44:47]
buffer_load_dwordx4 v198, s[36:39], s55 offen offset:3072 lds
v_mfma_f32_16x16x32_bf16 v[48:51], a[80:83], a[20:23], v[48:51]
v_mfma_f32_16x16x32_bf16 v[52:55], a[24:27], a[20:23], v[52:55]
v_mfma_f32_16x16x32_bf16 v[56:59], a[92:95], a[20:23], v[56:59]
v_mfma_f32_16x16x32_bf16 v[60:63], a[72:75], a[48:51], v[60:63]
ds_read_b128 a[0:3], v185 offset:8192
v_mfma_f32_16x16x32_bf16 v[64:67], a[32:35], a[48:51], v[64:67]
s_and_b32 s60, s68, 92160
s_add_u32 m0, s53, s60
v_mfma_f32_16x16x32_bf16 v[68:71], a[80:83], a[48:51], v[68:71]
s_waitcnt lgkmcnt(2)
v_mfma_f32_16x16x32_bf16 v[72:75], a[24:27], a[48:51], v[72:75]
buffer_load_dwordx4 v199, s[36:39], s56 offen lds
v_mfma_f32_16x16x32_bf16 v[76:79], a[92:95], a[48:51], v[76:79]
v_mfma_f32_16x16x32_bf16 v[80:83], a[72:75], a[56:59], v[80:83]
v_mfma_f32_16x16x32_bf16 v[84:87], a[32:35], a[56:59], v[84:87]
v_mfma_f32_16x16x32_bf16 v[88:91], a[80:83], a[56:59], v[88:91]
v_mfma_f32_16x16x32_bf16 v[92:95], a[24:27], a[56:59], v[92:95]
v_mfma_f32_16x16x32_bf16 v[96:99], a[92:95], a[56:59], v[96:99]
v_mfma_f32_16x16x32_bf16 v[100:103], a[72:75], a[60:63], v[100:103]
v_mfma_f32_16x16x32_bf16 v[104:107], a[32:35], a[60:63], v[104:107]
v_mfma_f32_16x16x32_bf16 v[108:111], a[80:83], a[60:63], v[108:111]
v_mfma_f32_16x16x32_bf16 v[112:115], a[24:27], a[60:63], v[112:115]
v_mfma_f32_16x16x32_bf16 v[116:119], a[92:95], a[60:63], v[116:119]
v_mfma_f32_16x16x32_bf16 v[120:123], a[72:75], a[52:55], v[120:123]
s_waitcnt vmcnt(8) lgkmcnt(0)
s_barrier
v_mfma_f32_16x16x32_bf16 v[124:127], a[32:35], a[52:55], v[124:127]
ds_read_b64_tr_b16 a[8:9], v180 offset:30720
v_mfma_f32_16x16x32_bf16 v[128:131], a[80:83], a[52:55], v[128:131]
ds_read_b64_tr_b16 a[10:11], v180 offset:36864
v_mfma_f32_16x16x32_bf16 v[132:135], a[24:27], a[52:55], v[132:135]
ds_read_b128 a[4:7], v185 offset:30720
v_mfma_f32_16x16x32_bf16 v[136:139], a[92:95], a[52:55], v[136:139]
ds_read_b64_tr_b16 a[88:89], v181 offset:30720
ds_read_b64_tr_b16 a[90:91], v181 offset:36864
v_mfma_f32_16x16x32_bf16 v[140:143], a[72:75], a[68:71], v[140:143]
ds_read_b64_tr_b16 a[12:13], v182 offset:30720
ds_read_b64_tr_b16 a[14:15], v182 offset:36864
v_mfma_f32_16x16x32_bf16 v[144:147], a[32:35], a[68:71], v[144:147]
ds_read_b64_tr_b16 a[64:65], v183 offset:30720
ds_read_b64_tr_b16 a[66:67], v183 offset:36864
v_mfma_f32_16x16x32_bf16 v[148:151], a[80:83], a[68:71], v[148:151]
ds_read_b64_tr_b16 a[16:17], v184 offset:30720
ds_read_b64_tr_b16 a[18:19], v184 offset:36864
v_mfma_f32_16x16x32_bf16 v[152:155], a[24:27], a[68:71], v[152:155]
ds_read_b128 a[84:87], v185 offset:31744
s_add_u32 s58, s73, 7
v_mfma_f32_16x16x32_bf16 v[156:159], a[92:95], a[68:71], v[156:159]
s_and_b32 s58, s58, 7
s_add_u32 s58, s58, 32
s_mul_i32 s59, s58, 40960
v_mfma_f32_16x16x32_bf16 v[160:163], a[72:75], a[0:3], v[160:163]
s_add_u32 s57, s76, s59
s_mul_i32 s59, s58, 294912
s_add_u32 s55, s77, s59
v_mfma_f32_16x16x32_bf16 v[164:167], a[32:35], a[0:3], v[164:167]
s_add_u32 s56, s78, s59
s_add_u32 m0, s54, 122880
v_mfma_f32_16x16x32_bf16 v[168:171], a[80:83], a[0:3], v[168:171]
s_waitcnt lgkmcnt(7)
v_mfma_f32_16x16x32_bf16 v[172:175], a[24:27], a[0:3], v[172:175]
buffer_load_dwordx4 v202, s[40:43], s57 offen lds
v_mfma_f32_16x16x32_bf16 v[176:179], a[92:95], a[0:3], v[176:179]
s_waitcnt lgkmcnt(3)
ds_read_b128 a[76:79], v185 offset:32768
v_mfma_f32_16x16x32_bf16 v[0:3], a[8:11], a[4:7], v[0:3]
buffer_load_dwordx4 v203, s[40:43], s57 offen offset:1024 lds
v_mfma_f32_16x16x32_bf16 v[4:7], a[88:91], a[4:7], v[4:7]
s_waitcnt lgkmcnt(1)
ds_read_b128 a[36:39], v185 offset:33792
v_mfma_f32_16x16x32_bf16 v[8:11], a[12:15], a[4:7], v[8:11]
buffer_load_dwordx4 v204, s[40:43], s57 offen offset:2048 lds
v_mfma_f32_16x16x32_bf16 v[12:15], a[64:67], a[4:7], v[12:15]
ds_read_b128 a[44:47], v185 offset:34816
s_add_u32 m0, s52, 122880
v_mfma_f32_16x16x32_bf16 v[16:19], a[16:19], a[4:7], v[16:19]
buffer_load_dwordx4 v198, s[36:39], s55 offen lds
v_mfma_f32_16x16x32_bf16 v[20:23], a[8:11], a[84:87], v[20:23]
ds_read_b128 a[40:43], v185 offset:35840
v_mfma_f32_16x16x32_bf16 v[24:27], a[88:91], a[84:87], v[24:27]
buffer_load_dwordx4 v198, s[36:39], s55 offen offset:1024 lds
v_mfma_f32_16x16x32_bf16 v[28:31], a[12:15], a[84:87], v[28:31]
s_waitcnt lgkmcnt(3)
ds_read_b128 a[28:31], v185 offset:36864
v_mfma_f32_16x16x32_bf16 v[32:35], a[64:67], a[84:87], v[32:35]
v_mfma_f32_16x16x32_bf16 v[36:39], a[16:19], a[84:87], v[36:39]
v_mfma_f32_16x16x32_bf16 v[40:43], a[8:11], a[76:79], v[40:43]
ds_read_b128 a[20:23], v185 offset:37888
v_mfma_f32_16x16x32_bf16 v[44:47], a[88:91], a[76:79], v[44:47]
buffer_load_dwordx4 v198, s[36:39], s55 offen offset:2048 lds
v_mfma_f32_16x16x32_bf16 v[48:51], a[12:15], a[76:79], v[48:51]
s_waitcnt lgkmcnt(3)
v_mfma_f32_16x16x32_bf16 v[52:55], a[64:67], a[76:79], v[52:55]
v_mfma_f32_16x16x32_bf16 v[56:59], a[16:19], a[76:79], v[56:59]
v_mfma_f32_16x16x32_bf16 v[60:63], a[8:11], a[36:39], v[60:63]
ds_read_b128 a[48:51], v185 offset:38912
v_mfma_f32_16x16x32_bf16 v[64:67], a[88:91], a[36:39], v[64:67]
buffer_load_dwordx4 v198, s[36:39], s55 offen offset:3072 lds
v_mfma_f32_16x16x32_bf16 v[68:71], a[12:15], a[36:39], v[68:71]
v_mfma_f32_16x16x32_bf16 v[72:75], a[64:67], a[36:39], v[72:75]
v_mfma_f32_16x16x32_bf16 v[76:79], a[16:19], a[36:39], v[76:79]
v_mfma_f32_16x16x32_bf16 v[80:83], a[8:11], a[44:47], v[80:83]
ds_read_b64_tr_b16 a[56:57], v186 offset:0
v_mfma_f32_16x16x32_bf16 v[84:87], a[88:91], a[44:47], v[84:87]
s_and_b32 s60, s68, 122880
s_add_u32 m0, s53, s60
v_mfma_f32_16x16x32_bf16 v[88:91], a[12:15], a[44:47], v[88:91]
s_waitcnt lgkmcnt(2)
v_mfma_f32_16x16x32_bf16 v[92:95], a[64:67], a[44:47], v[92:95]
buffer_load_dwordx4 v199, s[36:39], s56 offen lds
v_mfma_f32_16x16x32_bf16 v[96:99], a[16:19], a[44:47], v[96:99]
ds_read_b64_tr_b16 a[58:59], v186 offset:6144
v_mfma_f32_16x16x32_bf16 v[100:103], a[8:11], a[40:43], v[100:103]
ds_read_b128 a[60:63], v191 offset:0
v_mfma_f32_16x16x32_bf16 v[104:107], a[88:91], a[40:43], v[104:107]
ds_read_b64_tr_b16 a[52:53], v187 offset:0
v_mfma_f32_16x16x32_bf16 v[108:111], a[12:15], a[40:43], v[108:111]
ds_read_b64_tr_b16 a[54:55], v187 offset:6144
v_mfma_f32_16x16x32_bf16 v[112:115], a[64:67], a[40:43], v[112:115]
ds_read_b64_tr_b16 a[68:69], v188 offset:0
v_mfma_f32_16x16x32_bf16 v[116:119], a[16:19], a[40:43], v[116:119]
ds_read_b64_tr_b16 a[70:71], v188 offset:6144
v_mfma_f32_16x16x32_bf16 v[120:123], a[8:11], a[28:31], v[120:123]
ds_read_b64_tr_b16 a[72:73], v189 offset:0
v_mfma_f32_16x16x32_bf16 v[124:127], a[88:91], a[28:31], v[124:127]
ds_read_b64_tr_b16 a[74:75], v189 offset:6144
v_mfma_f32_16x16x32_bf16 v[128:131], a[12:15], a[28:31], v[128:131]
ds_read_b64_tr_b16 a[32:33], v190 offset:0
v_mfma_f32_16x16x32_bf16 v[132:135], a[64:67], a[28:31], v[132:135]
ds_read_b64_tr_b16 a[34:35], v190 offset:6144
s_add_u32 s58, s73, 0
v_mfma_f32_16x16x32_bf16 v[136:139], a[16:19], a[28:31], v[136:139]
s_and_b32 s58, s58, 7
s_add_u32 s58, s58, 40
s_mul_i32 s59, s58, 40960
v_mfma_f32_16x16x32_bf16 v[140:143], a[8:11], a[20:23], v[140:143]
s_add_u32 s57, s76, s59
s_mul_i32 s59, s58, 294912
s_add_u32 s55, s77, s59
v_mfma_f32_16x16x32_bf16 v[144:147], a[88:91], a[20:23], v[144:147]
s_add_u32 s56, s78, s59
s_add_u32 m0, s54, 0
v_mfma_f32_16x16x32_bf16 v[148:151], a[12:15], a[20:23], v[148:151]
s_waitcnt lgkmcnt(4)
v_mfma_f32_16x16x32_bf16 v[152:155], a[64:67], a[20:23], v[152:155]
buffer_load_dwordx4 v202, s[40:43], s57 offen lds
v_mfma_f32_16x16x32_bf16 v[156:159], a[16:19], a[20:23], v[156:159]
ds_read_b128 a[80:83], v191 offset:1024
v_mfma_f32_16x16x32_bf16 v[160:163], a[8:11], a[48:51], v[160:163]
buffer_load_dwordx4 v203, s[40:43], s57 offen offset:1024 lds
v_mfma_f32_16x16x32_bf16 v[164:167], a[88:91], a[48:51], v[164:167]
ds_read_b128 a[24:27], v191 offset:2048
v_mfma_f32_16x16x32_bf16 v[168:171], a[12:15], a[48:51], v[168:171]
buffer_load_dwordx4 v204, s[40:43], s57 offen offset:2048 lds
v_mfma_f32_16x16x32_bf16 v[172:175], a[64:67], a[48:51], v[172:175]
ds_read_b128 a[92:95], v191 offset:3072
s_add_u32 m0, s52, 0
v_mfma_f32_16x16x32_bf16 v[176:179], a[16:19], a[48:51], v[176:179]
buffer_load_dwordx4 v198, s[36:39], s55 offen lds
v_mfma_f32_16x16x32_bf16 v[0:3], a[56:59], a[60:63], v[0:3]
s_waitcnt lgkmcnt(3)
ds_read_b128 a[0:3], v191 offset:4096
v_mfma_f32_16x16x32_bf16 v[4:7], a[52:55], a[60:63], v[4:7]
buffer_load_dwordx4 v198, s[36:39], s55 offen offset:1024 lds
v_mfma_f32_16x16x32_bf16 v[8:11], a[68:71], a[60:63], v[8:11]
s_waitcnt lgkmcnt(3)
ds_read_b128 a[4:7], v191 offset:5120
v_mfma_f32_16x16x32_bf16 v[12:15], a[72:75], a[60:63], v[12:15]
v_mfma_f32_16x16x32_bf16 v[16:19], a[32:35], a[60:63], v[16:19]
v_mfma_f32_16x16x32_bf16 v[20:23], a[56:59], a[80:83], v[20:23]
ds_read_b128 a[84:87], v191 offset:6144
v_mfma_f32_16x16x32_bf16 v[24:27], a[52:55], a[80:83], v[24:27]
buffer_load_dwordx4 v198, s[36:39], s55 offen offset:2048 lds
v_mfma_f32_16x16x32_bf16 v[28:31], a[68:71], a[80:83], v[28:31]
s_waitcnt lgkmcnt(3)
v_mfma_f32_16x16x32_bf16 v[32:35], a[72:75], a[80:83], v[32:35]
v_mfma_f32_16x16x32_bf16 v[36:39], a[32:35], a[80:83], v[36:39]
v_mfma_f32_16x16x32_bf16 v[40:43], a[56:59], a[24:27], v[40:43]
ds_read_b128 a[76:79], v191 offset:7168
v_mfma_f32_16x16x32_bf16 v[44:47], a[52:55], a[24:27], v[44:47]
buffer_load_dwordx4 v198, s[36:39], s55 offen offset:3072 lds
v_mfma_f32_16x16x32_bf16 v[48:51], a[68:71], a[24:27], v[48:51]
v_mfma_f32_16x16x32_bf16 v[52:55], a[72:75], a[24:27], v[52:55]
v_mfma_f32_16x16x32_bf16 v[56:59], a[32:35], a[24:27], v[56:59]
v_mfma_f32_16x16x32_bf16 v[60:63], a[56:59], a[92:95], v[60:63]
ds_read_b128 a[36:39], v191 offset:8192
v_mfma_f32_16x16x32_bf16 v[64:67], a[52:55], a[92:95], v[64:67]
s_and_b32 s60, s68, 0
s_add_u32 m0, s53, s60
v_mfma_f32_16x16x32_bf16 v[68:71], a[68:71], a[92:95], v[68:71]
s_waitcnt lgkmcnt(2)
v_mfma_f32_16x16x32_bf16 v[72:75], a[72:75], a[92:95], v[72:75]
buffer_load_dwordx4 v199, s[36:39], s56 offen lds
v_mfma_f32_16x16x32_bf16 v[76:79], a[32:35], a[92:95], v[76:79]
v_mfma_f32_16x16x32_bf16 v[80:83], a[56:59], a[0:3], v[80:83]
v_mfma_f32_16x16x32_bf16 v[84:87], a[52:55], a[0:3], v[84:87]
v_mfma_f32_16x16x32_bf16 v[88:91], a[68:71], a[0:3], v[88:91]
v_mfma_f32_16x16x32_bf16 v[92:95], a[72:75], a[0:3], v[92:95]
v_mfma_f32_16x16x32_bf16 v[96:99], a[32:35], a[0:3], v[96:99]
v_mfma_f32_16x16x32_bf16 v[100:103], a[56:59], a[4:7], v[100:103]
v_mfma_f32_16x16x32_bf16 v[104:107], a[52:55], a[4:7], v[104:107]
v_mfma_f32_16x16x32_bf16 v[108:111], a[68:71], a[4:7], v[108:111]
v_mfma_f32_16x16x32_bf16 v[112:115], a[72:75], a[4:7], v[112:115]
v_mfma_f32_16x16x32_bf16 v[116:119], a[32:35], a[4:7], v[116:119]
v_mfma_f32_16x16x32_bf16 v[120:123], a[56:59], a[84:87], v[120:123]
s_waitcnt vmcnt(8) lgkmcnt(0)
s_barrier
v_mfma_f32_16x16x32_bf16 v[124:127], a[52:55], a[84:87], v[124:127]
ds_read_b64_tr_b16 a[44:45], v186 offset:30720
v_mfma_f32_16x16x32_bf16 v[128:131], a[68:71], a[84:87], v[128:131]
ds_read_b64_tr_b16 a[46:47], v186 offset:36864
v_mfma_f32_16x16x32_bf16 v[132:135], a[72:75], a[84:87], v[132:135]
ds_read_b128 a[40:43], v191 offset:30720
v_mfma_f32_16x16x32_bf16 v[136:139], a[32:35], a[84:87], v[136:139]
ds_read_b64_tr_b16 a[28:29], v187 offset:30720
ds_read_b64_tr_b16 a[30:31], v187 offset:36864
v_mfma_f32_16x16x32_bf16 v[140:143], a[56:59], a[76:79], v[140:143]
ds_read_b64_tr_b16 a[20:21], v188 offset:30720
ds_read_b64_tr_b16 a[22:23], v188 offset:36864
v_mfma_f32_16x16x32_bf16 v[144:147], a[52:55], a[76:79], v[144:147]
ds_read_b64_tr_b16 a[8:9], v189 offset:30720
ds_read_b64_tr_b16 a[10:11], v189 offset:36864
v_mfma_f32_16x16x32_bf16 v[148:151], a[68:71], a[76:79], v[148:151]
ds_read_b64_tr_b16 a[88:89], v190 offset:30720
ds_read_b64_tr_b16 a[90:91], v190 offset:36864
v_mfma_f32_16x16x32_bf16 v[152:155], a[72:75], a[76:79], v[152:155]
ds_read_b128 a[12:15], v191 offset:31744
s_add_u32 s58, s73, 1
v_mfma_f32_16x16x32_bf16 v[156:159], a[32:35], a[76:79], v[156:159]
s_and_b32 s58, s58, 7
s_add_u32 s58, s58, 40
s_mul_i32 s59, s58, 40960
v_mfma_f32_16x16x32_bf16 v[160:163], a[56:59], a[36:39], v[160:163]
s_add_u32 s57, s76, s59
s_mul_i32 s59, s58, 294912
s_add_u32 s55, s77, s59
v_mfma_f32_16x16x32_bf16 v[164:167], a[52:55], a[36:39], v[164:167]
s_add_u32 s56, s78, s59
s_add_u32 m0, s54, 30720
v_mfma_f32_16x16x32_bf16 v[168:171], a[68:71], a[36:39], v[168:171]
s_waitcnt lgkmcnt(7)
v_mfma_f32_16x16x32_bf16 v[172:175], a[72:75], a[36:39], v[172:175]
buffer_load_dwordx4 v202, s[40:43], s57 offen lds
v_mfma_f32_16x16x32_bf16 v[176:179], a[32:35], a[36:39], v[176:179]
s_waitcnt lgkmcnt(3)
ds_read_b128 a[64:67], v191 offset:32768
v_mfma_f32_16x16x32_bf16 v[0:3], a[44:47], a[40:43], v[0:3]
buffer_load_dwordx4 v203, s[40:43], s57 offen offset:1024 lds
v_mfma_f32_16x16x32_bf16 v[4:7], a[28:31], a[40:43], v[4:7]
s_waitcnt lgkmcnt(1)
ds_read_b128 a[16:19], v191 offset:33792
v_mfma_f32_16x16x32_bf16 v[8:11], a[20:23], a[40:43], v[8:11]
buffer_load_dwordx4 v204, s[40:43], s57 offen offset:2048 lds
v_mfma_f32_16x16x32_bf16 v[12:15], a[8:11], a[40:43], v[12:15]
ds_read_b128 a[48:51], v191 offset:34816
s_add_u32 m0, s52, 30720
v_mfma_f32_16x16x32_bf16 v[16:19], a[88:91], a[40:43], v[16:19]
buffer_load_dwordx4 v198, s[36:39], s55 offen lds
v_mfma_f32_16x16x32_bf16 v[20:23], a[44:47], a[12:15], v[20:23]
ds_read_b128 a[60:63], v191 offset:35840
v_mfma_f32_16x16x32_bf16 v[24:27], a[28:31], a[12:15], v[24:27]
buffer_load_dwordx4 v198, s[36:39], s55 offen offset:1024 lds
v_mfma_f32_16x16x32_bf16 v[28:31], a[20:23], a[12:15], v[28:31]
s_waitcnt lgkmcnt(3)
ds_read_b128 a[80:83], v191 offset:36864
v_mfma_f32_16x16x32_bf16 v[32:35], a[8:11], a[12:15], v[32:35]
v_mfma_f32_16x16x32_bf16 v[36:39], a[88:91], a[12:15], v[36:39]
v_mfma_f32_16x16x32_bf16 v[40:43], a[44:47], a[64:67], v[40:43]
ds_read_b128 a[24:27], v191 offset:37888
v_mfma_f32_16x16x32_bf16 v[44:47], a[28:31], a[64:67], v[44:47]
buffer_load_dwordx4 v198, s[36:39], s55 offen offset:2048 lds
v_mfma_f32_16x16x32_bf16 v[48:51], a[20:23], a[64:67], v[48:51]
s_waitcnt lgkmcnt(3)
v_mfma_f32_16x16x32_bf16 v[52:55], a[8:11], a[64:67], v[52:55]
v_mfma_f32_16x16x32_bf16 v[56:59], a[88:91], a[64:67], v[56:59]
v_mfma_f32_16x16x32_bf16 v[60:63], a[44:47], a[16:19], v[60:63]
ds_read_b128 a[92:95], v191 offset:38912
v_mfma_f32_16x16x32_bf16 v[64:67], a[28:31], a[16:19], v[64:67]
buffer_load_dwordx4 v198, s[36:39], s55 offen offset:3072 lds
v_mfma_f32_16x16x32_bf16 v[68:71], a[20:23], a[16:19], v[68:71]
v_mfma_f32_16x16x32_bf16 v[72:75], a[8:11], a[16:19], v[72:75]
v_mfma_f32_16x16x32_bf16 v[76:79], a[88:91], a[16:19], v[76:79]
v_mfma_f32_16x16x32_bf16 v[80:83], a[44:47], a[48:51], v[80:83]
ds_read_b64_tr_b16 a[0:1], v192 offset:0
v_mfma_f32_16x16x32_bf16 v[84:87], a[28:31], a[48:51], v[84:87]
s_and_b32 s60, s68, 30720
s_add_u32 m0, s53, s60
v_mfma_f32_16x16x32_bf16 v[88:91], a[20:23], a[48:51], v[88:91]
s_waitcnt lgkmcnt(2)
v_mfma_f32_16x16x32_bf16 v[92:95], a[8:11], a[48:51], v[92:95]
buffer_load_dwordx4 v199, s[36:39], s56 offen lds
v_mfma_f32_16x16x32_bf16 v[96:99], a[88:91], a[48:51], v[96:99]
ds_read_b64_tr_b16 a[2:3], v192 offset:6144
v_mfma_f32_16x16x32_bf16 v[100:103], a[44:47], a[60:63], v[100:103]
ds_read_b128 a[4:7], v197 offset:0
v_mfma_f32_16x16x32_bf16 v[104:107], a[28:31], a[60:63], v[104:107]
ds_read_b64_tr_b16 a[84:85], v193 offset:0
v_mfma_f32_16x16x32_bf16 v[108:111], a[20:23], a[60:63], v[108:111]
ds_read_b64_tr_b16 a[86:87], v193 offset:6144
v_mfma_f32_16x16x32_bf16 v[112:115], a[8:11], a[60:63], v[112:115]
ds_read_b64_tr_b16 a[76:77], v194 offset:0
v_mfma_f32_16x16x32_bf16 v[116:119], a[88:91], a[60:63], v[116:119]
ds_read_b64_tr_b16 a[78:79], v194 offset:6144
v_mfma_f32_16x16x32_bf16 v[120:123], a[44:47], a[80:83], v[120:123]
ds_read_b64_tr_b16 a[56:57], v195 offset:0
v_mfma_f32_16x16x32_bf16 v[124:127], a[28:31], a[80:83], v[124:127]
ds_read_b64_tr_b16 a[58:59], v195 offset:6144
v_mfma_f32_16x16x32_bf16 v[128:131], a[20:23], a[80:83], v[128:131]
ds_read_b64_tr_b16 a[52:53], v196 offset:0
v_mfma_f32_16x16x32_bf16 v[132:135], a[8:11], a[80:83], v[132:135]
ds_read_b64_tr_b16 a[54:55], v196 offset:6144
s_add_u32 s58, s73, 2
v_mfma_f32_16x16x32_bf16 v[136:139], a[88:91], a[80:83], v[136:139]
s_and_b32 s58, s58, 7
s_add_u32 s58, s58, 40
s_mul_i32 s59, s58, 40960
v_mfma_f32_16x16x32_bf16 v[140:143], a[44:47], a[24:27], v[140:143]
s_add_u32 s57, s76, s59
s_mul_i32 s59, s58, 294912
s_add_u32 s55, s77, s59
v_mfma_f32_16x16x32_bf16 v[144:147], a[28:31], a[24:27], v[144:147]
s_add_u32 s56, s78, s59
s_add_u32 m0, s54, 61440
v_mfma_f32_16x16x32_bf16 v[148:151], a[20:23], a[24:27], v[148:151]
s_waitcnt lgkmcnt(4)
v_mfma_f32_16x16x32_bf16 v[152:155], a[8:11], a[24:27], v[152:155]
buffer_load_dwordx4 v202, s[40:43], s57 offen lds
v_mfma_f32_16x16x32_bf16 v[156:159], a[88:91], a[24:27], v[156:159]
ds_read_b128 a[68:71], v197 offset:1024
v_mfma_f32_16x16x32_bf16 v[160:163], a[44:47], a[92:95], v[160:163]
buffer_load_dwordx4 v203, s[40:43], s57 offen offset:1024 lds
v_mfma_f32_16x16x32_bf16 v[164:167], a[28:31], a[92:95], v[164:167]
ds_read_b128 a[72:75], v197 offset:2048
v_mfma_f32_16x16x32_bf16 v[168:171], a[20:23], a[92:95], v[168:171]
buffer_load_dwordx4 v204, s[40:43], s57 offen offset:2048 lds
v_mfma_f32_16x16x32_bf16 v[172:175], a[8:11], a[92:95], v[172:175]
ds_read_b128 a[32:35], v197 offset:3072
s_add_u32 m0, s52, 61440
v_mfma_f32_16x16x32_bf16 v[176:179], a[88:91], a[92:95], v[176:179]
buffer_load_dwordx4 v198, s[36:39], s55 offen lds
v_mfma_f32_16x16x32_bf16 v[0:3], a[0:3], a[4:7], v[0:3]
s_waitcnt lgkmcnt(3)
ds_read_b128 a[36:39], v197 offset:4096
v_mfma_f32_16x16x32_bf16 v[4:7], a[84:87], a[4:7], v[4:7]
buffer_load_dwordx4 v198, s[36:39], s55 offen offset:1024 lds
v_mfma_f32_16x16x32_bf16 v[8:11], a[76:79], a[4:7], v[8:11]
s_waitcnt lgkmcnt(3)
ds_read_b128 a[40:43], v197 offset:5120
v_mfma_f32_16x16x32_bf16 v[12:15], a[56:59], a[4:7], v[12:15]
v_mfma_f32_16x16x32_bf16 v[16:19], a[52:55], a[4:7], v[16:19]
v_mfma_f32_16x16x32_bf16 v[20:23], a[0:3], a[68:71], v[20:23]
ds_read_b128 a[12:15], v197 offset:6144
v_mfma_f32_16x16x32_bf16 v[24:27], a[84:87], a[68:71], v[24:27]
buffer_load_dwordx4 v198, s[36:39], s55 offen offset:2048 lds
v_mfma_f32_16x16x32_bf16 v[28:31], a[76:79], a[68:71], v[28:31]
s_waitcnt lgkmcnt(3)
v_mfma_f32_16x16x32_bf16 v[32:35], a[56:59], a[68:71], v[32:35]
v_mfma_f32_16x16x32_bf16 v[36:39], a[52:55], a[68:71], v[36:39]
v_mfma_f32_16x16x32_bf16 v[40:43], a[0:3], a[72:75], v[40:43]
ds_read_b128 a[64:67], v197 offset:7168
v_mfma_f32_16x16x32_bf16 v[44:47], a[84:87], a[72:75], v[44:47]
buffer_load_dwordx4 v198, s[36:39], s55 offen offset:3072 lds
v_mfma_f32_16x16x32_bf16 v[48:51], a[76:79], a[72:75], v[48:51]
v_mfma_f32_16x16x32_bf16 v[52:55], a[56:59], a[72:75], v[52:55]
v_mfma_f32_16x16x32_bf16 v[56:59], a[52:55], a[72:75], v[56:59]
v_mfma_f32_16x16x32_bf16 v[60:63], a[0:3], a[32:35], v[60:63]
ds_read_b128 a[16:19], v197 offset:8192
v_mfma_f32_16x16x32_bf16 v[64:67], a[84:87], a[32:35], v[64:67]
s_and_b32 s60, s68, 61440
s_add_u32 m0, s53, s60
v_mfma_f32_16x16x32_bf16 v[68:71], a[76:79], a[32:35], v[68:71]
s_waitcnt lgkmcnt(2)
v_mfma_f32_16x16x32_bf16 v[72:75], a[56:59], a[32:35], v[72:75]
buffer_load_dwordx4 v199, s[36:39], s56 offen lds
v_mfma_f32_16x16x32_bf16 v[76:79], a[52:55], a[32:35], v[76:79]
v_mfma_f32_16x16x32_bf16 v[80:83], a[0:3], a[36:39], v[80:83]
v_mfma_f32_16x16x32_bf16 v[84:87], a[84:87], a[36:39], v[84:87]
v_mfma_f32_16x16x32_bf16 v[88:91], a[76:79], a[36:39], v[88:91]
v_mfma_f32_16x16x32_bf16 v[92:95], a[56:59], a[36:39], v[92:95]
v_mfma_f32_16x16x32_bf16 v[96:99], a[52:55], a[36:39], v[96:99]
v_mfma_f32_16x16x32_bf16 v[100:103], a[0:3], a[40:43], v[100:103]
v_mfma_f32_16x16x32_bf16 v[104:107], a[84:87], a[40:43], v[104:107]
v_mfma_f32_16x16x32_bf16 v[108:111], a[76:79], a[40:43], v[108:111]
v_mfma_f32_16x16x32_bf16 v[112:115], a[56:59], a[40:43], v[112:115]
v_mfma_f32_16x16x32_bf16 v[116:119], a[52:55], a[40:43], v[116:119]
v_mfma_f32_16x16x32_bf16 v[120:123], a[0:3], a[12:15], v[120:123]
s_waitcnt vmcnt(8) lgkmcnt(0)
s_barrier
v_mfma_f32_16x16x32_bf16 v[124:127], a[84:87], a[12:15], v[124:127]
ds_read_b64_tr_b16 a[48:49], v180 offset:0
v_mfma_f32_16x16x32_bf16 v[128:131], a[76:79], a[12:15], v[128:131]
ds_read_b64_tr_b16 a[50:51], v180 offset:6144
v_mfma_f32_16x16x32_bf16 v[132:135], a[56:59], a[12:15], v[132:135]
ds_read_b128 a[60:63], v185 offset:0
v_mfma_f32_16x16x32_bf16 v[136:139], a[52:55], a[12:15], v[136:139]
ds_read_b64_tr_b16 a[80:81], v181 offset:0
ds_read_b64_tr_b16 a[82:83], v181 offset:6144
v_mfma_f32_16x16x32_bf16 v[140:143], a[0:3], a[64:67], v[140:143]
ds_read_b64_tr_b16 a[24:25], v182 offset:0
ds_read_b64_tr_b16 a[26:27], v182 offset:6144
v_mfma_f32_16x16x32_bf16 v[144:147], a[84:87], a[64:67], v[144:147]
ds_read_b64_tr_b16 a[44:45], v183 offset:0
ds_read_b64_tr_b16 a[46:47], v183 offset:6144
v_mfma_f32_16x16x32_bf16 v[148:151], a[76:79], a[64:67], v[148:151]
ds_read_b64_tr_b16 a[28:29], v184 offset:0
ds_read_b64_tr_b16 a[30:31], v184 offset:6144
v_mfma_f32_16x16x32_bf16 v[152:155], a[56:59], a[64:67], v[152:155]
ds_read_b128 a[20:23], v185 offset:1024
s_add_u32 s58, s73, 3
v_mfma_f32_16x16x32_bf16 v[156:159], a[52:55], a[64:67], v[156:159]
s_and_b32 s58, s58, 7
s_add_u32 s58, s58, 40
s_mul_i32 s59, s58, 40960
v_mfma_f32_16x16x32_bf16 v[160:163], a[0:3], a[16:19], v[160:163]
s_add_u32 s57, s76, s59
s_mul_i32 s59, s58, 294912
s_add_u32 s55, s77, s59
v_mfma_f32_16x16x32_bf16 v[164:167], a[84:87], a[16:19], v[164:167]
s_add_u32 s56, s78, s59
s_add_u32 m0, s54, 92160
v_mfma_f32_16x16x32_bf16 v[168:171], a[76:79], a[16:19], v[168:171]
s_waitcnt lgkmcnt(7)
v_mfma_f32_16x16x32_bf16 v[172:175], a[56:59], a[16:19], v[172:175]
buffer_load_dwordx4 v202, s[40:43], s57 offen lds
v_mfma_f32_16x16x32_bf16 v[176:179], a[52:55], a[16:19], v[176:179]
s_waitcnt lgkmcnt(3)
ds_read_b128 a[8:11], v185 offset:2048
v_mfma_f32_16x16x32_bf16 v[0:3], a[48:51], a[60:63], v[0:3]
buffer_load_dwordx4 v203, s[40:43], s57 offen offset:1024 lds
v_mfma_f32_16x16x32_bf16 v[4:7], a[80:83], a[60:63], v[4:7]
s_waitcnt lgkmcnt(1)
ds_read_b128 a[88:91], v185 offset:3072
v_mfma_f32_16x16x32_bf16 v[8:11], a[24:27], a[60:63], v[8:11]
buffer_load_dwordx4 v204, s[40:43], s57 offen offset:2048 lds
v_mfma_f32_16x16x32_bf16 v[12:15], a[44:47], a[60:63], v[12:15]
ds_read_b128 a[92:95], v185 offset:4096
s_add_u32 m0, s52, 92160
v_mfma_f32_16x16x32_bf16 v[16:19], a[28:31], a[60:63], v[16:19]
buffer_load_dwordx4 v198, s[36:39], s55 offen lds
v_mfma_f32_16x16x32_bf16 v[20:23], a[48:51], a[20:23], v[20:23]
ds_read_b128 a[4:7], v185 offset:5120
v_mfma_f32_16x16x32_bf16 v[24:27], a[80:83], a[20:23], v[24:27]
buffer_load_dwordx4 v198, s[36:39], s55 offen offset:1024 lds
v_mfma_f32_16x16x32_bf16 v[28:31], a[24:27], a[20:23], v[28:31]
s_waitcnt lgkmcnt(3)
ds_read_b128 a[68:71], v185 offset:6144
v_mfma_f32_16x16x32_bf16 v[32:35], a[44:47], a[20:23], v[32:35]
v_mfma_f32_16x16x32_bf16 v[36:39], a[28:31], a[20:23], v[36:39]
v_mfma_f32_16x16x32_bf16 v[40:43], a[48:51], a[8:11], v[40:43]
ds_read_b128 a[72:75], v185 offset:7168
v_mfma_f32_16x16x32_bf16 v[44:47], a[80:83], a[8:11], v[44:47]
buffer_load_dwordx4 v198, s[36:39], s55 offen offset:2048 lds
v_mfma_f32_16x16x32_bf16 v[48:51], a[24:27], a[8:11], v[48:51]
s_waitcnt lgkmcnt(3)
v_mfma_f32_16x16x32_bf16 v[52:55], a[44:47], a[8:11], v[52:55]
v_mfma_f32_16x16x32_bf16 v[56:59], a[28:31], a[8:11], v[56:59]
v_mfma_f32_16x16x32_bf16 v[60:63], a[48:51], a[88:91], v[60:63]
ds_read_b128 a[32:35], v185 offset:8192
v_mfma_f32_16x16x32_bf16 v[64:67], a[80:83], a[88:91], v[64:67]
buffer_load_dwordx4 v198, s[36:39], s55 offen offset:3072 lds
v_mfma_f32_16x16x32_bf16 v[68:71], a[24:27], a[88:91], v[68:71]
v_mfma_f32_16x16x32_bf16 v[72:75], a[44:47], a[88:91], v[72:75]
v_mfma_f32_16x16x32_bf16 v[76:79], a[28:31], a[88:91], v[76:79]
v_mfma_f32_16x16x32_bf16 v[80:83], a[48:51], a[92:95], v[80:83]
ds_read_b64_tr_b16 a[36:37], v180 offset:30720
v_mfma_f32_16x16x32_bf16 v[84:87], a[80:83], a[92:95], v[84:87]
s_and_b32 s60, s68, 92160
s_add_u32 m0, s53, s60
v_mfma_f32_16x16x32_bf16 v[88:91], a[24:27], a[92:95], v[88:91]
s_waitcnt lgkmcnt(2)
v_mfma_f32_16x16x32_bf16 v[92:95], a[44:47], a[92:95], v[92:95]
buffer_load_dwordx4 v199, s[36:39], s56 offen lds
v_mfma_f32_16x16x32_bf16 v[96:99], a[28:31], a[92:95], v[96:99]
ds_read_b64_tr_b16 a[38:39], v180 offset:36864
v_mfma_f32_16x16x32_bf16 v[100:103], a[48:51], a[4:7], v[100:103]
ds_read_b128 a[40:43], v185 offset:30720
v_mfma_f32_16x16x32_bf16 v[104:107], a[80:83], a[4:7], v[104:107]
ds_read_b64_tr_b16 a[12:13], v181 offset:30720
v_mfma_f32_16x16x32_bf16 v[108:111], a[24:27], a[4:7], v[108:111]
ds_read_b64_tr_b16 a[14:15], v181 offset:36864
v_mfma_f32_16x16x32_bf16 v[112:115], a[44:47], a[4:7], v[112:115]
ds_read_b64_tr_b16 a[64:65], v182 offset:30720
v_mfma_f32_16x16x32_bf16 v[116:119], a[28:31], a[4:7], v[116:119]
ds_read_b64_tr_b16 a[66:67], v182 offset:36864
v_mfma_f32_16x16x32_bf16 v[120:123], a[48:51], a[68:71], v[120:123]
ds_read_b64_tr_b16 a[0:1], v183 offset:30720
v_mfma_f32_16x16x32_bf16 v[124:127], a[80:83], a[68:71], v[124:127]
ds_read_b64_tr_b16 a[2:3], v183 offset:36864
v_mfma_f32_16x16x32_bf16 v[128:131], a[24:27], a[68:71], v[128:131]
ds_read_b64_tr_b16 a[84:85], v184 offset:30720
v_mfma_f32_16x16x32_bf16 v[132:135], a[44:47], a[68:71], v[132:135]
ds_read_b64_tr_b16 a[86:87], v184 offset:36864
s_add_u32 s58, s73, 4
v_mfma_f32_16x16x32_bf16 v[136:139], a[28:31], a[68:71], v[136:139]
s_and_b32 s58, s58, 7
s_add_u32 s58, s58, 40
s_mul_i32 s59, s58, 40960
v_mfma_f32_16x16x32_bf16 v[140:143], a[48:51], a[72:75], v[140:143]
s_add_u32 s57, s76, s59
s_mul_i32 s59, s58, 294912
s_add_u32 s55, s77, s59
v_mfma_f32_16x16x32_bf16 v[144:147], a[80:83], a[72:75], v[144:147]
s_add_u32 s56, s78, s59
s_add_u32 m0, s54, 122880
v_mfma_f32_16x16x32_bf16 v[148:151], a[24:27], a[72:75], v[148:151]
s_waitcnt lgkmcnt(4)
v_mfma_f32_16x16x32_bf16 v[152:155], a[44:47], a[72:75], v[152:155]
buffer_load_dwordx4 v202, s[40:43], s57 offen lds
v_mfma_f32_16x16x32_bf16 v[156:159], a[28:31], a[72:75], v[156:159]
ds_read_b128 a[76:79], v185 offset:31744
v_mfma_f32_16x16x32_bf16 v[160:163], a[48:51], a[32:35], v[160:163]
buffer_load_dwordx4 v203, s[40:43], s57 offen offset:1024 lds
v_mfma_f32_16x16x32_bf16 v[164:167], a[80:83], a[32:35], v[164:167]
ds_read_b128 a[56:59], v185 offset:32768
v_mfma_f32_16x16x32_bf16 v[168:171], a[24:27], a[32:35], v[168:171]
buffer_load_dwordx4 v204, s[40:43], s57 offen offset:2048 lds
v_mfma_f32_16x16x32_bf16 v[172:175], a[44:47], a[32:35], v[172:175]
ds_read_b128 a[16:19], v185 offset:33792
s_add_u32 m0, s52, 122880
v_mfma_f32_16x16x32_bf16 v[176:179], a[28:31], a[32:35], v[176:179]
buffer_load_dwordx4 v198, s[36:39], s55 offen lds
v_mfma_f32_16x16x32_bf16 v[0:3], a[36:39], a[40:43], v[0:3]
s_waitcnt lgkmcnt(3)
ds_read_b128 a[52:55], v185 offset:34816
v_mfma_f32_16x16x32_bf16 v[4:7], a[12:15], a[40:43], v[4:7]
buffer_load_dwordx4 v198, s[36:39], s55 offen offset:1024 lds
v_mfma_f32_16x16x32_bf16 v[8:11], a[64:67], a[40:43], v[8:11]
s_waitcnt lgkmcnt(3)
ds_read_b128 a[60:63], v185 offset:35840
v_mfma_f32_16x16x32_bf16 v[12:15], a[0:3], a[40:43], v[12:15]
v_mfma_f32_16x16x32_bf16 v[16:19], a[84:87], a[40:43], v[16:19]
v_mfma_f32_16x16x32_bf16 v[20:23], a[36:39], a[76:79], v[20:23]
ds_read_b128 a[20:23], v185 offset:36864
v_mfma_f32_16x16x32_bf16 v[24:27], a[12:15], a[76:79], v[24:27]
buffer_load_dwordx4 v198, s[36:39], s55 offen offset:2048 lds
v_mfma_f32_16x16x32_bf16 v[28:31], a[64:67], a[76:79], v[28:31]
s_waitcnt lgkmcnt(3)
v_mfma_f32_16x16x32_bf16 v[32:35], a[0:3], a[76:79], v[32:35]
v_mfma_f32_16x16x32_bf16 v[36:39], a[84:87], a[76:79], v[36:39]
v_mfma_f32_16x16x32_bf16 v[40:43], a[36:39], a[56:59], v[40:43]
ds_read_b128 a[8:11], v185 offset:37888
v_mfma_f32_16x16x32_bf16 v[44:47], a[12:15], a[56:59], v[44:47]
buffer_load_dwordx4 v198, s[36:39], s55 offen offset:3072 lds
v_mfma_f32_16x16x32_bf16 v[48:51], a[64:67], a[56:59], v[48:51]
v_mfma_f32_16x16x32_bf16 v[52:55], a[0:3], a[56:59], v[52:55]
v_mfma_f32_16x16x32_bf16 v[56:59], a[84:87], a[56:59], v[56:59]
v_mfma_f32_16x16x32_bf16 v[60:63], a[36:39], a[16:19], v[60:63]
ds_read_b128 a[88:91], v185 offset:38912
v_mfma_f32_16x16x32_bf16 v[64:67], a[12:15], a[16:19], v[64:67]
s_and_b32 s60, s68, 122880
s_add_u32 m0, s53, s60
v_mfma_f32_16x16x32_bf16 v[68:71], a[64:67], a[16:19], v[68:71]
s_waitcnt lgkmcnt(2)
v_mfma_f32_16x16x32_bf16 v[72:75], a[0:3], a[16:19], v[72:75]
buffer_load_dwordx4 v199, s[36:39], s56 offen lds
v_mfma_f32_16x16x32_bf16 v[76:79], a[84:87], a[16:19], v[76:79]
v_mfma_f32_16x16x32_bf16 v[80:83], a[36:39], a[52:55], v[80:83]
v_mfma_f32_16x16x32_bf16 v[84:87], a[12:15], a[52:55], v[84:87]
v_mfma_f32_16x16x32_bf16 v[88:91], a[64:67], a[52:55], v[88:91]
v_mfma_f32_16x16x32_bf16 v[92:95], a[0:3], a[52:55], v[92:95]
v_mfma_f32_16x16x32_bf16 v[96:99], a[84:87], a[52:55], v[96:99]
v_mfma_f32_16x16x32_bf16 v[100:103], a[36:39], a[60:63], v[100:103]
v_mfma_f32_16x16x32_bf16 v[104:107], a[12:15], a[60:63], v[104:107]
v_mfma_f32_16x16x32_bf16 v[108:111], a[64:67], a[60:63], v[108:111]
v_mfma_f32_16x16x32_bf16 v[112:115], a[0:3], a[60:63], v[112:115]
v_mfma_f32_16x16x32_bf16 v[116:119], a[84:87], a[60:63], v[116:119]
v_mfma_f32_16x16x32_bf16 v[120:123], a[36:39], a[20:23], v[120:123]
s_waitcnt vmcnt(8) lgkmcnt(0)
s_barrier
v_mfma_f32_16x16x32_bf16 v[124:127], a[12:15], a[20:23], v[124:127]
ds_read_b64_tr_b16 a[92:93], v186 offset:0
v_mfma_f32_16x16x32_bf16 v[128:131], a[64:67], a[20:23], v[128:131]
ds_read_b64_tr_b16 a[94:95], v186 offset:6144
v_mfma_f32_16x16x32_bf16 v[132:135], a[0:3], a[20:23], v[132:135]
ds_read_b128 a[4:7], v191 offset:0
v_mfma_f32_16x16x32_bf16 v[136:139], a[84:87], a[20:23], v[136:139]
ds_read_b64_tr_b16 a[68:69], v187 offset:0
ds_read_b64_tr_b16 a[70:71], v187 offset:6144
v_mfma_f32_16x16x32_bf16 v[140:143], a[36:39], a[8:11], v[140:143]
ds_read_b64_tr_b16 a[72:73], v188 offset:0
ds_read_b64_tr_b16 a[74:75], v188 offset:6144
v_mfma_f32_16x16x32_bf16 v[144:147], a[12:15], a[8:11], v[144:147]
ds_read_b64_tr_b16 a[48:49], v189 offset:0
ds_read_b64_tr_b16 a[50:51], v189 offset:6144
v_mfma_f32_16x16x32_bf16 v[148:151], a[64:67], a[8:11], v[148:151]
ds_read_b64_tr_b16 a[80:81], v190 offset:0
ds_read_b64_tr_b16 a[82:83], v190 offset:6144
v_mfma_f32_16x16x32_bf16 v[152:155], a[0:3], a[8:11], v[152:155]
ds_read_b128 a[24:27], v191 offset:1024
s_add_u32 s58, s73, 5
v_mfma_f32_16x16x32_bf16 v[156:159], a[84:87], a[8:11], v[156:159]
s_and_b32 s58, s58, 7
s_add_u32 s58, s58, 40
s_mul_i32 s59, s58, 40960
v_mfma_f32_16x16x32_bf16 v[160:163], a[36:39], a[88:91], v[160:163]
s_add_u32 s57, s76, s59
s_mul_i32 s59, s58, 294912
s_add_u32 s55, s77, s59
v_mfma_f32_16x16x32_bf16 v[164:167], a[12:15], a[88:91], v[164:167]
s_add_u32 s56, s78, s59
s_add_u32 m0, s54, 0
v_mfma_f32_16x16x32_bf16 v[168:171], a[64:67], a[88:91], v[168:171]
s_waitcnt lgkmcnt(7)
v_mfma_f32_16x16x32_bf16 v[172:175], a[0:3], a[88:91], v[172:175]
buffer_load_dwordx4 v202, s[40:43], s57 offen lds
v_mfma_f32_16x16x32_bf16 v[176:179], a[84:87], a[88:91], v[176:179]
s_waitcnt lgkmcnt(3)
ds_read_b128 a[44:47], v191 offset:2048
v_mfma_f32_16x16x32_bf16 v[0:3], a[92:95], a[4:7], v[0:3]
buffer_load_dwordx4 v203, s[40:43], s57 offen offset:1024 lds
v_mfma_f32_16x16x32_bf16 v[4:7], a[68:71], a[4:7], v[4:7]
s_waitcnt lgkmcnt(1)
ds_read_b128 a[28:31], v191 offset:3072
v_mfma_f32_16x16x32_bf16 v[8:11], a[72:75], a[4:7], v[8:11]
buffer_load_dwordx4 v204, s[40:43], s57 offen offset:2048 lds
v_mfma_f32_16x16x32_bf16 v[12:15], a[48:51], a[4:7], v[12:15]
ds_read_b128 a[32:35], v191 offset:4096
s_add_u32 m0, s52, 0
v_mfma_f32_16x16x32_bf16 v[16:19], a[80:83], a[4:7], v[16:19]
buffer_load_dwordx4 v198, s[36:39], s55 offen lds
v_mfma_f32_16x16x32_bf16 v[20:23], a[92:95], a[24:27], v[20:23]
ds_read_b128 a[40:43], v191 offset:5120
v_mfma_f32_16x16x32_bf16 v[24:27], a[68:71], a[24:27], v[24:27]
buffer_load_dwordx4 v198, s[36:39], s55 offen offset:1024 lds
v_mfma_f32_16x16x32_bf16 v[28:31], a[72:75], a[24:27], v[28:31]
s_waitcnt lgkmcnt(3)
ds_read_b128 a[76:79], v191 offset:6144
v_mfma_f32_16x16x32_bf16 v[32:35], a[48:51], a[24:27], v[32:35]
v_mfma_f32_16x16x32_bf16 v[36:39], a[80:83], a[24:27], v[36:39]
v_mfma_f32_16x16x32_bf16 v[40:43], a[92:95], a[44:47], v[40:43]
ds_read_b128 a[56:59], v191 offset:7168
v_mfma_f32_16x16x32_bf16 v[44:47], a[68:71], a[44:47], v[44:47]
buffer_load_dwordx4 v198, s[36:39], s55 offen offset:2048 lds
v_mfma_f32_16x16x32_bf16 v[48:51], a[72:75], a[44:47], v[48:51]
s_waitcnt lgkmcnt(3)
v_mfma_f32_16x16x32_bf16 v[52:55], a[48:51], a[44:47], v[52:55]
v_mfma_f32_16x16x32_bf16 v[56:59], a[80:83], a[44:47], v[56:59]
v_mfma_f32_16x16x32_bf16 v[60:63], a[92:95], a[28:31], v[60:63]
ds_read_b128 a[16:19], v191 offset:8192
v_mfma_f32_16x16x32_bf16 v[64:67], a[68:71], a[28:31], v[64:67]
buffer_load_dwordx4 v198, s[36:39], s55 offen offset:3072 lds
v_mfma_f32_16x16x32_bf16 v[68:71], a[72:75], a[28:31], v[68:71]
v_mfma_f32_16x16x32_bf16 v[72:75], a[48:51], a[28:31], v[72:75]
v_mfma_f32_16x16x32_bf16 v[76:79], a[80:83], a[28:31], v[76:79]
v_mfma_f32_16x16x32_bf16 v[80:83], a[92:95], a[32:35], v[80:83]
ds_read_b64_tr_b16 a[52:53], v186 offset:30720
v_mfma_f32_16x16x32_bf16 v[84:87], a[68:71], a[32:35], v[84:87]
s_and_b32 s60, s68, 0
s_add_u32 m0, s53, s60
v_mfma_f32_16x16x32_bf16 v[88:91], a[72:75], a[32:35], v[88:91]
s_waitcnt lgkmcnt(2)
v_mfma_f32_16x16x32_bf16 v[92:95], a[48:51], a[32:35], v[92:95]
buffer_load_dwordx4 v199, s[36:39], s56 offen lds
v_mfma_f32_16x16x32_bf16 v[96:99], a[80:83], a[32:35], v[96:99]
ds_read_b64_tr_b16 a[54:55], v186 offset:36864
v_mfma_f32_16x16x32_bf16 v[100:103], a[92:95], a[40:43], v[100:103]
ds_read_b128 a[60:63], v191 offset:30720
v_mfma_f32_16x16x32_bf16 v[104:107], a[68:71], a[40:43], v[104:107]
ds_read_b64_tr_b16 a[20:21], v187 offset:30720
v_mfma_f32_16x16x32_bf16 v[108:111], a[72:75], a[40:43], v[108:111]
ds_read_b64_tr_b16 a[22:23], v187 offset:36864
v_mfma_f32_16x16x32_bf16 v[112:115], a[48:51], a[40:43], v[112:115]
ds_read_b64_tr_b16 a[8:9], v188 offset:30720
v_mfma_f32_16x16x32_bf16 v[116:119], a[80:83], a[40:43], v[116:119]
ds_read_b64_tr_b16 a[10:11], v188 offset:36864
v_mfma_f32_16x16x32_bf16 v[120:123], a[92:95], a[76:79], v[120:123]
ds_read_b64_tr_b16 a[36:37], v189 offset:30720
v_mfma_f32_16x16x32_bf16 v[124:127], a[68:71], a[76:79], v[124:127]
ds_read_b64_tr_b16 a[38:39], v189 offset:36864
v_mfma_f32_16x16x32_bf16 v[128:131], a[72:75], a[76:79], v[128:131]
ds_read_b64_tr_b16 a[12:13], v190 offset:30720
v_mfma_f32_16x16x32_bf16 v[132:135], a[48:51], a[76:79], v[132:135]
ds_read_b64_tr_b16 a[14:15], v190 offset:36864
s_add_u32 s58, s73, 6
v_mfma_f32_16x16x32_bf16 v[136:139], a[80:83], a[76:79], v[136:139]
s_and_b32 s58, s58, 7
s_add_u32 s58, s58, 40
s_mul_i32 s59, s58, 40960
v_mfma_f32_16x16x32_bf16 v[140:143], a[92:95], a[56:59], v[140:143]
s_add_u32 s57, s76, s59
s_mul_i32 s59, s58, 294912
s_add_u32 s55, s77, s59
v_mfma_f32_16x16x32_bf16 v[144:147], a[68:71], a[56:59], v[144:147]
s_add_u32 s56, s78, s59
s_add_u32 m0, s54, 30720
v_mfma_f32_16x16x32_bf16 v[148:151], a[72:75], a[56:59], v[148:151]
s_waitcnt lgkmcnt(4)
v_mfma_f32_16x16x32_bf16 v[152:155], a[48:51], a[56:59], v[152:155]
buffer_load_dwordx4 v202, s[40:43], s57 offen lds
v_mfma_f32_16x16x32_bf16 v[156:159], a[80:83], a[56:59], v[156:159]
ds_read_b128 a[64:67], v191 offset:31744
v_mfma_f32_16x16x32_bf16 v[160:163], a[92:95], a[16:19], v[160:163]
buffer_load_dwordx4 v203, s[40:43], s57 offen offset:1024 lds
v_mfma_f32_16x16x32_bf16 v[164:167], a[68:71], a[16:19], v[164:167]
ds_read_b128 a[0:3], v191 offset:32768
v_mfma_f32_16x16x32_bf16 v[168:171], a[72:75], a[16:19], v[168:171]
buffer_load_dwordx4 v204, s[40:43], s57 offen offset:2048 lds
v_mfma_f32_16x16x32_bf16 v[172:175], a[48:51], a[16:19], v[172:175]
ds_read_b128 a[84:87], v191 offset:33792
s_add_u32 m0, s52, 30720
v_mfma_f32_16x16x32_bf16 v[176:179], a[80:83], a[16:19], v[176:179]
buffer_load_dwordx4 v198, s[36:39], s55 offen lds
v_mfma_f32_16x16x32_bf16 v[0:3], a[52:55], a[60:63], v[0:3]
s_waitcnt lgkmcnt(3)
ds_read_b128 a[88:91], v191 offset:34816
v_mfma_f32_16x16x32_bf16 v[4:7], a[20:23], a[60:63], v[4:7]
buffer_load_dwordx4 v198, s[36:39], s55 offen offset:1024 lds
v_mfma_f32_16x16x32_bf16 v[8:11], a[8:11], a[60:63], v[8:11]
s_waitcnt lgkmcnt(3)
ds_read_b128 a[4:7], v191 offset:35840
v_mfma_f32_16x16x32_bf16 v[12:15], a[36:39], a[60:63], v[12:15]
v_mfma_f32_16x16x32_bf16 v[16:19], a[12:15], a[60:63], v[16:19]
v_mfma_f32_16x16x32_bf16 v[20:23], a[52:55], a[64:67], v[20:23]
ds_read_b128 a[24:27], v191 offset:36864
v_mfma_f32_16x16x32_bf16 v[24:27], a[20:23], a[64:67], v[24:27]
buffer_load_dwordx4 v198, s[36:39], s55 offen offset:2048 lds
v_mfma_f32_16x16x32_bf16 v[28:31], a[8:11], a[64:67], v[28:31]
s_waitcnt lgkmcnt(3)
v_mfma_f32_16x16x32_bf16 v[32:35], a[36:39], a[64:67], v[32:35]
v_mfma_f32_16x16x32_bf16 v[36:39], a[12:15], a[64:67], v[36:39]
v_mfma_f32_16x16x32_bf16 v[40:43], a[52:55], a[0:3], v[40:43]
ds_read_b128 a[44:47], v191 offset:37888
v_mfma_f32_16x16x32_bf16 v[44:47], a[20:23], a[0:3], v[44:47]
buffer_load_dwordx4 v198, s[36:39], s55 offen offset:3072 lds
v_mfma_f32_16x16x32_bf16 v[48:51], a[8:11], a[0:3], v[48:51]
v_mfma_f32_16x16x32_bf16 v[52:55], a[36:39], a[0:3], v[52:55]
v_mfma_f32_16x16x32_bf16 v[56:59], a[12:15], a[0:3], v[56:59]
v_mfma_f32_16x16x32_bf16 v[60:63], a[52:55], a[84:87], v[60:63]
ds_read_b128 a[28:31], v191 offset:38912
v_mfma_f32_16x16x32_bf16 v[64:67], a[20:23], a[84:87], v[64:67]
s_and_b32 s60, s68, 30720
s_add_u32 m0, s53, s60
v_mfma_f32_16x16x32_bf16 v[68:71], a[8:11], a[84:87], v[68:71]
s_waitcnt lgkmcnt(2)
v_mfma_f32_16x16x32_bf16 v[72:75], a[36:39], a[84:87], v[72:75]
buffer_load_dwordx4 v199, s[36:39], s56 offen lds
v_mfma_f32_16x16x32_bf16 v[76:79], a[12:15], a[84:87], v[76:79]
v_mfma_f32_16x16x32_bf16 v[80:83], a[52:55], a[88:91], v[80:83]
v_mfma_f32_16x16x32_bf16 v[84:87], a[20:23], a[88:91], v[84:87]
v_mfma_f32_16x16x32_bf16 v[88:91], a[8:11], a[88:91], v[88:91]
v_mfma_f32_16x16x32_bf16 v[92:95], a[36:39], a[88:91], v[92:95]
v_mfma_f32_16x16x32_bf16 v[96:99], a[12:15], a[88:91], v[96:99]
v_mfma_f32_16x16x32_bf16 v[100:103], a[52:55], a[4:7], v[100:103]
v_mfma_f32_16x16x32_bf16 v[104:107], a[20:23], a[4:7], v[104:107]
v_mfma_f32_16x16x32_bf16 v[108:111], a[8:11], a[4:7], v[108:111]
v_mfma_f32_16x16x32_bf16 v[112:115], a[36:39], a[4:7], v[112:115]
v_mfma_f32_16x16x32_bf16 v[116:119], a[12:15], a[4:7], v[116:119]
v_mfma_f32_16x16x32_bf16 v[120:123], a[52:55], a[24:27], v[120:123]
s_waitcnt vmcnt(8) lgkmcnt(0)
s_barrier
v_mfma_f32_16x16x32_bf16 v[124:127], a[20:23], a[24:27], v[124:127]
ds_read_b64_tr_b16 a[32:33], v192 offset:0
v_mfma_f32_16x16x32_bf16 v[128:131], a[8:11], a[24:27], v[128:131]
ds_read_b64_tr_b16 a[34:35], v192 offset:6144
v_mfma_f32_16x16x32_bf16 v[132:135], a[36:39], a[24:27], v[132:135]
ds_read_b128 a[40:43], v197 offset:0
v_mfma_f32_16x16x32_bf16 v[136:139], a[12:15], a[24:27], v[136:139]
ds_read_b64_tr_b16 a[76:77], v193 offset:0
ds_read_b64_tr_b16 a[78:79], v193 offset:6144
v_mfma_f32_16x16x32_bf16 v[140:143], a[52:55], a[44:47], v[140:143]
ds_read_b64_tr_b16 a[56:57], v194 offset:0
ds_read_b64_tr_b16 a[58:59], v194 offset:6144
v_mfma_f32_16x16x32_bf16 v[144:147], a[20:23], a[44:47], v[144:147]
ds_read_b64_tr_b16 a[92:93], v195 offset:0
ds_read_b64_tr_b16 a[94:95], v195 offset:6144
v_mfma_f32_16x16x32_bf16 v[148:151], a[8:11], a[44:47], v[148:151]
ds_read_b64_tr_b16 a[68:69], v196 offset:0
ds_read_b64_tr_b16 a[70:71], v196 offset:6144
v_mfma_f32_16x16x32_bf16 v[152:155], a[36:39], a[44:47], v[152:155]
ds_read_b128 a[72:75], v197 offset:1024
s_add_u32 s58, s73, 7
v_mfma_f32_16x16x32_bf16 v[156:159], a[12:15], a[44:47], v[156:159]
s_and_b32 s58, s58, 7
s_add_u32 s58, s58, 40
s_mul_i32 s59, s58, 40960
v_mfma_f32_16x16x32_bf16 v[160:163], a[52:55], a[28:31], v[160:163]
s_add_u32 s57, s76, s59
s_mul_i32 s59, s58, 294912
s_add_u32 s55, s77, s59
v_mfma_f32_16x16x32_bf16 v[164:167], a[20:23], a[28:31], v[164:167]
s_add_u32 s56, s78, s59
s_add_u32 m0, s54, 61440
v_mfma_f32_16x16x32_bf16 v[168:171], a[8:11], a[28:31], v[168:171]
s_waitcnt lgkmcnt(7)
v_mfma_f32_16x16x32_bf16 v[172:175], a[36:39], a[28:31], v[172:175]
buffer_load_dwordx4 v202, s[40:43], s57 offen lds
v_mfma_f32_16x16x32_bf16 v[176:179], a[12:15], a[28:31], v[176:179]
s_waitcnt lgkmcnt(3)
ds_read_b128 a[48:51], v197 offset:2048
v_mfma_f32_16x16x32_bf16 v[0:3], a[32:35], a[40:43], v[0:3]
buffer_load_dwordx4 v203, s[40:43], s57 offen offset:1024 lds
v_mfma_f32_16x16x32_bf16 v[4:7], a[76:79], a[40:43], v[4:7]
s_waitcnt lgkmcnt(1)
ds_read_b128 a[80:83], v197 offset:3072
v_mfma_f32_16x16x32_bf16 v[8:11], a[56:59], a[40:43], v[8:11]
buffer_load_dwordx4 v204, s[40:43], s57 offen offset:2048 lds
v_mfma_f32_16x16x32_bf16 v[12:15], a[92:95], a[40:43], v[12:15]
ds_read_b128 a[16:19], v197 offset:4096
s_add_u32 m0, s52, 61440
v_mfma_f32_16x16x32_bf16 v[16:19], a[68:71], a[40:43], v[16:19]
buffer_load_dwordx4 v198, s[36:39], s55 offen lds
v_mfma_f32_16x16x32_bf16 v[20:23], a[32:35], a[72:75], v[20:23]
ds_read_b128 a[60:63], v197 offset:5120
v_mfma_f32_16x16x32_bf16 v[24:27], a[76:79], a[72:75], v[24:27]
buffer_load_dwordx4 v198, s[36:39], s55 offen offset:1024 lds
v_mfma_f32_16x16x32_bf16 v[28:31], a[56:59], a[72:75], v[28:31]
s_waitcnt lgkmcnt(3)
ds_read_b128 a[64:67], v197 offset:6144
v_mfma_f32_16x16x32_bf16 v[32:35], a[92:95], a[72:75], v[32:35]
v_mfma_f32_16x16x32_bf16 v[36:39], a[68:71], a[72:75], v[36:39]
v_mfma_f32_16x16x32_bf16 v[40:43], a[32:35], a[48:51], v[40:43]
ds_read_b128 a[0:3], v197 offset:7168
v_mfma_f32_16x16x32_bf16 v[44:47], a[76:79], a[48:51], v[44:47]
buffer_load_dwordx4 v198, s[36:39], s55 offen offset:2048 lds
v_mfma_f32_16x16x32_bf16 v[48:51], a[56:59], a[48:51], v[48:51]
s_waitcnt lgkmcnt(3)
v_mfma_f32_16x16x32_bf16 v[52:55], a[92:95], a[48:51], v[52:55]
v_mfma_f32_16x16x32_bf16 v[56:59], a[68:71], a[48:51], v[56:59]
v_mfma_f32_16x16x32_bf16 v[60:63], a[32:35], a[80:83], v[60:63]
ds_read_b128 a[84:87], v197 offset:8192
v_mfma_f32_16x16x32_bf16 v[64:67], a[76:79], a[80:83], v[64:67]
buffer_load_dwordx4 v198, s[36:39], s55 offen offset:3072 lds
v_mfma_f32_16x16x32_bf16 v[68:71], a[56:59], a[80:83], v[68:71]
v_mfma_f32_16x16x32_bf16 v[72:75], a[92:95], a[80:83], v[72:75]
v_mfma_f32_16x16x32_bf16 v[76:79], a[68:71], a[80:83], v[76:79]
v_mfma_f32_16x16x32_bf16 v[80:83], a[32:35], a[16:19], v[80:83]
ds_read_b64_tr_b16 a[88:89], v180 offset:0
v_mfma_f32_16x16x32_bf16 v[84:87], a[76:79], a[16:19], v[84:87]
s_and_b32 s60, s68, 61440
s_add_u32 m0, s53, s60
v_mfma_f32_16x16x32_bf16 v[88:91], a[56:59], a[16:19], v[88:91]
s_waitcnt lgkmcnt(2)
v_mfma_f32_16x16x32_bf16 v[92:95], a[92:95], a[16:19], v[92:95]
buffer_load_dwordx4 v199, s[36:39], s56 offen lds
v_mfma_f32_16x16x32_bf16 v[96:99], a[68:71], a[16:19], v[96:99]
ds_read_b64_tr_b16 a[90:91], v180 offset:6144
v_mfma_f32_16x16x32_bf16 v[100:103], a[32:35], a[60:63], v[100:103]
ds_read_b128 a[4:7], v185 offset:0
v_mfma_f32_16x16x32_bf16 v[104:107], a[76:79], a[60:63], v[104:107]
ds_read_b64_tr_b16 a[24:25], v181 offset:0
v_mfma_f32_16x16x32_bf16 v[108:111], a[56:59], a[60:63], v[108:111]
ds_read_b64_tr_b16 a[26:27], v181 offset:6144
v_mfma_f32_16x16x32_bf16 v[112:115], a[92:95], a[60:63], v[112:115]
ds_read_b64_tr_b16 a[44:45], v182 offset:0
v_mfma_f32_16x16x32_bf16 v[116:119], a[68:71], a[60:63], v[116:119]
ds_read_b64_tr_b16 a[46:47], v182 offset:6144
v_mfma_f32_16x16x32_bf16 v[120:123], a[32:35], a[64:67], v[120:123]
ds_read_b64_tr_b16 a[52:53], v183 offset:0
v_mfma_f32_16x16x32_bf16 v[124:127], a[76:79], a[64:67], v[124:127]
ds_read_b64_tr_b16 a[54:55], v183 offset:6144
v_mfma_f32_16x16x32_bf16 v[128:131], a[56:59], a[64:67], v[128:131]
ds_read_b64_tr_b16 a[20:21], v184 offset:0
v_mfma_f32_16x16x32_bf16 v[132:135], a[92:95], a[64:67], v[132:135]
ds_read_b64_tr_b16 a[22:23], v184 offset:6144
v_mfma_f32_16x16x32_bf16 v[136:139], a[68:71], a[64:67], v[136:139]
ds_read_b128 a[8:11], v185 offset:1024
v_mfma_f32_16x16x32_bf16 v[140:143], a[32:35], a[0:3], v[140:143]
ds_read_b128 a[36:39], v185 offset:2048
v_mfma_f32_16x16x32_bf16 v[144:147], a[76:79], a[0:3], v[144:147]
ds_read_b128 a[12:15], v185 offset:3072
v_mfma_f32_16x16x32_bf16 v[148:151], a[56:59], a[0:3], v[148:151]
s_waitcnt lgkmcnt(7)
v_mfma_f32_16x16x32_bf16 v[152:155], a[92:95], a[0:3], v[152:155]
v_mfma_f32_16x16x32_bf16 v[156:159], a[68:71], a[0:3], v[156:159]
v_mfma_f32_16x16x32_bf16 v[160:163], a[32:35], a[84:87], v[160:163]
ds_read_b128 a[28:31], v185 offset:4096
v_mfma_f32_16x16x32_bf16 v[164:167], a[76:79], a[84:87], v[164:167]
v_mfma_f32_16x16x32_bf16 v[168:171], a[56:59], a[84:87], v[168:171]
v_mfma_f32_16x16x32_bf16 v[172:175], a[92:95], a[84:87], v[172:175]
v_mfma_f32_16x16x32_bf16 v[176:179], a[68:71], a[84:87], v[176:179]
v_mfma_f32_16x16x32_bf16 v[0:3], a[88:91], a[4:7], v[0:3]
s_waitcnt lgkmcnt(1)
ds_read_b128 a[40:43], v185 offset:5120
v_mfma_f32_16x16x32_bf16 v[4:7], a[24:27], a[4:7], v[4:7]
v_mfma_f32_16x16x32_bf16 v[8:11], a[44:47], a[4:7], v[8:11]
v_mfma_f32_16x16x32_bf16 v[12:15], a[52:55], a[4:7], v[12:15]
v_mfma_f32_16x16x32_bf16 v[16:19], a[20:23], a[4:7], v[16:19]
v_mfma_f32_16x16x32_bf16 v[20:23], a[88:91], a[8:11], v[20:23]
ds_read_b128 a[72:75], v185 offset:6144
v_mfma_f32_16x16x32_bf16 v[24:27], a[24:27], a[8:11], v[24:27]
v_mfma_f32_16x16x32_bf16 v[28:31], a[44:47], a[8:11], v[28:31]
v_mfma_f32_16x16x32_bf16 v[32:35], a[52:55], a[8:11], v[32:35]
v_mfma_f32_16x16x32_bf16 v[36:39], a[20:23], a[8:11], v[36:39]
v_mfma_f32_16x16x32_bf16 v[40:43], a[88:91], a[36:39], v[40:43]
ds_read_b128 a[48:51], v185 offset:7168
v_mfma_f32_16x16x32_bf16 v[44:47], a[24:27], a[36:39], v[44:47]
v_mfma_f32_16x16x32_bf16 v[48:51], a[44:47], a[36:39], v[48:51]
v_mfma_f32_16x16x32_bf16 v[52:55], a[52:55], a[36:39], v[52:55]
v_mfma_f32_16x16x32_bf16 v[56:59], a[20:23], a[36:39], v[56:59]
v_mfma_f32_16x16x32_bf16 v[60:63], a[88:91], a[12:15], v[60:63]
ds_read_b128 a[80:83], v185 offset:8192
v_mfma_f32_16x16x32_bf16 v[64:67], a[24:27], a[12:15], v[64:67]
v_mfma_f32_16x16x32_bf16 v[68:71], a[44:47], a[12:15], v[68:71]
s_waitcnt lgkmcnt(2)
v_mfma_f32_16x16x32_bf16 v[72:75], a[52:55], a[12:15], v[72:75]
v_mfma_f32_16x16x32_bf16 v[76:79], a[20:23], a[12:15], v[76:79]
v_mfma_f32_16x16x32_bf16 v[80:83], a[88:91], a[28:31], v[80:83]
v_mfma_f32_16x16x32_bf16 v[84:87], a[24:27], a[28:31], v[84:87]
v_mfma_f32_16x16x32_bf16 v[88:91], a[44:47], a[28:31], v[88:91]
v_mfma_f32_16x16x32_bf16 v[92:95], a[52:55], a[28:31], v[92:95]
v_mfma_f32_16x16x32_bf16 v[96:99], a[20:23], a[28:31], v[96:99]
v_mfma_f32_16x16x32_bf16 v[100:103], a[88:91], a[40:43], v[100:103]
v_mfma_f32_16x16x32_bf16 v[104:107], a[24:27], a[40:43], v[104:107]
v_mfma_f32_16x16x32_bf16 v[108:111], a[44:47], a[40:43], v[108:111]
v_mfma_f32_16x16x32_bf16 v[112:115], a[52:55], a[40:43], v[112:115]
v_mfma_f32_16x16x32_bf16 v[116:119], a[20:23], a[40:43], v[116:119]
v_mfma_f32_16x16x32_bf16 v[120:123], a[88:91], a[72:75], v[120:123]
s_waitcnt vmcnt(0) lgkmcnt(0)
s_barrier
v_mfma_f32_16x16x32_bf16 v[124:127], a[24:27], a[72:75], v[124:127]
ds_read_b64_tr_b16 a[16:17], v180 offset:30720
v_mfma_f32_16x16x32_bf16 v[128:131], a[44:47], a[72:75], v[128:131]
ds_read_b64_tr_b16 a[18:19], v180 offset:36864
v_mfma_f32_16x16x32_bf16 v[132:135], a[52:55], a[72:75], v[132:135]
ds_read_b128 a[60:63], v185 offset:30720
v_mfma_f32_16x16x32_bf16 v[136:139], a[20:23], a[72:75], v[136:139]
ds_read_b64_tr_b16 a[64:65], v181 offset:30720
ds_read_b64_tr_b16 a[66:67], v181 offset:36864
v_mfma_f32_16x16x32_bf16 v[140:143], a[88:91], a[48:51], v[140:143]
ds_read_b64_tr_b16 a[0:1], v182 offset:30720
ds_read_b64_tr_b16 a[2:3], v182 offset:36864
v_mfma_f32_16x16x32_bf16 v[144:147], a[24:27], a[48:51], v[144:147]
ds_read_b64_tr_b16 a[32:33], v183 offset:30720
ds_read_b64_tr_b16 a[34:35], v183 offset:36864
v_mfma_f32_16x16x32_bf16 v[148:151], a[44:47], a[48:51], v[148:151]
ds_read_b64_tr_b16 a[76:77], v184 offset:30720
ds_read_b64_tr_b16 a[78:79], v184 offset:36864
v_mfma_f32_16x16x32_bf16 v[152:155], a[52:55], a[48:51], v[152:155]
ds_read_b128 a[56:59], v185 offset:31744
v_mfma_f32_16x16x32_bf16 v[156:159], a[20:23], a[48:51], v[156:159]
ds_read_b128 a[92:95], v185 offset:32768
v_mfma_f32_16x16x32_bf16 v[160:163], a[88:91], a[80:83], v[160:163]
ds_read_b128 a[68:71], v185 offset:33792
v_mfma_f32_16x16x32_bf16 v[164:167], a[24:27], a[80:83], v[164:167]
ds_read_b128 a[84:87], v185 offset:34816
v_mfma_f32_16x16x32_bf16 v[168:171], a[44:47], a[80:83], v[168:171]
s_waitcnt lgkmcnt(10)
v_mfma_f32_16x16x32_bf16 v[172:175], a[52:55], a[80:83], v[172:175]
v_mfma_f32_16x16x32_bf16 v[176:179], a[20:23], a[80:83], v[176:179]
s_waitcnt lgkmcnt(6)
v_mfma_f32_16x16x32_bf16 v[0:3], a[16:19], a[60:63], v[0:3]
ds_read_b128 a[4:7], v185 offset:35840
v_mfma_f32_16x16x32_bf16 v[4:7], a[64:67], a[60:63], v[4:7]
s_waitcnt lgkmcnt(4)
v_mfma_f32_16x16x32_bf16 v[8:11], a[0:3], a[60:63], v[8:11]
v_mfma_f32_16x16x32_bf16 v[12:15], a[32:35], a[60:63], v[12:15]
v_mfma_f32_16x16x32_bf16 v[16:19], a[76:79], a[60:63], v[16:19]
v_mfma_f32_16x16x32_bf16 v[20:23], a[16:19], a[56:59], v[20:23]
ds_read_b128 a[8:11], v185 offset:36864
v_mfma_f32_16x16x32_bf16 v[24:27], a[64:67], a[56:59], v[24:27]
v_mfma_f32_16x16x32_bf16 v[28:31], a[0:3], a[56:59], v[28:31]
s_waitcnt lgkmcnt(2)
v_mfma_f32_16x16x32_bf16 v[32:35], a[32:35], a[56:59], v[32:35]
v_mfma_f32_16x16x32_bf16 v[36:39], a[76:79], a[56:59], v[36:39]
v_mfma_f32_16x16x32_bf16 v[40:43], a[16:19], a[92:95], v[40:43]
ds_read_b128 a[36:39], v185 offset:37888
v_mfma_f32_16x16x32_bf16 v[44:47], a[64:67], a[92:95], v[44:47]
v_mfma_f32_16x16x32_bf16 v[48:51], a[0:3], a[92:95], v[48:51]
v_mfma_f32_16x16x32_bf16 v[52:55], a[32:35], a[92:95], v[52:55]
v_mfma_f32_16x16x32_bf16 v[56:59], a[76:79], a[92:95], v[56:59]
v_mfma_f32_16x16x32_bf16 v[60:63], a[16:19], a[68:71], v[60:63]
ds_read_b128 a[12:15], v185 offset:38912
v_mfma_f32_16x16x32_bf16 v[64:67], a[64:67], a[68:71], v[64:67]
v_mfma_f32_16x16x32_bf16 v[68:71], a[0:3], a[68:71], v[68:71]
v_mfma_f32_16x16x32_bf16 v[72:75], a[32:35], a[68:71], v[72:75]
v_mfma_f32_16x16x32_bf16 v[76:79], a[76:79], a[68:71], v[76:79]
v_mfma_f32_16x16x32_bf16 v[80:83], a[16:19], a[84:87], v[80:83]
ds_read_b64_tr_b16 a[28:29], v186 offset:0
v_mfma_f32_16x16x32_bf16 v[84:87], a[64:67], a[84:87], v[84:87]
ds_read_b64_tr_b16 a[30:31], v186 offset:6144
v_mfma_f32_16x16x32_bf16 v[88:91], a[0:3], a[84:87], v[88:91]
s_waitcnt lgkmcnt(3)
ds_read_b128 a[40:43], v191 offset:0
v_mfma_f32_16x16x32_bf16 v[92:95], a[32:35], a[84:87], v[92:95]
ds_read_b64_tr_b16 a[72:73], v187 offset:0
v_mfma_f32_16x16x32_bf16 v[96:99], a[76:79], a[84:87], v[96:99]
ds_read_b64_tr_b16 a[74:75], v187 offset:6144
v_mfma_f32_16x16x32_bf16 v[100:103], a[16:19], a[4:7], v[100:103]
ds_read_b64_tr_b16 a[48:49], v188 offset:0
v_mfma_f32_16x16x32_bf16 v[104:107], a[64:67], a[4:7], v[104:107]
ds_read_b64_tr_b16 a[50:51], v188 offset:6144
v_mfma_f32_16x16x32_bf16 v[108:111], a[0:3], a[4:7], v[108:111]
ds_read_b64_tr_b16 a[88:89], v189 offset:0
v_mfma_f32_16x16x32_bf16 v[112:115], a[32:35], a[4:7], v[112:115]
ds_read_b64_tr_b16 a[90:91], v189 offset:6144
v_mfma_f32_16x16x32_bf16 v[116:119], a[76:79], a[4:7], v[116:119]
ds_read_b64_tr_b16 a[24:25], v190 offset:0
v_mfma_f32_16x16x32_bf16 v[120:123], a[16:19], a[8:11], v[120:123]
ds_read_b64_tr_b16 a[26:27], v190 offset:6144
v_mfma_f32_16x16x32_bf16 v[124:127], a[64:67], a[8:11], v[124:127]
ds_read_b128 a[44:47], v191 offset:1024
v_mfma_f32_16x16x32_bf16 v[128:131], a[0:3], a[8:11], v[128:131]
ds_read_b128 a[52:55], v191 offset:2048
v_mfma_f32_16x16x32_bf16 v[132:135], a[32:35], a[8:11], v[132:135]
v_mfma_f32_16x16x32_bf16 v[136:139], a[76:79], a[8:11], v[136:139]
v_mfma_f32_16x16x32_bf16 v[140:143], a[16:19], a[36:39], v[140:143]
ds_read_b128 a[80:83], v191 offset:3072
v_mfma_f32_16x16x32_bf16 v[144:147], a[64:67], a[36:39], v[144:147]
v_mfma_f32_16x16x32_bf16 v[148:151], a[0:3], a[36:39], v[148:151]
s_waitcnt lgkmcnt(4)
v_mfma_f32_16x16x32_bf16 v[152:155], a[32:35], a[36:39], v[152:155]
v_mfma_f32_16x16x32_bf16 v[156:159], a[76:79], a[36:39], v[156:159]
v_mfma_f32_16x16x32_bf16 v[160:163], a[16:19], a[12:15], v[160:163]
ds_read_b128 a[20:23], v191 offset:4096
v_mfma_f32_16x16x32_bf16 v[164:167], a[64:67], a[12:15], v[164:167]
v_mfma_f32_16x16x32_bf16 v[168:171], a[0:3], a[12:15], v[168:171]
v_mfma_f32_16x16x32_bf16 v[172:175], a[32:35], a[12:15], v[172:175]
v_mfma_f32_16x16x32_bf16 v[176:179], a[76:79], a[12:15], v[176:179]
v_mfma_f32_16x16x32_bf16 v[0:3], a[28:31], a[40:43], v[0:3]
ds_read_b128 a[60:63], v191 offset:5120
v_mfma_f32_16x16x32_bf16 v[4:7], a[72:75], a[40:43], v[4:7]
s_waitcnt lgkmcnt(2)
v_mfma_f32_16x16x32_bf16 v[8:11], a[48:51], a[40:43], v[8:11]
v_mfma_f32_16x16x32_bf16 v[12:15], a[88:91], a[40:43], v[12:15]
v_mfma_f32_16x16x32_bf16 v[16:19], a[24:27], a[40:43], v[16:19]
v_mfma_f32_16x16x32_bf16 v[20:23], a[28:31], a[44:47], v[20:23]
ds_read_b128 a[56:59], v191 offset:6144
v_mfma_f32_16x16x32_bf16 v[24:27], a[72:75], a[44:47], v[24:27]
v_mfma_f32_16x16x32_bf16 v[28:31], a[48:51], a[44:47], v[28:31]
v_mfma_f32_16x16x32_bf16 v[32:35], a[88:91], a[44:47], v[32:35]
v_mfma_f32_16x16x32_bf16 v[36:39], a[24:27], a[44:47], v[36:39]
v_mfma_f32_16x16x32_bf16 v[40:43], a[28:31], a[52:55], v[40:43]
ds_read_b128 a[92:95], v191 offset:7168
v_mfma_f32_16x16x32_bf16 v[44:47], a[72:75], a[52:55], v[44:47]
v_mfma_f32_16x16x32_bf16 v[48:51], a[48:51], a[52:55], v[48:51]
v_mfma_f32_16x16x32_bf16 v[52:55], a[88:91], a[52:55], v[52:55]
v_mfma_f32_16x16x32_bf16 v[56:59], a[24:27], a[52:55], v[56:59]
v_mfma_f32_16x16x32_bf16 v[60:63], a[28:31], a[80:83], v[60:63]
ds_read_b128 a[68:71], v191 offset:8192
v_mfma_f32_16x16x32_bf16 v[64:67], a[72:75], a[80:83], v[64:67]
v_mfma_f32_16x16x32_bf16 v[68:71], a[48:51], a[80:83], v[68:71]
s_waitcnt lgkmcnt(2)
v_mfma_f32_16x16x32_bf16 v[72:75], a[88:91], a[80:83], v[72:75]
v_mfma_f32_16x16x32_bf16 v[76:79], a[24:27], a[80:83], v[76:79]
v_mfma_f32_16x16x32_bf16 v[80:83], a[28:31], a[20:23], v[80:83]
v_mfma_f32_16x16x32_bf16 v[84:87], a[72:75], a[20:23], v[84:87]
v_mfma_f32_16x16x32_bf16 v[88:91], a[48:51], a[20:23], v[88:91]
v_mfma_f32_16x16x32_bf16 v[92:95], a[88:91], a[20:23], v[92:95]
v_mfma_f32_16x16x32_bf16 v[96:99], a[24:27], a[20:23], v[96:99]
v_mfma_f32_16x16x32_bf16 v[100:103], a[28:31], a[60:63], v[100:103]
v_mfma_f32_16x16x32_bf16 v[104:107], a[72:75], a[60:63], v[104:107]
v_mfma_f32_16x16x32_bf16 v[108:111], a[48:51], a[60:63], v[108:111]
v_mfma_f32_16x16x32_bf16 v[112:115], a[88:91], a[60:63], v[112:115]
v_mfma_f32_16x16x32_bf16 v[116:119], a[24:27], a[60:63], v[116:119]
v_mfma_f32_16x16x32_bf16 v[120:123], a[28:31], a[56:59], v[120:123]
s_waitcnt lgkmcnt(0)
s_barrier
v_mfma_f32_16x16x32_bf16 v[124:127], a[72:75], a[56:59], v[124:127]
v_mfma_f32_16x16x32_bf16 v[128:131], a[48:51], a[56:59], v[128:131]
v_mfma_f32_16x16x32_bf16 v[132:135], a[88:91], a[56:59], v[132:135]
v_mfma_f32_16x16x32_bf16 v[136:139], a[24:27], a[56:59], v[136:139]
v_mfma_f32_16x16x32_bf16 v[140:143], a[28:31], a[92:95], v[140:143]
v_mfma_f32_16x16x32_bf16 v[144:147], a[72:75], a[92:95], v[144:147]
v_mfma_f32_16x16x32_bf16 v[148:151], a[48:51], a[92:95], v[148:151]
v_mfma_f32_16x16x32_bf16 v[152:155], a[88:91], a[92:95], v[152:155]
v_mfma_f32_16x16x32_bf16 v[156:159], a[24:27], a[92:95], v[156:159]
v_mfma_f32_16x16x32_bf16 v[160:163], a[28:31], a[68:71], v[160:163]
v_mfma_f32_16x16x32_bf16 v[164:167], a[72:75], a[68:71], v[164:167]
v_mfma_f32_16x16x32_bf16 v[168:171], a[48:51], a[68:71], v[168:171]
v_mfma_f32_16x16x32_bf16 v[172:175], a[88:91], a[68:71], v[172:175]
v_mfma_f32_16x16x32_bf16 v[176:179], a[24:27], a[68:71], v[176:179]
s_nop 7
s_cmp_eq_u32 s63, 1
s_cbranch_scc1 K2_fin1
s_cmp_eq_u32 s63, 2
s_cbranch_scc1 K2_fin2
s_cmp_eq_u32 s63, 3
s_cbranch_scc1 K2_fin3
K2_fin0:
s_add_u32 s58, s71, 0
s_mul_i32 s58, s58, 24576
v_cvt_pk_bf16_f32 v4, v4, v5
v_cvt_pk_bf16_f32 v5, v6, v7
v_cvt_pk_bf16_f32 v6, v20, v21
v_cvt_pk_bf16_f32 v7, v22, v23
s_add_u32 s59, s58, 4096
buffer_store_dwordx4 v[4:7], v205, s[44:47], s59 offen offset:2048 sc1
v_cvt_pk_bf16_f32 v36, v36, v37
v_cvt_pk_bf16_f32 v37, v38, v39
v_cvt_pk_bf16_f32 v38, v52, v53
v_cvt_pk_bf16_f32 v39, v54, v55
s_add_u32 s59, s58, 4096
buffer_store_dwordx4 v[36:39], v205, s[44:47], s59 offen offset:3072 sc1
v_cvt_pk_bf16_f32 v68, v68, v69
v_cvt_pk_bf16_f32 v69, v70, v71
v_cvt_pk_bf16_f32 v70, v84, v85
v_cvt_pk_bf16_f32 v71, v86, v87
s_add_u32 s59, s58, 8192
buffer_store_dwordx4 v[68:71], v205, s[44:47], s59 offen sc1
v_cvt_pk_bf16_f32 v100, v100, v101
v_cvt_pk_bf16_f32 v101, v102, v103
v_cvt_pk_bf16_f32 v102, v116, v117
v_cvt_pk_bf16_f32 v103, v118, v119
s_add_u32 s59, s58, 8192
buffer_store_dwordx4 v[100:103], v205, s[44:47], s59 offen offset:1024 sc1
v_cvt_pk_bf16_f32 v132, v132, v133
v_cvt_pk_bf16_f32 v133, v134, v135
v_cvt_pk_bf16_f32 v134, v148, v149
v_cvt_pk_bf16_f32 v135, v150, v151
s_add_u32 s59, s58, 8192
buffer_store_dwordx4 v[132:135], v205, s[44:47], s59 offen offset:2048 sc1
v_cvt_pk_bf16_f32 v164, v164, v165
v_cvt_pk_bf16_f32 v165, v166, v167
s_add_u32 s59, s58, 8192
buffer_store_dwordx4 v[164:167], v205, s[44:47], s59 offen offset:3072 sc1
v_cvt_pk_bf16_f32 v8, v8, v9
v_cvt_pk_bf16_f32 v9, v10, v11
v_cvt_pk_bf16_f32 v10, v24, v25
v_cvt_pk_bf16_f32 v11, v26, v27
s_add_u32 s59, s58, 12288
buffer_store_dwordx4 v[8:11], v205, s[44:47], s59 offen sc1
v_cvt_pk_bf16_f32 v40, v40, v41
v_cvt_pk_bf16_f32 v41, v42, v43
v_cvt_pk_bf16_f32 v42, v56, v57
v_cvt_pk_bf16_f32 v43, v58, v59
s_add_u32 s59, s58, 12288
buffer_store_dwordx4 v[40:43], v205, s[44:47], s59 offen offset:1024 sc1
v_cvt_pk_bf16_f32 v72, v72, v73
v_cvt_pk_bf16_f32 v73, v74, v75
v_cvt_pk_bf16_f32 v74, v88, v89
v_cvt_pk_bf16_f32 v75, v90, v91
s_add_u32 s59, s58, 12288
buffer_store_dwordx4 v[72:75], v205, s[44:47], s59 offen offset:2048 sc1
v_cvt_pk_bf16_f32 v104, v104, v105
v_cvt_pk_bf16_f32 v105, v106, v107
v_cvt_pk_bf16_f32 v106, v120, v121
v_cvt_pk_bf16_f32 v107, v122, v123
s_add_u32 s59, s58, 12288
buffer_store_dwordx4 v[104:107], v205, s[44:47], s59 offen offset:3072 sc1
v_cvt_pk_bf16_f32 v136, v136, v137
v_cvt_pk_bf16_f32 v137, v138, v139
v_cvt_pk_bf16_f32 v138, v152, v153
v_cvt_pk_bf16_f32 v139, v154, v155
s_add_u32 s59, s58, 16384
buffer_store_dwordx4 v[136:139], v205, s[44:47], s59 offen sc1
v_cvt_pk_bf16_f32 v168, v168, v169
v_cvt_pk_bf16_f32 v169, v170, v171
s_add_u32 s59, s58, 16384
buffer_store_dwordx4 v[168:171], v205, s[44:47], s59 offen offset:1024 sc1
v_cvt_pk_bf16_f32 v12, v12, v13
v_cvt_pk_bf16_f32 v13, v14, v15
v_cvt_pk_bf16_f32 v14, v28, v29
v_cvt_pk_bf16_f32 v15, v30, v31
s_add_u32 s59, s58, 16384
buffer_store_dwordx4 v[12:15], v205, s[44:47], s59 offen offset:2048 sc1
v_cvt_pk_bf16_f32 v44, v44, v45
v_cvt_pk_bf16_f32 v45, v46, v47
v_cvt_pk_bf16_f32 v46, v60, v61
v_cvt_pk_bf16_f32 v47, v62, v63
s_add_u32 s59, s58, 16384
buffer_store_dwordx4 v[44:47], v205, s[44:47], s59 offen offset:3072 sc1
v_cvt_pk_bf16_f32 v76, v76, v77
v_cvt_pk_bf16_f32 v77, v78, v79
v_cvt_pk_bf16_f32 v78, v92, v93
v_cvt_pk_bf16_f32 v79, v94, v95
s_add_u32 s59, s58, 20480
buffer_store_dwordx4 v[76:79], v205, s[44:47], s59 offen sc1
v_cvt_pk_bf16_f32 v108, v108, v109
v_cvt_pk_bf16_f32 v109, v110, v111
v_cvt_pk_bf16_f32 v110, v124, v125
v_cvt_pk_bf16_f32 v111, v126, v127
s_add_u32 s59, s58, 20480
buffer_store_dwordx4 v[108:111], v205, s[44:47], s59 offen offset:1024 sc1
v_cvt_pk_bf16_f32 v140, v140, v141
v_cvt_pk_bf16_f32 v141, v142, v143
v_cvt_pk_bf16_f32 v142, v156, v157
v_cvt_pk_bf16_f32 v143, v158, v159
s_add_u32 s59, s58, 20480
buffer_store_dwordx4 v[140:143], v205, s[44:47], s59 offen offset:2048 sc1
v_cvt_pk_bf16_f32 v172, v172, v173
v_cvt_pk_bf16_f32 v173, v174, v175
s_add_u32 s59, s58, 20480
buffer_store_dwordx4 v[172:175], v205, s[44:47], s59 offen offset:3072 sc1
s_waitcnt vmcnt(0)
s_barrier
s_cmp_lg_u32 s19, 0
s_cbranch_scc1 K2_w0_skip
v_mov_b32 v208, 0
v_mov_b32 v209, 1
s_mov_b64 exec, 1
global_atomic_add v208, v209, s[74:75] sc1
s_mov_b32 s60, 0
K2_w0_poll:
global_load_dword v210, v208, s[74:75] sc1
s_waitcnt vmcnt(0)
v_readfirstlane_b32 s61, v210
s_add_u32 s60, s60, 1
s_cmp_ge_u32 s61, 4
s_cbranch_scc1 K2_w0_done
s_sleep 1
s_cmp_lt_u32 s60, 131072
s_cbranch_scc1 K2_w0_poll
K2_w0_done:
s_mov_b64 exec, -1
K2_w0_skip:
s_barrier
s_add_u32 s58, s71, 4
s_mul_i32 s58, s58, 24576
s_add_u32 s59, s58, 0
buffer_load_dwordx4 v[4:7], v205, s[44:47], s59 offen offset:0 sc1
s_add_u32 s59, s58, 0
buffer_load_dwordx4 v[8:11], v205, s[44:47], s59 offen offset:1024 sc1
s_add_u32 s59, s58, 0
buffer_load_dwordx4 v[12:15], v205, s[44:47], s59 offen offset:2048 sc1
s_add_u32 s59, s58, 0
buffer_load_dwordx4 v[20:23], v205, s[44:47], s59 offen offset:3072 sc1
s_add_u32 s59, s58, 4096
buffer_load_dwordx4 v[24:27], v205, s[44:47], s59 offen offset:0 sc1
s_add_u32 s59, s58, 4096
buffer_load_dwordx4 v[28:31], v205, s[44:47], s59 offen offset:1024 sc1
s_add_u32 s58, s71, 8
s_mul_i32 s58, s58, 24576
s_add_u32 s59, s58, 0
buffer_load_dwordx4 v[36:39], v205, s[44:47], s59 offen offset:0 sc1
s_add_u32 s59, s58, 0
buffer_load_dwordx4 v[40:43], v205, s[44:47], s59 offen offset:1024 sc1
s_add_u32 s59, s58, 0
buffer_load_dwordx4 v[44:47], v205, s[44:47], s59 offen offset:2048 sc1
s_add_u32 s59, s58, 0
buffer_load_dwordx4 v[52:55], v205, s[44:47], s59 offen offset:3072 sc1
s_add_u32 s59, s58, 4096
buffer_load_dwordx4 v[56:59], v205, s[44:47], s59 offen offset:0 sc1
s_add_u32 s59, s58, 4096
buffer_load_dwordx4 v[60:63], v205, s[44:47], s59 offen offset:1024 sc1
s_add_u32 s58, s71, 12
s_mul_i32 s58, s58, 24576
s_add_u32 s59, s58, 0
buffer_load_dwordx4 v[68:71], v205, s[44:47], s59 offen offset:0 sc1
s_add_u32 s59, s58, 0
buffer_load_dwordx4 v[72:75], v205, s[44:47], s59 offen offset:1024 sc1
s_add_u32 s59, s58, 0
buffer_load_dwordx4 v[76:79], v205, s[44:47], s59 offen offset:2048 sc1
s_add_u32 s59, s58, 0
buffer_load_dwordx4 v[84:87], v205, s[44:47], s59 offen offset:3072 sc1
s_add_u32 s59, s58, 4096
buffer_load_dwordx4 v[88:91], v205, s[44:47], s59 offen offset:0 sc1
s_add_u32 s59, s58, 4096
buffer_load_dwordx4 v[92:95], v205, s[44:47], s59 offen offset:1024 sc1
s_waitcnt vmcnt(0)
v_lshlrev_b32 v212, 16, v4
v_and_b32 v213, 4294901760, v4
v_lshlrev_b32 v214, 16, v5
v_and_b32 v215, 4294901760, v5
v_pk_add_f32 v[0:1], v[0:1], v[212:213]
v_pk_add_f32 v[2:3], v[2:3], v[214:215]
v_lshlrev_b32 v216, 16, v36
v_and_b32 v217, 4294901760, v36
v_lshlrev_b32 v218, 16, v37
v_and_b32 v219, 4294901760, v37
v_pk_add_f32 v[0:1], v[0:1], v[216:217]
v_pk_add_f32 v[2:3], v[2:3], v[218:219]
v_lshlrev_b32 v220, 16, v68
v_and_b32 v221, 4294901760, v68
v_lshlrev_b32 v222, 16, v69
v_and_b32 v223, 4294901760, v69
v_pk_add_f32 v[0:1], v[0:1], v[220:221]
v_pk_add_f32 v[2:3], v[2:3], v[222:223]
v_cvt_pk_bf16_f32 v0, v0, v1
v_cvt_pk_bf16_f32 v1, v2, v3
s_add_u32 s59, s72, 0
buffer_store_dwordx2 v[0:1], v206, s[48:51], s59 offen
v_lshlrev_b32 v224, 16, v6
v_and_b32 v225, 4294901760, v6
v_lshlrev_b32 v226, 16, v7
v_and_b32 v227, 4294901760, v7
v_pk_add_f32 v[16:17], v[16:17], v[224:225]
v_pk_add_f32 v[18:19], v[18:19], v[226:227]
v_lshlrev_b32 v212, 16, v38
v_and_b32 v213, 4294901760, v38
v_lshlrev_b32 v214, 16, v39
v_and_b32 v215, 4294901760, v39
v_pk_add_f32 v[16:17], v[16:17], v[212:213]
v_pk_add_f32 v[18:19], v[18:19], v[214:215]
v_lshlrev_b32 v216, 16, v70
v_and_b32 v217, 4294901760, v70
v_lshlrev_b32 v218, 16, v71
v_and_b32 v219, 4294901760, v71
v_pk_add_f32 v[16:17], v[16:17], v[216:217]
v_pk_add_f32 v[18:19], v[18:19], v[218:219]
v_cvt_pk_bf16_f32 v16, v16, v17
v_cvt_pk_bf16_f32 v17, v18, v19
s_add_u32 s59, s72, 128
buffer_store_dwordx2 v[16:17], v206, s[48:51], s59 offen
v_lshlrev_b32 v220, 16, v8
v_and_b32 v221, 4294901760, v8
v_lshlrev_b32 v222, 16, v9
v_and_b32 v223, 4294901760, v9
v_pk_add_f32 v[32:33], v[32:33], v[220:221]
v_pk_add_f32 v[34:35], v[34:35], v[222:223]
v_lshlrev_b32 v224, 16, v40
v_and_b32 v225, 4294901760, v40
v_lshlrev_b32 v226, 16, v41
v_and_b32 v227, 4294901760, v41
v_pk_add_f32 v[32:33], v[32:33], v[224:225]
v_pk_add_f32 v[34:35], v[34:35], v[226:227]
v_lshlrev_b32 v212, 16, v72
v_and_b32 v213, 4294901760, v72
v_lshlrev_b32 v214, 16, v73
v_and_b32 v215, 4294901760, v73
v_pk_add_f32 v[32:33], v[32:33], v[212:213]
v_pk_add_f32 v[34:35], v[34:35], v[214:215]
v_cvt_pk_bf16_f32 v32, v32, v33
v_cvt_pk_bf16_f32 v33, v34, v35
s_add_u32 s59, s72, 41056
buffer_store_dwordx2 v[32:33], v206, s[48:51], s59 offen
v_lshlrev_b32 v216, 16, v10
v_and_b32 v217, 4294901760, v10
v_lshlrev_b32 v218, 16, v11
v_and_b32 v219, 4294901760, v11
v_pk_add_f32 v[48:49], v[48:49], v[216:217]
v_pk_add_f32 v[50:51], v[50:51], v[218:219]
v_lshlrev_b32 v220, 16, v42
v_and_b32 v221, 4294901760, v42
v_lshlrev_b32 v222, 16, v43
v_and_b32 v223, 4294901760, v43
v_pk_add_f32 v[48:49], v[48:49], v[220:221]
v_pk_add_f32 v[50:51], v[50:51], v[222:223]
v_lshlrev_b32 v224, 16, v74
v_and_b32 v225, 4294901760, v74
v_lshlrev_b32 v226, 16, v75
v_and_b32 v227, 4294901760, v75
v_pk_add_f32 v[48:49], v[48:49], v[224:225]
v_pk_add_f32 v[50:51], v[50:51], v[226:227]
v_cvt_pk_bf16_f32 v48, v48, v49
v_cvt_pk_bf16_f32 v49, v50, v51
s_add_u32 s59, s72, 81984
buffer_store_dwordx2 v[48:49], v206, s[48:51], s59 offen
v_lshlrev_b32 v212, 16, v12
v_and_b32 v213, 4294901760, v12
v_lshlrev_b32 v214, 16, v13
v_and_b32 v215, 4294901760, v13
v_pk_add_f32 v[64:65], v[64:65], v[212:213]
v_pk_add_f32 v[66:67], v[66:67], v[214:215]
v_lshlrev_b32 v216, 16, v44
v_and_b32 v217, 4294901760, v44
v_lshlrev_b32 v218, 16, v45
v_and_b32 v219, 4294901760, v45
v_pk_add_f32 v[64:65], v[64:65], v[216:217]
v_pk_add_f32 v[66:67], v[66:67], v[218:219]
v_lshlrev_b32 v220, 16, v76
v_and_b32 v221, 4294901760, v76
v_lshlrev_b32 v222, 16, v77
v_and_b32 v223, 4294901760, v77
v_pk_add_f32 v[64:65], v[64:65], v[220:221]
v_pk_add_f32 v[66:67], v[66:67], v[222:223]
v_cvt_pk_bf16_f32 v64, v64, v65
v_cvt_pk_bf16_f32 v65, v66, v67
s_add_u32 s59, s72, 122912
buffer_store_dwordx2 v[64:65], v206, s[48:51], s59 offen
v_lshlrev_b32 v224, 16, v14
v_and_b32 v225, 4294901760, v14
v_lshlrev_b32 v226, 16, v15
v_and_b32 v227, 4294901760, v15
v_pk_add_f32 v[80:81], v[80:81], v[224:225]
v_pk_add_f32 v[82:83], v[82:83], v[226:227]
v_lshlrev_b32 v212, 16, v46
v_and_b32 v213, 4294901760, v46
v_lshlrev_b32 v214, 16, v47
v_and_b32 v215, 4294901760, v47
v_pk_add_f32 v[80:81], v[80:81], v[212:213]
v_pk_add_f32 v[82:83], v[82:83], v[214:215]
v_lshlrev_b32 v216, 16, v78
v_and_b32 v217, 4294901760, v78
v_lshlrev_b32 v218, 16, v79
v_and_b32 v219, 4294901760, v79
v_pk_add_f32 v[80:81], v[80:81], v[216:217]
v_pk_add_f32 v[82:83], v[82:83], v[218:219]
v_cvt_pk_bf16_f32 v80, v80, v81
v_cvt_pk_bf16_f32 v81, v82, v83
s_add_u32 s59, s72, 163840
buffer_store_dwordx2 v[80:81], v206, s[48:51], s59 offen
v_lshlrev_b32 v220, 16, v20
v_and_b32 v221, 4294901760, v20
v_lshlrev_b32 v222, 16, v21
v_and_b32 v223, 4294901760, v21
v_pk_add_f32 v[96:97], v[96:97], v[220:221]
v_pk_add_f32 v[98:99], v[98:99], v[222:223]
v_lshlrev_b32 v224, 16, v52
v_and_b32 v225, 4294901760, v52
v_lshlrev_b32 v226, 16, v53
v_and_b32 v227, 4294901760, v53
v_pk_add_f32 v[96:97], v[96:97], v[224:225]
v_pk_add_f32 v[98:99], v[98:99], v[226:227]
v_lshlrev_b32 v212, 16, v84
v_and_b32 v213, 4294901760, v84
v_lshlrev_b32 v214, 16, v85
v_and_b32 v215, 4294901760, v85
v_pk_add_f32 v[96:97], v[96:97], v[212:213]
v_pk_add_f32 v[98:99], v[98:99], v[214:215]
v_cvt_pk_bf16_f32 v96, v96, v97
v_cvt_pk_bf16_f32 v97, v98, v99
s_add_u32 s59, s72, 163968
buffer_store_dwordx2 v[96:97], v206, s[48:51], s59 offen
v_lshlrev_b32 v216, 16, v22
v_and_b32 v217, 4294901760, v22
v_lshlrev_b32 v218, 16, v23
v_and_b32 v219, 4294901760, v23
v_pk_add_f32 v[112:113], v[112:113], v[216:217]
v_pk_add_f32 v[114:115], v[114:115], v[218:219]
v_lshlrev_b32 v220, 16, v54
v_and_b32 v221, 4294901760, v54
v_lshlrev_b32 v222, 16, v55
v_and_b32 v223, 4294901760, v55
v_pk_add_f32 v[112:113], v[112:113], v[220:221]
v_pk_add_f32 v[114:115], v[114:115], v[222:223]
v_lshlrev_b32 v224, 16, v86
v_and_b32 v225, 4294901760, v86
v_lshlrev_b32 v226, 16, v87
v_and_b32 v227, 4294901760, v87
v_pk_add_f32 v[112:113], v[112:113], v[224:225]
v_pk_add_f32 v[114:115], v[114:115], v[226:227]
v_cvt_pk_bf16_f32 v112, v112, v113
v_cvt_pk_bf16_f32 v113, v114, v115
s_add_u32 s59, s72, 204896
buffer_store_dwordx2 v[112:113], v206, s[48:51], s59 offen
v_lshlrev_b32 v212, 16, v24
v_and_b32 v213, 4294901760, v24
v_lshlrev_b32 v214, 16, v25
v_and_b32 v215, 4294901760, v25
v_pk_add_f32 v[128:129], v[128:129], v[212:213]
v_pk_add_f32 v[130:131], v[130:131], v[214:215]
v_lshlrev_b32 v216, 16, v56
v_and_b32 v217, 4294901760, v56
v_lshlrev_b32 v218, 16, v57
v_and_b32 v219, 4294901760, v57
v_pk_add_f32 v[128:129], v[128:129], v[216:217]
v_pk_add_f32 v[130:131], v[130:131], v[218:219]
v_lshlrev_b32 v220, 16, v88
v_and_b32 v221, 4294901760, v88
v_lshlrev_b32 v222, 16, v89
v_and_b32 v223, 4294901760, v89
v_pk_add_f32 v[128:129], v[128:129], v[220:221]
v_pk_add_f32 v[130:131], v[130:131], v[222:223]
v_cvt_pk_bf16_f32 v128, v128, v129
v_cvt_pk_bf16_f32 v129, v130, v131
s_add_u32 s59, s72, 245824
buffer_store_dwordx2 v[128:129], v206, s[48:51], s59 offen
v_lshlrev_b32 v224, 16, v26
v_and_b32 v225, 4294901760, v26
v_lshlrev_b32 v226, 16, v27
v_and_b32 v227, 4294901760, v27
v_pk_add_f32 v[144:145], v[144:145], v[224:225]
v_pk_add_f32 v[146:147], v[146:147], v[226:227]
v_lshlrev_b32 v212, 16, v58
v_and_b32 v213, 4294901760, v58
v_lshlrev_b32 v214, 16, v59
v_and_b32 v215, 4294901760, v59
v_pk_add_f32 v[144:145], v[144:145], v[212:213]
v_pk_add_f32 v[146:147], v[146:147], v[214:215]
v_lshlrev_b32 v216, 16, v90
v_and_b32 v217, 4294901760, v90
v_lshlrev_b32 v218, 16, v91
v_and_b32 v219, 4294901760, v91
v_pk_add_f32 v[144:145], v[144:145], v[216:217]
v_pk_add_f32 v[146:147], v[146:147], v[218:219]
v_cvt_pk_bf16_f32 v144, v144, v145
v_cvt_pk_bf16_f32 v145, v146, v147
s_add_u32 s59, s72, 286752
buffer_store_dwordx2 v[144:145], v206, s[48:51], s59 offen
v_lshlrev_b32 v220, 16, v28
v_and_b32 v221, 4294901760, v28
v_lshlrev_b32 v222, 16, v29
v_and_b32 v223, 4294901760, v29
v_pk_add_f32 v[160:161], v[160:161], v[220:221]
v_pk_add_f32 v[162:163], v[162:163], v[222:223]
v_lshlrev_b32 v224, 16, v60
v_and_b32 v225, 4294901760, v60
v_lshlrev_b32 v226, 16, v61
v_and_b32 v227, 4294901760, v61
v_pk_add_f32 v[160:161], v[160:161], v[224:225]
v_pk_add_f32 v[162:163], v[162:163], v[226:227]
v_lshlrev_b32 v212, 16, v92
v_and_b32 v213, 4294901760, v92
v_lshlrev_b32 v214, 16, v93
v_and_b32 v215, 4294901760, v93
v_pk_add_f32 v[160:161], v[160:161], v[212:213]
v_pk_add_f32 v[162:163], v[162:163], v[214:215]
v_cvt_pk_bf16_f32 v160, v160, v161
v_cvt_pk_bf16_f32 v161, v162, v163
s_add_u32 s59, s72, 327680
buffer_store_dwordx2 v[160:161], v206, s[48:51], s59 offen
v_lshlrev_b32 v216, 16, v30
v_and_b32 v217, 4294901760, v30
v_lshlrev_b32 v218, 16, v31
v_and_b32 v219, 4294901760, v31
v_pk_add_f32 v[176:177], v[176:177], v[216:217]
v_pk_add_f32 v[178:179], v[178:179], v[218:219]
v_lshlrev_b32 v220, 16, v62
v_and_b32 v221, 4294901760, v62
v_lshlrev_b32 v222, 16, v63
v_and_b32 v223, 4294901760, v63
v_pk_add_f32 v[176:177], v[176:177], v[220:221]
v_pk_add_f32 v[178:179], v[178:179], v[222:223]
v_lshlrev_b32 v224, 16, v94
v_and_b32 v225, 4294901760, v94
v_lshlrev_b32 v226, 16, v95
v_and_b32 v227, 4294901760, v95
v_pk_add_f32 v[176:177], v[176:177], v[224:225]
v_pk_add_f32 v[178:179], v[178:179], v[226:227]
v_cvt_pk_bf16_f32 v176, v176, v177
v_cvt_pk_bf16_f32 v177, v178, v179
s_add_u32 s59, s72, 327808
buffer_store_dwordx2 v[176:177], v206, s[48:51], s59 offen
s_waitcnt vmcnt(0)
s_branch K2_end
K2_fin1:
s_add_u32 s58, s71, 4
s_mul_i32 s58, s58, 24576
v_cvt_pk_bf16_f32 v0, v0, v1
v_cvt_pk_bf16_f32 v1, v2, v3
v_cvt_pk_bf16_f32 v2, v16, v17
v_cvt_pk_bf16_f32 v3, v18, v19
s_add_u32 s59, s58, 0
buffer_store_dwordx4 v[0:3], v205, s[44:47], s59 offen sc1
v_cvt_pk_bf16_f32 v32, v32, v33
v_cvt_pk_bf16_f32 v33, v34, v35
v_cvt_pk_bf16_f32 v34, v48, v49
v_cvt_pk_bf16_f32 v35, v50, v51
s_add_u32 s59, s58, 0
buffer_store_dwordx4 v[32:35], v205, s[44:47], s59 offen offset:1024 sc1
v_cvt_pk_bf16_f32 v64, v64, v65
v_cvt_pk_bf16_f32 v65, v66, v67
v_cvt_pk_bf16_f32 v66, v80, v81
v_cvt_pk_bf16_f32 v67, v82, v83
s_add_u32 s59, s58, 0
buffer_store_dwordx4 v[64:67], v205, s[44:47], s59 offen offset:2048 sc1
v_cvt_pk_bf16_f32 v96, v96, v97
v_cvt_pk_bf16_f32 v97, v98, v99
v_cvt_pk_bf16_f32 v98, v112, v113
v_cvt_pk_bf16_f32 v99, v114, v115
s_add_u32 s59, s58, 0
buffer_store_dwordx4 v[96:99], v205, s[44:47], s59 offen offset:3072 sc1
v_cvt_pk_bf16_f32 v128, v128, v129
v_cvt_pk_bf16_f32 v129, v130, v131
v_cvt_pk_bf16_f32 v130, v144, v145
v_cvt_pk_bf16_f32 v131, v146, v147
s_add_u32 s59, s58, 4096
buffer_store_dwordx4 v[128:131], v205, s[44:47], s59 offen sc1
v_cvt_pk_bf16_f32 v160, v160, v161
v_cvt_pk_bf16_f32 v161, v162, v163
v_cvt_pk_bf16_f32 v162, v176, v177
v_cvt_pk_bf16_f32 v163, v178, v179
s_add_u32 s59, s58, 4096
buffer_store_dwordx4 v[160:163], v205, s[44:47], s59 offen offset:1024 sc1
v_cvt_pk_bf16_f32 v8, v8, v9
v_cvt_pk_bf16_f32 v9, v10, v11
v_cvt_pk_bf16_f32 v10, v24, v25
v_cvt_pk_bf16_f32 v11, v26, v27
s_add_u32 s59, s58, 12288
buffer_store_dwordx4 v[8:11], v205, s[44:47], s59 offen sc1
v_cvt_pk_bf16_f32 v40, v40, v41
v_cvt_pk_bf16_f32 v41, v42, v43
v_cvt_pk_bf16_f32 v42, v56, v57
v_cvt_pk_bf16_f32 v43, v58, v59
s_add_u32 s59, s58, 12288
buffer_store_dwordx4 v[40:43], v205, s[44:47], s59 offen offset:1024 sc1
v_cvt_pk_bf16_f32 v72, v72, v73
v_cvt_pk_bf16_f32 v73, v74, v75
v_cvt_pk_bf16_f32 v74, v88, v89
v_cvt_pk_bf16_f32 v75, v90, v91
s_add_u32 s59, s58, 12288
buffer_store_dwordx4 v[72:75], v205, s[44:47], s59 offen offset:2048 sc1
v_cvt_pk_bf16_f32 v104, v104, v105
v_cvt_pk_bf16_f32 v105, v106, v107
v_cvt_pk_bf16_f32 v106, v120, v121
v_cvt_pk_bf16_f32 v107, v122, v123
s_add_u32 s59, s58, 12288
buffer_store_dwordx4 v[104:107], v205, s[44:47], s59 offen offset:3072 sc1
v_cvt_pk_bf16_f32 v136, v136, v137
v_cvt_pk_bf16_f32 v137, v138, v139
v_cvt_pk_bf16_f32 v138, v152, v153
v_cvt_pk_bf16_f32 v139, v154, v155
s_add_u32 s59, s58, 16384
buffer_store_dwordx4 v[136:139], v205, s[44:47], s59 offen sc1
v_cvt_pk_bf16_f32 v168, v168, v169
v_cvt_pk_bf16_f32 v169, v170, v171
s_add_u32 s59, s58, 16384
buffer_store_dwordx4 v[168:171], v205, s[44:47], s59 offen offset:1024 sc1
v_cvt_pk_bf16_f32 v12, v12, v13
v_cvt_pk_bf16_f32 v13, v14, v15
v_cvt_pk_bf16_f32 v14, v28, v29
v_cvt_pk_bf16_f32 v15, v30, v31
s_add_u32 s59, s58, 16384
buffer_store_dwordx4 v[12:15], v205, s[44:47], s59 offen offset:2048 sc1
v_cvt_pk_bf16_f32 v44, v44, v45
v_cvt_pk_bf16_f32 v45, v46, v47
v_cvt_pk_bf16_f32 v46, v60, v61
v_cvt_pk_bf16_f32 v47, v62, v63
s_add_u32 s59, s58, 16384
buffer_store_dwordx4 v[44:47], v205, s[44:47], s59 offen offset:3072 sc1
v_cvt_pk_bf16_f32 v76, v76, v77
v_cvt_pk_bf16_f32 v77, v78, v79
v_cvt_pk_bf16_f32 v78, v92, v93
v_cvt_pk_bf16_f32 v79, v94, v95
s_add_u32 s59, s58, 20480
buffer_store_dwordx4 v[76:79], v205, s[44:47], s59 offen sc1
v_cvt_pk_bf16_f32 v108, v108, v109
v_cvt_pk_bf16_f32 v109, v110, v111
v_cvt_pk_bf16_f32 v110, v124, v125
v_cvt_pk_bf16_f32 v111, v126, v127
s_add_u32 s59, s58, 20480
buffer_store_dwordx4 v[108:111], v205, s[44:47], s59 offen offset:1024 sc1
v_cvt_pk_bf16_f32 v140, v140, v141
v_cvt_pk_bf16_f32 v141, v142, v143
v_cvt_pk_bf16_f32 v142, v156, v157
v_cvt_pk_bf16_f32 v143, v158, v159
s_add_u32 s59, s58, 20480
buffer_store_dwordx4 v[140:143], v205, s[44:47], s59 offen offset:2048 sc1
v_cvt_pk_bf16_f32 v172, v172, v173
v_cvt_pk_bf16_f32 v173, v174, v175
s_add_u32 s59, s58, 20480
buffer_store_dwordx4 v[172:175], v205, s[44:47], s59 offen offset:3072 sc1
s_waitcnt vmcnt(0)
s_barrier
s_cmp_lg_u32 s19, 0
s_cbranch_scc1 K2_w1_skip
v_mov_b32 v208, 0
v_mov_b32 v209, 1
s_mov_b64 exec, 1
global_atomic_add v208, v209, s[74:75] sc1
s_mov_b32 s60, 0
K2_w1_poll:
global_load_dword v210, v208, s[74:75] sc1
s_waitcnt vmcnt(0)
v_readfirstlane_b32 s61, v210
s_add_u32 s60, s60, 1
s_cmp_ge_u32 s61, 4
s_cbranch_scc1 K2_w1_done
s_sleep 1
s_cmp_lt_u32 s60, 131072
s_cbranch_scc1 K2_w1_poll
K2_w1_done:
s_mov_b64 exec, -1
K2_w1_skip:
s_barrier
s_add_u32 s58, s71, 0
s_mul_i32 s58, s58, 24576
s_add_u32 s59, s58, 4096
buffer_load_dwordx4 v[0:3], v205, s[44:47], s59 offen offset:2048 sc1
s_add_u32 s59, s58, 4096
buffer_load_dwordx4 v[8:11], v205, s[44:47], s59 offen offset:3072 sc1
s_add_u32 s59, s58, 8192
buffer_load_dwordx4 v[12:15], v205, s[44:47], s59 offen offset:0 sc1
s_add_u32 s59, s58, 8192
buffer_load_dwordx4 v[16:19], v205, s[44:47], s59 offen offset:1024 sc1
s_add_u32 s59, s58, 8192
buffer_load_dwordx4 v[24:27], v205, s[44:47], s59 offen offset:2048 sc1
s_add_u32 s59, s58, 8192
buffer_load_dwordx4 v[28:31], v205, s[44:47], s59 offen offset:3072 sc1
s_add_u32 s58, s71, 8
s_mul_i32 s58, s58, 24576
s_add_u32 s59, s58, 4096
buffer_load_dwordx4 v[32:35], v205, s[44:47], s59 offen offset:2048 sc1
s_add_u32 s59, s58, 4096
buffer_load_dwordx4 v[40:43], v205, s[44:47], s59 offen offset:3072 sc1
s_add_u32 s59, s58, 8192
buffer_load_dwordx4 v[44:47], v205, s[44:47], s59 offen offset:0 sc1
s_add_u32 s59, s58, 8192
buffer_load_dwordx4 v[48:51], v205, s[44:47], s59 offen offset:1024 sc1
s_add_u32 s59, s58, 8192
buffer_load_dwordx4 v[56:59], v205, s[44:47], s59 offen offset:2048 sc1
s_add_u32 s59, s58, 8192
buffer_load_dwordx4 v[60:63], v205, s[44:47], s59 offen offset:3072 sc1
s_add_u32 s58, s71, 12
s_mul_i32 s58, s58, 24576
s_add_u32 s59, s58, 4096
buffer_load_dwordx4 v[64:67], v205, s[44:47], s59 offen offset:2048 sc1
s_add_u32 s59, s58, 4096
buffer_load_dwordx4 v[72:75], v205, s[44:47], s59 offen offset:3072 sc1
s_add_u32 s59, s58, 8192
buffer_load_dwordx4 v[76:79], v205, s[44:47], s59 offen offset:0 sc1
s_add_u32 s59, s58, 8192
buffer_load_dwordx4 v[80:83], v205, s[44:47], s59 offen offset:1024 sc1
s_add_u32 s59, s58, 8192
buffer_load_dwordx4 v[88:91], v205, s[44:47], s59 offen offset:2048 sc1
s_add_u32 s59, s58, 8192
buffer_load_dwordx4 v[92:95], v205, s[44:47], s59 offen offset:3072 sc1
s_waitcnt vmcnt(0)
v_lshlrev_b32 v212, 16, v0
v_and_b32 v213, 4294901760, v0
v_lshlrev_b32 v214, 16, v1
v_and_b32 v215, 4294901760, v1
v_pk_add_f32 v[4:5], v[4:5], v[212:213]
v_pk_add_f32 v[6:7], v[6:7], v[214:215]
v_lshlrev_b32 v216, 16, v32
v_and_b32 v217, 4294901760, v32
v_lshlrev_b32 v218, 16, v33
v_and_b32 v219, 4294901760, v33
v_pk_add_f32 v[4:5], v[4:5], v[216:217]
v_pk_add_f32 v[6:7], v[6:7], v[218:219]
v_lshlrev_b32 v220, 16, v64
v_and_b32 v221, 4294901760, v64
v_lshlrev_b32 v222, 16, v65
v_and_b32 v223, 4294901760, v65
v_pk_add_f32 v[4:5], v[4:5], v[220:221]
v_pk_add_f32 v[6:7], v[6:7], v[222:223]
v_cvt_pk_bf16_f32 v4, v4, v5
v_cvt_pk_bf16_f32 v5, v6, v7
s_add_u32 s59, s72, 32
buffer_store_dwordx2 v[4:5], v206, s[48:51], s59 offen
v_lshlrev_b32 v224, 16, v2
v_and_b32 v225, 4294901760, v2
v_lshlrev_b32 v226, 16, v3
v_and_b32 v227, 4294901760, v3
v_pk_add_f32 v[20:21], v[20:21], v[224:225]
v_pk_add_f32 v[22:23], v[22:23], v[226:227]
v_lshlrev_b32 v212, 16, v34
v_and_b32 v213, 4294901760, v34
v_lshlrev_b32 v214, 16, v35
v_and_b32 v215, 4294901760, v35
v_pk_add_f32 v[20:21], v[20:21], v[212:213]
v_pk_add_f32 v[22:23], v[22:23], v[214:215]
v_lshlrev_b32 v216, 16, v66
v_and_b32 v217, 4294901760, v66
v_lshlrev_b32 v218, 16, v67
v_and_b32 v219, 4294901760, v67
v_pk_add_f32 v[20:21], v[20:21], v[216:217]
v_pk_add_f32 v[22:23], v[22:23], v[218:219]
v_cvt_pk_bf16_f32 v20, v20, v21
v_cvt_pk_bf16_f32 v21, v22, v23
s_add_u32 s59, s72, 40960
buffer_store_dwordx2 v[20:21], v206, s[48:51], s59 offen
v_lshlrev_b32 v220, 16, v8
v_and_b32 v221, 4294901760, v8
v_lshlrev_b32 v222, 16, v9
v_and_b32 v223, 4294901760, v9
v_pk_add_f32 v[36:37], v[36:37], v[220:221]
v_pk_add_f32 v[38:39], v[38:39], v[222:223]
v_lshlrev_b32 v224, 16, v40
v_and_b32 v225, 4294901760, v40
v_lshlrev_b32 v226, 16, v41
v_and_b32 v227, 4294901760, v41
v_pk_add_f32 v[36:37], v[36:37], v[224:225]
v_pk_add_f32 v[38:39], v[38:39], v[226:227]
v_lshlrev_b32 v212, 16, v72
v_and_b32 v213, 4294901760, v72
v_lshlrev_b32 v214, 16, v73
v_and_b32 v215, 4294901760, v73
v_pk_add_f32 v[36:37], v[36:37], v[212:213]
v_pk_add_f32 v[38:39], v[38:39], v[214:215]
v_cvt_pk_bf16_f32 v36, v36, v37
v_cvt_pk_bf16_f32 v37, v38, v39
s_add_u32 s59, s72, 41088
buffer_store_dwordx2 v[36:37], v206, s[48:51], s59 offen
v_lshlrev_b32 v216, 16, v10
v_and_b32 v217, 4294901760, v10
v_lshlrev_b32 v218, 16, v11
v_and_b32 v219, 4294901760, v11
v_pk_add_f32 v[52:53], v[52:53], v[216:217]
v_pk_add_f32 v[54:55], v[54:55], v[218:219]
v_lshlrev_b32 v220, 16, v42
v_and_b32 v221, 4294901760, v42
v_lshlrev_b32 v222, 16, v43
v_and_b32 v223, 4294901760, v43
v_pk_add_f32 v[52:53], v[52:53], v[220:221]
v_pk_add_f32 v[54:55], v[54:55], v[222:223]
v_lshlrev_b32 v224, 16, v74
v_and_b32 v225, 4294901760, v74
v_lshlrev_b32 v226, 16, v75
v_and_b32 v227, 4294901760, v75
v_pk_add_f32 v[52:53], v[52:53], v[224:225]
v_pk_add_f32 v[54:55], v[54:55], v[226:227]
v_cvt_pk_bf16_f32 v52, v52, v53
v_cvt_pk_bf16_f32 v53, v54, v55
s_add_u32 s59, s72, 82016
buffer_store_dwordx2 v[52:53], v206, s[48:51], s59 offen
v_lshlrev_b32 v212, 16, v12
v_and_b32 v213, 4294901760, v12
v_lshlrev_b32 v214, 16, v13
v_and_b32 v215, 4294901760, v13
v_pk_add_f32 v[68:69], v[68:69], v[212:213]
v_pk_add_f32 v[70:71], v[70:71], v[214:215]
v_lshlrev_b32 v216, 16, v44
v_and_b32 v217, 4294901760, v44
v_lshlrev_b32 v218, 16, v45
v_and_b32 v219, 4294901760, v45
v_pk_add_f32 v[68:69], v[68:69], v[216:217]
v_pk_add_f32 v[70:71], v[70:71], v[218:219]
v_lshlrev_b32 v220, 16, v76
v_and_b32 v221, 4294901760, v76
v_lshlrev_b32 v222, 16, v77
v_and_b32 v223, 4294901760, v77
v_pk_add_f32 v[68:69], v[68:69], v[220:221]
v_pk_add_f32 v[70:71], v[70:71], v[222:223]
v_cvt_pk_bf16_f32 v68, v68, v69
v_cvt_pk_bf16_f32 v69, v70, v71
s_add_u32 s59, s72, 122944
buffer_store_dwordx2 v[68:69], v206, s[48:51], s59 offen
v_lshlrev_b32 v224, 16, v14
v_and_b32 v225, 4294901760, v14
v_lshlrev_b32 v226, 16, v15
v_and_b32 v227, 4294901760, v15
v_pk_add_f32 v[84:85], v[84:85], v[224:225]
v_pk_add_f32 v[86:87], v[86:87], v[226:227]
v_lshlrev_b32 v212, 16, v46
v_and_b32 v213, 4294901760, v46
v_lshlrev_b32 v214, 16, v47
v_and_b32 v215, 4294901760, v47
v_pk_add_f32 v[84:85], v[84:85], v[212:213]
v_pk_add_f32 v[86:87], v[86:87], v[214:215]
v_lshlrev_b32 v216, 16, v78
v_and_b32 v217, 4294901760, v78
v_lshlrev_b32 v218, 16, v79
v_and_b32 v219, 4294901760, v79
v_pk_add_f32 v[84:85], v[84:85], v[216:217]
v_pk_add_f32 v[86:87], v[86:87], v[218:219]
v_cvt_pk_bf16_f32 v84, v84, v85
v_cvt_pk_bf16_f32 v85, v86, v87
s_add_u32 s59, s72, 163872
buffer_store_dwordx2 v[84:85], v206, s[48:51], s59 offen
v_lshlrev_b32 v220, 16, v16
v_and_b32 v221, 4294901760, v16
v_lshlrev_b32 v222, 16, v17
v_and_b32 v223, 4294901760, v17
v_pk_add_f32 v[100:101], v[100:101], v[220:221]
v_pk_add_f32 v[102:103], v[102:103], v[222:223]
v_lshlrev_b32 v224, 16, v48
v_and_b32 v225, 4294901760, v48
v_lshlrev_b32 v226, 16, v49
v_and_b32 v227, 4294901760, v49
v_pk_add_f32 v[100:101], v[100:101], v[224:225]
v_pk_add_f32 v[102:103], v[102:103], v[226:227]
v_lshlrev_b32 v212, 16, v80
v_and_b32 v213, 4294901760, v80
v_lshlrev_b32 v214, 16, v81
v_and_b32 v215, 4294901760, v81
v_pk_add_f32 v[100:101], v[100:101], v[212:213]
v_pk_add_f32 v[102:103], v[102:103], v[214:215]
v_cvt_pk_bf16_f32 v100, v100, v101
v_cvt_pk_bf16_f32 v101, v102, v103
s_add_u32 s59, s72, 204800
buffer_store_dwordx2 v[100:101], v206, s[48:51], s59 offen
v_lshlrev_b32 v216, 16, v18
v_and_b32 v217, 4294901760, v18
v_lshlrev_b32 v218, 16, v19
v_and_b32 v219, 4294901760, v19
v_pk_add_f32 v[116:117], v[116:117], v[216:217]
v_pk_add_f32 v[118:119], v[118:119], v[218:219]
v_lshlrev_b32 v220, 16, v50
v_and_b32 v221, 4294901760, v50
v_lshlrev_b32 v222, 16, v51
v_and_b32 v223, 4294901760, v51
v_pk_add_f32 v[116:117], v[116:117], v[220:221]
v_pk_add_f32 v[118:119], v[118:119], v[222:223]
v_lshlrev_b32 v224, 16, v82
v_and_b32 v225, 4294901760, v82
v_lshlrev_b32 v226, 16, v83
v_and_b32 v227, 4294901760, v83
v_pk_add_f32 v[116:117], v[116:117], v[224:225]
v_pk_add_f32 v[118:119], v[118:119], v[226:227]
v_cvt_pk_bf16_f32 v116, v116, v117
v_cvt_pk_bf16_f32 v117, v118, v119
s_add_u32 s59, s72, 204928
buffer_store_dwordx2 v[116:117], v206, s[48:51], s59 offen
v_lshlrev_b32 v212, 16, v24
v_and_b32 v213, 4294901760, v24
v_lshlrev_b32 v214, 16, v25
v_and_b32 v215, 4294901760, v25
v_pk_add_f32 v[132:133], v[132:133], v[212:213]
v_pk_add_f32 v[134:135], v[134:135], v[214:215]
v_lshlrev_b32 v216, 16, v56
v_and_b32 v217, 4294901760, v56
v_lshlrev_b32 v218, 16, v57
v_and_b32 v219, 4294901760, v57
v_pk_add_f32 v[132:133], v[132:133], v[216:217]
v_pk_add_f32 v[134:135], v[134:135], v[218:219]
v_lshlrev_b32 v220, 16, v88
v_and_b32 v221, 4294901760, v88
v_lshlrev_b32 v222, 16, v89
v_and_b32 v223, 4294901760, v89
v_pk_add_f32 v[132:133], v[132:133], v[220:221]
v_pk_add_f32 v[134:135], v[134:135], v[222:223]
v_cvt_pk_bf16_f32 v132, v132, v133
v_cvt_pk_bf16_f32 v133, v134, v135
s_add_u32 s59, s72, 245856
buffer_store_dwordx2 v[132:133], v206, s[48:51], s59 offen
v_lshlrev_b32 v224, 16, v26
v_and_b32 v225, 4294901760, v26
v_lshlrev_b32 v226, 16, v27
v_and_b32 v227, 4294901760, v27
v_pk_add_f32 v[148:149], v[148:149], v[224:225]
v_pk_add_f32 v[150:151], v[150:151], v[226:227]
v_lshlrev_b32 v212, 16, v58
v_and_b32 v213, 4294901760, v58
v_lshlrev_b32 v214, 16, v59
v_and_b32 v215, 4294901760, v59
v_pk_add_f32 v[148:149], v[148:149], v[212:213]
v_pk_add_f32 v[150:151], v[150:151], v[214:215]
v_lshlrev_b32 v216, 16, v90
v_and_b32 v217, 4294901760, v90
v_lshlrev_b32 v218, 16, v91
v_and_b32 v219, 4294901760, v91
v_pk_add_f32 v[148:149], v[148:149], v[216:217]
v_pk_add_f32 v[150:151], v[150:151], v[218:219]
v_cvt_pk_bf16_f32 v148, v148, v149
v_cvt_pk_bf16_f32 v149, v150, v151
s_add_u32 s59, s72, 286784
buffer_store_dwordx2 v[148:149], v206, s[48:51], s59 offen
v_lshlrev_b32 v220, 16, v28
v_and_b32 v221, 4294901760, v28
v_lshlrev_b32 v222, 16, v29
v_and_b32 v223, 4294901760, v29
v_pk_add_f32 v[164:165], v[164:165], v[220:221]
v_pk_add_f32 v[166:167], v[166:167], v[222:223]
v_lshlrev_b32 v224, 16, v60
v_and_b32 v225, 4294901760, v60
v_lshlrev_b32 v226, 16, v61
v_and_b32 v227, 4294901760, v61
v_pk_add_f32 v[164:165], v[164:165], v[224:225]
v_pk_add_f32 v[166:167], v[166:167], v[226:227]
v_lshlrev_b32 v212, 16, v92
v_and_b32 v213, 4294901760, v92
v_lshlrev_b32 v214, 16, v93
v_and_b32 v215, 4294901760, v93
v_pk_add_f32 v[164:165], v[164:165], v[212:213]
v_pk_add_f32 v[166:167], v[166:167], v[214:215]
v_cvt_pk_bf16_f32 v164, v164, v165
v_cvt_pk_bf16_f32 v165, v166, v167
s_add_u32 s59, s72, 327712
buffer_store_dwordx2 v[164:165], v206, s[48:51], s59 offen
s_waitcnt vmcnt(0)
s_branch K2_end
K2_fin2:
s_add_u32 s58, s71, 8
s_mul_i32 s58, s58, 24576
v_cvt_pk_bf16_f32 v0, v0, v1
v_cvt_pk_bf16_f32 v1, v2, v3
v_cvt_pk_bf16_f32 v2, v16, v17
v_cvt_pk_bf16_f32 v3, v18, v19
s_add_u32 s59, s58, 0
buffer_store_dwordx4 v[0:3], v205, s[44:47], s59 offen sc1
v_cvt_pk_bf16_f32 v32, v32, v33
v_cvt_pk_bf16_f32 v33, v34, v35
v_cvt_pk_bf16_f32 v34, v48, v49
v_cvt_pk_bf16_f32 v35, v50, v51
s_add_u32 s59, s58, 0
buffer_store_dwordx4 v[32:35], v205, s[44:47], s59 offen offset:1024 sc1
v_cvt_pk_bf16_f32 v64, v64, v65
v_cvt_pk_bf16_f32 v65, v66, v67
v_cvt_pk_bf16_f32 v66, v80, v81
v_cvt_pk_bf16_f32 v67, v82, v83
s_add_u32 s59, s58, 0
buffer_store_dwordx4 v[64:67], v205, s[44:47], s59 offen offset:2048 sc1
v_cvt_pk_bf16_f32 v96, v96, v97
v_cvt_pk_bf16_f32 v97, v98, v99
v_cvt_pk_bf16_f32 v98, v112, v113
v_cvt_pk_bf16_f32 v99, v114, v115
s_add_u32 s59, s58, 0
buffer_store_dwordx4 v[96:99], v205, s[44:47], s59 offen offset:3072 sc1
v_cvt_pk_bf16_f32 v128, v128, v129
v_cvt_pk_bf16_f32 v129, v130, v131
v_cvt_pk_bf16_f32 v130, v144, v145
v_cvt_pk_bf16_f32 v131, v146, v147
s_add_u32 s59, s58, 4096
buffer_store_dwordx4 v[128:131], v205, s[44:47], s59 offen sc1
v_cvt_pk_bf16_f32 v160, v160, v161
v_cvt_pk_bf16_f32 v161, v162, v163
v_cvt_pk_bf16_f32 v162, v176, v177
v_cvt_pk_bf16_f32 v163, v178, v179
s_add_u32 s59, s58, 4096
buffer_store_dwordx4 v[160:163], v205, s[44:47], s59 offen offset:1024 sc1
v_cvt_pk_bf16_f32 v4, v4, v5
v_cvt_pk_bf16_f32 v5, v6, v7
v_cvt_pk_bf16_f32 v6, v20, v21
v_cvt_pk_bf16_f32 v7, v22, v23
s_add_u32 s59, s58, 4096
buffer_store_dwordx4 v[4:7], v205, s[44:47], s59 offen offset:2048 sc1
v_cvt_pk_bf16_f32 v36, v36, v37
v_cvt_pk_bf16_f32 v37, v38, v39
v_cvt_pk_bf16_f32 v38, v52, v53
v_cvt_pk_bf16_f32 v39, v54, v55
s_add_u32 s59, s58, 4096
buffer_store_dwordx4 v[36:39], v205, s[44:47], s59 offen offset:3072 sc1
v_cvt_pk_bf16_f32 v68, v68, v69
v_cvt_pk_bf16_f32 v69, v70, v71
v_cvt_pk_bf16_f32 v70, v84, v85
v_cvt_pk_bf16_f32 v71, v86, v87
s_add_u32 s59, s58, 8192
buffer_store_dwordx4 v[68:71], v205, s[44:47], s59 offen sc1
v_cvt_pk_bf16_f32 v100, v100, v101
v_cvt_pk_bf16_f32 v101, v102, v103
v_cvt_pk_bf16_f32 v102, v116, v117
v_cvt_pk_bf16_f32 v103, v118, v119
s_add_u32 s59, s58, 8192
buffer_store_dwordx4 v[100:103], v205, s[44:47], s59 offen offset:1024 sc1
v_cvt_pk_bf16_f32 v132, v132, v133
v_cvt_pk_bf16_f32 v133, v134, v135
v_cvt_pk_bf16_f32 v134, v148, v149
v_cvt_pk_bf16_f32 v135, v150, v151
s_add_u32 s59, s58, 8192
buffer_store_dwordx4 v[132:135], v205, s[44:47], s59 offen offset:2048 sc1
v_cvt_pk_bf16_f32 v164, v164, v165
v_cvt_pk_bf16_f32 v165, v166, v167
s_add_u32 s59, s58, 8192
buffer_store_dwordx4 v[164:167], v205, s[44:47], s59 offen offset:3072 sc1
v_cvt_pk_bf16_f32 v12, v12, v13
v_cvt_pk_bf16_f32 v13, v14, v15
v_cvt_pk_bf16_f32 v14, v28, v29
v_cvt_pk_bf16_f32 v15, v30, v31
s_add_u32 s59, s58, 16384
buffer_store_dwordx4 v[12:15], v205, s[44:47], s59 offen offset:2048 sc1
v_cvt_pk_bf16_f32 v44, v44, v45
v_cvt_pk_bf16_f32 v45, v46, v47
v_cvt_pk_bf16_f32 v46, v60, v61
v_cvt_pk_bf16_f32 v47, v62, v63
s_add_u32 s59, s58, 16384
buffer_store_dwordx4 v[44:47], v205, s[44:47], s59 offen offset:3072 sc1
v_cvt_pk_bf16_f32 v76, v76, v77
v_cvt_pk_bf16_f32 v77, v78, v79
v_cvt_pk_bf16_f32 v78, v92, v93
v_cvt_pk_bf16_f32 v79, v94, v95
s_add_u32 s59, s58, 20480
buffer_store_dwordx4 v[76:79], v205, s[44:47], s59 offen sc1
v_cvt_pk_bf16_f32 v108, v108, v109
v_cvt_pk_bf16_f32 v109, v110, v111
v_cvt_pk_bf16_f32 v110, v124, v125
v_cvt_pk_bf16_f32 v111, v126, v127
s_add_u32 s59, s58, 20480
buffer_store_dwordx4 v[108:111], v205, s[44:47], s59 offen offset:1024 sc1
v_cvt_pk_bf16_f32 v140, v140, v141
v_cvt_pk_bf16_f32 v141, v142, v143
v_cvt_pk_bf16_f32 v142, v156, v157
v_cvt_pk_bf16_f32 v143, v158, v159
s_add_u32 s59, s58, 20480
buffer_store_dwordx4 v[140:143], v205, s[44:47], s59 offen offset:2048 sc1
v_cvt_pk_bf16_f32 v172, v172, v173
v_cvt_pk_bf16_f32 v173, v174, v175
s_add_u32 s59, s58, 20480
buffer_store_dwordx4 v[172:175], v205, s[44:47], s59 offen offset:3072 sc1
s_waitcnt vmcnt(0)
s_barrier
s_cmp_lg_u32 s19, 0
s_cbranch_scc1 K2_w2_skip
v_mov_b32 v208, 0
v_mov_b32 v209, 1
s_mov_b64 exec, 1
global_atomic_add v208, v209, s[74:75] sc1
s_mov_b32 s60, 0
K2_w2_poll:
global_load_dword v210, v208, s[74:75] sc1
s_waitcnt vmcnt(0)
v_readfirstlane_b32 s61, v210
s_add_u32 s60, s60, 1
s_cmp_ge_u32 s61, 4
s_cbranch_scc1 K2_w2_done
s_sleep 1
s_cmp_lt_u32 s60, 131072
s_cbranch_scc1 K2_w2_poll
K2_w2_done:
s_mov_b64 exec, -1
K2_w2_skip:
s_barrier
s_add_u32 s58, s71, 0
s_mul_i32 s58, s58, 24576
s_add_u32 s59, s58, 12288
buffer_load_dwordx4 v[0:3], v205, s[44:47], s59 offen offset:0 sc1
s_add_u32 s59, s58, 12288
buffer_load_dwordx4 v[4:7], v205, s[44:47], s59 offen offset:1024 sc1
s_add_u32 s59, s58, 12288
buffer_load_dwordx4 v[12:15], v205, s[44:47], s59 offen offset:2048 sc1
s_add_u32 s59, s58, 12288
buffer_load_dwordx4 v[16:19], v205, s[44:47], s59 offen offset:3072 sc1
s_add_u32 s59, s58, 16384
buffer_load_dwordx4 v[20:23], v205, s[44:47], s59 offen offset:0 sc1
s_add_u32 s59, s58, 16384
buffer_load_dwordx4 v[28:31], v205, s[44:47], s59 offen offset:1024 sc1
s_add_u32 s58, s71, 4
s_mul_i32 s58, s58, 24576
s_add_u32 s59, s58, 12288
buffer_load_dwordx4 v[32:35], v205, s[44:47], s59 offen offset:0 sc1
s_add_u32 s59, s58, 12288
buffer_load_dwordx4 v[36:39], v205, s[44:47], s59 offen offset:1024 sc1
s_add_u32 s59, s58, 12288
buffer_load_dwordx4 v[44:47], v205, s[44:47], s59 offen offset:2048 sc1
s_add_u32 s59, s58, 12288
buffer_load_dwordx4 v[48:51], v205, s[44:47], s59 offen offset:3072 sc1
s_add_u32 s59, s58, 16384
buffer_load_dwordx4 v[52:55], v205, s[44:47], s59 offen offset:0 sc1
s_add_u32 s59, s58, 16384
buffer_load_dwordx4 v[60:63], v205, s[44:47], s59 offen offset:1024 sc1
s_add_u32 s58, s71, 12
s_mul_i32 s58, s58, 24576
s_add_u32 s59, s58, 12288
buffer_load_dwordx4 v[64:67], v205, s[44:47], s59 offen offset:0 sc1
s_add_u32 s59, s58, 12288
buffer_load_dwordx4 v[68:71], v205, s[44:47], s59 offen offset:1024 sc1
s_add_u32 s59, s58, 12288
buffer_load_dwordx4 v[76:79], v205, s[44:47], s59 offen offset:2048 sc1
s_add_u32 s59, s58, 12288
buffer_load_dwordx4 v[80:83], v205, s[44:47], s59 offen offset:3072 sc1
s_add_u32 s59, s58, 16384
buffer_load_dwordx4 v[84:87], v205, s[44:47], s59 offen offset:0 sc1
s_add_u32 s59, s58, 16384
buffer_load_dwordx4 v[92:95], v205, s[44:47], s59 offen offset:1024 sc1
s_waitcnt vmcnt(0)
v_lshlrev_b32 v212, 16, v0
v_and_b32 v213, 4294901760, v0
v_lshlrev_b32 v214, 16, v1
v_and_b32 v215, 4294901760, v1
v_pk_add_f32 v[8:9], v[8:9], v[212:213]
v_pk_add_f32 v[10:11], v[10:11], v[214:215]
v_lshlrev_b32 v216, 16, v32
v_and_b32 v217, 4294901760, v32
v_lshlrev_b32 v218, 16, v33
v_and_b32 v219, 4294901760, v33
v_pk_add_f32 v[8:9], v[8:9], v[216:217]
v_pk_add_f32 v[10:11], v[10:11], v[218:219]
v_lshlrev_b32 v220, 16, v64
v_and_b32 v221, 4294901760, v64
v_lshlrev_b32 v222, 16, v65
v_and_b32 v223, 4294901760, v65
v_pk_add_f32 v[8:9], v[8:9], v[220:221]
v_pk_add_f32 v[10:11], v[10:11], v[222:223]
v_cvt_pk_bf16_f32 v8, v8, v9
v_cvt_pk_bf16_f32 v9, v10, v11
s_add_u32 s59, s72, 64
buffer_store_dwordx2 v[8:9], v206, s[48:51], s59 offen
v_lshlrev_b32 v224, 16, v2
v_and_b32 v225, 4294901760, v2
v_lshlrev_b32 v226, 16, v3
v_and_b32 v227, 4294901760, v3
v_pk_add_f32 v[24:25], v[24:25], v[224:225]
v_pk_add_f32 v[26:27], v[26:27], v[226:227]
v_lshlrev_b32 v212, 16, v34
v_and_b32 v213, 4294901760, v34
v_lshlrev_b32 v214, 16, v35
v_and_b32 v215, 4294901760, v35
v_pk_add_f32 v[24:25], v[24:25], v[212:213]
v_pk_add_f32 v[26:27], v[26:27], v[214:215]
v_lshlrev_b32 v216, 16, v66
v_and_b32 v217, 4294901760, v66
v_lshlrev_b32 v218, 16, v67
v_and_b32 v219, 4294901760, v67
v_pk_add_f32 v[24:25], v[24:25], v[216:217]
v_pk_add_f32 v[26:27], v[26:27], v[218:219]
v_cvt_pk_bf16_f32 v24, v24, v25
v_cvt_pk_bf16_f32 v25, v26, v27
s_add_u32 s59, s72, 40992
buffer_store_dwordx2 v[24:25], v206, s[48:51], s59 offen
v_lshlrev_b32 v220, 16, v4
v_and_b32 v221, 4294901760, v4
v_lshlrev_b32 v222, 16, v5
v_and_b32 v223, 4294901760, v5
v_pk_add_f32 v[40:41], v[40:41], v[220:221]
v_pk_add_f32 v[42:43], v[42:43], v[222:223]
v_lshlrev_b32 v224, 16, v36
v_and_b32 v225, 4294901760, v36
v_lshlrev_b32 v226, 16, v37
v_and_b32 v227, 4294901760, v37
v_pk_add_f32 v[40:41], v[40:41], v[224:225]
v_pk_add_f32 v[42:43], v[42:43], v[226:227]
v_lshlrev_b32 v212, 16, v68
v_and_b32 v213, 4294901760, v68
v_lshlrev_b32 v214, 16, v69
v_and_b32 v215, 4294901760, v69
v_pk_add_f32 v[40:41], v[40:41], v[212:213]
v_pk_add_f32 v[42:43], v[42:43], v[214:215]
v_cvt_pk_bf16_f32 v40, v40, v41
v_cvt_pk_bf16_f32 v41, v42, v43
s_add_u32 s59, s72, 81920
buffer_store_dwordx2 v[40:41], v206, s[48:51], s59 offen
v_lshlrev_b32 v216, 16, v6
v_and_b32 v217, 4294901760, v6
v_lshlrev_b32 v218, 16, v7
v_and_b32 v219, 4294901760, v7
v_pk_add_f32 v[56:57], v[56:57], v[216:217]
v_pk_add_f32 v[58:59], v[58:59], v[218:219]
v_lshlrev_b32 v220, 16, v38
v_and_b32 v221, 4294901760, v38
v_lshlrev_b32 v222, 16, v39
v_and_b32 v223, 4294901760, v39
v_pk_add_f32 v[56:57], v[56:57], v[220:221]
v_pk_add_f32 v[58:59], v[58:59], v[222:223]
v_lshlrev_b32 v224, 16, v70
v_and_b32 v225, 4294901760, v70
v_lshlrev_b32 v226, 16, v71
v_and_b32 v227, 4294901760, v71
v_pk_add_f32 v[56:57], v[56:57], v[224:225]
v_pk_add_f32 v[58:59], v[58:59], v[226:227]
v_cvt_pk_bf16_f32 v56, v56, v57
v_cvt_pk_bf16_f32 v57, v58, v59
s_add_u32 s59, s72, 82048
buffer_store_dwordx2 v[56:57], v206, s[48:51], s59 offen
v_lshlrev_b32 v212, 16, v12
v_and_b32 v213, 4294901760, v12
v_lshlrev_b32 v214, 16, v13
v_and_b32 v215, 4294901760, v13
v_pk_add_f32 v[72:73], v[72:73], v[212:213]
v_pk_add_f32 v[74:75], v[74:75], v[214:215]
v_lshlrev_b32 v216, 16, v44
v_and_b32 v217, 4294901760, v44
v_lshlrev_b32 v218, 16, v45
v_and_b32 v219, 4294901760, v45
v_pk_add_f32 v[72:73], v[72:73], v[216:217]
v_pk_add_f32 v[74:75], v[74:75], v[218:219]
v_lshlrev_b32 v220, 16, v76
v_and_b32 v221, 4294901760, v76
v_lshlrev_b32 v222, 16, v77
v_and_b32 v223, 4294901760, v77
v_pk_add_f32 v[72:73], v[72:73], v[220:221]
v_pk_add_f32 v[74:75], v[74:75], v[222:223]
v_cvt_pk_bf16_f32 v72, v72, v73
v_cvt_pk_bf16_f32 v73, v74, v75
s_add_u32 s59, s72, 122976
buffer_store_dwordx2 v[72:73], v206, s[48:51], s59 offen
v_lshlrev_b32 v224, 16, v14
v_and_b32 v225, 4294901760, v14
v_lshlrev_b32 v226, 16, v15
v_and_b32 v227, 4294901760, v15
v_pk_add_f32 v[88:89], v[88:89], v[224:225]
v_pk_add_f32 v[90:91], v[90:91], v[226:227]
v_lshlrev_b32 v212, 16, v46
v_and_b32 v213, 4294901760, v46
v_lshlrev_b32 v214, 16, v47
v_and_b32 v215, 4294901760, v47
v_pk_add_f32 v[88:89], v[88:89], v[212:213]
v_pk_add_f32 v[90:91], v[90:91], v[214:215]
v_lshlrev_b32 v216, 16, v78
v_and_b32 v217, 4294901760, v78
v_lshlrev_b32 v218, 16, v79
v_and_b32 v219, 4294901760, v79
v_pk_add_f32 v[88:89], v[88:89], v[216:217]
v_pk_add_f32 v[90:91], v[90:91], v[218:219]
v_cvt_pk_bf16_f32 v88, v88, v89
v_cvt_pk_bf16_f32 v89, v90, v91
s_add_u32 s59, s72, 163904
buffer_store_dwordx2 v[88:89], v206, s[48:51], s59 offen
v_lshlrev_b32 v220, 16, v16
v_and_b32 v221, 4294901760, v16
v_lshlrev_b32 v222, 16, v17
v_and_b32 v223, 4294901760, v17
v_pk_add_f32 v[104:105], v[104:105], v[220:221]
v_pk_add_f32 v[106:107], v[106:107], v[222:223]
v_lshlrev_b32 v224, 16, v48
v_and_b32 v225, 4294901760, v48
v_lshlrev_b32 v226, 16, v49
v_and_b32 v227, 4294901760, v49
v_pk_add_f32 v[104:105], v[104:105], v[224:225]
v_pk_add_f32 v[106:107], v[106:107], v[226:227]
v_lshlrev_b32 v212, 16, v80
v_and_b32 v213, 4294901760, v80
v_lshlrev_b32 v214, 16, v81
v_and_b32 v215, 4294901760, v81
v_pk_add_f32 v[104:105], v[104:105], v[212:213]
v_pk_add_f32 v[106:107], v[106:107], v[214:215]
v_cvt_pk_bf16_f32 v104, v104, v105
v_cvt_pk_bf16_f32 v105, v106, v107
s_add_u32 s59, s72, 204832
buffer_store_dwordx2 v[104:105], v206, s[48:51], s59 offen
v_lshlrev_b32 v216, 16, v18
v_and_b32 v217, 4294901760, v18
v_lshlrev_b32 v218, 16, v19
v_and_b32 v219, 4294901760, v19
v_pk_add_f32 v[120:121], v[120:121], v[216:217]
v_pk_add_f32 v[122:123], v[122:123], v[218:219]
v_lshlrev_b32 v220, 16, v50
v_and_b32 v221, 4294901760, v50
v_lshlrev_b32 v222, 16, v51
v_and_b32 v223, 4294901760, v51
v_pk_add_f32 v[120:121], v[120:121], v[220:221]
v_pk_add_f32 v[122:123], v[122:123], v[222:223]
v_lshlrev_b32 v224, 16, v82
v_and_b32 v225, 4294901760, v82
v_lshlrev_b32 v226, 16, v83
v_and_b32 v227, 4294901760, v83
v_pk_add_f32 v[120:121], v[120:121], v[224:225]
v_pk_add_f32 v[122:123], v[122:123], v[226:227]
v_cvt_pk_bf16_f32 v120, v120, v121
v_cvt_pk_bf16_f32 v121, v122, v123
s_add_u32 s59, s72, 245760
buffer_store_dwordx2 v[120:121], v206, s[48:51], s59 offen
v_lshlrev_b32 v212, 16, v20
v_and_b32 v213, 4294901760, v20
v_lshlrev_b32 v214, 16, v21
v_and_b32 v215, 4294901760, v21
v_pk_add_f32 v[136:137], v[136:137], v[212:213]
v_pk_add_f32 v[138:139], v[138:139], v[214:215]
v_lshlrev_b32 v216, 16, v52
v_and_b32 v217, 4294901760, v52
v_lshlrev_b32 v218, 16, v53
v_and_b32 v219, 4294901760, v53
v_pk_add_f32 v[136:137], v[136:137], v[216:217]
v_pk_add_f32 v[138:139], v[138:139], v[218:219]
v_lshlrev_b32 v220, 16, v84
v_and_b32 v221, 4294901760, v84
v_lshlrev_b32 v222, 16, v85
v_and_b32 v223, 4294901760, v85
v_pk_add_f32 v[136:137], v[136:137], v[220:221]
v_pk_add_f32 v[138:139], v[138:139], v[222:223]
v_cvt_pk_bf16_f32 v136, v136, v137
v_cvt_pk_bf16_f32 v137, v138, v139
s_add_u32 s59, s72, 245888
buffer_store_dwordx2 v[136:137], v206, s[48:51], s59 offen
v_lshlrev_b32 v224, 16, v22
v_and_b32 v225, 4294901760, v22
v_lshlrev_b32 v226, 16, v23
v_and_b32 v227, 4294901760, v23
v_pk_add_f32 v[152:153], v[152:153], v[224:225]
v_pk_add_f32 v[154:155], v[154:155], v[226:227]
v_lshlrev_b32 v212, 16, v54
v_and_b32 v213, 4294901760, v54
v_lshlrev_b32 v214, 16, v55
v_and_b32 v215, 4294901760, v55
v_pk_add_f32 v[152:153], v[152:153], v[212:213]
v_pk_add_f32 v[154:155], v[154:155], v[214:215]
v_lshlrev_b32 v216, 16, v86
v_and_b32 v217, 4294901760, v86
v_lshlrev_b32 v218, 16, v87
v_and_b32 v219, 4294901760, v87
v_pk_add_f32 v[152:153], v[152:153], v[216:217]
v_pk_add_f32 v[154:155], v[154:155], v[218:219]
v_cvt_pk_bf16_f32 v152, v152, v153
v_cvt_pk_bf16_f32 v153, v154, v155
s_add_u32 s59, s72, 286816
buffer_store_dwordx2 v[152:153], v206, s[48:51], s59 offen
v_lshlrev_b32 v220, 16, v28
v_and_b32 v221, 4294901760, v28
v_lshlrev_b32 v222, 16, v29
v_and_b32 v223, 4294901760, v29
v_pk_add_f32 v[168:169], v[168:169], v[220:221]
v_pk_add_f32 v[170:171], v[170:171], v[222:223]
v_lshlrev_b32 v224, 16, v60
v_and_b32 v225, 4294901760, v60
v_lshlrev_b32 v226, 16, v61
v_and_b32 v227, 4294901760, v61
v_pk_add_f32 v[168:169], v[168:169], v[224:225]
v_pk_add_f32 v[170:171], v[170:171], v[226:227]
v_lshlrev_b32 v212, 16, v92
v_and_b32 v213, 4294901760, v92
v_lshlrev_b32 v214, 16, v93
v_and_b32 v215, 4294901760, v93
v_pk_add_f32 v[168:169], v[168:169], v[212:213]
v_pk_add_f32 v[170:171], v[170:171], v[214:215]
v_cvt_pk_bf16_f32 v168, v168, v169
v_cvt_pk_bf16_f32 v169, v170, v171
s_add_u32 s59, s72, 327744
buffer_store_dwordx2 v[168:169], v206, s[48:51], s59 offen
s_waitcnt vmcnt(0)
s_branch K2_end
K2_fin3:
s_add_u32 s58, s71, 12
s_mul_i32 s58, s58, 24576
v_cvt_pk_bf16_f32 v0, v0, v1
v_cvt_pk_bf16_f32 v1, v2, v3
v_cvt_pk_bf16_f32 v2, v16, v17
v_cvt_pk_bf16_f32 v3, v18, v19
s_add_u32 s59, s58, 0
buffer_store_dwordx4 v[0:3], v205, s[44:47], s59 offen sc1
v_cvt_pk_bf16_f32 v32, v32, v33
v_cvt_pk_bf16_f32 v33, v34, v35
v_cvt_pk_bf16_f32 v34, v48, v49
v_cvt_pk_bf16_f32 v35, v50, v51
s_add_u32 s59, s58, 0
buffer_store_dwordx4 v[32:35], v205, s[44:47], s59 offen offset:1024 sc1
v_cvt_pk_bf16_f32 v64, v64, v65
v_cvt_pk_bf16_f32 v65, v66, v67
v_cvt_pk_bf16_f32 v66, v80, v81
v_cvt_pk_bf16_f32 v67, v82, v83
s_add_u32 s59, s58, 0
buffer_store_dwordx4 v[64:67], v205, s[44:47], s59 offen offset:2048 sc1
v_cvt_pk_bf16_f32 v96, v96, v97
v_cvt_pk_bf16_f32 v97, v98, v99
v_cvt_pk_bf16_f32 v98, v112, v113
v_cvt_pk_bf16_f32 v99, v114, v115
s_add_u32 s59, s58, 0
buffer_store_dwordx4 v[96:99], v205, s[44:47], s59 offen offset:3072 sc1
v_cvt_pk_bf16_f32 v128, v128, v129
v_cvt_pk_bf16_f32 v129, v130, v131
v_cvt_pk_bf16_f32 v130, v144, v145
v_cvt_pk_bf16_f32 v131, v146, v147
s_add_u32 s59, s58, 4096
buffer_store_dwordx4 v[128:131], v205, s[44:47], s59 offen sc1
v_cvt_pk_bf16_f32 v160, v160, v161
v_cvt_pk_bf16_f32 v161, v162, v163
v_cvt_pk_bf16_f32 v162, v176, v177
v_cvt_pk_bf16_f32 v163, v178, v179
s_add_u32 s59, s58, 4096
buffer_store_dwordx4 v[160:163], v205, s[44:47], s59 offen offset:1024 sc1
v_cvt_pk_bf16_f32 v4, v4, v5
v_cvt_pk_bf16_f32 v5, v6, v7
v_cvt_pk_bf16_f32 v6, v20, v21
v_cvt_pk_bf16_f32 v7, v22, v23
s_add_u32 s59, s58, 4096
buffer_store_dwordx4 v[4:7], v205, s[44:47], s59 offen offset:2048 sc1
v_cvt_pk_bf16_f32 v36, v36, v37
v_cvt_pk_bf16_f32 v37, v38, v39
v_cvt_pk_bf16_f32 v38, v52, v53
v_cvt_pk_bf16_f32 v39, v54, v55
s_add_u32 s59, s58, 4096
buffer_store_dwordx4 v[36:39], v205, s[44:47], s59 offen offset:3072 sc1
v_cvt_pk_bf16_f32 v68, v68, v69
v_cvt_pk_bf16_f32 v69, v70, v71
v_cvt_pk_bf16_f32 v70, v84, v85
v_cvt_pk_bf16_f32 v71, v86, v87
s_add_u32 s59, s58, 8192
buffer_store_dwordx4 v[68:71], v205, s[44:47], s59 offen sc1
v_cvt_pk_bf16_f32 v100, v100, v101
v_cvt_pk_bf16_f32 v101, v102, v103
v_cvt_pk_bf16_f32 v102, v116, v117
v_cvt_pk_bf16_f32 v103, v118, v119
s_add_u32 s59, s58, 8192
buffer_store_dwordx4 v[100:103], v205, s[44:47], s59 offen offset:1024 sc1
v_cvt_pk_bf16_f32 v132, v132, v133
v_cvt_pk_bf16_f32 v133, v134, v135
v_cvt_pk_bf16_f32 v134, v148, v149
v_cvt_pk_bf16_f32 v135, v150, v151
s_add_u32 s59, s58, 8192
buffer_store_dwordx4 v[132:135], v205, s[44:47], s59 offen offset:2048 sc1
v_cvt_pk_bf16_f32 v164, v164, v165
v_cvt_pk_bf16_f32 v165, v166, v167
s_add_u32 s59, s58, 8192
buffer_store_dwordx4 v[164:167], v205, s[44:47], s59 offen offset:3072 sc1
v_cvt_pk_bf16_f32 v8, v8, v9
v_cvt_pk_bf16_f32 v9, v10, v11
v_cvt_pk_bf16_f32 v10, v24, v25
v_cvt_pk_bf16_f32 v11, v26, v27
s_add_u32 s59, s58, 12288
buffer_store_dwordx4 v[8:11], v205, s[44:47], s59 offen sc1
v_cvt_pk_bf16_f32 v40, v40, v41
v_cvt_pk_bf16_f32 v41, v42, v43
v_cvt_pk_bf16_f32 v42, v56, v57
v_cvt_pk_bf16_f32 v43, v58, v59
s_add_u32 s59, s58, 12288
buffer_store_dwordx4 v[40:43], v205, s[44:47], s59 offen offset:1024 sc1
v_cvt_pk_bf16_f32 v72, v72, v73
v_cvt_pk_bf16_f32 v73, v74, v75
v_cvt_pk_bf16_f32 v74, v88, v89
v_cvt_pk_bf16_f32 v75, v90, v91
s_add_u32 s59, s58, 12288
buffer_store_dwordx4 v[72:75], v205, s[44:47], s59 offen offset:2048 sc1
v_cvt_pk_bf16_f32 v104, v104, v105
v_cvt_pk_bf16_f32 v105, v106, v107
v_cvt_pk_bf16_f32 v106, v120, v121
v_cvt_pk_bf16_f32 v107, v122, v123
s_add_u32 s59, s58, 12288
buffer_store_dwordx4 v[104:107], v205, s[44:47], s59 offen offset:3072 sc1
v_cvt_pk_bf16_f32 v136, v136, v137
v_cvt_pk_bf16_f32 v137, v138, v139
v_cvt_pk_bf16_f32 v138, v152, v153
v_cvt_pk_bf16_f32 v139, v154, v155
s_add_u32 s59, s58, 16384
buffer_store_dwordx4 v[136:139], v205, s[44:47], s59 offen sc1
v_cvt_pk_bf16_f32 v168, v168, v169
v_cvt_pk_bf16_f32 v169, v170, v171
s_add_u32 s59, s58, 16384
buffer_store_dwordx4 v[168:171], v205, s[44:47], s59 offen offset:1024 sc1
s_waitcnt vmcnt(0)
s_barrier
s_cmp_lg_u32 s19, 0
s_cbranch_scc1 K2_w3_skip
v_mov_b32 v208, 0
v_mov_b32 v209, 1
s_mov_b64 exec, 1
global_atomic_add v208, v209, s[74:75] sc1
s_mov_b32 s60, 0
K2_w3_poll:
global_load_dword v210, v208, s[74:75] sc1
s_waitcnt vmcnt(0)
v_readfirstlane_b32 s61, v210
s_add_u32 s60, s60, 1
s_cmp_ge_u32 s61, 4
s_cbranch_scc1 K2_w3_done
s_sleep 1
s_cmp_lt_u32 s60, 131072
s_cbranch_scc1 K2_w3_poll
K2_w3_done:
s_mov_b64 exec, -1
K2_w3_skip:
s_barrier
s_add_u32 s58, s71, 0
s_mul_i32 s58, s58, 24576
s_add_u32 s59, s58, 16384
buffer_load_dwordx4 v[0:3], v205, s[44:47], s59 offen offset:2048 sc1
s_add_u32 s59, s58, 16384
buffer_load_dwordx4 v[4:7], v205, s[44:47], s59 offen offset:3072 sc1
s_add_u32 s59, s58, 20480
buffer_load_dwordx4 v[8:11], v205, s[44:47], s59 offen offset:0 sc1
s_add_u32 s59, s58, 20480
buffer_load_dwordx4 v[16:19], v205, s[44:47], s59 offen offset:1024 sc1
s_add_u32 s59, s58, 20480
buffer_load_dwordx4 v[20:23], v205, s[44:47], s59 offen offset:2048 sc1
s_add_u32 s59, s58, 20480
buffer_load_dwordx4 v[24:27], v205, s[44:47], s59 offen offset:3072 sc1
s_add_u32 s58, s71, 4
s_mul_i32 s58, s58, 24576
s_add_u32 s59, s58, 16384
buffer_load_dwordx4 v[32:35], v205, s[44:47], s59 offen offset:2048 sc1
s_add_u32 s59, s58, 16384
buffer_load_dwordx4 v[36:39], v205, s[44:47], s59 offen offset:3072 sc1
s_add_u32 s59, s58, 20480
buffer_load_dwordx4 v[40:43], v205, s[44:47], s59 offen offset:0 sc1
s_add_u32 s59, s58, 20480
buffer_load_dwordx4 v[48:51], v205, s[44:47], s59 offen offset:1024 sc1
s_add_u32 s59, s58, 20480
buffer_load_dwordx4 v[52:55], v205, s[44:47], s59 offen offset:2048 sc1
s_add_u32 s59, s58, 20480
buffer_load_dwordx4 v[56:59], v205, s[44:47], s59 offen offset:3072 sc1
s_add_u32 s58, s71, 8
s_mul_i32 s58, s58, 24576
s_add_u32 s59, s58, 16384
buffer_load_dwordx4 v[64:67], v205, s[44:47], s59 offen offset:2048 sc1
s_add_u32 s59, s58, 16384
buffer_load_dwordx4 v[68:71], v205, s[44:47], s59 offen offset:3072 sc1
s_add_u32 s59, s58, 20480
buffer_load_dwordx4 v[72:75], v205, s[44:47], s59 offen offset:0 sc1
s_add_u32 s59, s58, 20480
buffer_load_dwordx4 v[80:83], v205, s[44:47], s59 offen offset:1024 sc1
s_add_u32 s59, s58, 20480
buffer_load_dwordx4 v[84:87], v205, s[44:47], s59 offen offset:2048 sc1
s_add_u32 s59, s58, 20480
buffer_load_dwordx4 v[88:91], v205, s[44:47], s59 offen offset:3072 sc1
s_waitcnt vmcnt(0)
v_lshlrev_b32 v212, 16, v0
v_and_b32 v213, 4294901760, v0
v_lshlrev_b32 v214, 16, v1
v_and_b32 v215, 4294901760, v1
v_pk_add_f32 v[12:13], v[12:13], v[212:213]
v_pk_add_f32 v[14:15], v[14:15], v[214:215]
v_lshlrev_b32 v216, 16, v32
v_and_b32 v217, 4294901760, v32
v_lshlrev_b32 v218, 16, v33
v_and_b32 v219, 4294901760, v33
v_pk_add_f32 v[12:13], v[12:13], v[216:217]
v_pk_add_f32 v[14:15], v[14:15], v[218:219]
v_lshlrev_b32 v220, 16, v64
v_and_b32 v221, 4294901760, v64
v_lshlrev_b32 v222, 16, v65
v_and_b32 v223, 4294901760, v65
v_pk_add_f32 v[12:13], v[12:13], v[220:221]
v_pk_add_f32 v[14:15], v[14:15], v[222:223]
v_cvt_pk_bf16_f32 v12, v12, v13
v_cvt_pk_bf16_f32 v13, v14, v15
s_add_u32 s59, s72, 96
buffer_store_dwordx2 v[12:13], v206, s[48:51], s59 offen
v_lshlrev_b32 v224, 16, v2
v_and_b32 v225, 4294901760, v2
v_lshlrev_b32 v226, 16, v3
v_and_b32 v227, 4294901760, v3
v_pk_add_f32 v[28:29], v[28:29], v[224:225]
v_pk_add_f32 v[30:31], v[30:31], v[226:227]
v_lshlrev_b32 v212, 16, v34
v_and_b32 v213, 4294901760, v34
v_lshlrev_b32 v214, 16, v35
v_and_b32 v215, 4294901760, v35
v_pk_add_f32 v[28:29], v[28:29], v[212:213]
v_pk_add_f32 v[30:31], v[30:31], v[214:215]
v_lshlrev_b32 v216, 16, v66
v_and_b32 v217, 4294901760, v66
v_lshlrev_b32 v218, 16, v67
v_and_b32 v219, 4294901760, v67
v_pk_add_f32 v[28:29], v[28:29], v[216:217]
v_pk_add_f32 v[30:31], v[30:31], v[218:219]
v_cvt_pk_bf16_f32 v28, v28, v29
v_cvt_pk_bf16_f32 v29, v30, v31
s_add_u32 s59, s72, 41024
buffer_store_dwordx2 v[28:29], v206, s[48:51], s59 offen
v_lshlrev_b32 v220, 16, v4
v_and_b32 v221, 4294901760, v4
v_lshlrev_b32 v222, 16, v5
v_and_b32 v223, 4294901760, v5
v_pk_add_f32 v[44:45], v[44:45], v[220:221]
v_pk_add_f32 v[46:47], v[46:47], v[222:223]
v_lshlrev_b32 v224, 16, v36
v_and_b32 v225, 4294901760, v36
v_lshlrev_b32 v226, 16, v37
v_and_b32 v227, 4294901760, v37
v_pk_add_f32 v[44:45], v[44:45], v[224:225]
v_pk_add_f32 v[46:47], v[46:47], v[226:227]
v_lshlrev_b32 v212, 16, v68
v_and_b32 v213, 4294901760, v68
v_lshlrev_b32 v214, 16, v69
v_and_b32 v215, 4294901760, v69
v_pk_add_f32 v[44:45], v[44:45], v[212:213]
v_pk_add_f32 v[46:47], v[46:47], v[214:215]
v_cvt_pk_bf16_f32 v44, v44, v45
v_cvt_pk_bf16_f32 v45, v46, v47
s_add_u32 s59, s72, 81952
buffer_store_dwordx2 v[44:45], v206, s[48:51], s59 offen
v_lshlrev_b32 v216, 16, v6
v_and_b32 v217, 4294901760, v6
v_lshlrev_b32 v218, 16, v7
v_and_b32 v219, 4294901760, v7
v_pk_add_f32 v[60:61], v[60:61], v[216:217]
v_pk_add_f32 v[62:63], v[62:63], v[218:219]
v_lshlrev_b32 v220, 16, v38
v_and_b32 v221, 4294901760, v38
v_lshlrev_b32 v222, 16, v39
v_and_b32 v223, 4294901760, v39
v_pk_add_f32 v[60:61], v[60:61], v[220:221]
v_pk_add_f32 v[62:63], v[62:63], v[222:223]
v_lshlrev_b32 v224, 16, v70
v_and_b32 v225, 4294901760, v70
v_lshlrev_b32 v226, 16, v71
v_and_b32 v227, 4294901760, v71
v_pk_add_f32 v[60:61], v[60:61], v[224:225]
v_pk_add_f32 v[62:63], v[62:63], v[226:227]
v_cvt_pk_bf16_f32 v60, v60, v61
v_cvt_pk_bf16_f32 v61, v62, v63
s_add_u32 s59, s72, 122880
buffer_store_dwordx2 v[60:61], v206, s[48:51], s59 offen
v_lshlrev_b32 v212, 16, v8
v_and_b32 v213, 4294901760, v8
v_lshlrev_b32 v214, 16, v9
v_and_b32 v215, 4294901760, v9
v_pk_add_f32 v[76:77], v[76:77], v[212:213]
v_pk_add_f32 v[78:79], v[78:79], v[214:215]
v_lshlrev_b32 v216, 16, v40
v_and_b32 v217, 4294901760, v40
v_lshlrev_b32 v218, 16, v41
v_and_b32 v219, 4294901760, v41
v_pk_add_f32 v[76:77], v[76:77], v[216:217]
v_pk_add_f32 v[78:79], v[78:79], v[218:219]
v_lshlrev_b32 v220, 16, v72
v_and_b32 v221, 4294901760, v72
v_lshlrev_b32 v222, 16, v73
v_and_b32 v223, 4294901760, v73
v_pk_add_f32 v[76:77], v[76:77], v[220:221]
v_pk_add_f32 v[78:79], v[78:79], v[222:223]
v_cvt_pk_bf16_f32 v76, v76, v77
v_cvt_pk_bf16_f32 v77, v78, v79
s_add_u32 s59, s72, 123008
buffer_store_dwordx2 v[76:77], v206, s[48:51], s59 offen
v_lshlrev_b32 v224, 16, v10
v_and_b32 v225, 4294901760, v10
v_lshlrev_b32 v226, 16, v11
v_and_b32 v227, 4294901760, v11
v_pk_add_f32 v[92:93], v[92:93], v[224:225]
v_pk_add_f32 v[94:95], v[94:95], v[226:227]
v_lshlrev_b32 v212, 16, v42
v_and_b32 v213, 4294901760, v42
v_lshlrev_b32 v214, 16, v43
v_and_b32 v215, 4294901760, v43
v_pk_add_f32 v[92:93], v[92:93], v[212:213]
v_pk_add_f32 v[94:95], v[94:95], v[214:215]
v_lshlrev_b32 v216, 16, v74
v_and_b32 v217, 4294901760, v74
v_lshlrev_b32 v218, 16, v75
v_and_b32 v219, 4294901760, v75
v_pk_add_f32 v[92:93], v[92:93], v[216:217]
v_pk_add_f32 v[94:95], v[94:95], v[218:219]
v_cvt_pk_bf16_f32 v92, v92, v93
v_cvt_pk_bf16_f32 v93, v94, v95
s_add_u32 s59, s72, 163936
buffer_store_dwordx2 v[92:93], v206, s[48:51], s59 offen
v_lshlrev_b32 v220, 16, v16
v_and_b32 v221, 4294901760, v16
v_lshlrev_b32 v222, 16, v17
v_and_b32 v223, 4294901760, v17
v_pk_add_f32 v[108:109], v[108:109], v[220:221]
v_pk_add_f32 v[110:111], v[110:111], v[222:223]
v_lshlrev_b32 v224, 16, v48
v_and_b32 v225, 4294901760, v48
v_lshlrev_b32 v226, 16, v49
v_and_b32 v227, 4294901760, v49
v_pk_add_f32 v[108:109], v[108:109], v[224:225]
v_pk_add_f32 v[110:111], v[110:111], v[226:227]
v_lshlrev_b32 v212, 16, v80
v_and_b32 v213, 4294901760, v80
v_lshlrev_b32 v214, 16, v81
v_and_b32 v215, 4294901760, v81
v_pk_add_f32 v[108:109], v[108:109], v[212:213]
v_pk_add_f32 v[110:111], v[110:111], v[214:215]
v_cvt_pk_bf16_f32 v108, v108, v109
v_cvt_pk_bf16_f32 v109, v110, v111
s_add_u32 s59, s72, 204864
buffer_store_dwordx2 v[108:109], v206, s[48:51], s59 offen
v_lshlrev_b32 v216, 16, v18
v_and_b32 v217, 4294901760, v18
v_lshlrev_b32 v218, 16, v19
v_and_b32 v219, 4294901760, v19
v_pk_add_f32 v[124:125], v[124:125], v[216:217]
v_pk_add_f32 v[126:127], v[126:127], v[218:219]
v_lshlrev_b32 v220, 16, v50
v_and_b32 v221, 4294901760, v50
v_lshlrev_b32 v222, 16, v51
v_and_b32 v223, 4294901760, v51
v_pk_add_f32 v[124:125], v[124:125], v[220:221]
v_pk_add_f32 v[126:127], v[126:127], v[222:223]
v_lshlrev_b32 v224, 16, v82
v_and_b32 v225, 4294901760, v82
v_lshlrev_b32 v226, 16, v83
v_and_b32 v227, 4294901760, v83
v_pk_add_f32 v[124:125], v[124:125], v[224:225]
v_pk_add_f32 v[126:127], v[126:127], v[226:227]
v_cvt_pk_bf16_f32 v124, v124, v125
v_cvt_pk_bf16_f32 v125, v126, v127
s_add_u32 s59, s72, 245792
buffer_store_dwordx2 v[124:125], v206, s[48:51], s59 offen
v_lshlrev_b32 v212, 16, v20
v_and_b32 v213, 4294901760, v20
v_lshlrev_b32 v214, 16, v21
v_and_b32 v215, 4294901760, v21
v_pk_add_f32 v[140:141], v[140:141], v[212:213]
v_pk_add_f32 v[142:143], v[142:143], v[214:215]
v_lshlrev_b32 v216, 16, v52
v_and_b32 v217, 4294901760, v52
v_lshlrev_b32 v218, 16, v53
v_and_b32 v219, 4294901760, v53
v_pk_add_f32 v[140:141], v[140:141], v[216:217]
v_pk_add_f32 v[142:143], v[142:143], v[218:219]
v_lshlrev_b32 v220, 16, v84
v_and_b32 v221, 4294901760, v84
v_lshlrev_b32 v222, 16, v85
v_and_b32 v223, 4294901760, v85
v_pk_add_f32 v[140:141], v[140:141], v[220:221]
v_pk_add_f32 v[142:143], v[142:143], v[222:223]
v_cvt_pk_bf16_f32 v140, v140, v141
v_cvt_pk_bf16_f32 v141, v142, v143
s_add_u32 s59, s72, 286720
buffer_store_dwordx2 v[140:141], v206, s[48:51], s59 offen
v_lshlrev_b32 v224, 16, v22
v_and_b32 v225, 4294901760, v22
v_lshlrev_b32 v226, 16, v23
v_and_b32 v227, 4294901760, v23
v_pk_add_f32 v[156:157], v[156:157], v[224:225]
v_pk_add_f32 v[158:159], v[158:159], v[226:227]
v_lshlrev_b32 v212, 16, v54
v_and_b32 v213, 4294901760, v54
v_lshlrev_b32 v214, 16, v55
v_and_b32 v215, 4294901760, v55
v_pk_add_f32 v[156:157], v[156:157], v[212:213]
v_pk_add_f32 v[158:159], v[158:159], v[214:215]
v_lshlrev_b32 v216, 16, v86
v_and_b32 v217, 4294901760, v86
v_lshlrev_b32 v218, 16, v87
v_and_b32 v219, 4294901760, v87
v_pk_add_f32 v[156:157], v[156:157], v[216:217]
v_pk_add_f32 v[158:159], v[158:159], v[218:219]
v_cvt_pk_bf16_f32 v156, v156, v157
v_cvt_pk_bf16_f32 v157, v158, v159
s_add_u32 s59, s72, 286848
buffer_store_dwordx2 v[156:157], v206, s[48:51], s59 offen
v_lshlrev_b32 v220, 16, v24
v_and_b32 v221, 4294901760, v24
v_lshlrev_b32 v222, 16, v25
v_and_b32 v223, 4294901760, v25
v_pk_add_f32 v[172:173], v[172:173], v[220:221]
v_pk_add_f32 v[174:175], v[174:175], v[222:223]
v_lshlrev_b32 v224, 16, v56
v_and_b32 v225, 4294901760, v56
v_lshlrev_b32 v226, 16, v57
v_and_b32 v227, 4294901760, v57
v_pk_add_f32 v[172:173], v[172:173], v[224:225]
v_pk_add_f32 v[174:175], v[174:175], v[226:227]
v_lshlrev_b32 v212, 16, v88
v_and_b32 v213, 4294901760, v88
v_lshlrev_b32 v214, 16, v89
v_and_b32 v215, 4294901760, v89
v_pk_add_f32 v[172:173], v[172:173], v[212:213]
v_pk_add_f32 v[174:175], v[174:175], v[214:215]
v_cvt_pk_bf16_f32 v172, v172, v173
v_cvt_pk_bf16_f32 v173, v174, v175
s_add_u32 s59, s72, 327776
buffer_store_dwordx2 v[172:173], v206, s[48:51], s59 offen
s_waitcnt vmcnt(0)
s_branch K2_end
K2_end:
)ASM"
    : "+{v0}"(tid)
    : "{s[16:17]}"(__builtin_amdgcn_kernarg_segment_ptr()), "{s18}"(blockIdx.x)
    : "v1", "v2", "v3", "v4", "v5", "v6", "v7", "v8", "v9", "v10", "v11", "v12", "v13", "v14", "v15", "v16", "v17", "v18", "v19", "v20", "v21", "v22", "v23", "v24", "v25", "v26", "v27", "v28", "v29", "v30", "v31", "v32", "v33", "v34", "v35", "v36", "v37", "v38", "v39", "v40", "v41", "v42", "v43", "v44", "v45", "v46", "v47", "v48", "v49", "v50", "v51", "v52", "v53", "v54", "v55", "v56", "v57", "v58", "v59", "v60", "v61", "v62", "v63", "v64", "v65", "v66", "v67", "v68", "v69", "v70", "v71", "v72", "v73", "v74", "v75", "v76", "v77", "v78", "v79", "v80", "v81", "v82", "v83", "v84", "v85", "v86", "v87", "v88", "v89", "v90", "v91", "v92", "v93", "v94", "v95", "v96", "v97", "v98", "v99", "v100", "v101", "v102", "v103", "v104", "v105", "v106", "v107", "v108", "v109", "v110", "v111", "v112", "v113", "v114", "v115", "v116", "v117", "v118", "v119", "v120", "v121", "v122", "v123", "v124", "v125", "v126", "v127", "v128", "v129", "v130", "v131", "v132", "v133", "v134", "v135", "v136", "v137", "v138", "v139", "v140", "v141", "v142", "v143", "v144", "v145", "v146", "v147", "v148", "v149", "v150", "v151", "v152", "v153", "v154", "v155", "v156", "v157", "v158", "v159", "v160", "v161", "v162", "v163", "v164", "v165", "v166", "v167", "v168", "v169", "v170", "v171", "v172", "v173", "v174", "v175", "v176", "v177", "v178", "v179", "v180", "v181", "v182", "v183", "v184", "v185", "v186", "v187", "v188", "v189", "v190", "v191", "v192", "v193", "v194", "v195", "v196", "v197", "v198", "v199", "v200", "v201", "v202", "v203", "v204", "v205", "v206", "v207", "v208", "v209", "v210", "v211", "v212", "v213", "v214", "v215", "v216", "v217", "v218", "v219", "v220", "v221", "v222", "v223", "v224", "v225", "v226", "v227", "v228", "v229", "v230", "v231", "v232", "v233", "v234", "v235", "v236", "v237", "v238", "v239", "v240", "v241", "v242", "v243", "v244", "v245", "v246", "v247", "v248", "v249", "v250", "v251", "v252", "v253", "v254", "v255", "a0", "a1", "a2", "a3", "a4", "a5", "a6", "a7", "a8", "a9", "a10", "a11", "a12", "a13", "a14", "a15", "a16", "a17", "a18", "a19", "a20", "a21", "a22", "a23", "a24", "a25", "a26", "a27", "a28", "a29", "a30", "a31", "a32", "a33", "a34", "a35", "a36", "a37", "a38", "a39", "a40", "a41", "a42", "a43", "a44", "a45", "a46", "a47", "a48", "a49", "a50", "a51", "a52", "a53", "a54", "a55", "a56", "a57", "a58", "a59", "a60", "a61", "a62", "a63", "a64", "a65", "a66", "a67", "a68", "a69", "a70", "a71", "a72", "a73", "a74", "a75", "a76", "a77", "a78", "a79", "a80", "a81", "a82", "a83", "a84", "a85", "a86", "a87", "a88", "a89", "a90", "a91", "a92", "a93", "a94", "a95", "a96", "a97", "a98", "a99", "a100", "a101", "a102", "a103", "a104", "a105", "a106", "a107", "a108", "a109", "a110", "a111", "a112", "a113", "a114", "a115", "a116", "a117", "a118", "a119", "a120", "a121", "a122", "a123", "a124", "a125", "a126", "a127", "a128", "a129", "a130", "a131", "a132", "a133", "a134", "a135", "a136", "a137", "a138", "a139", "a140", "a141", "a142", "a143", "a144", "a145", "a146", "a147", "a148", "a149", "a150", "a151", "a152", "a153", "a154", "a155", "a156", "a157", "a158", "a159", "a160", "a161", "a162", "a163", "a164", "a165", "a166", "a167", "a168", "a169", "a170", "a171", "a172", "a173", "a174", "a175", "a176", "a177", "a178", "a179", "a180", "a181", "a182", "a183", "a184", "a185", "a186", "a187", "a188", "a189", "a190", "a191", "a192", "a193", "a194", "a195", "a196", "a197", "a198", "a199", "a200", "a201", "a202", "a203", "a204", "a205", "a206", "a207", "a208", "a209", "a210", "a211", "a212", "a213", "a214", "a215", "a216", "a217", "a218", "a219", "a220", "a221", "a222", "a223", "a224", "a225", "a226", "a227", "a228", "a229", "a230", "a231", "a232", "a233", "a234", "a235", "a236", "a237", "a238", "a239", "a240", "a241", "a242", "a243", "a244", "a245", "a246", "a247", "a248", "a249", "a250", "a251", "a252", "a253", "a254", "a255", "s4", "s5", "s6", "s7", "s8", "s9", "s10", "s11", "s12", "s13", "s14", "s15", "s19", "s20", "s21", "s22", "s23", "s24", "s25", "s26", "s27", "s28", "s29", "s30", "s31", "s32", "s33", "s34", "s35", "s36", "s37", "s38", "s39", "s40", "s41", "s42", "s43", "s44", "s45", "s46", "s47", "s48", "s49", "s50", "s51", "s52", "s53", "s54", "s55", "s56", "s57", "s58", "s59", "s60", "s61", "s62", "s63", "s64", "s65", "s66", "s67", "s68", "s69", "s70", "s71", "s72", "s73", "s74", "s75", "s76", "s77", "s78", "s79", "s80", "s81", "s82", "s83", "s84", "s85", "s86", "s87", "s88", "s89", "s90", "s91", "s92", "s93", "s94", "s95", "s96", "s97", "s98", "s99", "vcc", "scc", "memory");
}

extern "C" void kernel_launch(void* const* d_in, const int* in_sizes, int n_in, void* d_out, int out_size, void* d_ws,
                              size_t ws_size, hipStream_t stream) {
  (void)in_sizes; (void)n_in; (void)out_size; (void)ws_size;
  (void)hipFuncSetAttribute((const void*)k1_attn, hipFuncAttributeMaxDynamicSharedMemorySize, 163840);
  (void)hipFuncSetAttribute((const void*)k2_oproj, hipFuncAttributeMaxDynamicSharedMemorySize, 163840);
  k1_attn<<<256, 256, 163840, stream>>>(d_in[0], d_in[1], d_in[2], d_in[3], d_in[5], d_in[6], d_in[7], d_in[8], d_ws);
  k2_oproj<<<256, 256, 163840, stream>>>(d_ws, d_in[4], d_in[9], d_out);
}
```
